```python
import torch

def _softmax(x, axis=-1):
    return torch.softmax(x, dim=axis)

def _gelu(x):
    return torch.nn.functional.gelu(x, approximate='tanh')

def _silu(x):
    return torch.nn.functional.silu(x)

def _sigmoid(x):
    return torch.sigmoid(x)

def _layernorm(x, axis=-1, eps=1e-05):
    m = x.mean(dim=axis, keepdim=True)
    v = x.var(dim=axis, keepdim=True, unbiased=False)
    return (x - m) / torch.sqrt(v + eps)

def _scatter_add(tgt, idx, src, axis=0):
    return tgt.clone().index_add_(axis, idx, src)

def _segment_sum(src, seg, n):
    out = torch.zeros((n,) + src.shape[1:], dtype=src.dtype)
    return out.index_add_(0, seg, src)

def _pad(x, pw):
    flat = tuple((v for pair in reversed(pw) for v in pair))
    return torch.nn.functional.pad(x, flat)
kernel_name = 'adaLN_cond__mha_block_cached__glu_gated_geglu__prompt'

def setup_inputs():

    _w = lambda a: (lambda t: t.to(torch.bfloat16) if t.is_floating_point() else t)(torch.as_tensor(a))

    def _A_setup():
        B, Sq, D = (1, 2048, 448)
        H, Sk, Dh = (16, 2048, 256)
        return {'x': _w((lambda *s: torch.randn(*s, dtype=torch.bfloat16) * 0.05)(B, Sq, D)), 'k_cache': _w((lambda *s: torch.randn(*s, dtype=torch.bfloat16) * 0.8)(B, H, Sk, Dh)), 'v_cache': _w((lambda *s: torch.randn(*s, dtype=torch.bfloat16) * 3.2)(B, H, Sk, Dh)), 'wq': _w((lambda *s: torch.randn(*s, dtype=torch.bfloat16) * 0.8)(D, H * Dh) * D ** (-0.5)), 'wo': _w((lambda *s: torch.randn(*s, dtype=torch.bfloat16) * 3.2)(H * Dh, D) * (H * Dh) ** (-0.5))}
    _A_in = _A_setup()

    def _B_setup():
        B, S, Dm, Df = (1, 2048, 448, 1792)
        return {'x': _w((lambda *s: torch.randn(*s, dtype=torch.bfloat16) * 0.05)(B, S, Dm)), 'w_gate': _w((lambda *s: torch.randn(*s, dtype=torch.bfloat16) * 3.2)(Dm, Df) * Dm ** (-0.5)), 'w_up': _w((lambda *s: torch.randn(*s, dtype=torch.bfloat16) * 3.2)(Dm, Df) * Dm ** (-0.5)), 'w_down': _w((lambda *s: torch.randn(*s, dtype=torch.bfloat16) * 3.2)(Df, Dm) * Df ** (-0.5))}
    _B_in = _B_setup()
    return {'A_x': _A_in['x'], 'A_k_cache': _A_in['k_cache'], 'A_v_cache': _A_in['v_cache'], 'A_wq': _A_in['wq'], 'A_wo': _A_in['wo'], 'B_x': _B_in['x'], 'B_w_gate': _B_in['w_gate'], 'B_w_up': _B_in['w_up'], 'B_w_down': _B_in['w_down'], 'c': _w((lambda *s: torch.randn(*s, dtype=torch.bfloat16) * 0.4)(1, 448) * 8.0), 'w_mod': _w((lambda *s: torch.randn(*s, dtype=torch.bfloat16) * 0.4)(448, 6 * 448) * 448 ** (-0.5) * 8.0)}

def reference(A_x, A_k_cache, A_v_cache, A_wq, A_wo, B_x, B_w_gate, B_w_up, B_w_down, c, w_mod):

    def _A_ref(x, k_cache, v_cache, wq, wo):
        B, Sq, D = x.shape
        H, Dh = (16, 256)
        q = (lambda x, *d: x.permute(*d))((x @ wq).reshape(B, Sq, H, Dh), 0, 2, 1, 3)
        scores = torch.einsum('bhqd,bhkd->bhqk', q, k_cache) * Dh ** (-0.5)
        p = _softmax(scores, axis=-1)
        o = torch.einsum('bhqk,bhkd->bhqd', p, v_cache)
        return (lambda x, *d: x.permute(*d))(o, 0, 2, 1, 3).reshape(B, Sq, H * Dh) @ wo

    def _B_ref(x, w_gate, w_up, w_down):
        g = torch.einsum('bsd,df->bsf', x, w_gate)
        u = torch.einsum('bsd,df->bsf', x, w_up)
        h = _gelu(g) * u
        return torch.einsum('bsf,fd->bsd', h, w_down)
    x = A_x
    mod = (_silu(c) @ w_mod).reshape(c.shape[0], 6, 1, -1)
    sa, ca, ga, sb, cb, gb = (mod[:, 0].clone(), mod[:, 1].clone(), mod[:, 2].clone(), mod[:, 3].clone(), mod[:, 4].clone(), mod[:, 5].clone())
    h = _layernorm(x, axis=-1) * (1 + ca) + sa
    x = x + ga * _A_ref(h, A_k_cache, A_v_cache, A_wq, A_wo)
    h = _layernorm(x, axis=-1) * (1 + cb) + sb
    x = x + gb * _B_ref(h, B_w_gate, B_w_up, B_w_down)
    return x
```

```cpp
#include <hip/hip_runtime.h>
#include <hip/hip_bf16.h>
#include <stdint.h>

typedef unsigned short u16;
static __device__ __forceinline__ float bf2f(u16 v) { return __uint_as_float(((unsigned)v) << 16); }
static __device__ __forceinline__ u16 f2bf(float f) { __bf16 b = (__bf16)f; return __builtin_bit_cast(u16, b); }

constexpr int S = 2048, D = 448, H = 16, DH = 256, DF = 1792;

__global__ void k_mod(const u16* c, const u16* wmod, float* mod) {
  int n = blockIdx.x * 64 + threadIdx.x;
  if (n >= 6 * D) return;
  float acc = 0.f;
  for (int k = 0; k < D; ++k) { float cv = bf2f(c[k]); float s = cv / (1.f + __expf(-cv)); s = bf2f(f2bf(s)); acc += s * bf2f(wmod[k * 6 * D + n]); }
  mod[n] = bf2f(f2bf(acc));
}
__global__ void k_ln(const u16* x, const float* shift, const float* scale, u16* h) {
  int row = blockIdx.x; int l = threadIdx.x;
  float v[7]; float s = 0;
  for (int i = 0; i < 7; ++i) { v[i] = bf2f(x[row * D + l + 64 * i]); s += v[i]; }
  for (int o = 32; o; o >>= 1) s += __shfl_xor(s, o);
  float m = s / D; float q = 0;
  for (int i = 0; i < 7; ++i) { float d = v[i] - m; q += d * d; }
  for (int o = 32; o; o >>= 1) q += __shfl_xor(q, o);
  float r = rsqrtf(q / D + 1e-5f);
  for (int i = 0; i < 7; ++i) { int cidx = l + 64 * i; h[row * D + cidx] = f2bf((v[i] - m) * r * (1.f + scale[cidx]) + shift[cidx]); }
}
__global__ void k_gemm(const u16* A, const u16* B, float* C, int M, int N, int K) {
  __shared__ float As[16][65], Bs[16][65];
  int tx = threadIdx.x & 15, ty = threadIdx.x >> 4;
  int m0 = blockIdx.y * 64, n0 = blockIdx.x * 64;
  float acc[4][4] = {};
  for (int k0 = 0; k0 < K; k0 += 16) {
    for (int i = threadIdx.x; i < 1024; i += 256) { int r = i >> 4, kk = i & 15; As[kk][r] = bf2f(A[(size_t)(m0 + r) * K + k0 + kk]); }
    for (int i = threadIdx.x; i < 1024; i += 256) { int kk = i >> 6, cc = i & 63; Bs[kk][cc] = bf2f(B[(size_t)(k0 + kk) * N + n0 + cc]); }
    __syncthreads();
    for (int kk = 0; kk < 16; ++kk) {
      float a[4], b[4];
      for (int i = 0; i < 4; ++i) { a[i] = As[kk][ty * 4 + i]; b[i] = Bs[kk][tx * 4 + i]; }
      for (int i = 0; i < 4; ++i) for (int j = 0; j < 4; ++j) acc[i][j] += a[i] * b[j];
    }
    __syncthreads();
  }
  for (int i = 0; i < 4; ++i) for (int j = 0; j < 4; ++j) C[(size_t)(m0 + ty * 4 + i) * N + n0 + tx * 4 + j] = acc[i][j];
}
__global__ void k_attn(const float* q, const u16* kc, const u16* vc, u16* o) {
  __shared__ float qs[8][DH]; __shared__ float p[8][S]; __shared__ float red[8][4];
  int h = blockIdx.y, r0 = blockIdx.x * 8, t = threadIdx.x;
  for (int i = t; i < 8 * DH; i += 256) qs[i >> 8][i & 255] = bf2f(f2bf(q[(size_t)(r0 + (i >> 8)) * (H * DH) + h * DH + (i & 255)]));
  __syncthreads();
  for (int k = t; k < S; k += 256) {
    float acc[8] = {};
    const u16* kr = kc + ((size_t)h * S + k) * DH;
    for (int d = 0; d < DH; ++d) { float kv = bf2f(kr[d]); for (int r = 0; r < 8; ++r) acc[r] += kv * qs[r][d]; }
    for (int r = 0; r < 8; ++r) p[r][k] = acc[r] * 0.0625f;
  }
  __syncthreads();
  for (int r = 0; r < 8; ++r) {
    float m = -1e30f; for (int k = t; k < S; k += 256) m = fmaxf(m, p[r][k]);
    for (int o2 = 32; o2; o2 >>= 1) m = fmaxf(m, __shfl_xor(m, o2));
    if ((t & 63) == 0) red[r][t >> 6] = m;
    __syncthreads();
    m = fmaxf(fmaxf(red[r][0], red[r][1]), fmaxf(red[r][2], red[r][3]));
    __syncthreads();
    float s = 0; for (int k = t; k < S; k += 256) { float e = __expf(p[r][k] - m); p[r][k] = e; s += e; }
    for (int o2 = 32; o2; o2 >>= 1) s += __shfl_xor(s, o2);
    if ((t & 63) == 0) red[r][t >> 6] = s;
    __syncthreads();
    s = red[r][0] + red[r][1] + red[r][2] + red[r][3];
    __syncthreads();
    for (int k = t; k < S; k += 256) p[r][k] = p[r][k] / s;
  }
  __syncthreads();
  float acc[8] = {};
  for (int k = 0; k < S; ++k) { float vv = bf2f(vc[((size_t)h * S + k) * DH + t]); for (int r = 0; r < 8; ++r) acc[r] += p[r][k] * vv; }
  for (int r = 0; r < 8; ++r) o[(size_t)(r0 + r) * (H * DH) + h * DH + t] = f2bf(acc[r]);
}
__global__ void k_res(const u16* x, const float* g, const float* y, u16* out) {
  int i = blockIdx.x * 256 + threadIdx.x;
  out[i] = f2bf(bf2f(x[i]) + g[i % D] * y[i]);
}
__global__ void k_act(const float* g, const float* u, u16* a) {
  int i = blockIdx.x * 256 + threadIdx.x;
  float x = g[i]; float t = tanhf(0.7978845608028654f * (x + 0.044715f * x * x * x));
  a[i] = f2bf(0.5f * x * (1.f + t) * u[i]);
}

extern "C" void kernel_launch(void* const* d_in, const int* in_sizes, int n_in, void* d_out, int out_size, void* d_ws, size_t ws_size, hipStream_t stream) {
  const u16* x = (const u16*)d_in[0]; const u16* kc = (const u16*)d_in[1]; const u16* vc = (const u16*)d_in[2];
  const u16* wq = (const u16*)d_in[3]; const u16* wo = (const u16*)d_in[4];
  const u16* wg = (const u16*)d_in[6]; const u16* wu = (const u16*)d_in[7]; const u16* wd = (const u16*)d_in[8];
  const u16* c = (const u16*)d_in[9]; const u16* wmod = (const u16*)d_in[10];
  char* ws = (char*)d_ws;
  float* mod = (float*)ws;
  u16* h = (u16*)(ws + (1 << 20));
  u16* x1 = (u16*)(ws + (4 << 20));
  float* q = (float*)(ws + (8 << 20));
  u16* o = (u16*)(ws + (48 << 20));
  float* y = (float*)(ws + (72 << 20));
  float* g = (float*)(ws + (80 << 20));
  float* u = (float*)(ws + (100 << 20));
  u16* a = (u16*)(ws + (120 << 20));
  k_mod<<<42, 64, 0, stream>>>(c, wmod, mod);
  k_ln<<<S, 64, 0, stream>>>(x, mod, mod + D, h);
  k_gemm<<<dim3(H * DH / 64, S / 64), 256, 0, stream>>>(h, wq, q, S, H * DH, D);
  k_attn<<<dim3(S / 8, H), 256, 0, stream>>>(q, kc, vc, o);
  k_gemm<<<dim3(D / 64, S / 64), 256, 0, stream>>>(o, wo, y, S, D, H * DH);
  k_res<<<S * D / 256, 256, 0, stream>>>(x, mod + 2 * D, y, x1);
  k_ln<<<S, 64, 0, stream>>>(x1, mod + 3 * D, mod + 4 * D, h);
  k_gemm<<<dim3(DF / 64, S / 64), 256, 0, stream>>>(h, wg, g, S, DF, D);
  k_gemm<<<dim3(DF / 64, S / 64), 256, 0, stream>>>(h, wu, u, S, DF, D);
  k_act<<<S * DF / 256, 256, 0, stream>>>(g, u, a);
  k_gemm<<<dim3(D / 64, S / 64), 256, 0, stream>>>(a, wd, y, S, D, DF);
  k_res<<<S * D / 256, 256, 0, stream>>>(x1, mod + 5 * D, y, (u16*)d_out);
}
```

```cpp
#include <hip/hip_runtime.h>

extern "C" __global__ __launch_bounds__(256, 1) void fused_block(const void* x, const void* kc, const void* vc, const void* wq, const void* wo,
    const void* wg, const void* wu, const void* wd, const void* c, const void* wm, void* ws, void* out) {
  unsigned w = __builtin_amdgcn_readfirstlane(threadIdx.x >> 6);
  unsigned b = blockIdx.x;
  asm volatile(
    "s_load_dwordx16 s[8:23], %0, 0\n"
    "s_load_dwordx8 s[24:31], %0, 64\n"
    "s_mov_b32 s50, %2\n"
    "s_and_b32 s51, %1, 15\n"
    "s_lshr_b32 s52, %1, 4\n"
    "v_mbcnt_lo_u32_b32 v250, -1, 0\n"
    "v_mbcnt_hi_u32_b32 v250, -1, v250\n"
    "s_getpc_b64 s[56:57]\n"
    "L_pc_%=:\n"
    "s_bfe_u32 s54, %1, 0x30003\n"
    "s_lshl_b32 s54, s54, 2\n"
    "s_add_u32 s54, s54, s50\n"
    "s_lshl_b32 s54, s54, 7\n"
    "v_min_u32 v1, 27, v250\n"
    "v_lshl_add_u32 v1, v1, 12, s54\n"
    "s_mov_b32 s54, L_cmb_%=_redo-L_pc_%=-4\n"
    "v_min_u32 v2, s54, v1\n"
    "global_load_dword a255, v2, s[56:57]\n"
    "v_bfe_u32 v0, v250, 2, 3\n"
    "s_lshl_b32 s54, s52, 7\n"
    "s_lshl_b32 s55, s50, 5\n"
    "s_add_u32 s54, s54, s55\n"
    "v_add_u32 v0, s54, v0\n"
    "v_mul_u32_u24 v0, 896, v0\n"
    "v_lshrrev_b32 v1, 5, v250\n"
    "v_lshl_add_u32 v0, v1, 6, v0\n"
    "v_bfe_u32 v1, v250, 4, 1\n"
    "v_sub_u32 v1, 0, v1\n"
    "v_xor_b32 v1, v1, v250\n"
    "v_and_b32 v1, 3, v1\n"
    "v_lshl_add_u32 v216, v1, 4, v0\n"
    "v_xor_b32 v217, 32, v216\n"
    "v_add_u32 v217, 7168, v217\n"
    "v_add_u32 v218, 14336, v216\n"
    "v_add_u32 v219, 14336, v217\n"
    "s_mul_i32 s49, s50, 28672\n"
    "s_cmp_lt_u32 s50, 2\n"
    "s_cselect_b32 s54, 0, 40960\n"
    "s_add_u32 s49, s49, s54\n"
    "v_lshrrev_b32 v4, 2, v250\n"
    "s_lshl_b32 s54, s50, 4\n"
    "v_add_u32 v4, s54, v4\n"
    "v_lshlrev_b32 v5, 1, v4\n"
    "v_mul_u32_u24 v4, 5376, v4\n"
    "v_and_b32 v6, 3, v250\n"
    "v_min_u32 v7, 2, v6\n"
    "v_lshl_add_u32 v7, v7, 7, v5\n"
    "v_add_u32 v7, 512, v7\n"
    "v_lshl_add_u32 v5, v6, 7, v5\n"
    "v_lshlrev_b32 v6, 4, v6\n"
    "s_lshl_b32 s54, %1, 6\n"
    "v_add3_u32 v4, v4, v6, s54\n"
    "s_and_b32 s54, s50, 1\n"
    "s_lshl_b32 s54, s54, 1\n"
    "v_and_b32 v0, 3, v250\n"
    "v_xor_b32 v0, s54, v0\n"
    "s_waitcnt lgkmcnt(0)\n"
    "s_and_b32 s28, s28, 0xffe00000\n"
    "s_mov_b64 s[92:93], s[8:9]\n"
    "s_and_b32 s93, s93, 0xffff\n"
    "s_mov_b32 s94, 1835008\n"
    "s_mov_b32 s95, 0x20000\n"
    "s_lshr_b32 s6, %1, 3\n"
    "s_max_u32 s6, s6, 10\n"
    "s_sub_u32 s6, s6, 10\n"
    "s_lshl_b32 s6, s6, 21\n"
    "s_add_u32 s6, s6, s28\n"
    "s_addc_u32 s7, s29, 0\n"
    "s_load_dword s4, s[6:7], 253184\n"
    "s_load_dword s53, s[28:29], 0\n"
    "s_cmp_lt_u32 %1, 84\n"
    "s_cbranch_scc0 L_nog1_%=\n"
    "global_load_ushort v180, v5, s[24:25]\n"
    "global_load_ushort v181, v7, s[24:25]\n"
    "global_load_dwordx4 v[128:131], v4, s[26:27]\n"
    "v_add_u32 v4, 344064, v4\n"
    "global_load_dwordx4 v[132:135], v4, s[26:27]\n"
    "v_add_u32 v4, 344064, v4\n"
    "global_load_dwordx4 v[136:139], v4, s[26:27]\n"
    "v_add_u32 v4, 344064, v4\n"
    "global_load_dwordx4 v[140:143], v4, s[26:27]\n"
    "v_add_u32 v4, 344064, v4\n"
    "global_load_dwordx4 v[144:147], v4, s[26:27]\n"
    "v_add_u32 v4, 344064, v4\n"
    "global_load_dwordx4 v[148:151], v4, s[26:27]\n"
    "v_add_u32 v4, 344064, v4\n"
    "global_load_dwordx4 v[152:155], v4, s[26:27]\n"
    "s_branch L_nog3_%=\n"
    "L_nog1_%=:\n"
    "s_add_u32 m0, s49, 0\n"
    "s_nop 0\n"
    "buffer_load_dwordx4 v216, s[92:95], 0 offen offset:0 lds\n"
    "s_add_u32 m0, s49, 7168\n"
    "s_nop 0\n"
    "buffer_load_dwordx4 v217, s[92:95], 0 offen offset:0 lds\n"
    "s_add_u32 m0, s49, 14336\n"
    "s_nop 0\n"
    "buffer_load_dwordx4 v218, s[92:95], 0 offen offset:0 lds\n"
    "s_add_u32 m0, s49, 21504\n"
    "s_nop 0\n"
    "buffer_load_dwordx4 v219, s[92:95], 0 offen offset:0 lds\n"
    "s_add_u32 m0, s49, 896\n"
    "s_nop 0\n"
    "buffer_load_dwordx4 v216, s[92:95], 0 offen offset:128 lds\n"
    "s_add_u32 m0, s49, 8064\n"
    "s_nop 0\n"
    "buffer_load_dwordx4 v217, s[92:95], 0 offen offset:128 lds\n"
    "s_add_u32 m0, s49, 15232\n"
    "s_nop 0\n"
    "buffer_load_dwordx4 v218, s[92:95], 0 offen offset:128 lds\n"
    "s_add_u32 m0, s49, 22400\n"
    "s_nop 0\n"
    "buffer_load_dwordx4 v219, s[92:95], 0 offen offset:128 lds\n"
    "L_nog3_%=:\n"
    "v_lshlrev_b32 v0, 4, v0\n"
    "v_lshrrev_b32 v2, 5, v250\n"
    "v_lshl_add_u32 v0, v2, 6, v0\n"
    "v_bfe_u32 v2, v250, 2, 3\n"
    "v_lshl_add_u32 v0, v2, 13, v0\n"
    "s_lshl_b32 s54, s50, 16\n"
    "v_add_u32 v246, s54, v0\n"
    "s_lshl_b32 s69, s50, 12\n"
    "v_bfe_u32 v248, v250, 2, 2\n"
    "v_sub_u32 v248, 0, v248\n"
    "v_lshrrev_b32 v249, 4, v250\n"
    "v_xor_b32 v248, v248, v249\n"
    "v_and_b32 v248, 3, v248\n"
    "v_lshlrev_b32 v248, 4, v248\n"
    "v_and_b32 v249, 7, v250\n"
    "v_lshl_add_u32 v248, v249, 6, v248\n"
    "v_bfe_u32 v249, v250, 3, 1\n"
    "v_mul_u32_u24 v249, 7168, v249\n"
    "v_add3_u32 v248, v248, v249, s49\n"
    "v_bfe_u32 v232, v250, 2, 2\n"
    "v_sub_u32 v232, 0, v232\n"
    "v_lshrrev_b32 v233, 4, v250\n"
    "v_xor_b32 v232, v232, v233\n"
    "v_and_b32 v232, 3, v232\n"
    "v_lshlrev_b32 v232, 4, v232\n"
    "v_and_b32 v234, 7, v250\n"
    "v_lshl_add_u32 v232, v234, 6, v232\n"
    "v_bfe_u32 v234, v250, 3, 1\n"
    "v_lshl_add_u32 v232, v234, 10, v232\n"
    "s_and_b32 s54, s50, 1\n"
    "s_lshl_b32 s54, s54, 13\n"
    "v_add_u32 v251, s54, v232\n"
    "v_lshrrev_b32 v232, 5, v250\n"
    "v_lshlrev_b32 v232, 11, v232\n"
    "v_bfe_u32 v234, v250, 2, 3\n"
    "v_lshl_add_u32 v232, v234, 6, v232\n"
    "v_and_b32 v234, 3, v250\n"
    "v_lshl_add_u32 v232, v234, 3, v232\n"
    "v_bfe_u32 v234, v250, 4, 1\n"
    "v_lshl_add_u32 v232, v234, 5, v232\n"
    "s_add_u32 s54, s54, 0x10000\n"
    "v_add_u32 v252, s54, v232\n"
    "v_xor_b32 v253, 32, v252\n"
    "s_and_b32 s54, s50, 1\n"
    "s_lshl_b32 s55, s54, 1\n"
    "v_bfe_u32 v232, v250, 4, 1\n"
    "v_add_u32 v232, s55, v232\n"
    "v_sub_u32 v232, 0, v232\n"
    "v_xor_b32 v232, v232, v250\n"
    "v_and_b32 v232, 3, v232\n"
    "v_lshlrev_b32 v232, 4, v232\n"
    "v_lshrrev_b32 v233, 5, v250\n"
    "v_lshlrev_b32 v233, 6, v233\n"
    "v_bfe_u32 v234, v250, 2, 3\n"
    "v_lshl_add_u32 v233, v234, 9, v233\n"
    "v_add_u32 v232, v232, v233\n"
    "s_lshl_b32 s54, s54, 12\n"
    "s_lshr_b32 s55, s50, 1\n"
    "s_lshl_b32 s55, s55, 14\n"
    "s_add_u32 s54, s54, s55\n"
    "v_add_u32 v254, s54, v232\n"
    "v_bfe_u32 v232, v250, 4, 1\n"
    "v_lshlrev_b32 v232, 1, v232\n"
    "v_xor_b32 v232, v232, v250\n"
    "v_and_b32 v232, 3, v232\n"
    "v_lshl_add_u32 v232, v232, 4, v233\n"
    "s_lshl_b32 s54, s50, 13\n"
    "v_add_u32 v255, s54, v232\n"
    "s_lshr_b32 s54, s50, 1\n"
    "s_lshl_b32 s54, s54, 13\n"
    "s_and_b32 s55, s50, 1\n"
    "s_lshl_b32 s55, s55, 10\n"
    "s_add_u32 s47, s54, s55\n"
    "s_lshl_b32 s54, s50, 12\n"
    "s_add_u32 s48, s54, 0x10000\n"
    "v_accvgpr_write_b32 a237, v252\n"
    "v_accvgpr_write_b32 a238, v255\n"
    "v_bfe_u32 v8, v250, 4, 1\n"
    "v_lshlrev_b32 v8, 1, v8\n"
    "v_xor_b32 v8, v8, v250\n"
    "v_and_b32 v8, 3, v8\n"
    "v_lshlrev_b32 v8, 4, v8\n"
    "v_lshrrev_b32 v9, 5, v250\n"
    "v_lshl_add_u32 v8, v9, 6, v8\n"
    "s_cmp_lt_u32 %1, 196\n"
    "s_cselect_b32 s57, 0, 0x40000000\n"
    "v_add_u32 v8, s57, v8\n"
    "v_bfe_u32 v10, v250, 2, 3\n"
    "s_lshl_b32 s54, s50, 3\n"
    "v_add_u32 v10, s54, v10\n"
    "s_mul_i32 s54, %1, 2341\n"
    "s_lshr_b32 s54, s54, 16\n"
    "s_mul_i32 s55, s54, 28\n"
    "s_sub_u32 s55, %1, s55\n"
    "s_lshl_b32 s45, s55, 2\n"
    "s_add_u32 s45, s45, s50\n"
    "s_mul_i32 s45, s45, 28\n"
    "s_add_u32 s45, s45, s54\n"
    "s_lshl_b32 s45, s45, 10\n"
    "s_add_u32 s45, s45, s57\n"
    "s_mul_i32 s54, s54, 114688\n"
    "s_lshl_b32 s55, s55, 7\n"
    "s_add_u32 s54, s54, s55\n"
    "v_mul_u32_u24 v252, 3584, v10\n"
    "v_add3_u32 v252, v252, v8, s54\n"
    "s_mul_i32 s54, %1, 9363\n"
    "s_lshr_b32 s54, s54, 16\n"
    "s_mul_i32 s55, s54, 7\n"
    "s_sub_u32 s55, %1, s55\n"
    "s_mul_i32 s46, s55, 56\n"
    "s_add_u32 s46, s46, s54\n"
    "s_lshl_b32 s46, s46, 2\n"
    "s_add_u32 s46, s46, s50\n"
    "s_lshl_b32 s46, s46, 10\n"
    "s_add_u32 s46, s46, s57\n"
    "s_mul_i32 s54, s54, 28672\n"
    "s_lshl_b32 s55, s55, 7\n"
    "s_add_u32 s54, s54, s55\n"
    "v_mul_u32_u24 v253, 896, v10\n"
    "v_add3_u32 v253, v253, v8, s54\n"
    "v_lshlrev_b32 v8, 10, v9\n"
    "v_bfe_u32 v10, v250, 2, 3\n"
    "v_lshl_add_u32 v8, v10, 6, v8\n"
    "v_and_b32 v10, 3, v250\n"
    "v_lshl_add_u32 v8, v10, 3, v8\n"
    "v_bfe_u32 v10, v250, 4, 1\n"
    "s_and_b32 s54, s50, 1\n"
    "v_xor_b32 v10, s54, v10\n"
    "v_lshl_add_u32 v8, v10, 5, v8\n"
    "s_lshr_b32 s54, s50, 1\n"
    "s_lshl_b32 s54, s54, 9\n"
    "s_add_u32 s54, s54, 147456\n"
    "v_add_u32 v255, s54, v8\n"
    "v_lshrrev_b32 v1, 4, v250\n"
    "v_lshlrev_b32 v0, 12, v1\n"
    "v_bfe_u32 v2, v250, 2, 2\n"
    "v_lshl_add_u32 v0, v2, 6, v0\n"
    "v_and_b32 v2, 3, v250\n"
    "v_lshl_add_u32 v0, v2, 3, v0\n"
    "v_bfe_u32 v2, v250, 4, 1\n"
    "v_lshl_add_u32 v240, v2, 5, v0\n"
    "v_xor_b32 v241, 32, v240\n"
    "v_add_u32 v242, 0x10000, v240\n"
    "v_add_u32 v243, 0x10000, v241\n"
    "v_add_u32 v244, 0x20000, v240\n"
    "v_add_u32 v245, 0x20000, v241\n"
    "v_lshlrev_b32 v1, 5, v1\n"
    "v_add_u32 v247, 155648, v1\n"
    "s_and_b32 s54, %1, 7\n"
    "s_lshr_b32 s55, %1, 3\n"
    "s_lshl_b32 s70, s54, 5\n"
    "s_add_u32 s70, s70, s55\n"
    "s_lshl_b32 s97, s54, 2\n"
    "s_lshr_b32 s98, s55, 3\n"
    "s_add_u32 s97, s97, s98\n"
    "s_and_b32 s98, s55, 7\n"
    "s_lshr_b32 s71, s54, 2\n"
    "s_lshl_b32 s71, s71, 3\n"
    "s_lshr_b32 s96, s55, 2\n"
    "s_add_u32 s71, s71, s96\n"
    "s_and_b32 s96, s54, 3\n"
    "s_lshl_b32 s96, s96, 2\n"
    "s_and_b32 s55, s55, 3\n"
    "s_add_u32 s96, s96, s55\n"
    "v_bfe_u32 v8, v250, 4, 1\n"
    "v_lshlrev_b32 v8, 1, v8\n"
    "v_xor_b32 v8, v8, v250\n"
    "v_and_b32 v8, 3, v8\n"
    "v_lshlrev_b32 v8, 4, v8\n"
    "v_lshrrev_b32 v9, 5, v250\n"
    "v_lshl_add_u32 v8, v9, 6, v8\n"
    "v_bfe_u32 v10, v250, 2, 3\n"
    "s_lshl_b32 s54, s51, 8\n"
    "s_lshl_b32 s55, s50, 6\n"
    "s_add_u32 s54, s54, s55\n"
    "v_add_u32 v10, s54, v10\n"
    "v_mul_u32_u24 v10, 896, v10\n"
    "v_add_u32 v10, v8, v10\n"
    "v_accvgpr_write_b32 a233, v10\n"
    "v_lshlrev_b32 v8, 10, v9\n"
    "v_bfe_u32 v10, v250, 2, 3\n"
    "v_lshl_add_u32 v8, v10, 6, v8\n"
    "v_and_b32 v10, 3, v250\n"
    "v_lshl_add_u32 v8, v10, 3, v8\n"
    "v_bfe_u32 v10, v250, 4, 1\n"
    "v_lshl_add_u32 v10, v10, 5, v8\n"
    "v_accvgpr_write_b32 a232, v10\n"
    "v_bfe_u32 v8, v250, 3, 1\n"
    "s_and_b32 s54, s52, 7\n"
    "s_lshl_b32 s54, s54, 4\n"
    "s_lshl_b32 s55, s50, 2\n"
    "s_add_u32 s54, s54, s55\n"
    "v_add_u32 v8, s54, v8\n"
    "v_lshlrev_b32 v8, 18, v8\n"
    "v_lshrrev_b32 v9, 4, v250\n"
    "v_lshl_add_u32 v8, v9, 7, v8\n"
    "v_and_b32 v9, 7, v250\n"
    "v_lshl_add_u32 v8, v9, 4, v8\n"
    "s_lshl_b32 s54, s51, 9\n"
    "v_add_u32 v8, s54, v8\n"
    "s_cmp_lt_u32 %1, 84\n"
    "s_cbranch_scc1 L_nog4_%=\n"
    "s_add_u32 m0, s49, 1792\n"
    "s_nop 0\n"
    "buffer_load_dwordx4 v216, s[92:95], 0 offen offset:256 lds\n"
    "s_add_u32 m0, s49, 8960\n"
    "s_nop 0\n"
    "buffer_load_dwordx4 v217, s[92:95], 0 offen offset:256 lds\n"
    "s_add_u32 m0, s49, 16128\n"
    "s_nop 0\n"
    "buffer_load_dwordx4 v218, s[92:95], 0 offen offset:256 lds\n"
    "s_add_u32 m0, s49, 23296\n"
    "s_nop 0\n"
    "buffer_load_dwordx4 v219, s[92:95], 0 offen offset:256 lds\n"
    "s_add_u32 m0, s49, 2688\n"
    "s_nop 0\n"
    "buffer_load_dwordx4 v216, s[92:95], 0 offen offset:384 lds\n"
    "s_add_u32 m0, s49, 9856\n"
    "s_nop 0\n"
    "buffer_load_dwordx4 v217, s[92:95], 0 offen offset:384 lds\n"
    "s_add_u32 m0, s49, 17024\n"
    "s_nop 0\n"
    "buffer_load_dwordx4 v218, s[92:95], 0 offen offset:384 lds\n"
    "s_add_u32 m0, s49, 24192\n"
    "s_nop 0\n"
    "buffer_load_dwordx4 v219, s[92:95], 0 offen offset:384 lds\n"
    "s_branch L_nog5_%=\n"
    "L_nog4_%=:\n"
    "s_add_u32 m0, s49, 0\n"
    "s_nop 0\n"
    "buffer_load_dwordx4 v216, s[92:95], 0 offen offset:0 lds\n"
    "s_add_u32 m0, s49, 7168\n"
    "s_nop 0\n"
    "buffer_load_dwordx4 v217, s[92:95], 0 offen offset:0 lds\n"
    "s_add_u32 m0, s49, 14336\n"
    "s_nop 0\n"
    "buffer_load_dwordx4 v218, s[92:95], 0 offen offset:0 lds\n"
    "s_add_u32 m0, s49, 21504\n"
    "s_nop 0\n"
    "buffer_load_dwordx4 v219, s[92:95], 0 offen offset:0 lds\n"
    "L_nog5_%=:\n"
    "v_accvgpr_write_b32 a234, v8\n"
    "v_lshlrev_b32 v8, 4, v250\n"
    "s_lshl_b32 s54, s50, 14\n"
    "v_add_u32 v9, s54, v8\n"
    "v_accvgpr_write_b32 a235, v9\n"
    "v_and_b32 v8, 0xff, v8\n"
    "s_lshl_b32 s54, s50, 8\n"
    "s_add_u32 s54, s54, 159744\n"
    "v_add_u32 v8, s54, v8\n"
    "v_accvgpr_write_b32 a236, v8\n"
    "v_and_b32 v8, 15, v250\n"
    "v_lshlrev_b32 v8, 4, v8\n"
    "v_lshrrev_b32 v9, 5, v250\n"
    "v_lshl_add_u32 v8, v9, 8, v8\n"
    "v_bfe_u32 v9, v250, 4, 1\n"
    "v_lshl_add_u32 v8, v9, 3, v8\n"
    "s_lshl_b32 s54, s50, 12\n"
    "s_add_u32 s54, s54, 32768\n"
    "v_add_u32 v8, s54, v8\n"
    "v_lshlrev_b32 v9, 4, v250\n"
    "v_add_u32 v9, s54, v9\n"
    "v_accvgpr_write_b32 a240, v8\n"
    "v_accvgpr_write_b32 a241, v9\n"
    "v_mov_b32 v8, 0x3f803f80\n"
    "v_accvgpr_write_b32 a128, v8\n"
    "v_accvgpr_write_b32 a129, v8\n"
    "v_accvgpr_write_b32 a130, v8\n"
    "v_accvgpr_write_b32 a131, v8\n"
    "v_mov_b32 v8, 0x3f80\n"
    "v_bfe_u32 v9, v250, 2, 2\n"
    "v_lshrrev_b32 v11, 4, v250\n"
    "v_cmp_eq_u32 vcc, v9, v11\n"
    "v_and_b32 v9, 1, v250\n"
    "v_lshlrev_b32 v9, 4, v9\n"
    "v_lshlrev_b32 v9, v9, v8\n"
    "v_cndmask_b32 v9, 0, v9, vcc\n"
    "v_bfe_i32 v11, v250, 1, 1\n"
    "v_and_b32 v10, v9, v11\n"
    "v_xor_b32 v9, v9, v10\n"
    "v_accvgpr_write_b32 a132, 0\n"
    "v_accvgpr_write_b32 a133, 0\n"
    "v_accvgpr_write_b32 a134, v9\n"
    "v_accvgpr_write_b32 a135, v10\n"
    "v_accvgpr_write_b32 a136, 0\n"
    "v_accvgpr_write_b32 a137, 0\n"
    "v_min_u32 v249, 55, v250\n"
    "v_lshlrev_b32 v249, 4, v249\n"
    "s_lshl_b32 s54, s51, 9\n"
    "s_mov_b64 s[60:61], s[14:15]\n"
    "s_add_u32 s60, s60, s54\n"
    "s_addc_u32 s61, s61, 0\n"
    "s_and_b32 s61, s61, 0xffff\n"
    "s_mov_b32 s62, 3662336\n"
    "s_mov_b32 s63, 0x20000\n"
    "s_mov_b64 s[64:65], s[28:29]\n"
    "s_and_b32 s65, s65, 0xffff\n"
    "s_mov_b32 s66, 54525952\n"
    "s_mov_b32 s67, 0x20000\n"
    "s_lshl_b32 s54, s51, 20\n"
    "s_mov_b64 s[36:37], s[10:11]\n"
    "s_add_u32 s36, s36, s54\n"
    "s_addc_u32 s37, s37, 0\n"
    "s_and_b32 s37, s37, 0xffff\n"
    "s_mov_b32 s38, 1048576\n"
    "s_mov_b32 s39, 0x20000\n"
    "s_mov_b64 s[40:41], s[12:13]\n"
    "s_add_u32 s40, s40, s54\n"
    "s_addc_u32 s41, s41, 0\n"
    "s_and_b32 s41, s41, 0xffff\n"
    "s_mov_b32 s42, 1048576\n"
    "s_mov_b32 s43, 0x20000\n"
    "s_lshl_b32 s54, s51, 6\n"
    "s_lshl_b32 s55, s52, 2\n"
    "s_add_u32 s54, s54, s55\n"
    "s_add_u32 s54, s54, s50\n"
    "s_mul_i32 s55, s54, 1568\n"
    "s_mul_i32 s54, s54, 3584\n"
    "s_mov_b64 s[56:57], s[16:17]\n"
    "s_add_u32 s56, s56, s54\n"
    "s_addc_u32 s57, s57, 0\n"
    "v_mov_b32 v10, s56\n"
    "v_mov_b32 v11, s57\n"
    "v_mov_b32 v13, 116\n"
    "s_lshr_b32 s58, %1, 3\n"
    "s_lshl_b32 s58, s58, 2\n"
    "s_add_u32 s58, s58, s50\n"
    "s_mul_i32 s54, s58, 6272\n"
    "s_lshr_b32 s55, s96, 2\n"
    "s_mul_i32 s55, s55, 802816\n"
    "s_add_u32 s54, s54, s55\n"
    "s_add_u32 s54, s54, 48228224\n"
    "s_mov_b64 s[56:57], s[28:29]\n"
    "s_add_u32 s56, s56, s54\n"
    "s_addc_u32 s57, s57, 0\n"
    "v_cmp_le_u32 vcc, 16, v250\n"
    "v_mov_b32 v12, s56\n"
    "v_mov_b32 v14, 104\n"
    "v_cndmask_b32 v10, v10, v12, vcc\n"
    "v_mov_b32 v12, s57\n"
    "v_cndmask_b32 v13, v13, v14, vcc\n"
    "v_cndmask_b32 v11, v11, v12, vcc\n"
    "v_lshrrev_b32 v14, 3, v250\n"
    "v_min_u32 v14, 3, v14\n"
    "v_cmp_le_u32 vcc, 16, v250\n"
    "v_mul_u32_u24 v14, 3136, v14\n"
    "s_nop 0\n"
    "v_cndmask_b32 v14, 0, v14, vcc\n"
    "v_add_co_u32 v10, vcc, v10, v14\n"
    "s_nop 1\n"
    "v_addc_co_u32 v11, vcc, 0, v11, vcc\n"
    "v_accvgpr_write_b32 a250, v10\n"
    "v_accvgpr_write_b32 a251, v11\n"
    "v_accvgpr_write_b32 a252, v13\n"
    "s_waitcnt lgkmcnt(0)\n"
    "s_add_u32 s53, s53, 4\n"
    "s_mov_b32 s99, 500000\n"
    "s_mov_b32 s91, 0\n"
    "s_mov_b32 s5, 0\n"
    "s_cmp_lt_u32 %1, 84\n"
    "s_cbranch_scc1 L_nopr_%=\n"
    "s_cmp_eq_u32 s50, 0\n"
    "s_cbranch_scc0 L_nopr_%=\n"
    "s_memtime s[54:55]\n"
    "s_waitcnt lgkmcnt(0)\n"
    "s_load_dword s4, s[6:7], 252928\n"
    "s_waitcnt lgkmcnt(0)\n"
    "s_memtime s[56:57]\n"
    "s_waitcnt lgkmcnt(0)\n"
    "s_load_dword s4, s[6:7], 261120\n"
    "s_waitcnt lgkmcnt(0)\n"
    "s_memtime s[58:59]\n"
    "s_waitcnt lgkmcnt(0)\n"
    "s_sub_u32 s54, s56, s54\n"
    "s_sub_u32 s58, s58, s56\n"
    "s_lshr_b32 s5, %1, 2\n"
    "s_lshr_b32 s56, %1, 3\n"
    "s_add_u32 s5, s5, s56\n"
    "s_cmp_lt_u32 s58, s54\n"
    "s_addc_u32 s5, s5, 0\n"
    "s_and_b32 s5, s5, 1\n"
    "L_nopr_%=:\n"
    "s_cmp_lt_u32 %1, 84\n"
    "s_cbranch_scc0 L_nog2_%=\n"
    "s_waitcnt vmcnt(11)\n"
    "v_lshlrev_b32 v180, 16, v180\n"
    "v_lshlrev_b32 v181, 16, v181\n"
    "v_mul_f32 v172, 0xbfb8aa3b, v180\n"
    "v_mul_f32 v173, 0xbfb8aa3b, v181\n"
    "v_exp_f32 v172, v172\n"
    "v_exp_f32 v173, v173\n"
    "v_add_f32 v172, 1.0, v172\n"
    "v_add_f32 v173, 1.0, v173\n"
    "v_rcp_f32 v172, v172\n"
    "v_rcp_f32 v173, v173\n"
    "v_mul_f32 v172, v172, v180\n"
    "v_mul_f32 v173, v173, v181\n"
    "v_cvt_pk_bf16_f32 v172, v172, v172\n"
    "v_cvt_pk_bf16_f32 v173, v173, v173\n"
    "v_lshlrev_b32 v180, 16, v172\n"
    "v_lshlrev_b32 v181, 16, v173\n"
    "s_nop 0\n"
    "v_mov_b32_dpp v156, v180 quad_perm:[0,0,0,0] row_mask:0xf bank_mask:0xf\n"
    "v_mov_b32_dpp v157, v180 quad_perm:[1,1,1,1] row_mask:0xf bank_mask:0xf\n"
    "v_mov_b32_dpp v158, v180 quad_perm:[2,2,2,2] row_mask:0xf bank_mask:0xf\n"
    "v_mov_b32_dpp v159, v180 quad_perm:[3,3,3,3] row_mask:0xf bank_mask:0xf\n"
    "v_mov_b32_dpp v160, v181 quad_perm:[0,0,0,0] row_mask:0xf bank_mask:0xf\n"
    "v_mov_b32_dpp v161, v181 quad_perm:[1,1,1,1] row_mask:0xf bank_mask:0xf\n"
    "v_mov_b32_dpp v162, v181 quad_perm:[2,2,2,2] row_mask:0xf bank_mask:0xf\n"
    "s_waitcnt vmcnt(10)\n"
    "s_add_u32 m0, s49, 896\n"
    "s_nop 0\n"
    "buffer_load_dwordx4 v216, s[92:95], 0 offen offset:128 lds\n"
    "v_lshlrev_b32 v172, 16, v128\n"
    "v_and_b32 v173, 0xffff0000, v128\n"
    "v_lshlrev_b32 v174, 16, v129\n"
    "v_and_b32 v175, 0xffff0000, v129\n"
    "v_lshlrev_b32 v176, 16, v130\n"
    "v_and_b32 v177, 0xffff0000, v130\n"
    "v_lshlrev_b32 v178, 16, v131\n"
    "v_and_b32 v179, 0xffff0000, v131\n"
    "v_pk_mul_f32 v[164:165], v[172:173], v[156:157] op_sel:[0,0] op_sel_hi:[1,0]\n"
    "v_pk_mul_f32 v[166:167], v[174:175], v[156:157] op_sel:[0,0] op_sel_hi:[1,0]\n"
    "v_pk_mul_f32 v[168:169], v[176:177], v[156:157] op_sel:[0,0] op_sel_hi:[1,0]\n"
    "v_pk_mul_f32 v[170:171], v[178:179], v[156:157] op_sel:[0,0] op_sel_hi:[1,0]\n"
    "s_waitcnt vmcnt(10)\n"
    "s_add_u32 m0, s49, 8064\n"
    "s_nop 0\n"
    "buffer_load_dwordx4 v217, s[92:95], 0 offen offset:128 lds\n"
    "v_lshlrev_b32 v172, 16, v132\n"
    "v_and_b32 v173, 0xffff0000, v132\n"
    "v_lshlrev_b32 v174, 16, v133\n"
    "v_and_b32 v175, 0xffff0000, v133\n"
    "v_lshlrev_b32 v176, 16, v134\n"
    "v_and_b32 v177, 0xffff0000, v134\n"
    "v_lshlrev_b32 v178, 16, v135\n"
    "v_and_b32 v179, 0xffff0000, v135\n"
    "v_pk_fma_f32 v[164:165], v[172:173], v[156:157], v[164:165] op_sel:[0,1,0] op_sel_hi:[1,1,1]\n"
    "v_pk_fma_f32 v[166:167], v[174:175], v[156:157], v[166:167] op_sel:[0,1,0] op_sel_hi:[1,1,1]\n"
    "v_pk_fma_f32 v[168:169], v[176:177], v[156:157], v[168:169] op_sel:[0,1,0] op_sel_hi:[1,1,1]\n"
    "v_pk_fma_f32 v[170:171], v[178:179], v[156:157], v[170:171] op_sel:[0,1,0] op_sel_hi:[1,1,1]\n"
    "s_waitcnt vmcnt(10)\n"
    "s_add_u32 m0, s49, 15232\n"
    "s_nop 0\n"
    "buffer_load_dwordx4 v218, s[92:95], 0 offen offset:128 lds\n"
    "v_lshlrev_b32 v172, 16, v136\n"
    "v_and_b32 v173, 0xffff0000, v136\n"
    "v_lshlrev_b32 v174, 16, v137\n"
    "v_and_b32 v175, 0xffff0000, v137\n"
    "v_lshlrev_b32 v176, 16, v138\n"
    "v_and_b32 v177, 0xffff0000, v138\n"
    "v_lshlrev_b32 v178, 16, v139\n"
    "v_and_b32 v179, 0xffff0000, v139\n"
    "v_pk_fma_f32 v[164:165], v[172:173], v[158:159], v[164:165] op_sel:[0,0,0] op_sel_hi:[1,0,1]\n"
    "v_pk_fma_f32 v[166:167], v[174:175], v[158:159], v[166:167] op_sel:[0,0,0] op_sel_hi:[1,0,1]\n"
    "v_pk_fma_f32 v[168:169], v[176:177], v[158:159], v[168:169] op_sel:[0,0,0] op_sel_hi:[1,0,1]\n"
    "v_pk_fma_f32 v[170:171], v[178:179], v[158:159], v[170:171] op_sel:[0,0,0] op_sel_hi:[1,0,1]\n"
    "s_waitcnt vmcnt(10)\n"
    "s_add_u32 m0, s49, 22400\n"
    "s_nop 0\n"
    "buffer_load_dwordx4 v219, s[92:95], 0 offen offset:128 lds\n"
    "v_lshlrev_b32 v172, 16, v140\n"
    "v_and_b32 v173, 0xffff0000, v140\n"
    "v_lshlrev_b32 v174, 16, v141\n"
    "v_and_b32 v175, 0xffff0000, v141\n"
    "v_lshlrev_b32 v176, 16, v142\n"
    "v_and_b32 v177, 0xffff0000, v142\n"
    "v_lshlrev_b32 v178, 16, v143\n"
    "v_and_b32 v179, 0xffff0000, v143\n"
    "v_pk_fma_f32 v[164:165], v[172:173], v[158:159], v[164:165] op_sel:[0,1,0] op_sel_hi:[1,1,1]\n"
    "v_pk_fma_f32 v[166:167], v[174:175], v[158:159], v[166:167] op_sel:[0,1,0] op_sel_hi:[1,1,1]\n"
    "v_pk_fma_f32 v[168:169], v[176:177], v[158:159], v[168:169] op_sel:[0,1,0] op_sel_hi:[1,1,1]\n"
    "v_pk_fma_f32 v[170:171], v[178:179], v[158:159], v[170:171] op_sel:[0,1,0] op_sel_hi:[1,1,1]\n"
    "s_waitcnt vmcnt(10)\n"
    "s_add_u32 m0, s49, 1792\n"
    "s_nop 0\n"
    "buffer_load_dwordx4 v216, s[92:95], 0 offen offset:256 lds\n"
    "v_lshlrev_b32 v172, 16, v144\n"
    "v_and_b32 v173, 0xffff0000, v144\n"
    "v_lshlrev_b32 v174, 16, v145\n"
    "v_and_b32 v175, 0xffff0000, v145\n"
    "v_lshlrev_b32 v176, 16, v146\n"
    "v_and_b32 v177, 0xffff0000, v146\n"
    "v_lshlrev_b32 v178, 16, v147\n"
    "v_and_b32 v179, 0xffff0000, v147\n"
    "v_pk_fma_f32 v[164:165], v[172:173], v[160:161], v[164:165] op_sel:[0,0,0] op_sel_hi:[1,0,1]\n"
    "v_pk_fma_f32 v[166:167], v[174:175], v[160:161], v[166:167] op_sel:[0,0,0] op_sel_hi:[1,0,1]\n"
    "v_pk_fma_f32 v[168:169], v[176:177], v[160:161], v[168:169] op_sel:[0,0,0] op_sel_hi:[1,0,1]\n"
    "v_pk_fma_f32 v[170:171], v[178:179], v[160:161], v[170:171] op_sel:[0,0,0] op_sel_hi:[1,0,1]\n"
    "s_waitcnt vmcnt(10)\n"
    "s_add_u32 m0, s49, 8960\n"
    "s_nop 0\n"
    "buffer_load_dwordx4 v217, s[92:95], 0 offen offset:256 lds\n"
    "v_lshlrev_b32 v172, 16, v148\n"
    "v_and_b32 v173, 0xffff0000, v148\n"
    "v_lshlrev_b32 v174, 16, v149\n"
    "v_and_b32 v175, 0xffff0000, v149\n"
    "v_lshlrev_b32 v176, 16, v150\n"
    "v_and_b32 v177, 0xffff0000, v150\n"
    "v_lshlrev_b32 v178, 16, v151\n"
    "v_and_b32 v179, 0xffff0000, v151\n"
    "v_pk_fma_f32 v[164:165], v[172:173], v[160:161], v[164:165] op_sel:[0,1,0] op_sel_hi:[1,1,1]\n"
    "v_pk_fma_f32 v[166:167], v[174:175], v[160:161], v[166:167] op_sel:[0,1,0] op_sel_hi:[1,1,1]\n"
    "v_pk_fma_f32 v[168:169], v[176:177], v[160:161], v[168:169] op_sel:[0,1,0] op_sel_hi:[1,1,1]\n"
    "v_pk_fma_f32 v[170:171], v[178:179], v[160:161], v[170:171] op_sel:[0,1,0] op_sel_hi:[1,1,1]\n"
    "s_waitcnt vmcnt(10)\n"
    "s_add_u32 m0, s49, 16128\n"
    "s_nop 0\n"
    "buffer_load_dwordx4 v218, s[92:95], 0 offen offset:256 lds\n"
    "v_lshlrev_b32 v172, 16, v152\n"
    "v_and_b32 v173, 0xffff0000, v152\n"
    "v_lshlrev_b32 v174, 16, v153\n"
    "v_and_b32 v175, 0xffff0000, v153\n"
    "v_lshlrev_b32 v176, 16, v154\n"
    "v_and_b32 v177, 0xffff0000, v154\n"
    "v_lshlrev_b32 v178, 16, v155\n"
    "v_and_b32 v179, 0xffff0000, v155\n"
    "v_pk_fma_f32 v[164:165], v[172:173], v[162:163], v[164:165] op_sel:[0,0,0] op_sel_hi:[1,0,1]\n"
    "v_pk_fma_f32 v[166:167], v[174:175], v[162:163], v[166:167] op_sel:[0,0,0] op_sel_hi:[1,0,1]\n"
    "v_pk_fma_f32 v[168:169], v[176:177], v[162:163], v[168:169] op_sel:[0,0,0] op_sel_hi:[1,0,1]\n"
    "v_pk_fma_f32 v[170:171], v[178:179], v[162:163], v[170:171] op_sel:[0,0,0] op_sel_hi:[1,0,1]\n"
    "s_nop 0\n"
    "v_permlane32_swap_b32 v164, v168\n"
    "v_permlane32_swap_b32 v165, v169\n"
    "v_permlane32_swap_b32 v166, v170\n"
    "v_permlane32_swap_b32 v167, v171\n"
    "v_pk_add_f32 v[164:165], v[164:165], v[168:169]\n"
    "v_pk_add_f32 v[166:167], v[166:167], v[170:171]\n"
    "s_nop 1\n"
    "v_permlane16_swap_b32 v164, v166\n"
    "v_permlane16_swap_b32 v165, v167\n"
    "v_pk_add_f32 v[164:165], v[164:165], v[166:167]\n"
    "s_nop 1\n"
    "v_add_f32_dpp v164, v164, v164 row_ror:4 row_mask:0xf bank_mask:0xf\n"
    "v_add_f32_dpp v165, v165, v165 row_ror:4 row_mask:0xf bank_mask:0xf\n"
    "s_nop 0\n"
    "v_add_f32_dpp v164, v164, v164 row_ror:8 row_mask:0xf bank_mask:0xf\n"
    "v_add_f32_dpp v165, v165, v165 row_ror:8 row_mask:0xf bank_mask:0xf\n"
    "v_lshlrev_b32 v172, 3, v250\n"
    "v_add_u32 v172, 159744, v172\n"
    "s_lshl_b32 s54, s50, 9\n"
    "v_add_u32 v173, s54, v172\n"
    "ds_write_b64 v173, v[164:165]\n"
    "v_and_b32 v174, 3, v250\n"
    "v_lshlrev_b32 v174, 2, v174\n"
    "v_lshrrev_b32 v175, 4, v250\n"
    "v_add_lshl_u32 v174, v174, v175, 3\n"
    "s_lshl_b32 s54, %1, 7\n"
    "s_add_u32 s54, s54, 4096\n"
    "s_waitcnt lgkmcnt(0)\n"
    "s_barrier\n"
    "ds_read_b64 v[164:165], v172 offset:0\n"
    "ds_read_b64 v[166:167], v172 offset:512\n"
    "ds_read_b64 v[168:169], v172 offset:1024\n"
    "ds_read_b64 v[170:171], v172 offset:1536\n"
    "s_waitcnt lgkmcnt(0)\n"
    "v_pk_add_f32 v[164:165], v[164:165], v[166:167]\n"
    "v_pk_add_f32 v[168:169], v[168:169], v[170:171]\n"
    "v_pk_add_f32 v[164:165], v[164:165], v[168:169]\n"
    "v_cvt_pk_bf16_f32 v164, v164, v165\n"
    "v_mov_b32 v165, s53\n"
    "s_cmp_eq_u32 s50, 0\n"
    "s_cselect_b32 s54, s54, 2146435072\n"
    "s_mov_b32 exec_lo, 0x000f000f\n"
    "s_mov_b32 exec_hi, 0x000f000f\n"
    "buffer_store_dwordx2 v[164:165], v174, s[64:67], s54 offen sc1\n"
    "s_mov_b64 exec, -1\n"
    "s_add_u32 m0, s49, 23296\n"
    "s_nop 0\n"
    "buffer_load_dwordx4 v219, s[92:95], 0 offen offset:256 lds\n"
    "s_add_u32 m0, s49, 2688\n"
    "s_nop 0\n"
    "buffer_load_dwordx4 v216, s[92:95], 0 offen offset:384 lds\n"
    "s_add_u32 m0, s49, 9856\n"
    "s_nop 0\n"
    "buffer_load_dwordx4 v217, s[92:95], 0 offen offset:384 lds\n"
    "s_add_u32 m0, s49, 17024\n"
    "s_nop 0\n"
    "buffer_load_dwordx4 v218, s[92:95], 0 offen offset:384 lds\n"
    "s_add_u32 m0, s49, 24192\n"
    "s_nop 0\n"
    "buffer_load_dwordx4 v219, s[92:95], 0 offen offset:384 lds\n"
    "L_nog2_%=:\n"
    "s_waitcnt vmcnt(12)\n"
    "ds_read_b128 a[16:19], v248 offset:0\n"
    "ds_read_b128 a[20:23], v248 offset:14336\n"
    "ds_read_b128 a[24:27], v248 offset:512\n"
    "ds_read_b128 a[28:31], v248 offset:14848\n"
    "s_waitcnt lgkmcnt(3)\n"
    "v_mfma_f32_16x16x32_bf16 v[224:227], a[128:131], a[16:19], 0\n"
    "v_mfma_f32_16x16x32_bf16 v[232:235], a[16:19], a[16:19], 0\n"
    "v_mfma_f32_16x16x32_bf16 v[0:3], a[134:137], a[16:19], 0\n"
    "s_waitcnt vmcnt(8)\n"
    "ds_read_b128 a[32:35], v248 offset:1024\n"
    "v_mfma_f32_16x16x32_bf16 v[4:7], a[132:135], a[16:19], 0\n"
    "s_waitcnt lgkmcnt(3)\n"
    "ds_read_b128 a[36:39], v248 offset:15360\n"
    "v_mfma_f32_16x16x32_bf16 v[228:231], a[128:131], a[20:23], 0\n"
    "ds_read_b128 a[40:43], v248 offset:1536\n"
    "v_mfma_f32_16x16x32_bf16 v[236:239], a[20:23], a[20:23], 0\n"
    "ds_read_b128 a[44:47], v248 offset:15872\n"
    "v_mfma_f32_16x16x32_bf16 v[112:115], a[134:137], a[20:23], 0\n"
    "s_waitcnt lgkmcnt(5)\n"
    "s_add_u32 m0, s49, 3584\n"
    "v_mfma_f32_16x16x32_bf16 v[116:119], a[132:135], a[20:23], 0\n"
    "buffer_load_dwordx4 v216, s[92:95], 0 offen offset:512 lds\n"
    "v_mfma_f32_16x16x32_bf16 v[224:227], a[128:131], a[24:27], v[224:227]\n"
    "s_add_u32 m0, s49, 10752\n"
    "v_mfma_f32_16x16x32_bf16 v[232:235], a[24:27], a[24:27], v[232:235]\n"
    "buffer_load_dwordx4 v217, s[92:95], 0 offen offset:512 lds\n"
    "v_mfma_f32_16x16x32_bf16 v[8:11], a[134:137], a[24:27], 0\n"
    "s_waitcnt lgkmcnt(4)\n"
    "s_add_u32 m0, s49, 17920\n"
    "v_mfma_f32_16x16x32_bf16 v[12:15], a[132:135], a[24:27], 0\n"
    "buffer_load_dwordx4 v218, s[92:95], 0 offen offset:512 lds\n"
    "v_mfma_f32_16x16x32_bf16 v[228:231], a[128:131], a[28:31], v[228:231]\n"
    "s_add_u32 m0, s49, 25088\n"
    "v_mfma_f32_16x16x32_bf16 v[236:239], a[28:31], a[28:31], v[236:239]\n"
    "buffer_load_dwordx4 v219, s[92:95], 0 offen offset:512 lds\n"
    "v_mfma_f32_16x16x32_bf16 v[120:123], a[134:137], a[28:31], 0\n"
    "v_mfma_f32_16x16x32_bf16 v[124:127], a[132:135], a[28:31], 0\n"
    "s_waitcnt lgkmcnt(3)\n"
    "v_mfma_f32_16x16x32_bf16 v[224:227], a[128:131], a[32:35], v[224:227]\n"
    "v_mfma_f32_16x16x32_bf16 v[232:235], a[32:35], a[32:35], v[232:235]\n"
    "v_mfma_f32_16x16x32_bf16 v[16:19], a[134:137], a[32:35], 0\n"
    "s_waitcnt vmcnt(8)\n"
    "ds_read_b128 a[48:51], v248 offset:2048\n"
    "v_mfma_f32_16x16x32_bf16 v[20:23], a[132:135], a[32:35], 0\n"
    "s_waitcnt lgkmcnt(3)\n"
    "ds_read_b128 a[52:55], v248 offset:16384\n"
    "v_mfma_f32_16x16x32_bf16 v[228:231], a[128:131], a[36:39], v[228:231]\n"
    "ds_read_b128 a[56:59], v248 offset:2560\n"
    "v_mfma_f32_16x16x32_bf16 v[236:239], a[36:39], a[36:39], v[236:239]\n"
    "ds_read_b128 a[60:63], v248 offset:16896\n"
    "v_mfma_f32_16x16x32_bf16 v[128:131], a[134:137], a[36:39], 0\n"
    "s_waitcnt lgkmcnt(5)\n"
    "s_add_u32 m0, s49, 4480\n"
    "v_mfma_f32_16x16x32_bf16 v[132:135], a[132:135], a[36:39], 0\n"
    "buffer_load_dwordx4 v216, s[92:95], 0 offen offset:640 lds\n"
    "v_mfma_f32_16x16x32_bf16 v[224:227], a[128:131], a[40:43], v[224:227]\n"
    "s_add_u32 m0, s49, 11648\n"
    "v_mfma_f32_16x16x32_bf16 v[232:235], a[40:43], a[40:43], v[232:235]\n"
    "buffer_load_dwordx4 v217, s[92:95], 0 offen offset:640 lds\n"
    "v_mfma_f32_16x16x32_bf16 v[24:27], a[134:137], a[40:43], 0\n"
    "s_waitcnt lgkmcnt(4)\n"
    "s_add_u32 m0, s49, 18816\n"
    "v_mfma_f32_16x16x32_bf16 v[28:31], a[132:135], a[40:43], 0\n"
    "buffer_load_dwordx4 v218, s[92:95], 0 offen offset:640 lds\n"
    "v_mfma_f32_16x16x32_bf16 v[228:231], a[128:131], a[44:47], v[228:231]\n"
    "s_add_u32 m0, s49, 25984\n"
    "v_mfma_f32_16x16x32_bf16 v[236:239], a[44:47], a[44:47], v[236:239]\n"
    "buffer_load_dwordx4 v219, s[92:95], 0 offen offset:640 lds\n"
    "v_mfma_f32_16x16x32_bf16 v[136:139], a[134:137], a[44:47], 0\n"
    "v_mfma_f32_16x16x32_bf16 v[140:143], a[132:135], a[44:47], 0\n"
    "s_waitcnt lgkmcnt(3)\n"
    "v_mfma_f32_16x16x32_bf16 v[224:227], a[128:131], a[48:51], v[224:227]\n"
    "v_mfma_f32_16x16x32_bf16 v[232:235], a[48:51], a[48:51], v[232:235]\n"
    "v_mfma_f32_16x16x32_bf16 v[32:35], a[134:137], a[48:51], 0\n"
    "s_waitcnt vmcnt(8)\n"
    "ds_read_b128 a[64:67], v248 offset:3072\n"
    "v_mfma_f32_16x16x32_bf16 v[36:39], a[132:135], a[48:51], 0\n"
    "s_waitcnt lgkmcnt(3)\n"
    "ds_read_b128 a[68:71], v248 offset:17408\n"
    "v_mfma_f32_16x16x32_bf16 v[228:231], a[128:131], a[52:55], v[228:231]\n"
    "ds_read_b128 a[72:75], v248 offset:3584\n"
    "v_mfma_f32_16x16x32_bf16 v[236:239], a[52:55], a[52:55], v[236:239]\n"
    "ds_read_b128 a[76:79], v248 offset:17920\n"
    "v_mfma_f32_16x16x32_bf16 v[144:147], a[134:137], a[52:55], 0\n"
    "s_waitcnt lgkmcnt(5)\n"
    "s_add_u32 m0, s49, 5376\n"
    "v_mfma_f32_16x16x32_bf16 v[148:151], a[132:135], a[52:55], 0\n"
    "buffer_load_dwordx4 v216, s[92:95], 0 offen offset:768 lds\n"
    "v_mfma_f32_16x16x32_bf16 v[224:227], a[128:131], a[56:59], v[224:227]\n"
    "s_add_u32 m0, s49, 12544\n"
    "v_mfma_f32_16x16x32_bf16 v[232:235], a[56:59], a[56:59], v[232:235]\n"
    "buffer_load_dwordx4 v217, s[92:95], 0 offen offset:768 lds\n"
    "v_mfma_f32_16x16x32_bf16 v[40:43], a[134:137], a[56:59], 0\n"
    "s_waitcnt lgkmcnt(4)\n"
    "s_add_u32 m0, s49, 19712\n"
    "v_mfma_f32_16x16x32_bf16 v[44:47], a[132:135], a[56:59], 0\n"
    "buffer_load_dwordx4 v218, s[92:95], 0 offen offset:768 lds\n"
    "v_mfma_f32_16x16x32_bf16 v[228:231], a[128:131], a[60:63], v[228:231]\n"
    "s_add_u32 m0, s49, 26880\n"
    "v_mfma_f32_16x16x32_bf16 v[236:239], a[60:63], a[60:63], v[236:239]\n"
    "buffer_load_dwordx4 v219, s[92:95], 0 offen offset:768 lds\n"
    "v_mfma_f32_16x16x32_bf16 v[152:155], a[134:137], a[60:63], 0\n"
    "v_mfma_f32_16x16x32_bf16 v[156:159], a[132:135], a[60:63], 0\n"
    "s_waitcnt lgkmcnt(3)\n"
    "v_mfma_f32_16x16x32_bf16 v[224:227], a[128:131], a[64:67], v[224:227]\n"
    "v_mfma_f32_16x16x32_bf16 v[232:235], a[64:67], a[64:67], v[232:235]\n"
    "v_mfma_f32_16x16x32_bf16 v[48:51], a[134:137], a[64:67], 0\n"
    "s_waitcnt vmcnt(8)\n"
    "ds_read_b128 a[16:19], v248 offset:4096\n"
    "v_mfma_f32_16x16x32_bf16 v[52:55], a[132:135], a[64:67], 0\n"
    "s_waitcnt lgkmcnt(3)\n"
    "ds_read_b128 a[20:23], v248 offset:18432\n"
    "v_mfma_f32_16x16x32_bf16 v[228:231], a[128:131], a[68:71], v[228:231]\n"
    "ds_read_b128 a[24:27], v248 offset:4608\n"
    "v_mfma_f32_16x16x32_bf16 v[236:239], a[68:71], a[68:71], v[236:239]\n"
    "ds_read_b128 a[28:31], v248 offset:18944\n"
    "v_mfma_f32_16x16x32_bf16 v[160:163], a[134:137], a[68:71], 0\n"
    "s_waitcnt lgkmcnt(5)\n"
    "s_mov_b32 s68, 0\n"
    "s_add_u32 m0, s69, 65536\n"
    "v_mfma_f32_16x16x32_bf16 v[164:167], a[132:135], a[68:71], 0\n"
    "buffer_load_dwordx4 v246, s[60:63], s68 offen offset:0 nt lds\n"
    "v_mfma_f32_16x16x32_bf16 v[224:227], a[128:131], a[72:75], v[224:227]\n"
    "s_add_u32 m0, s69, 66432\n"
    "v_mfma_f32_16x16x32_bf16 v[232:235], a[72:75], a[72:75], v[232:235]\n"
    "buffer_load_dwordx4 v246, s[60:63], s68 offen offset:128 nt lds\n"
    "v_mfma_f32_16x16x32_bf16 v[56:59], a[134:137], a[72:75], 0\n"
    "s_waitcnt lgkmcnt(4)\n"
    "s_mov_b32 s68, 262144\n"
    "s_add_u32 m0, s69, 67584\n"
    "v_mfma_f32_16x16x32_bf16 v[60:63], a[132:135], a[72:75], 0\n"
    "buffer_load_dwordx4 v246, s[60:63], s68 offen offset:0 nt lds\n"
    "v_mfma_f32_16x16x32_bf16 v[228:231], a[128:131], a[76:79], v[228:231]\n"
    "s_add_u32 m0, s69, 68480\n"
    "v_mfma_f32_16x16x32_bf16 v[236:239], a[76:79], a[76:79], v[236:239]\n"
    "buffer_load_dwordx4 v246, s[60:63], s68 offen offset:128 nt lds\n"
    "v_mfma_f32_16x16x32_bf16 v[168:171], a[134:137], a[76:79], 0\n"
    "v_mfma_f32_16x16x32_bf16 v[172:175], a[132:135], a[76:79], 0\n"
    "s_waitcnt lgkmcnt(3)\n"
    "v_mfma_f32_16x16x32_bf16 v[224:227], a[128:131], a[16:19], v[224:227]\n"
    "v_mfma_f32_16x16x32_bf16 v[232:235], a[16:19], a[16:19], v[232:235]\n"
    "v_mfma_f32_16x16x32_bf16 v[64:67], a[134:137], a[16:19], 0\n"
    "s_waitcnt vmcnt(8)\n"
    "ds_read_b128 a[32:35], v248 offset:5120\n"
    "v_mfma_f32_16x16x32_bf16 v[68:71], a[132:135], a[16:19], 0\n"
    "s_waitcnt lgkmcnt(3)\n"
    "ds_read_b128 a[36:39], v248 offset:19456\n"
    "v_mfma_f32_16x16x32_bf16 v[228:231], a[128:131], a[20:23], v[228:231]\n"
    "ds_read_b128 a[40:43], v248 offset:5632\n"
    "v_mfma_f32_16x16x32_bf16 v[236:239], a[20:23], a[20:23], v[236:239]\n"
    "ds_read_b128 a[44:47], v248 offset:19968\n"
    "v_mfma_f32_16x16x32_bf16 v[176:179], a[134:137], a[20:23], 0\n"
    "s_waitcnt lgkmcnt(5)\n"
    "s_mov_b32 s68, 524288\n"
    "s_add_u32 m0, s69, 81920\n"
    "v_mfma_f32_16x16x32_bf16 v[180:183], a[132:135], a[20:23], 0\n"
    "buffer_load_dwordx4 v246, s[60:63], s68 offen offset:0 nt lds\n"
    "v_mfma_f32_16x16x32_bf16 v[224:227], a[128:131], a[24:27], v[224:227]\n"
    "s_add_u32 m0, s69, 82816\n"
    "v_mfma_f32_16x16x32_bf16 v[232:235], a[24:27], a[24:27], v[232:235]\n"
    "buffer_load_dwordx4 v246, s[60:63], s68 offen offset:128 nt lds\n"
    "v_mfma_f32_16x16x32_bf16 v[72:75], a[134:137], a[24:27], 0\n"
    "s_waitcnt lgkmcnt(4)\n"
    "s_mov_b32 s68, 786432\n"
    "s_add_u32 m0, s69, 83968\n"
    "v_mfma_f32_16x16x32_bf16 v[76:79], a[132:135], a[24:27], 0\n"
    "buffer_load_dwordx4 v246, s[60:63], s68 offen offset:0 nt lds\n"
    "v_mfma_f32_16x16x32_bf16 v[228:231], a[128:131], a[28:31], v[228:231]\n"
    "s_add_u32 m0, s69, 84864\n"
    "v_mfma_f32_16x16x32_bf16 v[236:239], a[28:31], a[28:31], v[236:239]\n"
    "buffer_load_dwordx4 v246, s[60:63], s68 offen offset:128 nt lds\n"
    "v_mfma_f32_16x16x32_bf16 v[184:187], a[134:137], a[28:31], 0\n"
    "v_mfma_f32_16x16x32_bf16 v[188:191], a[132:135], a[28:31], 0\n"
    "s_waitcnt lgkmcnt(3)\n"
    "v_mfma_f32_16x16x32_bf16 v[224:227], a[128:131], a[32:35], v[224:227]\n"
    "s_waitcnt vmcnt(8)\n"
    "ds_read_b128 a[48:51], v248 offset:6144\n"
    "ds_read_b128 a[52:55], v248 offset:20480\n"
    "v_mfma_f32_16x16x32_bf16 v[232:235], a[32:35], a[32:35], v[232:235]\n"
    "s_waitcnt lgkmcnt(4)\n"
    "ds_read_b128 a[56:59], v248 offset:6656\n"
    "ds_read_b128 a[60:63], v248 offset:20992\n"
    "v_mfma_f32_16x16x32_bf16 v[228:231], a[128:131], a[36:39], v[228:231]\n"
    "v_mfma_f32_16x16x32_bf16 v[236:239], a[36:39], a[36:39], v[236:239]\n"
    "s_waitcnt lgkmcnt(5)\n"
    "v_mfma_f32_16x16x32_bf16 v[224:227], a[128:131], a[40:43], v[224:227]\n"
    "v_mfma_f32_16x16x32_bf16 v[232:235], a[40:43], a[40:43], v[232:235]\n"
    "s_waitcnt lgkmcnt(4)\n"
    "v_mfma_f32_16x16x32_bf16 v[228:231], a[128:131], a[44:47], v[228:231]\n"
    "v_mfma_f32_16x16x32_bf16 v[236:239], a[44:47], a[44:47], v[236:239]\n"
    "s_waitcnt lgkmcnt(3)\n"
    "v_mfma_f32_16x16x32_bf16 v[224:227], a[128:131], a[48:51], v[224:227]\n"
    "v_mfma_f32_16x16x32_bf16 v[232:235], a[48:51], a[48:51], v[232:235]\n"
    "s_waitcnt lgkmcnt(2)\n"
    "v_mfma_f32_16x16x32_bf16 v[228:231], a[128:131], a[52:55], v[228:231]\n"
    "v_mfma_f32_16x16x32_bf16 v[236:239], a[52:55], a[52:55], v[236:239]\n"
    "s_waitcnt lgkmcnt(1)\n"
    "v_mfma_f32_16x16x32_bf16 v[224:227], a[128:131], a[56:59], v[224:227]\n"
    "v_mfma_f32_16x16x32_bf16 v[232:235], a[56:59], a[56:59], v[232:235]\n"
    "s_waitcnt lgkmcnt(0)\n"
    "v_mfma_f32_16x16x32_bf16 v[228:231], a[128:131], a[60:63], v[228:231]\n"
    "v_mfma_f32_16x16x32_bf16 v[236:239], a[60:63], a[60:63], v[236:239]\n"
    "s_barrier\n"
    "s_mul_i32 s58, s50, 896\n"
    "v_mfma_f32_16x16x32_bf16 v[80:83], a[134:137], a[32:35], 0\n"
    "s_add_u32 s58, s58, 4096\n"
    "v_lshrrev_b32 v248, 4, v250\n"
    "v_lshlrev_b32 v248, 2, v248\n"
    "v_mfma_f32_16x16x32_bf16 v[84:87], a[132:135], a[32:35], 0\n"
    "buffer_load_dwordx4 a[244:247], v249, s[64:67], s58 offen sc1\n"
    "v_and_b32 v245, 15, v250\n"
    "v_sub_u32 v248, v245, v248\n"
    "v_mfma_f32_16x16x32_bf16 v[192:195], a[134:137], a[36:39], 0\n"
    "s_mov_b32 s68, 1048576\n"
    "v_cmp_eq_u32 vcc, 0, v248\n"
    "v_mfma_f32_16x16x32_bf16 v[196:199], a[132:135], a[36:39], 0\n"
    "s_add_u32 m0, s69, 16384\n"
    "v_cndmask_b32 v225, 0, v232, vcc\n"
    "v_cndmask_b32 v229, 0, v236, vcc\n"
    "v_cmp_eq_u32 vcc, 1, v248\n"
    "s_nop 1\n"
    "v_cndmask_b32 v225, v225, v233, vcc\n"
    "v_cndmask_b32 v229, v229, v237, vcc\n"
    "v_mfma_f32_16x16x32_bf16 v[88:91], a[134:137], a[40:43], 0\n"
    "buffer_load_dwordx4 v246, s[60:63], s68 offen offset:0 nt lds\n"
    "v_cmp_eq_u32 vcc, 2, v248\n"
    "s_nop 1\n"
    "v_cndmask_b32 v225, v225, v234, vcc\n"
    "v_cndmask_b32 v229, v229, v238, vcc\n"
    "v_mfma_f32_16x16x32_bf16 v[92:95], a[132:135], a[40:43], 0\n"
    "v_cmp_eq_u32 vcc, 3, v248\n"
    "s_nop 1\n"
    "v_cndmask_b32 v225, v225, v235, vcc\n"
    "v_cndmask_b32 v229, v229, v239, vcc\n"
    "v_mfma_f32_16x16x32_bf16 v[200:203], a[134:137], a[44:47], 0\n"
    "s_add_u32 m0, s69, 17280\n"
    "v_mov_b32 v226, v225\n"
    "v_mov_b32 v230, v229\n"
    "s_nop 0\n"
    "v_permlane32_swap_b32 v226, v225\n"
    "v_mfma_f32_16x16x32_bf16 v[204:207], a[132:135], a[44:47], 0\n"
    "buffer_load_dwordx4 v246, s[60:63], s68 offen offset:128 nt lds\n"
    "v_permlane32_swap_b32 v230, v229\n"
    "v_add_f32 v225, v225, v226\n"
    "v_add_f32 v229, v229, v230\n"
    "v_mfma_f32_16x16x32_bf16 v[96:99], a[134:137], a[48:51], 0\n"
    "s_mov_b32 s68, 1310720\n"
    "v_mov_b32 v226, v225\n"
    "v_mov_b32 v230, v229\n"
    "s_nop 0\n"
    "v_permlane16_swap_b32 v226, v225\n"
    "v_mfma_f32_16x16x32_bf16 v[100:103], a[132:135], a[48:51], 0\n"
    "s_add_u32 m0, s69, 18432\n"
    "v_permlane16_swap_b32 v230, v229\n"
    "v_add_f32 v225, v225, v226\n"
    "v_add_f32 v229, v229, v230\n"
    "v_mfma_f32_16x16x32_bf16 v[208:211], a[134:137], a[52:55], 0\n"
    "buffer_load_dwordx4 v246, s[60:63], s68 offen offset:0 nt lds\n"
    "v_mul_f32 v224, 0x3b124925, v224\n"
    "v_mul_f32 v225, 0x3b124925, v225\n"
    "v_fma_f32 v225, -v224, v224, v225\n"
    "v_mfma_f32_16x16x32_bf16 v[212:215], a[132:135], a[52:55], 0\n"
    "v_max_f32 v225, 0, v225\n"
    "v_add_f32 v225, 0x3727c5ac, v225\n"
    "v_rsq_f32 v225, v225\n"
    "v_mfma_f32_16x16x32_bf16 v[104:107], a[134:137], a[56:59], 0\n"
    "s_add_u32 m0, s69, 19328\n"
    "v_mul_f32 v224, v224, v225\n"
    "v_mul_f32 v228, 0x3b124925, v228\n"
    "v_mul_f32 v229, 0x3b124925, v229\n"
    "v_mfma_f32_16x16x32_bf16 v[108:111], a[132:135], a[56:59], 0\n"
    "buffer_load_dwordx4 v246, s[60:63], s68 offen offset:128 nt lds\n"
    "v_fma_f32 v229, -v228, v228, v229\n"
    "v_max_f32 v229, 0, v229\n"
    "v_add_f32 v229, 0x3727c5ac, v229\n"
    "v_mfma_f32_16x16x32_bf16 v[216:219], a[134:137], a[60:63], 0\n"
    "v_rsq_f32 v229, v229\n"
    "s_nop 0\n"
    "v_mul_f32 v228, v228, v229\n"
    "v_add_u32 v245, 0x20000, v241\n"
    "v_mfma_f32_16x16x32_bf16 v[220:223], a[132:135], a[60:63], 0\n"
    "s_mov_b32 s68, 1572864\n"
    "s_add_u32 m0, s69, 98304\n"
    "s_nop 0\n"
    "buffer_load_dwordx4 v246, s[60:63], s68 offen offset:0 nt lds\n"
    "v_pk_fma_f32 v[0:1], v[0:1], v[224:225], v[224:225] op_sel:[0,1,0] op_sel_hi:[1,1,0] neg_lo:[0,0,1] neg_hi:[0,0,1]\n"
    "v_pk_fma_f32 v[2:3], v[2:3], v[224:225], v[224:225] op_sel:[0,1,0] op_sel_hi:[1,1,0] neg_lo:[0,0,1] neg_hi:[0,0,1]\n"
    "v_pk_fma_f32 v[4:5], v[4:5], v[224:225], v[224:225] op_sel:[0,1,0] op_sel_hi:[1,1,0] neg_lo:[0,0,1] neg_hi:[0,0,1]\n"
    "v_pk_fma_f32 v[6:7], v[6:7], v[224:225], v[224:225] op_sel:[0,1,0] op_sel_hi:[1,1,0] neg_lo:[0,0,1] neg_hi:[0,0,1]\n"
    "v_pk_fma_f32 v[112:113], v[112:113], v[228:229], v[228:229] op_sel:[0,1,0] op_sel_hi:[1,1,0] neg_lo:[0,0,1] neg_hi:[0,0,1]\n"
    "v_pk_fma_f32 v[114:115], v[114:115], v[228:229], v[228:229] op_sel:[0,1,0] op_sel_hi:[1,1,0] neg_lo:[0,0,1] neg_hi:[0,0,1]\n"
    "v_pk_fma_f32 v[116:117], v[116:117], v[228:229], v[228:229] op_sel:[0,1,0] op_sel_hi:[1,1,0] neg_lo:[0,0,1] neg_hi:[0,0,1]\n"
    "v_pk_fma_f32 v[118:119], v[118:119], v[228:229], v[228:229] op_sel:[0,1,0] op_sel_hi:[1,1,0] neg_lo:[0,0,1] neg_hi:[0,0,1]\n"
    "v_pk_fma_f32 v[8:9], v[8:9], v[224:225], v[224:225] op_sel:[0,1,0] op_sel_hi:[1,1,0] neg_lo:[0,0,1] neg_hi:[0,0,1]\n"
    "v_pk_fma_f32 v[10:11], v[10:11], v[224:225], v[224:225] op_sel:[0,1,0] op_sel_hi:[1,1,0] neg_lo:[0,0,1] neg_hi:[0,0,1]\n"
    "s_add_u32 m0, s69, 99200\n"
    "s_nop 0\n"
    "buffer_load_dwordx4 v246, s[60:63], s68 offen offset:128 nt lds\n"
    "v_pk_fma_f32 v[12:13], v[12:13], v[224:225], v[224:225] op_sel:[0,1,0] op_sel_hi:[1,1,0] neg_lo:[0,0,1] neg_hi:[0,0,1]\n"
    "v_pk_fma_f32 v[14:15], v[14:15], v[224:225], v[224:225] op_sel:[0,1,0] op_sel_hi:[1,1,0] neg_lo:[0,0,1] neg_hi:[0,0,1]\n"
    "v_pk_fma_f32 v[120:121], v[120:121], v[228:229], v[228:229] op_sel:[0,1,0] op_sel_hi:[1,1,0] neg_lo:[0,0,1] neg_hi:[0,0,1]\n"
    "v_pk_fma_f32 v[122:123], v[122:123], v[228:229], v[228:229] op_sel:[0,1,0] op_sel_hi:[1,1,0] neg_lo:[0,0,1] neg_hi:[0,0,1]\n"
    "v_pk_fma_f32 v[124:125], v[124:125], v[228:229], v[228:229] op_sel:[0,1,0] op_sel_hi:[1,1,0] neg_lo:[0,0,1] neg_hi:[0,0,1]\n"
    "v_pk_fma_f32 v[126:127], v[126:127], v[228:229], v[228:229] op_sel:[0,1,0] op_sel_hi:[1,1,0] neg_lo:[0,0,1] neg_hi:[0,0,1]\n"
    "v_pk_fma_f32 v[16:17], v[16:17], v[224:225], v[224:225] op_sel:[0,1,0] op_sel_hi:[1,1,0] neg_lo:[0,0,1] neg_hi:[0,0,1]\n"
    "v_pk_fma_f32 v[18:19], v[18:19], v[224:225], v[224:225] op_sel:[0,1,0] op_sel_hi:[1,1,0] neg_lo:[0,0,1] neg_hi:[0,0,1]\n"
    "v_pk_fma_f32 v[20:21], v[20:21], v[224:225], v[224:225] op_sel:[0,1,0] op_sel_hi:[1,1,0] neg_lo:[0,0,1] neg_hi:[0,0,1]\n"
    "v_pk_fma_f32 v[22:23], v[22:23], v[224:225], v[224:225] op_sel:[0,1,0] op_sel_hi:[1,1,0] neg_lo:[0,0,1] neg_hi:[0,0,1]\n"
    "s_mov_b32 s68, 1835008\n"
    "s_add_u32 m0, s69, 100352\n"
    "s_nop 0\n"
    "buffer_load_dwordx4 v246, s[60:63], s68 offen offset:0 nt lds\n"
    "v_pk_fma_f32 v[128:129], v[128:129], v[228:229], v[228:229] op_sel:[0,1,0] op_sel_hi:[1,1,0] neg_lo:[0,0,1] neg_hi:[0,0,1]\n"
    "v_pk_fma_f32 v[130:131], v[130:131], v[228:229], v[228:229] op_sel:[0,1,0] op_sel_hi:[1,1,0] neg_lo:[0,0,1] neg_hi:[0,0,1]\n"
    "v_pk_fma_f32 v[132:133], v[132:133], v[228:229], v[228:229] op_sel:[0,1,0] op_sel_hi:[1,1,0] neg_lo:[0,0,1] neg_hi:[0,0,1]\n"
    "v_pk_fma_f32 v[134:135], v[134:135], v[228:229], v[228:229] op_sel:[0,1,0] op_sel_hi:[1,1,0] neg_lo:[0,0,1] neg_hi:[0,0,1]\n"
    "v_pk_fma_f32 v[24:25], v[24:25], v[224:225], v[224:225] op_sel:[0,1,0] op_sel_hi:[1,1,0] neg_lo:[0,0,1] neg_hi:[0,0,1]\n"
    "v_pk_fma_f32 v[26:27], v[26:27], v[224:225], v[224:225] op_sel:[0,1,0] op_sel_hi:[1,1,0] neg_lo:[0,0,1] neg_hi:[0,0,1]\n"
    "v_pk_fma_f32 v[28:29], v[28:29], v[224:225], v[224:225] op_sel:[0,1,0] op_sel_hi:[1,1,0] neg_lo:[0,0,1] neg_hi:[0,0,1]\n"
    "v_pk_fma_f32 v[30:31], v[30:31], v[224:225], v[224:225] op_sel:[0,1,0] op_sel_hi:[1,1,0] neg_lo:[0,0,1] neg_hi:[0,0,1]\n"
    "v_pk_fma_f32 v[136:137], v[136:137], v[228:229], v[228:229] op_sel:[0,1,0] op_sel_hi:[1,1,0] neg_lo:[0,0,1] neg_hi:[0,0,1]\n"
    "v_pk_fma_f32 v[138:139], v[138:139], v[228:229], v[228:229] op_sel:[0,1,0] op_sel_hi:[1,1,0] neg_lo:[0,0,1] neg_hi:[0,0,1]\n"
    "s_add_u32 m0, s69, 101248\n"
    "s_nop 0\n"
    "buffer_load_dwordx4 v246, s[60:63], s68 offen offset:128 nt lds\n"
    "v_pk_fma_f32 v[140:141], v[140:141], v[228:229], v[228:229] op_sel:[0,1,0] op_sel_hi:[1,1,0] neg_lo:[0,0,1] neg_hi:[0,0,1]\n"
    "v_pk_fma_f32 v[142:143], v[142:143], v[228:229], v[228:229] op_sel:[0,1,0] op_sel_hi:[1,1,0] neg_lo:[0,0,1] neg_hi:[0,0,1]\n"
    "v_pk_fma_f32 v[32:33], v[32:33], v[224:225], v[224:225] op_sel:[0,1,0] op_sel_hi:[1,1,0] neg_lo:[0,0,1] neg_hi:[0,0,1]\n"
    "v_pk_fma_f32 v[34:35], v[34:35], v[224:225], v[224:225] op_sel:[0,1,0] op_sel_hi:[1,1,0] neg_lo:[0,0,1] neg_hi:[0,0,1]\n"
    "v_pk_fma_f32 v[36:37], v[36:37], v[224:225], v[224:225] op_sel:[0,1,0] op_sel_hi:[1,1,0] neg_lo:[0,0,1] neg_hi:[0,0,1]\n"
    "v_pk_fma_f32 v[38:39], v[38:39], v[224:225], v[224:225] op_sel:[0,1,0] op_sel_hi:[1,1,0] neg_lo:[0,0,1] neg_hi:[0,0,1]\n"
    "v_pk_fma_f32 v[144:145], v[144:145], v[228:229], v[228:229] op_sel:[0,1,0] op_sel_hi:[1,1,0] neg_lo:[0,0,1] neg_hi:[0,0,1]\n"
    "v_pk_fma_f32 v[146:147], v[146:147], v[228:229], v[228:229] op_sel:[0,1,0] op_sel_hi:[1,1,0] neg_lo:[0,0,1] neg_hi:[0,0,1]\n"
    "v_pk_fma_f32 v[148:149], v[148:149], v[228:229], v[228:229] op_sel:[0,1,0] op_sel_hi:[1,1,0] neg_lo:[0,0,1] neg_hi:[0,0,1]\n"
    "v_pk_fma_f32 v[150:151], v[150:151], v[228:229], v[228:229] op_sel:[0,1,0] op_sel_hi:[1,1,0] neg_lo:[0,0,1] neg_hi:[0,0,1]\n"
    "s_mov_b32 s68, 2097152\n"
    "s_add_u32 m0, s69, 114688\n"
    "s_nop 0\n"
    "buffer_load_dwordx4 v246, s[60:63], s68 offen offset:0 nt lds\n"
    "v_pk_fma_f32 v[40:41], v[40:41], v[224:225], v[224:225] op_sel:[0,1,0] op_sel_hi:[1,1,0] neg_lo:[0,0,1] neg_hi:[0,0,1]\n"
    "v_pk_fma_f32 v[42:43], v[42:43], v[224:225], v[224:225] op_sel:[0,1,0] op_sel_hi:[1,1,0] neg_lo:[0,0,1] neg_hi:[0,0,1]\n"
    "v_pk_fma_f32 v[44:45], v[44:45], v[224:225], v[224:225] op_sel:[0,1,0] op_sel_hi:[1,1,0] neg_lo:[0,0,1] neg_hi:[0,0,1]\n"
    "v_pk_fma_f32 v[46:47], v[46:47], v[224:225], v[224:225] op_sel:[0,1,0] op_sel_hi:[1,1,0] neg_lo:[0,0,1] neg_hi:[0,0,1]\n"
    "v_pk_fma_f32 v[152:153], v[152:153], v[228:229], v[228:229] op_sel:[0,1,0] op_sel_hi:[1,1,0] neg_lo:[0,0,1] neg_hi:[0,0,1]\n"
    "v_pk_fma_f32 v[154:155], v[154:155], v[228:229], v[228:229] op_sel:[0,1,0] op_sel_hi:[1,1,0] neg_lo:[0,0,1] neg_hi:[0,0,1]\n"
    "v_pk_fma_f32 v[156:157], v[156:157], v[228:229], v[228:229] op_sel:[0,1,0] op_sel_hi:[1,1,0] neg_lo:[0,0,1] neg_hi:[0,0,1]\n"
    "v_pk_fma_f32 v[158:159], v[158:159], v[228:229], v[228:229] op_sel:[0,1,0] op_sel_hi:[1,1,0] neg_lo:[0,0,1] neg_hi:[0,0,1]\n"
    "v_pk_fma_f32 v[48:49], v[48:49], v[224:225], v[224:225] op_sel:[0,1,0] op_sel_hi:[1,1,0] neg_lo:[0,0,1] neg_hi:[0,0,1]\n"
    "v_pk_fma_f32 v[50:51], v[50:51], v[224:225], v[224:225] op_sel:[0,1,0] op_sel_hi:[1,1,0] neg_lo:[0,0,1] neg_hi:[0,0,1]\n"
    "s_add_u32 m0, s69, 115584\n"
    "s_nop 0\n"
    "buffer_load_dwordx4 v246, s[60:63], s68 offen offset:128 nt lds\n"
    "v_pk_fma_f32 v[52:53], v[52:53], v[224:225], v[224:225] op_sel:[0,1,0] op_sel_hi:[1,1,0] neg_lo:[0,0,1] neg_hi:[0,0,1]\n"
    "v_pk_fma_f32 v[54:55], v[54:55], v[224:225], v[224:225] op_sel:[0,1,0] op_sel_hi:[1,1,0] neg_lo:[0,0,1] neg_hi:[0,0,1]\n"
    "v_pk_fma_f32 v[160:161], v[160:161], v[228:229], v[228:229] op_sel:[0,1,0] op_sel_hi:[1,1,0] neg_lo:[0,0,1] neg_hi:[0,0,1]\n"
    "v_pk_fma_f32 v[162:163], v[162:163], v[228:229], v[228:229] op_sel:[0,1,0] op_sel_hi:[1,1,0] neg_lo:[0,0,1] neg_hi:[0,0,1]\n"
    "v_pk_fma_f32 v[164:165], v[164:165], v[228:229], v[228:229] op_sel:[0,1,0] op_sel_hi:[1,1,0] neg_lo:[0,0,1] neg_hi:[0,0,1]\n"
    "v_pk_fma_f32 v[166:167], v[166:167], v[228:229], v[228:229] op_sel:[0,1,0] op_sel_hi:[1,1,0] neg_lo:[0,0,1] neg_hi:[0,0,1]\n"
    "v_pk_fma_f32 v[56:57], v[56:57], v[224:225], v[224:225] op_sel:[0,1,0] op_sel_hi:[1,1,0] neg_lo:[0,0,1] neg_hi:[0,0,1]\n"
    "v_pk_fma_f32 v[58:59], v[58:59], v[224:225], v[224:225] op_sel:[0,1,0] op_sel_hi:[1,1,0] neg_lo:[0,0,1] neg_hi:[0,0,1]\n"
    "v_pk_fma_f32 v[60:61], v[60:61], v[224:225], v[224:225] op_sel:[0,1,0] op_sel_hi:[1,1,0] neg_lo:[0,0,1] neg_hi:[0,0,1]\n"
    "v_pk_fma_f32 v[62:63], v[62:63], v[224:225], v[224:225] op_sel:[0,1,0] op_sel_hi:[1,1,0] neg_lo:[0,0,1] neg_hi:[0,0,1]\n"
    "s_mov_b32 s68, 2359296\n"
    "s_add_u32 m0, s69, 116736\n"
    "s_nop 0\n"
    "buffer_load_dwordx4 v246, s[60:63], s68 offen offset:0 nt lds\n"
    "v_pk_fma_f32 v[168:169], v[168:169], v[228:229], v[228:229] op_sel:[0,1,0] op_sel_hi:[1,1,0] neg_lo:[0,0,1] neg_hi:[0,0,1]\n"
    "v_pk_fma_f32 v[170:171], v[170:171], v[228:229], v[228:229] op_sel:[0,1,0] op_sel_hi:[1,1,0] neg_lo:[0,0,1] neg_hi:[0,0,1]\n"
    "v_pk_fma_f32 v[172:173], v[172:173], v[228:229], v[228:229] op_sel:[0,1,0] op_sel_hi:[1,1,0] neg_lo:[0,0,1] neg_hi:[0,0,1]\n"
    "v_pk_fma_f32 v[174:175], v[174:175], v[228:229], v[228:229] op_sel:[0,1,0] op_sel_hi:[1,1,0] neg_lo:[0,0,1] neg_hi:[0,0,1]\n"
    "v_pk_fma_f32 v[64:65], v[64:65], v[224:225], v[224:225] op_sel:[0,1,0] op_sel_hi:[1,1,0] neg_lo:[0,0,1] neg_hi:[0,0,1]\n"
    "v_pk_fma_f32 v[66:67], v[66:67], v[224:225], v[224:225] op_sel:[0,1,0] op_sel_hi:[1,1,0] neg_lo:[0,0,1] neg_hi:[0,0,1]\n"
    "v_pk_fma_f32 v[68:69], v[68:69], v[224:225], v[224:225] op_sel:[0,1,0] op_sel_hi:[1,1,0] neg_lo:[0,0,1] neg_hi:[0,0,1]\n"
    "v_pk_fma_f32 v[70:71], v[70:71], v[224:225], v[224:225] op_sel:[0,1,0] op_sel_hi:[1,1,0] neg_lo:[0,0,1] neg_hi:[0,0,1]\n"
    "v_pk_fma_f32 v[176:177], v[176:177], v[228:229], v[228:229] op_sel:[0,1,0] op_sel_hi:[1,1,0] neg_lo:[0,0,1] neg_hi:[0,0,1]\n"
    "v_pk_fma_f32 v[178:179], v[178:179], v[228:229], v[228:229] op_sel:[0,1,0] op_sel_hi:[1,1,0] neg_lo:[0,0,1] neg_hi:[0,0,1]\n"
    "s_add_u32 m0, s69, 117632\n"
    "s_nop 0\n"
    "buffer_load_dwordx4 v246, s[60:63], s68 offen offset:128 nt lds\n"
    "v_pk_fma_f32 v[180:181], v[180:181], v[228:229], v[228:229] op_sel:[0,1,0] op_sel_hi:[1,1,0] neg_lo:[0,0,1] neg_hi:[0,0,1]\n"
    "v_pk_fma_f32 v[182:183], v[182:183], v[228:229], v[228:229] op_sel:[0,1,0] op_sel_hi:[1,1,0] neg_lo:[0,0,1] neg_hi:[0,0,1]\n"
    "v_pk_fma_f32 v[72:73], v[72:73], v[224:225], v[224:225] op_sel:[0,1,0] op_sel_hi:[1,1,0] neg_lo:[0,0,1] neg_hi:[0,0,1]\n"
    "v_pk_fma_f32 v[74:75], v[74:75], v[224:225], v[224:225] op_sel:[0,1,0] op_sel_hi:[1,1,0] neg_lo:[0,0,1] neg_hi:[0,0,1]\n"
    "v_pk_fma_f32 v[76:77], v[76:77], v[224:225], v[224:225] op_sel:[0,1,0] op_sel_hi:[1,1,0] neg_lo:[0,0,1] neg_hi:[0,0,1]\n"
    "v_pk_fma_f32 v[78:79], v[78:79], v[224:225], v[224:225] op_sel:[0,1,0] op_sel_hi:[1,1,0] neg_lo:[0,0,1] neg_hi:[0,0,1]\n"
    "v_pk_fma_f32 v[184:185], v[184:185], v[228:229], v[228:229] op_sel:[0,1,0] op_sel_hi:[1,1,0] neg_lo:[0,0,1] neg_hi:[0,0,1]\n"
    "v_pk_fma_f32 v[186:187], v[186:187], v[228:229], v[228:229] op_sel:[0,1,0] op_sel_hi:[1,1,0] neg_lo:[0,0,1] neg_hi:[0,0,1]\n"
    "v_pk_fma_f32 v[188:189], v[188:189], v[228:229], v[228:229] op_sel:[0,1,0] op_sel_hi:[1,1,0] neg_lo:[0,0,1] neg_hi:[0,0,1]\n"
    "v_pk_fma_f32 v[190:191], v[190:191], v[228:229], v[228:229] op_sel:[0,1,0] op_sel_hi:[1,1,0] neg_lo:[0,0,1] neg_hi:[0,0,1]\n"
    "s_mov_b64 s[92:93], s[18:19]\n"
    "s_and_b32 s93, s93, 0xffff\n"
    "s_mov_b32 s94, 1605632\n"
    "s_mov_b32 s59, 0\n"
    "s_lshl_b32 m0, s50, 10\n"
    "s_add_u32 m0, m0, 147456\n"
    "s_nop 0\n"
    "buffer_load_dwordx4 v252, s[92:95], s59 offen nt lds\n"
    "v_pk_fma_f32 v[80:81], v[80:81], v[224:225], v[224:225] op_sel:[0,1,0] op_sel_hi:[1,1,0] neg_lo:[0,0,1] neg_hi:[0,0,1]\n"
    "v_pk_fma_f32 v[82:83], v[82:83], v[224:225], v[224:225] op_sel:[0,1,0] op_sel_hi:[1,1,0] neg_lo:[0,0,1] neg_hi:[0,0,1]\n"
    "v_pk_fma_f32 v[84:85], v[84:85], v[224:225], v[224:225] op_sel:[0,1,0] op_sel_hi:[1,1,0] neg_lo:[0,0,1] neg_hi:[0,0,1]\n"
    "v_pk_fma_f32 v[86:87], v[86:87], v[224:225], v[224:225] op_sel:[0,1,0] op_sel_hi:[1,1,0] neg_lo:[0,0,1] neg_hi:[0,0,1]\n"
    "v_pk_fma_f32 v[192:193], v[192:193], v[228:229], v[228:229] op_sel:[0,1,0] op_sel_hi:[1,1,0] neg_lo:[0,0,1] neg_hi:[0,0,1]\n"
    "v_pk_fma_f32 v[194:195], v[194:195], v[228:229], v[228:229] op_sel:[0,1,0] op_sel_hi:[1,1,0] neg_lo:[0,0,1] neg_hi:[0,0,1]\n"
    "v_pk_fma_f32 v[196:197], v[196:197], v[228:229], v[228:229] op_sel:[0,1,0] op_sel_hi:[1,1,0] neg_lo:[0,0,1] neg_hi:[0,0,1]\n"
    "v_pk_fma_f32 v[198:199], v[198:199], v[228:229], v[228:229] op_sel:[0,1,0] op_sel_hi:[1,1,0] neg_lo:[0,0,1] neg_hi:[0,0,1]\n"
    "v_pk_fma_f32 v[88:89], v[88:89], v[224:225], v[224:225] op_sel:[0,1,0] op_sel_hi:[1,1,0] neg_lo:[0,0,1] neg_hi:[0,0,1]\n"
    "v_pk_fma_f32 v[90:91], v[90:91], v[224:225], v[224:225] op_sel:[0,1,0] op_sel_hi:[1,1,0] neg_lo:[0,0,1] neg_hi:[0,0,1]\n"
    "s_mov_b64 s[92:93], s[20:21]\n"
    "s_and_b32 s93, s93, 0xffff\n"
    "s_mov_b32 s94, 1605632\n"
    "s_mov_b32 s59, 0\n"
    "s_lshl_b32 m0, s50, 10\n"
    "s_add_u32 m0, m0, 151552\n"
    "s_nop 0\n"
    "buffer_load_dwordx4 v252, s[92:95], s59 offen nt lds\n"
    "v_pk_fma_f32 v[92:93], v[92:93], v[224:225], v[224:225] op_sel:[0,1,0] op_sel_hi:[1,1,0] neg_lo:[0,0,1] neg_hi:[0,0,1]\n"
    "v_pk_fma_f32 v[94:95], v[94:95], v[224:225], v[224:225] op_sel:[0,1,0] op_sel_hi:[1,1,0] neg_lo:[0,0,1] neg_hi:[0,0,1]\n"
    "v_pk_fma_f32 v[200:201], v[200:201], v[228:229], v[228:229] op_sel:[0,1,0] op_sel_hi:[1,1,0] neg_lo:[0,0,1] neg_hi:[0,0,1]\n"
    "v_pk_fma_f32 v[202:203], v[202:203], v[228:229], v[228:229] op_sel:[0,1,0] op_sel_hi:[1,1,0] neg_lo:[0,0,1] neg_hi:[0,0,1]\n"
    "v_pk_fma_f32 v[204:205], v[204:205], v[228:229], v[228:229] op_sel:[0,1,0] op_sel_hi:[1,1,0] neg_lo:[0,0,1] neg_hi:[0,0,1]\n"
    "v_pk_fma_f32 v[206:207], v[206:207], v[228:229], v[228:229] op_sel:[0,1,0] op_sel_hi:[1,1,0] neg_lo:[0,0,1] neg_hi:[0,0,1]\n"
    "v_pk_fma_f32 v[96:97], v[96:97], v[224:225], v[224:225] op_sel:[0,1,0] op_sel_hi:[1,1,0] neg_lo:[0,0,1] neg_hi:[0,0,1]\n"
    "v_pk_fma_f32 v[98:99], v[98:99], v[224:225], v[224:225] op_sel:[0,1,0] op_sel_hi:[1,1,0] neg_lo:[0,0,1] neg_hi:[0,0,1]\n"
    "v_pk_fma_f32 v[100:101], v[100:101], v[224:225], v[224:225] op_sel:[0,1,0] op_sel_hi:[1,1,0] neg_lo:[0,0,1] neg_hi:[0,0,1]\n"
    "v_pk_fma_f32 v[102:103], v[102:103], v[224:225], v[224:225] op_sel:[0,1,0] op_sel_hi:[1,1,0] neg_lo:[0,0,1] neg_hi:[0,0,1]\n"
    "s_mov_b64 s[92:93], s[22:23]\n"
    "s_and_b32 s93, s93, 0xffff\n"
    "s_mov_b32 s94, 1605632\n"
    "s_mov_b32 s59, 0\n"
    "s_lshl_b32 m0, s50, 10\n"
    "s_add_u32 m0, m0, 159744\n"
    "s_nop 0\n"
    "buffer_load_dwordx4 v253, s[92:95], s59 offen nt lds\n"
    "v_pk_fma_f32 v[208:209], v[208:209], v[228:229], v[228:229] op_sel:[0,1,0] op_sel_hi:[1,1,0] neg_lo:[0,0,1] neg_hi:[0,0,1]\n"
    "v_pk_fma_f32 v[210:211], v[210:211], v[228:229], v[228:229] op_sel:[0,1,0] op_sel_hi:[1,1,0] neg_lo:[0,0,1] neg_hi:[0,0,1]\n"
    "v_pk_fma_f32 v[212:213], v[212:213], v[228:229], v[228:229] op_sel:[0,1,0] op_sel_hi:[1,1,0] neg_lo:[0,0,1] neg_hi:[0,0,1]\n"
    "v_pk_fma_f32 v[214:215], v[214:215], v[228:229], v[228:229] op_sel:[0,1,0] op_sel_hi:[1,1,0] neg_lo:[0,0,1] neg_hi:[0,0,1]\n"
    "v_pk_fma_f32 v[104:105], v[104:105], v[224:225], v[224:225] op_sel:[0,1,0] op_sel_hi:[1,1,0] neg_lo:[0,0,1] neg_hi:[0,0,1]\n"
    "v_pk_fma_f32 v[106:107], v[106:107], v[224:225], v[224:225] op_sel:[0,1,0] op_sel_hi:[1,1,0] neg_lo:[0,0,1] neg_hi:[0,0,1]\n"
    "v_pk_fma_f32 v[108:109], v[108:109], v[224:225], v[224:225] op_sel:[0,1,0] op_sel_hi:[1,1,0] neg_lo:[0,0,1] neg_hi:[0,0,1]\n"
    "v_pk_fma_f32 v[110:111], v[110:111], v[224:225], v[224:225] op_sel:[0,1,0] op_sel_hi:[1,1,0] neg_lo:[0,0,1] neg_hi:[0,0,1]\n"
    "v_pk_fma_f32 v[216:217], v[216:217], v[228:229], v[228:229] op_sel:[0,1,0] op_sel_hi:[1,1,0] neg_lo:[0,0,1] neg_hi:[0,0,1]\n"
    "v_pk_fma_f32 v[218:219], v[218:219], v[228:229], v[228:229] op_sel:[0,1,0] op_sel_hi:[1,1,0] neg_lo:[0,0,1] neg_hi:[0,0,1]\n"
    "v_pk_fma_f32 v[220:221], v[220:221], v[228:229], v[228:229] op_sel:[0,1,0] op_sel_hi:[1,1,0] neg_lo:[0,0,1] neg_hi:[0,0,1]\n"
    "v_pk_fma_f32 v[222:223], v[222:223], v[228:229], v[228:229] op_sel:[0,1,0] op_sel_hi:[1,1,0] neg_lo:[0,0,1] neg_hi:[0,0,1]\n"
    "s_waitcnt vmcnt(15)\n"
    "v_accvgpr_read_b32 v232, a244\n"
    "v_accvgpr_read_b32 v233, a245\n"
    "v_accvgpr_read_b32 v234, a246\n"
    "v_accvgpr_read_b32 v235, a247\n"
    "L_gr2_%=:\n"
    "v_xor_b32 v236, s53, v233\n"
    "v_xor_b32 v237, s53, v235\n"
    "v_or_b32 v236, v236, v237\n"
    "v_cmp_ne_u32 vcc, 0, v236\n"
    "s_nop 1\n"
    "s_cbranch_vccz L_grd3_%=\n"
    "s_sub_u32 s99, s99, 1\n"
    "s_cbranch_scc1 L_grd3_%=\n"
    "s_nop 0\n"
    "s_mul_i32 s58, s50, 896\n"
    "s_add_u32 s58, s58, 4096\n"
    "buffer_load_dwordx4 v[232:235], v249, s[64:67], s58 offen sc1\n"
    "s_waitcnt vmcnt(0)\n"
    "s_branch L_gr2_%=\n"
    "L_grd3_%=:\n"
    "s_max_i32 s99, s99, 0\n"
    "v_and_b32 v233, 0xffff0000, v232\n"
    "v_lshlrev_b32 v232, 16, v232\n"
    "v_and_b32 v235, 0xffff0000, v234\n"
    "v_lshlrev_b32 v234, 16, v234\n"
    "s_cmp_lt_u32 s50, 2\n"
    "s_cbranch_scc1 L_ms1_%=\n"
    "v_pk_add_f32 v[232:233], v[232:233], 1.0 op_sel_hi:[1,0]\n"
    "v_cvt_pk_bf16_f32 v236, v232, v233\n"
    "v_and_b32 v233, 0xffff0000, v236\n"
    "v_lshlrev_b32 v232, 16, v236\n"
    "v_pk_add_f32 v[234:235], v[234:235], 1.0 op_sel_hi:[1,0]\n"
    "v_cvt_pk_bf16_f32 v236, v234, v235\n"
    "v_and_b32 v235, 0xffff0000, v236\n"
    "v_lshlrev_b32 v234, 16, v236\n"
    "L_ms1_%=:\n"
    "s_nop 0\n"
    "v_mul_f32 v232, 0x3db8aa3b, v232\n"
    "v_mul_f32 v233, 0x3db8aa3b, v233\n"
    "v_mul_f32 v234, 0x3db8aa3b, v234\n"
    "v_mul_f32 v235, 0x3db8aa3b, v235\n"
    "s_xor_b32 s54, s50, 2\n"
    "s_mul_i32 s54, s54, 896\n"
    "s_add_u32 s54, s54, 155648\n"
    "v_add_u32 v236, s54, v249\n"
    "ds_write_b128 v236, v[232:235]\n"
    "s_waitcnt lgkmcnt(0)\n"
    "s_barrier\n"
    "v_lshlrev_b32 v248, 4, v250\n"
    "ds_read_b128 v[224:227], v247 offset:0\n"
    "ds_read_b128 v[228:231], v247 offset:16\n"
    "ds_read_b128 v[232:235], v247 offset:1792\n"
    "ds_read_b128 v[236:239], v247 offset:1808\n"
    "ds_read_b64_tr_b16 a[128:129], v242 offset:0\n"
    "ds_read_b64_tr_b16 a[130:131], v242 offset:256\n"
    "ds_read_b64_tr_b16 a[132:133], v242 offset:2048\n"
    "ds_read_b64_tr_b16 a[134:135], v242 offset:2304\n"
    "ds_read_b64_tr_b16 a[136:137], v243 offset:0\n"
    "ds_read_b64_tr_b16 a[138:139], v243 offset:256\n"
    "ds_read_b64_tr_b16 a[140:141], v243 offset:2048\n"
    "ds_read_b64_tr_b16 a[142:143], v243 offset:2304\n"
    "ds_read_b64_tr_b16 a[144:145], v242 offset:512\n"
    "ds_read_b64_tr_b16 a[146:147], v242 offset:768\n"
    "ds_read_b64_tr_b16 a[148:149], v242 offset:2560\n"
    "ds_read_b64_tr_b16 a[150:151], v242 offset:2816\n"
    "ds_read_b64_tr_b16 a[152:153], v243 offset:512\n"
    "ds_read_b64_tr_b16 a[154:155], v243 offset:768\n"
    "ds_read_b64_tr_b16 a[156:157], v243 offset:2560\n"
    "ds_read_b64_tr_b16 a[158:159], v243 offset:2816\n"
    "s_waitcnt lgkmcnt(14)\n"
    "v_pk_fma_f32 v[0:1], v[0:1], v[224:225], v[232:233]\n"
    "v_pk_fma_f32 v[2:3], v[2:3], v[226:227], v[234:235]\n"
    "v_pk_fma_f32 v[4:5], v[4:5], v[228:229], v[236:237]\n"
    "v_pk_fma_f32 v[6:7], v[6:7], v[230:231], v[238:239]\n"
    "v_pk_fma_f32 v[112:113], v[112:113], v[224:225], v[232:233]\n"
    "v_pk_fma_f32 v[114:115], v[114:115], v[226:227], v[234:235]\n"
    "v_pk_fma_f32 v[116:117], v[116:117], v[228:229], v[236:237]\n"
    "v_pk_fma_f32 v[118:119], v[118:119], v[230:231], v[238:239]\n"
    "ds_read_b128 v[224:227], v247 offset:128\n"
    "ds_read_b128 v[228:231], v247 offset:144\n"
    "ds_read_b128 v[232:235], v247 offset:1920\n"
    "ds_read_b128 v[236:239], v247 offset:1936\n"
    "v_cvt_pk_bf16_f32 v0, v0, v1\n"
    "v_cvt_pk_bf16_f32 v1, v2, v3\n"
    "v_cvt_pk_bf16_f32 v2, v4, v5\n"
    "v_cvt_pk_bf16_f32 v3, v6, v7\n"
    "v_cvt_pk_bf16_f32 v112, v112, v113\n"
    "v_cvt_pk_bf16_f32 v113, v114, v115\n"
    "v_cvt_pk_bf16_f32 v114, v116, v117\n"
    "v_cvt_pk_bf16_f32 v115, v118, v119\n"
    "s_waitcnt lgkmcnt(1)\n"
    "v_pk_fma_f32 v[8:9], v[8:9], v[224:225], v[232:233]\n"
    "v_pk_fma_f32 v[10:11], v[10:11], v[226:227], v[234:235]\n"
    "s_waitcnt lgkmcnt(0)\n"
    "v_pk_fma_f32 v[12:13], v[12:13], v[228:229], v[236:237]\n"
    "v_pk_fma_f32 v[14:15], v[14:15], v[230:231], v[238:239]\n"
    "v_pk_fma_f32 v[120:121], v[120:121], v[224:225], v[232:233]\n"
    "v_pk_fma_f32 v[122:123], v[122:123], v[226:227], v[234:235]\n"
    "v_pk_fma_f32 v[124:125], v[124:125], v[228:229], v[236:237]\n"
    "v_pk_fma_f32 v[126:127], v[126:127], v[230:231], v[238:239]\n"
    "ds_read_b128 v[224:227], v247 offset:256\n"
    "ds_read_b128 v[228:231], v247 offset:272\n"
    "ds_read_b128 v[232:235], v247 offset:2048\n"
    "ds_read_b128 v[236:239], v247 offset:2064\n"
    "v_cvt_pk_bf16_f32 v8, v8, v9\n"
    "v_cvt_pk_bf16_f32 v9, v10, v11\n"
    "v_cvt_pk_bf16_f32 v10, v12, v13\n"
    "v_cvt_pk_bf16_f32 v11, v14, v15\n"
    "v_cvt_pk_bf16_f32 v120, v120, v121\n"
    "v_cvt_pk_bf16_f32 v121, v122, v123\n"
    "v_cvt_pk_bf16_f32 v122, v124, v125\n"
    "v_cvt_pk_bf16_f32 v123, v126, v127\n"
    "ds_read_b128 v[4:7], v247 offset:384\n"
    "ds_read_b128 v[116:119], v247 offset:400\n"
    "ds_read_b128 v[12:15], v247 offset:2176\n"
    "ds_read_b128 v[124:127], v247 offset:2192\n"
    "s_waitcnt lgkmcnt(5)\n"
    "v_pk_fma_f32 v[16:17], v[16:17], v[224:225], v[232:233]\n"
    "v_pk_fma_f32 v[18:19], v[18:19], v[226:227], v[234:235]\n"
    "s_waitcnt lgkmcnt(4)\n"
    "v_pk_fma_f32 v[20:21], v[20:21], v[228:229], v[236:237]\n"
    "v_pk_fma_f32 v[22:23], v[22:23], v[230:231], v[238:239]\n"
    "v_pk_fma_f32 v[128:129], v[128:129], v[224:225], v[232:233]\n"
    "v_pk_fma_f32 v[130:131], v[130:131], v[226:227], v[234:235]\n"
    "v_pk_fma_f32 v[132:133], v[132:133], v[228:229], v[236:237]\n"
    "v_pk_fma_f32 v[134:135], v[134:135], v[230:231], v[238:239]\n"
    "v_cvt_pk_bf16_f32 v16, v16, v17\n"
    "v_cvt_pk_bf16_f32 v17, v18, v19\n"
    "v_cvt_pk_bf16_f32 v18, v20, v21\n"
    "v_cvt_pk_bf16_f32 v19, v22, v23\n"
    "v_cvt_pk_bf16_f32 v128, v128, v129\n"
    "v_cvt_pk_bf16_f32 v129, v130, v131\n"
    "v_cvt_pk_bf16_f32 v130, v132, v133\n"
    "v_cvt_pk_bf16_f32 v131, v134, v135\n"
    "ds_read_b128 v[224:227], v247 offset:512\n"
    "ds_read_b128 v[228:231], v247 offset:528\n"
    "ds_read_b128 v[232:235], v247 offset:2304\n"
    "ds_read_b128 v[236:239], v247 offset:2320\n"
    "s_waitcnt lgkmcnt(5)\n"
    "v_pk_fma_f32 v[24:25], v[24:25], v[4:5], v[12:13]\n"
    "v_pk_fma_f32 v[26:27], v[26:27], v[6:7], v[14:15]\n"
    "s_waitcnt lgkmcnt(4)\n"
    "v_pk_fma_f32 v[28:29], v[28:29], v[116:117], v[124:125]\n"
    "v_pk_fma_f32 v[30:31], v[30:31], v[118:119], v[126:127]\n"
    "v_pk_fma_f32 v[136:137], v[136:137], v[4:5], v[12:13]\n"
    "v_pk_fma_f32 v[138:139], v[138:139], v[6:7], v[14:15]\n"
    "v_pk_fma_f32 v[140:141], v[140:141], v[116:117], v[124:125]\n"
    "v_pk_fma_f32 v[142:143], v[142:143], v[118:119], v[126:127]\n"
    "v_cvt_pk_bf16_f32 v24, v24, v25\n"
    "v_cvt_pk_bf16_f32 v25, v26, v27\n"
    "v_cvt_pk_bf16_f32 v26, v28, v29\n"
    "v_cvt_pk_bf16_f32 v27, v30, v31\n"
    "v_cvt_pk_bf16_f32 v136, v136, v137\n"
    "v_cvt_pk_bf16_f32 v137, v138, v139\n"
    "v_cvt_pk_bf16_f32 v138, v140, v141\n"
    "v_cvt_pk_bf16_f32 v139, v142, v143\n"
    "ds_read_b128 v[4:7], v247 offset:640\n"
    "ds_read_b128 v[116:119], v247 offset:656\n"
    "ds_read_b128 v[12:15], v247 offset:2432\n"
    "ds_read_b128 v[124:127], v247 offset:2448\n"
    "s_waitcnt lgkmcnt(5)\n"
    "v_pk_fma_f32 v[32:33], v[32:33], v[224:225], v[232:233]\n"
    "v_pk_fma_f32 v[34:35], v[34:35], v[226:227], v[234:235]\n"
    "s_waitcnt lgkmcnt(4)\n"
    "v_pk_fma_f32 v[36:37], v[36:37], v[228:229], v[236:237]\n"
    "v_pk_fma_f32 v[38:39], v[38:39], v[230:231], v[238:239]\n"
    "v_pk_fma_f32 v[144:145], v[144:145], v[224:225], v[232:233]\n"
    "v_pk_fma_f32 v[146:147], v[146:147], v[226:227], v[234:235]\n"
    "v_pk_fma_f32 v[148:149], v[148:149], v[228:229], v[236:237]\n"
    "v_pk_fma_f32 v[150:151], v[150:151], v[230:231], v[238:239]\n"
    "v_cvt_pk_bf16_f32 v32, v32, v33\n"
    "v_cvt_pk_bf16_f32 v33, v34, v35\n"
    "v_cvt_pk_bf16_f32 v34, v36, v37\n"
    "v_cvt_pk_bf16_f32 v35, v38, v39\n"
    "v_cvt_pk_bf16_f32 v144, v144, v145\n"
    "v_cvt_pk_bf16_f32 v145, v146, v147\n"
    "v_cvt_pk_bf16_f32 v146, v148, v149\n"
    "v_cvt_pk_bf16_f32 v147, v150, v151\n"
    "ds_read_b128 v[224:227], v247 offset:768\n"
    "ds_read_b128 v[228:231], v247 offset:784\n"
    "ds_read_b128 v[232:235], v247 offset:2560\n"
    "ds_read_b128 v[236:239], v247 offset:2576\n"
    "s_waitcnt lgkmcnt(5)\n"
    "v_pk_fma_f32 v[40:41], v[40:41], v[4:5], v[12:13]\n"
    "v_pk_fma_f32 v[42:43], v[42:43], v[6:7], v[14:15]\n"
    "s_waitcnt lgkmcnt(4)\n"
    "v_pk_fma_f32 v[44:45], v[44:45], v[116:117], v[124:125]\n"
    "v_pk_fma_f32 v[46:47], v[46:47], v[118:119], v[126:127]\n"
    "v_pk_fma_f32 v[152:153], v[152:153], v[4:5], v[12:13]\n"
    "v_pk_fma_f32 v[154:155], v[154:155], v[6:7], v[14:15]\n"
    "v_pk_fma_f32 v[156:157], v[156:157], v[116:117], v[124:125]\n"
    "v_pk_fma_f32 v[158:159], v[158:159], v[118:119], v[126:127]\n"
    "v_cvt_pk_bf16_f32 v40, v40, v41\n"
    "v_cvt_pk_bf16_f32 v41, v42, v43\n"
    "v_cvt_pk_bf16_f32 v42, v44, v45\n"
    "v_cvt_pk_bf16_f32 v43, v46, v47\n"
    "v_cvt_pk_bf16_f32 v152, v152, v153\n"
    "v_cvt_pk_bf16_f32 v153, v154, v155\n"
    "v_cvt_pk_bf16_f32 v154, v156, v157\n"
    "v_cvt_pk_bf16_f32 v155, v158, v159\n"
    "ds_read_b128 v[4:7], v247 offset:896\n"
    "ds_read_b128 v[116:119], v247 offset:912\n"
    "ds_read_b128 v[12:15], v247 offset:2688\n"
    "ds_read_b128 v[124:127], v247 offset:2704\n"
    "s_waitcnt lgkmcnt(5)\n"
    "v_pk_fma_f32 v[48:49], v[48:49], v[224:225], v[232:233]\n"
    "v_pk_fma_f32 v[50:51], v[50:51], v[226:227], v[234:235]\n"
    "s_waitcnt lgkmcnt(4)\n"
    "v_pk_fma_f32 v[52:53], v[52:53], v[228:229], v[236:237]\n"
    "v_pk_fma_f32 v[54:55], v[54:55], v[230:231], v[238:239]\n"
    "v_pk_fma_f32 v[160:161], v[160:161], v[224:225], v[232:233]\n"
    "v_pk_fma_f32 v[162:163], v[162:163], v[226:227], v[234:235]\n"
    "v_pk_fma_f32 v[164:165], v[164:165], v[228:229], v[236:237]\n"
    "v_pk_fma_f32 v[166:167], v[166:167], v[230:231], v[238:239]\n"
    "v_cvt_pk_bf16_f32 v48, v48, v49\n"
    "v_cvt_pk_bf16_f32 v49, v50, v51\n"
    "v_cvt_pk_bf16_f32 v50, v52, v53\n"
    "v_cvt_pk_bf16_f32 v51, v54, v55\n"
    "v_cvt_pk_bf16_f32 v160, v160, v161\n"
    "v_cvt_pk_bf16_f32 v161, v162, v163\n"
    "v_cvt_pk_bf16_f32 v162, v164, v165\n"
    "v_cvt_pk_bf16_f32 v163, v166, v167\n"
    "ds_read_b128 v[224:227], v247 offset:1024\n"
    "ds_read_b128 v[228:231], v247 offset:1040\n"
    "ds_read_b128 v[232:235], v247 offset:2816\n"
    "ds_read_b128 v[236:239], v247 offset:2832\n"
    "s_waitcnt lgkmcnt(5)\n"
    "v_pk_fma_f32 v[56:57], v[56:57], v[4:5], v[12:13]\n"
    "v_pk_fma_f32 v[58:59], v[58:59], v[6:7], v[14:15]\n"
    "s_waitcnt lgkmcnt(4)\n"
    "v_pk_fma_f32 v[60:61], v[60:61], v[116:117], v[124:125]\n"
    "v_pk_fma_f32 v[62:63], v[62:63], v[118:119], v[126:127]\n"
    "v_pk_fma_f32 v[168:169], v[168:169], v[4:5], v[12:13]\n"
    "v_pk_fma_f32 v[170:171], v[170:171], v[6:7], v[14:15]\n"
    "v_pk_fma_f32 v[172:173], v[172:173], v[116:117], v[124:125]\n"
    "v_pk_fma_f32 v[174:175], v[174:175], v[118:119], v[126:127]\n"
    "v_cvt_pk_bf16_f32 v56, v56, v57\n"
    "v_cvt_pk_bf16_f32 v57, v58, v59\n"
    "v_cvt_pk_bf16_f32 v58, v60, v61\n"
    "v_cvt_pk_bf16_f32 v59, v62, v63\n"
    "v_cvt_pk_bf16_f32 v168, v168, v169\n"
    "v_cvt_pk_bf16_f32 v169, v170, v171\n"
    "v_cvt_pk_bf16_f32 v170, v172, v173\n"
    "v_cvt_pk_bf16_f32 v171, v174, v175\n"
    "s_waitcnt lgkmcnt(14)\n"
    "v_mfma_f32_16x16x32_bf16 a[16:19], a[128:131], v[0:3], 0\n"
    "s_waitcnt lgkmcnt(1)\n"
    "s_mov_b32 s68, 2621440\n"
    "v_fma_f32 v64, v64, v224, v232\n"
    "v_fma_f32 v65, v65, v225, v233\n"
    "v_mfma_f32_16x16x32_bf16 a[16:19], a[132:135], v[8:11], a[16:19]\n"
    "s_add_u32 m0, s69, 131072\n"
    "v_cvt_pk_bf16_f32 v64, v64, v65\n"
    "v_fma_f32 v66, v66, v226, v234\n"
    "v_mfma_f32_16x16x32_bf16 a[20:23], a[128:131], v[112:115], 0\n"
    "buffer_load_dwordx4 v246, s[60:63], s68 offen offset:0 nt lds\n"
    "v_mfma_f32_16x16x32_bf16 a[20:23], a[132:135], v[120:123], a[20:23]\n"
    "s_nop 0\n"
    "ds_read_b128 v[4:7], v247 offset:1152\n"
    "ds_read_b64_tr_b16 a[160:161], v242 offset:1024\n"
    "v_mfma_f32_16x16x32_bf16 a[24:27], a[136:139], v[0:3], 0\n"
    "ds_read_b128 v[116:119], v247 offset:1168\n"
    "v_fma_f32 v67, v67, v227, v235\n"
    "v_mfma_f32_16x16x32_bf16 a[24:27], a[140:143], v[8:11], a[24:27]\n"
    "ds_read_b64_tr_b16 a[162:163], v242 offset:1280\n"
    "v_cvt_pk_bf16_f32 v65, v66, v67\n"
    "v_mfma_f32_16x16x32_bf16 a[28:31], a[136:139], v[112:115], 0\n"
    "s_waitcnt lgkmcnt(4)\n"
    "ds_read_b128 v[12:15], v247 offset:2944\n"
    "ds_read_b64_tr_b16 a[164:165], v242 offset:3072\n"
    "v_mfma_f32_16x16x32_bf16 a[28:31], a[140:143], v[120:123], a[28:31]\n"
    "s_add_u32 m0, s69, 131968\n"
    "ds_read_b64_tr_b16 a[166:167], v242 offset:3328\n"
    "v_fma_f32 v68, v68, v228, v236\n"
    "v_mfma_f32_16x16x32_bf16 a[32:35], a[144:147], v[0:3], 0\n"
    "buffer_load_dwordx4 v246, s[60:63], s68 offen offset:128 nt lds\n"
    "v_mfma_f32_16x16x32_bf16 a[32:35], a[148:151], v[8:11], a[32:35]\n"
    "s_nop 0\n"
    "ds_read_b128 v[124:127], v247 offset:2960\n"
    "ds_read_b64_tr_b16 a[168:169], v243 offset:1024\n"
    "v_mfma_f32_16x16x32_bf16 a[36:39], a[144:147], v[112:115], 0\n"
    "ds_read_b64_tr_b16 a[170:171], v243 offset:1280\n"
    "v_fma_f32 v69, v69, v229, v237\n"
    "v_mfma_f32_16x16x32_bf16 a[36:39], a[148:151], v[120:123], a[36:39]\n"
    "ds_read_b64_tr_b16 a[172:173], v243 offset:3072\n"
    "v_cvt_pk_bf16_f32 v66, v68, v69\n"
    "v_mfma_f32_16x16x32_bf16 a[40:43], a[152:155], v[0:3], 0\n"
    "s_mov_b32 s68, 2883584\n"
    "ds_read_b64_tr_b16 a[174:175], v243 offset:3328\n"
    "v_fma_f32 v70, v70, v230, v238\n"
    "v_mfma_f32_16x16x32_bf16 a[40:43], a[156:159], v[8:11], a[40:43]\n"
    "s_add_u32 m0, s69, 133120\n"
    "ds_read_b64_tr_b16 a[176:177], v242 offset:1536\n"
    "v_fma_f32 v71, v71, v231, v239\n"
    "v_mfma_f32_16x16x32_bf16 a[44:47], a[152:155], v[112:115], 0\n"
    "buffer_load_dwordx4 v246, s[60:63], s68 offen offset:0 nt lds\n"
    "v_mfma_f32_16x16x32_bf16 a[44:47], a[156:159], v[120:123], a[44:47]\n"
    "s_waitcnt lgkmcnt(9)\n"
    "ds_read_b64_tr_b16 a[178:179], v242 offset:1792\n"
    "v_cvt_pk_bf16_f32 v67, v70, v71\n"
    "v_mfma_f32_16x16x32_bf16 a[48:51], a[160:163], v[0:3], 0\n"
    "s_waitcnt lgkmcnt(7)\n"
    "ds_read_b64_tr_b16 a[180:181], v242 offset:3584\n"
    "ds_read_b64_tr_b16 a[182:183], v242 offset:3840\n"
    "v_mfma_f32_16x16x32_bf16 a[48:51], a[164:167], v[8:11], a[48:51]\n"
    "ds_read_b64_tr_b16 a[184:185], v243 offset:1536\n"
    "v_fma_f32 v176, v176, v224, v232\n"
    "v_mfma_f32_16x16x32_bf16 a[52:55], a[160:163], v[112:115], 0\n"
    "s_waitcnt lgkmcnt(5)\n"
    "ds_read_b64_tr_b16 a[186:187], v243 offset:1792\n"
    "v_fma_f32 v177, v177, v225, v233\n"
    "v_mfma_f32_16x16x32_bf16 a[52:55], a[164:167], v[120:123], a[52:55]\n"
    "s_add_u32 m0, s69, 134016\n"
    "ds_read_b64_tr_b16 a[188:189], v243 offset:3584\n"
    "v_cvt_pk_bf16_f32 v176, v176, v177\n"
    "v_mfma_f32_16x16x32_bf16 a[56:59], a[168:171], v[0:3], 0\n"
    "buffer_load_dwordx4 v246, s[60:63], s68 offen offset:128 nt lds\n"
    "v_mfma_f32_16x16x32_bf16 a[56:59], a[172:175], v[8:11], a[56:59]\n"
    "ds_read_b64_tr_b16 a[190:191], v243 offset:3840\n"
    "v_fma_f32 v178, v178, v226, v234\n"
    "v_mfma_f32_16x16x32_bf16 a[60:63], a[168:171], v[112:115], 0\n"
    "ds_read_b64_tr_b16 a[128:129], v242 offset:16384\n"
    "v_fma_f32 v179, v179, v227, v235\n"
    "v_mfma_f32_16x16x32_bf16 a[60:63], a[172:175], v[120:123], a[60:63]\n"
    "s_waitcnt lgkmcnt(7)\n"
    "ds_read_b64_tr_b16 a[130:131], v242 offset:16640\n"
    "v_cvt_pk_bf16_f32 v177, v178, v179\n"
    "v_mfma_f32_16x16x32_bf16 a[64:67], a[176:179], v[0:3], 0\n"
    "s_waitcnt lgkmcnt(4)\n"
    "ds_read_b64_tr_b16 a[132:133], v242 offset:18432\n"
    "ds_read_b64_tr_b16 a[134:135], v242 offset:18688\n"
    "v_mfma_f32_16x16x32_bf16 a[64:67], a[180:183], v[8:11], a[64:67]\n"
    "s_mov_b32 s68, 3145728\n"
    "ds_read_b64_tr_b16 a[136:137], v243 offset:16384\n"
    "v_fma_f32 v180, v180, v228, v236\n"
    "v_mfma_f32_16x16x32_bf16 a[68:71], a[176:179], v[112:115], 0\n"
    "s_add_u32 m0, s69, 49152\n"
    "ds_read_b64_tr_b16 a[138:139], v243 offset:16640\n"
    "v_fma_f32 v181, v181, v229, v237\n"
    "v_mfma_f32_16x16x32_bf16 a[68:71], a[180:183], v[120:123], a[68:71]\n"
    "buffer_load_dwordx4 v246, s[60:63], s68 offen offset:0 nt lds\n"
    "v_cvt_pk_bf16_f32 v178, v180, v181\n"
    "v_mfma_f32_16x16x32_bf16 a[72:75], a[184:187], v[0:3], 0\n"
    "s_waitcnt lgkmcnt(6)\n"
    "ds_read_b64_tr_b16 a[140:141], v243 offset:18432\n"
    "ds_read_b64_tr_b16 a[142:143], v243 offset:18688\n"
    "v_mfma_f32_16x16x32_bf16 a[72:75], a[188:191], v[8:11], a[72:75]\n"
    "ds_read_b64_tr_b16 a[144:145], v242 offset:16896\n"
    "v_fma_f32 v182, v182, v230, v238\n"
    "v_mfma_f32_16x16x32_bf16 a[76:79], a[184:187], v[112:115], 0\n"
    "ds_read_b64_tr_b16 a[146:147], v242 offset:17152\n"
    "v_fma_f32 v183, v183, v231, v239\n"
    "v_mfma_f32_16x16x32_bf16 a[76:79], a[188:191], v[120:123], a[76:79]\n"
    "s_waitcnt lgkmcnt(6)\n"
    "ds_read_b64_tr_b16 a[148:149], v242 offset:18944\n"
    "v_cvt_pk_bf16_f32 v179, v182, v183\n"
    "v_mfma_f32_16x16x32_bf16 a[16:19], a[128:131], v[16:19], a[16:19]\n"
    "s_add_u32 m0, s69, 50048\n"
    "ds_read_b64_tr_b16 a[150:151], v242 offset:19200\n"
    "v_fma_f32 v72, v72, v4, v12\n"
    "v_mfma_f32_16x16x32_bf16 a[16:19], a[132:135], v[24:27], a[16:19]\n"
    "buffer_load_dwordx4 v246, s[60:63], s68 offen offset:128 nt lds\n"
    "v_mfma_f32_16x16x32_bf16 a[20:23], a[128:131], v[128:131], a[20:23]\n"
    "s_nop 0\n"
    "ds_read_b64_tr_b16 a[152:153], v243 offset:16896\n"
    "ds_read_b128 v[224:227], v247 offset:1280\n"
    "v_fma_f32 v73, v73, v5, v13\n"
    "v_mfma_f32_16x16x32_bf16 a[20:23], a[132:135], v[136:139], a[20:23]\n"
    "s_waitcnt lgkmcnt(8)\n"
    "ds_read_b64_tr_b16 a[154:155], v243 offset:17152\n"
    "v_cvt_pk_bf16_f32 v72, v72, v73\n"
    "v_mfma_f32_16x16x32_bf16 a[24:27], a[136:139], v[16:19], a[24:27]\n"
    "s_waitcnt lgkmcnt(5)\n"
    "ds_read_b64_tr_b16 a[156:157], v243 offset:18944\n"
    "ds_read_b128 v[228:231], v247 offset:1296\n"
    "v_fma_f32 v74, v74, v6, v14\n"
    "v_mfma_f32_16x16x32_bf16 a[24:27], a[140:143], v[24:27], a[24:27]\n"
    "s_mov_b32 s68, 3407872\n"
    "ds_read_b64_tr_b16 a[158:159], v243 offset:19200\n"
    "ds_read_b64_tr_b16 a[160:161], v242 offset:17408\n"
    "v_mfma_f32_16x16x32_bf16 a[28:31], a[136:139], v[128:131], a[28:31]\n"
    "s_add_u32 m0, s69, 51200\n"
    "ds_read_b128 v[232:235], v247 offset:3072\n"
    "v_fma_f32 v75, v75, v7, v15\n"
    "v_mfma_f32_16x16x32_bf16 a[28:31], a[140:143], v[136:139], a[28:31]\n"
    "buffer_load_dwordx4 v246, s[60:63], s68 offen offset:0 nt lds\n"
    "ds_read_b64_tr_b16 a[162:163], v242 offset:17664\n"
    "v_cvt_pk_bf16_f32 v73, v74, v75\n"
    "v_mfma_f32_16x16x32_bf16 a[32:35], a[144:147], v[16:19], a[32:35]\n"
    "s_waitcnt lgkmcnt(9)\n"
    "ds_read_b64_tr_b16 a[164:165], v242 offset:19456\n"
    "ds_read_b128 v[236:239], v247 offset:3088\n"
    "v_fma_f32 v76, v76, v116, v124\n"
    "v_mfma_f32_16x16x32_bf16 a[32:35], a[148:151], v[24:27], a[32:35]\n"
    "s_nop 0\n"
    "ds_read_b64_tr_b16 a[166:167], v242 offset:19712\n"
    "ds_read_b64_tr_b16 a[168:169], v243 offset:17408\n"
    "v_mfma_f32_16x16x32_bf16 a[36:39], a[144:147], v[128:131], a[36:39]\n"
    "ds_read_b64_tr_b16 a[170:171], v243 offset:17664\n"
    "v_fma_f32 v77, v77, v117, v125\n"
    "v_mfma_f32_16x16x32_bf16 a[36:39], a[148:151], v[136:139], a[36:39]\n"
    "s_waitcnt lgkmcnt(8)\n"
    "ds_read_b64_tr_b16 a[172:173], v243 offset:19456\n"
    "v_cvt_pk_bf16_f32 v74, v76, v77\n"
    "v_mfma_f32_16x16x32_bf16 a[40:43], a[152:155], v[16:19], a[40:43]\n"
    "s_add_u32 m0, s69, 52096\n"
    "ds_read_b64_tr_b16 a[174:175], v243 offset:19712\n"
    "v_fma_f32 v78, v78, v118, v126\n"
    "v_mfma_f32_16x16x32_bf16 a[40:43], a[156:159], v[24:27], a[40:43]\n"
    "buffer_load_dwordx4 v246, s[60:63], s68 offen offset:128 nt lds\n"
    "v_mfma_f32_16x16x32_bf16 a[44:47], a[152:155], v[128:131], a[44:47]\n"
    "s_waitcnt vmcnt(15)\n"
    "ds_read_b64_tr_b16 a[176:177], v242 offset:17920\n"
    "v_fma_f32 v79, v79, v119, v127\n"
    "v_mfma_f32_16x16x32_bf16 a[44:47], a[156:159], v[136:139], a[44:47]\n"
    "s_waitcnt lgkmcnt(8)\n"
    "s_barrier\n"
    "ds_read_b64_tr_b16 a[178:179], v242 offset:18176\n"
    "v_cvt_pk_bf16_f32 v75, v78, v79\n"
    "v_mfma_f32_16x16x32_bf16 a[48:51], a[160:163], v[16:19], a[48:51]\n"
    "s_waitcnt lgkmcnt(6)\n"
    "ds_read_b64_tr_b16 a[180:181], v242 offset:19968\n"
    "v_fma_f32 v184, v184, v4, v12\n"
    "v_mfma_f32_16x16x32_bf16 a[48:51], a[164:167], v[24:27], a[48:51]\n"
    "s_waitcnt lgkmcnt(3)\n"
    "ds_read_b64_tr_b16 a[182:183], v242 offset:20224\n"
    "ds_read_b64_tr_b16 a[184:185], v243 offset:17920\n"
    "v_mfma_f32_16x16x32_bf16 a[52:55], a[160:163], v[128:131], a[52:55]\n"
    "s_mov_b32 s68, 256\n"
    "ds_read_b64_tr_b16 a[186:187], v243 offset:18176\n"
    "v_fma_f32 v185, v185, v5, v13\n"
    "v_mfma_f32_16x16x32_bf16 a[52:55], a[164:167], v[136:139], a[52:55]\n"
    "s_add_u32 m0, s69, 32768\n"
    "ds_read_b64_tr_b16 a[188:189], v243 offset:19968\n"
    "v_cvt_pk_bf16_f32 v184, v184, v185\n"
    "v_mfma_f32_16x16x32_bf16 a[56:59], a[168:171], v[16:19], a[56:59]\n"
    "buffer_load_dwordx4 v246, s[60:63], s68 offen offset:0 nt lds\n"
    "v_fma_f32 v186, v186, v6, v14\n"
    "v_mfma_f32_16x16x32_bf16 a[56:59], a[172:175], v[24:27], a[56:59]\n"
    "s_nop 0\n"
    "ds_read_b64_tr_b16 a[190:191], v243 offset:20224\n"
    "ds_read_b64_tr_b16 a[128:129], v240 offset:16384\n"
    "v_mfma_f32_16x16x32_bf16 a[60:63], a[168:171], v[128:131], a[60:63]\n"
    "ds_read_b64_tr_b16 a[130:131], v240 offset:16640\n"
    "v_fma_f32 v187, v187, v7, v15\n"
    "v_mfma_f32_16x16x32_bf16 a[60:63], a[172:175], v[136:139], a[60:63]\n"
    "s_waitcnt lgkmcnt(8)\n"
    "ds_read_b64_tr_b16 a[132:133], v240 offset:18432\n"
    "v_cvt_pk_bf16_f32 v185, v186, v187\n"
    "v_mfma_f32_16x16x32_bf16 a[64:67], a[176:179], v[16:19], a[64:67]\n"
    "s_waitcnt lgkmcnt(7)\n"
    "ds_read_b64_tr_b16 a[134:135], v240 offset:18688\n"
    "v_fma_f32 v188, v188, v116, v124\n"
    "v_mfma_f32_16x16x32_bf16 a[64:67], a[180:183], v[24:27], a[64:67]\n"
    "s_add_u32 m0, s69, 33664\n"
    "ds_read_b64_tr_b16 a[136:137], v241 offset:16384\n"
    "v_fma_f32 v189, v189, v117, v125\n"
    "v_mfma_f32_16x16x32_bf16 a[68:71], a[176:179], v[128:131], a[68:71]\n"
    "buffer_load_dwordx4 v246, s[60:63], s68 offen offset:128 nt lds\n"
    "v_mfma_f32_16x16x32_bf16 a[68:71], a[180:183], v[136:139], a[68:71]\n"
    "s_waitcnt lgkmcnt(7)\n"
    "ds_read_b64_tr_b16 a[138:139], v241 offset:16640\n"
    "v_cvt_pk_bf16_f32 v186, v188, v189\n"
    "v_mfma_f32_16x16x32_bf16 a[72:75], a[184:187], v[16:19], a[72:75]\n"
    "s_waitcnt lgkmcnt(6)\n"
    "ds_read_b64_tr_b16 a[140:141], v241 offset:18432\n"
    "v_fma_f32 v190, v190, v118, v126\n"
    "v_mfma_f32_16x16x32_bf16 a[72:75], a[188:191], v[24:27], a[72:75]\n"
    "s_waitcnt lgkmcnt(3)\n"
    "ds_read_b64_tr_b16 a[142:143], v241 offset:18688\n"
    "ds_read_b64_tr_b16 a[144:145], v240 offset:16896\n"
    "v_mfma_f32_16x16x32_bf16 a[76:79], a[184:187], v[128:131], a[76:79]\n"
    "s_mov_b32 s68, 262400\n"
    "ds_read_b64_tr_b16 a[146:147], v240 offset:17152\n"
    "v_fma_f32 v191, v191, v119, v127\n"
    "v_mfma_f32_16x16x32_bf16 a[76:79], a[188:191], v[136:139], a[76:79]\n"
    "s_add_u32 m0, s69, 34816\n"
    "ds_read_b64_tr_b16 a[148:149], v240 offset:18944\n"
    "v_cvt_pk_bf16_f32 v187, v190, v191\n"
    "v_mfma_f32_16x16x32_bf16 a[16:19], a[128:131], v[32:35], a[16:19]\n"
    "buffer_load_dwordx4 v246, s[60:63], s68 offen offset:0 nt lds\n"
    "v_fma_f32 v80, v80, v224, v232\n"
    "v_mfma_f32_16x16x32_bf16 a[16:19], a[132:135], v[40:43], a[16:19]\n"
    "ds_read_b64_tr_b16 a[150:151], v240 offset:19200\n"
    "v_fma_f32 v81, v81, v225, v233\n"
    "v_mfma_f32_16x16x32_bf16 a[20:23], a[128:131], v[144:147], a[20:23]\n"
    "s_nop 0\n"
    "ds_read_b64_tr_b16 a[152:153], v241 offset:16896\n"
    "ds_read_b128 v[4:7], v247 offset:1408\n"
    "v_mfma_f32_16x16x32_bf16 a[20:23], a[132:135], v[152:155], a[20:23]\n"
    "s_waitcnt lgkmcnt(8)\n"
    "ds_read_b64_tr_b16 a[154:155], v241 offset:17152\n"
    "v_cvt_pk_bf16_f32 v80, v80, v81\n"
    "v_mfma_f32_16x16x32_bf16 a[24:27], a[136:139], v[32:35], a[24:27]\n"
    "s_waitcnt lgkmcnt(7)\n"
    "ds_read_b64_tr_b16 a[156:157], v241 offset:18944\n"
    "ds_read_b128 v[116:119], v247 offset:1424\n"
    "v_fma_f32 v82, v82, v226, v234\n"
    "v_mfma_f32_16x16x32_bf16 a[24:27], a[140:143], v[40:43], a[24:27]\n"
    "s_add_u32 m0, s69, 35712\n"
    "ds_read_b64_tr_b16 a[158:159], v241 offset:19200\n"
    "v_fma_f32 v83, v83, v227, v235\n"
    "v_mfma_f32_16x16x32_bf16 a[28:31], a[136:139], v[144:147], a[28:31]\n"
    "s_nop 0\n"
    "buffer_load_dwordx4 v246, s[60:63], s68 offen offset:128 nt lds\n"
    "ds_read_b64_tr_b16 a[160:161], v240 offset:17408\n"
    "ds_read_b128 v[12:15], v247 offset:3200\n"
    "v_mfma_f32_16x16x32_bf16 a[28:31], a[140:143], v[152:155], a[28:31]\n"
    "s_waitcnt lgkmcnt(10)\n"
    "ds_read_b64_tr_b16 a[162:163], v240 offset:17664\n"
    "v_cvt_pk_bf16_f32 v81, v82, v83\n"
    "v_mfma_f32_16x16x32_bf16 a[32:35], a[144:147], v[32:35], a[32:35]\n"
    "s_waitcnt lgkmcnt(9)\n"
    "ds_read_b64_tr_b16 a[164:165], v240 offset:19456\n"
    "ds_read_b128 v[124:127], v247 offset:3216\n"
    "v_fma_f32 v84, v84, v228, v236\n"
    "v_mfma_f32_16x16x32_bf16 a[32:35], a[148:151], v[40:43], a[32:35]\n"
    "ds_read_b64_tr_b16 a[166:167], v240 offset:19712\n"
    "v_fma_f32 v85, v85, v229, v237\n"
    "v_mfma_f32_16x16x32_bf16 a[36:39], a[144:147], v[144:147], a[36:39]\n"
    "s_waitcnt lgkmcnt(6)\n"
    "ds_read_b64_tr_b16 a[168:169], v241 offset:17408\n"
    "ds_read_b64_tr_b16 a[170:171], v241 offset:17664\n"
    "v_mfma_f32_16x16x32_bf16 a[36:39], a[148:151], v[152:155], a[36:39]\n"
    "s_mov_b32 s68, 524544\n"
    "ds_read_b64_tr_b16 a[172:173], v241 offset:19456\n"
    "v_cvt_pk_bf16_f32 v82, v84, v85\n"
    "v_mfma_f32_16x16x32_bf16 a[40:43], a[152:155], v[32:35], a[40:43]\n"
    "s_add_u32 m0, s69, 0\n"
    "ds_read_b64_tr_b16 a[174:175], v241 offset:19712\n"
    "v_fma_f32 v86, v86, v230, v238\n"
    "v_mfma_f32_16x16x32_bf16 a[40:43], a[156:159], v[40:43], a[40:43]\n"
    "buffer_load_dwordx4 v246, s[60:63], s68 offen offset:0 nt lds\n"
    "v_fma_f32 v87, v87, v231, v239\n"
    "v_mfma_f32_16x16x32_bf16 a[44:47], a[152:155], v[144:147], a[44:47]\n"
    "s_nop 0\n"
    "ds_read_b64_tr_b16 a[176:177], v240 offset:17920\n"
    "ds_read_b64_tr_b16 a[178:179], v240 offset:18176\n"
    "v_mfma_f32_16x16x32_bf16 a[44:47], a[156:159], v[152:155], a[44:47]\n"
    "s_waitcnt lgkmcnt(9)\n"
    "ds_read_b64_tr_b16 a[180:181], v240 offset:19968\n"
    "v_cvt_pk_bf16_f32 v83, v86, v87\n"
    "v_mfma_f32_16x16x32_bf16 a[48:51], a[160:163], v[32:35], a[48:51]\n"
    "s_waitcnt lgkmcnt(7)\n"
    "ds_read_b64_tr_b16 a[182:183], v240 offset:20224\n"
    "v_fma_f32 v192, v192, v224, v232\n"
    "v_mfma_f32_16x16x32_bf16 a[48:51], a[164:167], v[40:43], a[48:51]\n"
    "s_waitcnt lgkmcnt(6)\n"
    "ds_read_b64_tr_b16 a[184:185], v241 offset:17920\n"
    "v_fma_f32 v193, v193, v225, v233\n"
    "v_mfma_f32_16x16x32_bf16 a[52:55], a[160:163], v[144:147], a[52:55]\n"
    "s_add_u32 m0, s69, 896\n"
    "ds_read_b64_tr_b16 a[186:187], v241 offset:18176\n"
    "v_cvt_pk_bf16_f32 v192, v192, v193\n"
    "v_mfma_f32_16x16x32_bf16 a[52:55], a[164:167], v[152:155], a[52:55]\n"
    "buffer_load_dwordx4 v246, s[60:63], s68 offen offset:128 nt lds\n"
    "v_mfma_f32_16x16x32_bf16 a[56:59], a[168:171], v[32:35], a[56:59]\n"
    "s_waitcnt lgkmcnt(6)\n"
    "ds_read_b64_tr_b16 a[188:189], v241 offset:19968\n"
    "v_fma_f32 v194, v194, v226, v234\n"
    "v_mfma_f32_16x16x32_bf16 a[56:59], a[172:175], v[40:43], a[56:59]\n"
    "ds_read_b64_tr_b16 a[190:191], v241 offset:20224\n"
    "v_fma_f32 v195, v195, v227, v235\n"
    "v_mfma_f32_16x16x32_bf16 a[60:63], a[168:171], v[144:147], a[60:63]\n"
    "s_waitcnt lgkmcnt(4)\n"
    "ds_read_b64_tr_b16 a[128:129], v242 offset:32768\n"
    "ds_read_b64_tr_b16 a[130:131], v242 offset:33024\n"
    "v_mfma_f32_16x16x32_bf16 a[60:63], a[172:175], v[152:155], a[60:63]\n"
    "s_mov_b32 s68, 786688\n"
    "ds_read_b64_tr_b16 a[132:133], v242 offset:34816\n"
    "v_cvt_pk_bf16_f32 v193, v194, v195\n"
    "v_mfma_f32_16x16x32_bf16 a[64:67], a[176:179], v[32:35], a[64:67]\n"
    "s_add_u32 m0, s69, 2048\n"
    "ds_read_b64_tr_b16 a[134:135], v242 offset:35072\n"
    "v_fma_f32 v196, v196, v228, v236\n"
    "v_mfma_f32_16x16x32_bf16 a[64:67], a[180:183], v[40:43], a[64:67]\n"
    "buffer_load_dwordx4 v246, s[60:63], s68 offen offset:0 nt lds\n"
    "v_fma_f32 v197, v197, v229, v237\n"
    "v_mfma_f32_16x16x32_bf16 a[68:71], a[176:179], v[144:147], a[68:71]\n"
    "s_nop 0\n"
    "ds_read_b64_tr_b16 a[136:137], v243 offset:32768\n"
    "ds_read_b64_tr_b16 a[138:139], v243 offset:33024\n"
    "v_mfma_f32_16x16x32_bf16 a[68:71], a[180:183], v[152:155], a[68:71]\n"
    "s_waitcnt lgkmcnt(8)\n"
    "ds_read_b64_tr_b16 a[140:141], v243 offset:34816\n"
    "v_cvt_pk_bf16_f32 v194, v196, v197\n"
    "v_mfma_f32_16x16x32_bf16 a[72:75], a[184:187], v[32:35], a[72:75]\n"
    "s_waitcnt lgkmcnt(7)\n"
    "ds_read_b64_tr_b16 a[142:143], v243 offset:35072\n"
    "v_fma_f32 v198, v198, v230, v238\n"
    "v_mfma_f32_16x16x32_bf16 a[72:75], a[188:191], v[40:43], a[72:75]\n"
    "s_waitcnt lgkmcnt(6)\n"
    "ds_read_b64_tr_b16 a[144:145], v242 offset:33280\n"
    "v_fma_f32 v199, v199, v231, v239\n"
    "v_mfma_f32_16x16x32_bf16 a[76:79], a[184:187], v[144:147], a[76:79]\n"
    "s_add_u32 m0, s69, 2944\n"
    "ds_read_b64_tr_b16 a[146:147], v242 offset:33536\n"
    "v_cvt_pk_bf16_f32 v195, v198, v199\n"
    "v_mfma_f32_16x16x32_bf16 a[76:79], a[188:191], v[152:155], a[76:79]\n"
    "buffer_load_dwordx4 v246, s[60:63], s68 offen offset:128 nt lds\n"
    "v_mfma_f32_16x16x32_bf16 a[16:19], a[128:131], v[48:51], a[16:19]\n"
    "s_waitcnt lgkmcnt(6)\n"
    "ds_read_b64_tr_b16 a[148:149], v242 offset:35328\n"
    "v_fma_f32 v88, v88, v4, v12\n"
    "v_mfma_f32_16x16x32_bf16 a[16:19], a[132:135], v[56:59], a[16:19]\n"
    "ds_read_b64_tr_b16 a[150:151], v242 offset:35584\n"
    "v_fma_f32 v89, v89, v5, v13\n"
    "v_mfma_f32_16x16x32_bf16 a[20:23], a[128:131], v[160:163], a[20:23]\n"
    "s_nop 0\n"
    "ds_read_b64_tr_b16 a[152:153], v243 offset:33280\n"
    "ds_read_b128 v[224:227], v247 offset:1536\n"
    "v_cvt_pk_bf16_f32 v88, v88, v89\n"
    "v_mfma_f32_16x16x32_bf16 a[20:23], a[132:135], v[168:171], a[20:23]\n"
    "s_waitcnt lgkmcnt(8)\n"
    "ds_read_b64_tr_b16 a[154:155], v243 offset:33536\n"
    "ds_read_b64_tr_b16 a[156:157], v243 offset:35328\n"
    "v_mfma_f32_16x16x32_bf16 a[24:27], a[136:139], v[48:51], a[24:27]\n"
    "s_waitcnt lgkmcnt(8)\n"
    "ds_read_b128 v[228:231], v247 offset:1552\n"
    "v_fma_f32 v90, v90, v6, v14\n"
    "v_mfma_f32_16x16x32_bf16 a[24:27], a[140:143], v[56:59], a[24:27]\n"
    "ds_read_b64_tr_b16 a[158:159], v243 offset:35584\n"
    "v_fma_f32 v91, v91, v7, v15\n"
    "v_mfma_f32_16x16x32_bf16 a[28:31], a[136:139], v[160:163], a[28:31]\n"
    "s_nop 0\n"
    "ds_read_b64_tr_b16 a[160:161], v242 offset:33792\n"
    "ds_read_b128 v[232:235], v247 offset:3328\n"
    "v_cvt_pk_bf16_f32 v89, v90, v91\n"
    "v_mfma_f32_16x16x32_bf16 a[28:31], a[140:143], v[168:171], a[28:31]\n"
    "s_waitcnt lgkmcnt(10)\n"
    "ds_read_b64_tr_b16 a[162:163], v242 offset:34048\n"
    "ds_read_b64_tr_b16 a[164:165], v242 offset:35840\n"
    "v_mfma_f32_16x16x32_bf16 a[32:35], a[144:147], v[48:51], a[32:35]\n"
    "s_waitcnt lgkmcnt(10)\n"
    "ds_read_b128 v[236:239], v247 offset:3344\n"
    "v_fma_f32 v92, v92, v116, v124\n"
    "v_mfma_f32_16x16x32_bf16 a[32:35], a[148:151], v[56:59], a[32:35]\n"
    "ds_read_b64_tr_b16 a[166:167], v242 offset:36096\n"
    "v_fma_f32 v93, v93, v117, v125\n"
    "v_mfma_f32_16x16x32_bf16 a[36:39], a[144:147], v[160:163], a[36:39]\n"
    "ds_read_b64_tr_b16 a[168:169], v243 offset:33792\n"
    "v_cvt_pk_bf16_f32 v90, v92, v93\n"
    "v_mfma_f32_16x16x32_bf16 a[36:39], a[148:151], v[168:171], a[36:39]\n"
    "s_waitcnt lgkmcnt(10)\n"
    "ds_read_b64_tr_b16 a[170:171], v243 offset:34048\n"
    "ds_read_b64_tr_b16 a[172:173], v243 offset:35840\n"
    "v_mfma_f32_16x16x32_bf16 a[40:43], a[152:155], v[48:51], a[40:43]\n"
    "s_waitcnt lgkmcnt(6)\n"
    "ds_read_b64_tr_b16 a[174:175], v243 offset:36096\n"
    "v_fma_f32 v94, v94, v118, v126\n"
    "v_mfma_f32_16x16x32_bf16 a[40:43], a[156:159], v[56:59], a[40:43]\n"
    "s_waitcnt vmcnt(16)\n"
    "ds_read_b64_tr_b16 a[176:177], v242 offset:34304\n"
    "v_fma_f32 v95, v95, v119, v127\n"
    "v_mfma_f32_16x16x32_bf16 a[44:47], a[152:155], v[160:163], a[44:47]\n"
    "s_waitcnt vmcnt(12)\n"
    "ds_read_b64_tr_b16 a[178:179], v242 offset:34560\n"
    "v_cvt_pk_bf16_f32 v91, v94, v95\n"
    "v_mfma_f32_16x16x32_bf16 a[44:47], a[156:159], v[168:171], a[44:47]\n"
    "s_barrier\n"
    "ds_read_b64_tr_b16 a[180:181], v242 offset:36352\n"
    "v_fma_f32 v200, v200, v4, v12\n"
    "v_mfma_f32_16x16x32_bf16 a[48:51], a[160:163], v[48:51], a[48:51]\n"
    "s_waitcnt lgkmcnt(7)\n"
    "s_mov_b32 s68, 1048832\n"
    "ds_read_b64_tr_b16 a[182:183], v242 offset:36608\n"
    "v_fma_f32 v201, v201, v5, v13\n"
    "v_mfma_f32_16x16x32_bf16 a[48:51], a[164:167], v[56:59], a[48:51]\n"
    "s_add_u32 m0, s69, 65536\n"
    "ds_read_b64_tr_b16 a[184:185], v243 offset:34304\n"
    "v_cvt_pk_bf16_f32 v200, v200, v201\n"
    "v_mfma_f32_16x16x32_bf16 a[52:55], a[160:163], v[160:163], a[52:55]\n"
    "buffer_load_dwordx4 v246, s[60:63], s68 offen offset:0 nt lds\n"
    "v_mfma_f32_16x16x32_bf16 a[52:55], a[164:167], v[168:171], a[52:55]\n"
    "s_waitcnt lgkmcnt(7)\n"
    "ds_read_b64_tr_b16 a[186:187], v243 offset:34560\n"
    "ds_read_b64_tr_b16 a[188:189], v243 offset:36352\n"
    "v_mfma_f32_16x16x32_bf16 a[56:59], a[168:171], v[48:51], a[56:59]\n"
    "s_waitcnt lgkmcnt(7)\n"
    "ds_read_b64_tr_b16 a[190:191], v243 offset:36608\n"
    "v_fma_f32 v202, v202, v6, v14\n"
    "v_mfma_f32_16x16x32_bf16 a[56:59], a[172:175], v[56:59], a[56:59]\n"
    "ds_read_b64_tr_b16 a[128:129], v242 offset:49152\n"
    "v_fma_f32 v203, v203, v7, v15\n"
    "v_mfma_f32_16x16x32_bf16 a[60:63], a[168:171], v[160:163], a[60:63]\n"
    "s_waitcnt lgkmcnt(5)\n"
    "ds_read_b64_tr_b16 a[130:131], v242 offset:49408\n"
    "v_cvt_pk_bf16_f32 v201, v202, v203\n"
    "v_mfma_f32_16x16x32_bf16 a[60:63], a[172:175], v[168:171], a[60:63]\n"
    "s_add_u32 m0, s69, 66432\n"
    "ds_read_b64_tr_b16 a[132:133], v242 offset:51200\n"
    "v_fma_f32 v204, v204, v116, v124\n"
    "v_mfma_f32_16x16x32_bf16 a[64:67], a[176:179], v[48:51], a[64:67]\n"
    "buffer_load_dwordx4 v246, s[60:63], s68 offen offset:128 nt lds\n"
    "v_mfma_f32_16x16x32_bf16 a[64:67], a[180:183], v[56:59], a[64:67]\n"
    "ds_read_b64_tr_b16 a[134:135], v242 offset:51456\n"
    "v_fma_f32 v205, v205, v117, v125\n"
    "v_mfma_f32_16x16x32_bf16 a[68:71], a[176:179], v[160:163], a[68:71]\n"
    "ds_read_b64_tr_b16 a[136:137], v243 offset:49152\n"
    "v_cvt_pk_bf16_f32 v202, v204, v205\n"
    "v_mfma_f32_16x16x32_bf16 a[68:71], a[180:183], v[168:171], a[68:71]\n"
    "s_waitcnt lgkmcnt(5)\n"
    "ds_read_b64_tr_b16 a[138:139], v243 offset:49408\n"
    "ds_read_b64_tr_b16 a[0:1], v255 offset:0\n"
    "ds_read_b64_tr_b16 a[2:3], v255 offset:2048\n"
    "ds_read_b64_tr_b16 a[4:5], v255 offset:4096\n"
    "ds_read_b64_tr_b16 a[6:7], v255 offset:6144\n"
    "ds_read_b64_tr_b16 a[8:9], v255 offset:12288\n"
    "ds_read_b64_tr_b16 a[10:11], v255 offset:14336\n"
    "v_mfma_f32_16x16x32_bf16 a[72:75], a[184:187], v[48:51], a[72:75]\n"
    "s_mov_b32 s68, 1310976\n"
    "ds_read_b64_tr_b16 a[140:141], v243 offset:51200\n"
    "v_fma_f32 v206, v206, v118, v126\n"
    "v_mfma_f32_16x16x32_bf16 a[72:75], a[188:191], v[56:59], a[72:75]\n"
    "s_add_u32 m0, s69, 67584\n"
    "ds_read_b64_tr_b16 a[142:143], v243 offset:51456\n"
    "v_fma_f32 v207, v207, v119, v127\n"
    "v_mfma_f32_16x16x32_bf16 a[76:79], a[184:187], v[160:163], a[76:79]\n"
    "buffer_load_dwordx4 v246, s[60:63], s68 offen offset:0 nt lds\n"
    "ds_read_b64_tr_b16 a[144:145], v242 offset:49664\n"
    "v_cvt_pk_bf16_f32 v203, v206, v207\n"
    "v_mfma_f32_16x16x32_bf16 a[76:79], a[188:191], v[168:171], a[76:79]\n"
    "s_waitcnt lgkmcnt(13)\n"
    "ds_read_b64_tr_b16 a[146:147], v242 offset:49920\n"
    "v_fma_f32 v96, v96, v224, v232\n"
    "v_mfma_f32_16x16x32_bf16 a[16:19], a[128:131], v[64:67], a[16:19]\n"
    "s_waitcnt lgkmcnt(12)\n"
    "ds_read_b64_tr_b16 a[148:149], v242 offset:51712\n"
    "ds_read_b64_tr_b16 a[150:151], v242 offset:51968\n"
    "v_mfma_f32_16x16x32_bf16 a[16:19], a[132:135], v[72:75], a[16:19]\n"
    "ds_read_b128 v[4:7], v247 offset:1664\n"
    "v_fma_f32 v97, v97, v225, v233\n"
    "v_mfma_f32_16x16x32_bf16 a[20:23], a[128:131], v[176:179], a[20:23]\n"
    "s_waitcnt lgkmcnt(5)\n"
    "ds_read_b64_tr_b16 a[152:153], v243 offset:49664\n"
    "v_cvt_pk_bf16_f32 v96, v96, v97\n"
    "v_mfma_f32_16x16x32_bf16 a[20:23], a[132:135], v[184:187], a[20:23]\n"
    "s_add_u32 m0, s69, 68480\n"
    "ds_read_b64_tr_b16 a[154:155], v243 offset:49920\n"
    "ds_read_b128 v[116:119], v247 offset:1680\n"
    "v_fma_f32 v98, v98, v226, v234\n"
    "v_mfma_f32_16x16x32_bf16 a[24:27], a[136:139], v[64:67], a[24:27]\n"
    "buffer_load_dwordx4 v246, s[60:63], s68 offen offset:128 nt lds\n"
    "ds_read_b64_tr_b16 a[156:157], v243 offset:51712\n"
    "v_mfma_f32_16x16x32_bf16 a[24:27], a[140:143], v[72:75], a[24:27]\n"
    "s_waitcnt lgkmcnt(5)\n"
    "ds_read_b64_tr_b16 a[158:159], v243 offset:51968\n"
    "ds_read_b128 v[12:15], v247 offset:3456\n"
    "v_fma_f32 v99, v99, v227, v235\n"
    "v_mfma_f32_16x16x32_bf16 a[28:31], a[136:139], v[176:179], a[28:31]\n"
    "s_add_u32 s59, s45, 48234496\n"
    "ds_read_b64_tr_b16 a[160:161], v242 offset:50176\n"
    "v_cvt_pk_bf16_f32 v97, v98, v99\n"
    "v_mfma_f32_16x16x32_bf16 a[28:31], a[140:143], v[184:187], a[28:31]\n"
    "s_nop 0\n"
    "buffer_store_dwordx4 a[0:3], v248, s[64:67], s59 offen sc1\n"
    "ds_read_b64_tr_b16 a[162:163], v242 offset:50432\n"
    "ds_read_b128 v[124:127], v247 offset:3472\n"
    "v_fma_f32 v100, v100, v228, v236\n"
    "v_mfma_f32_16x16x32_bf16 a[32:35], a[144:147], v[64:67], a[32:35]\n"
    "s_mov_b32 s68, 1573120\n"
    "ds_read_b64_tr_b16 a[164:165], v242 offset:52224\n"
    "ds_read_b64_tr_b16 a[166:167], v242 offset:52480\n"
    "v_mfma_f32_16x16x32_bf16 a[32:35], a[148:151], v[72:75], a[32:35]\n"
    "s_add_u32 m0, s69, 81920\n"
    "ds_read_b64_tr_b16 a[168:169], v243 offset:50176\n"
    "v_fma_f32 v101, v101, v229, v237\n"
    "v_mfma_f32_16x16x32_bf16 a[36:39], a[144:147], v[176:179], a[36:39]\n"
    "buffer_load_dwordx4 v246, s[60:63], s68 offen offset:0 nt lds\n"
    "v_cvt_pk_bf16_f32 v98, v100, v101\n"
    "v_mfma_f32_16x16x32_bf16 a[36:39], a[148:151], v[184:187], a[36:39]\n"
    "s_waitcnt lgkmcnt(10)\n"
    "ds_read_b64_tr_b16 a[170:171], v243 offset:50432\n"
    "v_fma_f32 v102, v102, v230, v238\n"
    "v_mfma_f32_16x16x32_bf16 a[40:43], a[152:155], v[64:67], a[40:43]\n"
    "s_waitcnt lgkmcnt(8)\n"
    "ds_read_b64_tr_b16 a[172:173], v243 offset:52224\n"
    "ds_read_b64_tr_b16 a[174:175], v243 offset:52480\n"
    "v_mfma_f32_16x16x32_bf16 a[40:43], a[156:159], v[72:75], a[40:43]\n"
    "ds_read_b64_tr_b16 a[176:177], v242 offset:50688\n"
    "v_fma_f32 v103, v103, v231, v239\n"
    "v_mfma_f32_16x16x32_bf16 a[44:47], a[152:155], v[176:179], a[44:47]\n"
    "s_waitcnt lgkmcnt(5)\n"
    "ds_read_b64_tr_b16 a[178:179], v242 offset:50944\n"
    "v_cvt_pk_bf16_f32 v99, v102, v103\n"
    "v_mfma_f32_16x16x32_bf16 a[44:47], a[156:159], v[184:187], a[44:47]\n"
    "s_add_u32 m0, s69, 82816\n"
    "ds_read_b64_tr_b16 a[180:181], v242 offset:52736\n"
    "v_fma_f32 v208, v208, v224, v232\n"
    "v_mfma_f32_16x16x32_bf16 a[48:51], a[160:163], v[64:67], a[48:51]\n"
    "buffer_load_dwordx4 v246, s[60:63], s68 offen offset:128 nt lds\n"
    "v_mfma_f32_16x16x32_bf16 a[48:51], a[164:167], v[72:75], a[48:51]\n"
    "s_add_u32 s59, s45, 48248832\n"
    "ds_read_b64_tr_b16 a[182:183], v242 offset:52992\n"
    "v_fma_f32 v209, v209, v225, v233\n"
    "v_mfma_f32_16x16x32_bf16 a[52:55], a[160:163], v[176:179], a[52:55]\n"
    "buffer_store_dwordx4 a[4:7], v248, s[64:67], s59 offen sc1\n"
    "ds_read_b64_tr_b16 a[184:185], v243 offset:50688\n"
    "v_cvt_pk_bf16_f32 v208, v208, v209\n"
    "v_mfma_f32_16x16x32_bf16 a[52:55], a[164:167], v[184:187], a[52:55]\n"
    "s_waitcnt lgkmcnt(5)\n"
    "ds_read_b64_tr_b16 a[186:187], v243 offset:50944\n"
    "v_fma_f32 v210, v210, v226, v234\n"
    "v_mfma_f32_16x16x32_bf16 a[56:59], a[168:171], v[64:67], a[56:59]\n"
    "s_mov_b32 s68, 1835264\n"
    "ds_read_b64_tr_b16 a[188:189], v243 offset:52736\n"
    "ds_read_b64_tr_b16 a[190:191], v243 offset:52992\n"
    "v_mfma_f32_16x16x32_bf16 a[56:59], a[172:175], v[72:75], a[56:59]\n"
    "s_add_u32 m0, s69, 83968\n"
    "ds_read_b64_tr_b16 a[128:129], v244 offset:0\n"
    "v_fma_f32 v211, v211, v227, v235\n"
    "v_mfma_f32_16x16x32_bf16 a[60:63], a[168:171], v[176:179], a[60:63]\n"
    "buffer_load_dwordx4 v246, s[60:63], s68 offen offset:0 nt lds\n"
    "v_cvt_pk_bf16_f32 v209, v210, v211\n"
    "v_mfma_f32_16x16x32_bf16 a[60:63], a[172:175], v[184:187], a[60:63]\n"
    "s_waitcnt lgkmcnt(5)\n"
    "ds_read_b64_tr_b16 a[130:131], v244 offset:256\n"
    "v_fma_f32 v212, v212, v228, v236\n"
    "v_mfma_f32_16x16x32_bf16 a[64:67], a[176:179], v[64:67], a[64:67]\n"
    "s_add_u32 s59, s46, 51904512\n"
    "ds_read_b64_tr_b16 a[132:133], v244 offset:2048\n"
    "ds_read_b64_tr_b16 a[134:135], v244 offset:2304\n"
    "v_mfma_f32_16x16x32_bf16 a[64:67], a[180:183], v[72:75], a[64:67]\n"
    "buffer_store_dwordx4 a[8:11], v248, s[64:67], s59 offen sc1\n"
    "v_fma_f32 v213, v213, v229, v237\n"
    "v_mfma_f32_16x16x32_bf16 a[68:71], a[176:179], v[176:179], a[68:71]\n"
    "s_waitcnt lgkmcnt(4)\n"
    "ds_read_b64_tr_b16 a[136:137], v245 offset:0\n"
    "v_cvt_pk_bf16_f32 v210, v212, v213\n"
    "v_mfma_f32_16x16x32_bf16 a[68:71], a[180:183], v[184:187], a[68:71]\n"
    "s_add_u32 m0, s69, 84864\n"
    "ds_read_b64_tr_b16 a[138:139], v245 offset:256\n"
    "v_fma_f32 v214, v214, v230, v238\n"
    "v_mfma_f32_16x16x32_bf16 a[72:75], a[184:187], v[64:67], a[72:75]\n"
    "buffer_load_dwordx4 v246, s[60:63], s68 offen offset:128 nt lds\n"
    "ds_read_b64_tr_b16 a[140:141], v245 offset:2048\n"
    "v_mfma_f32_16x16x32_bf16 a[72:75], a[188:191], v[72:75], a[72:75]\n"
    "ds_read_b64_tr_b16 a[142:143], v245 offset:2304\n"
    "v_fma_f32 v215, v215, v231, v239\n"
    "v_mfma_f32_16x16x32_bf16 a[76:79], a[184:187], v[176:179], a[76:79]\n"
    "ds_read_b64_tr_b16 a[144:145], v244 offset:512\n"
    "v_cvt_pk_bf16_f32 v211, v214, v215\n"
    "v_mfma_f32_16x16x32_bf16 a[76:79], a[188:191], v[184:187], a[76:79]\n"
    "s_waitcnt lgkmcnt(7)\n"
    "ds_read_b64_tr_b16 a[146:147], v244 offset:768\n"
    "v_fma_f32 v104, v104, v4, v12\n"
    "v_mfma_f32_16x16x32_bf16 a[16:19], a[128:131], v[80:83], a[16:19]\n"
    "s_waitcnt lgkmcnt(6)\n"
    "ds_read_b64_tr_b16 a[148:149], v244 offset:2560\n"
    "v_fma_f32 v105, v105, v5, v13\n"
    "v_mfma_f32_16x16x32_bf16 a[16:19], a[132:135], v[88:91], a[16:19]\n"
    "s_nop 0\n"
    "ds_read_b64_tr_b16 a[150:151], v244 offset:2816\n"
    "ds_read_b64_tr_b16 a[152:153], v245 offset:512\n"
    "v_mfma_f32_16x16x32_bf16 a[20:23], a[128:131], v[192:195], a[20:23]\n"
    "ds_read_b64_tr_b16 a[154:155], v245 offset:768\n"
    "v_cvt_pk_bf16_f32 v104, v104, v105\n"
    "v_mfma_f32_16x16x32_bf16 a[20:23], a[132:135], v[200:203], a[20:23]\n"
    "s_waitcnt lgkmcnt(8)\n"
    "ds_read_b64_tr_b16 a[156:157], v245 offset:2560\n"
    "v_fma_f32 v106, v106, v6, v14\n"
    "v_mfma_f32_16x16x32_bf16 a[24:27], a[136:139], v[80:83], a[24:27]\n"
    "s_waitcnt lgkmcnt(7)\n"
    "ds_read_b64_tr_b16 a[158:159], v245 offset:2816\n"
    "v_fma_f32 v107, v107, v7, v15\n"
    "v_mfma_f32_16x16x32_bf16 a[24:27], a[140:143], v[88:91], a[24:27]\n"
    "ds_read_b64_tr_b16 a[160:161], v244 offset:1024\n"
    "v_cvt_pk_bf16_f32 v105, v106, v107\n"
    "v_mfma_f32_16x16x32_bf16 a[28:31], a[136:139], v[192:195], a[28:31]\n"
    "ds_read_b64_tr_b16 a[162:163], v244 offset:1280\n"
    "v_fma_f32 v108, v108, v116, v124\n"
    "v_mfma_f32_16x16x32_bf16 a[28:31], a[140:143], v[200:203], a[28:31]\n"
    "s_waitcnt lgkmcnt(8)\n"
    "ds_read_b64_tr_b16 a[164:165], v244 offset:3072\n"
    "v_fma_f32 v109, v109, v117, v125\n"
    "v_mfma_f32_16x16x32_bf16 a[32:35], a[144:147], v[80:83], a[32:35]\n"
    "s_waitcnt lgkmcnt(7)\n"
    "ds_read_b64_tr_b16 a[166:167], v244 offset:3328\n"
    "ds_read_b64_tr_b16 a[168:169], v245 offset:1024\n"
    "v_mfma_f32_16x16x32_bf16 a[32:35], a[148:151], v[88:91], a[32:35]\n"
    "ds_read_b64_tr_b16 a[170:171], v245 offset:1280\n"
    "v_cvt_pk_bf16_f32 v106, v108, v109\n"
    "v_mfma_f32_16x16x32_bf16 a[36:39], a[144:147], v[192:195], a[36:39]\n"
    "ds_read_b64_tr_b16 a[172:173], v245 offset:3072\n"
    "v_fma_f32 v110, v110, v118, v126\n"
    "v_mfma_f32_16x16x32_bf16 a[36:39], a[148:151], v[200:203], a[36:39]\n"
    "s_waitcnt lgkmcnt(9)\n"
    "ds_read_b64_tr_b16 a[174:175], v245 offset:3328\n"
    "v_fma_f32 v111, v111, v119, v127\n"
    "v_mfma_f32_16x16x32_bf16 a[40:43], a[152:155], v[80:83], a[40:43]\n"
    "s_waitcnt lgkmcnt(8)\n"
    "ds_read_b64_tr_b16 a[176:177], v244 offset:1536\n"
    "v_cvt_pk_bf16_f32 v107, v110, v111\n"
    "v_mfma_f32_16x16x32_bf16 a[40:43], a[156:159], v[88:91], a[40:43]\n"
    "s_waitcnt lgkmcnt(7)\n"
    "ds_read_b64_tr_b16 a[178:179], v244 offset:1792\n"
    "v_fma_f32 v216, v216, v4, v12\n"
    "v_mfma_f32_16x16x32_bf16 a[44:47], a[152:155], v[192:195], a[44:47]\n"
    "s_waitcnt vmcnt(15)\n"
    "ds_read_b64_tr_b16 a[180:181], v244 offset:3584\n"
    "v_fma_f32 v217, v217, v5, v13\n"
    "v_mfma_f32_16x16x32_bf16 a[44:47], a[156:159], v[200:203], a[44:47]\n"
    "s_barrier\n"
    "ds_read_b64_tr_b16 a[182:183], v244 offset:3840\n"
    "ds_read_b64_tr_b16 a[184:185], v245 offset:1536\n"
    "v_mfma_f32_16x16x32_bf16 a[48:51], a[160:163], v[80:83], a[48:51]\n"
    "s_waitcnt lgkmcnt(9)\n"
    "ds_read_b64_tr_b16 a[186:187], v245 offset:1792\n"
    "v_cvt_pk_bf16_f32 v216, v216, v217\n"
    "v_mfma_f32_16x16x32_bf16 a[48:51], a[164:167], v[88:91], a[48:51]\n"
    "ds_read_b64_tr_b16 a[188:189], v245 offset:3584\n"
    "v_fma_f32 v218, v218, v6, v14\n"
    "v_mfma_f32_16x16x32_bf16 a[52:55], a[160:163], v[192:195], a[52:55]\n"
    "ds_read_b64_tr_b16 a[190:191], v245 offset:3840\n"
    "v_fma_f32 v219, v219, v7, v15\n"
    "v_mfma_f32_16x16x32_bf16 a[52:55], a[164:167], v[200:203], a[52:55]\n"
    "s_waitcnt lgkmcnt(10)\n"
    "ds_read_b64_tr_b16 a[128:129], v240 offset:49152\n"
    "v_cvt_pk_bf16_f32 v217, v218, v219\n"
    "v_mfma_f32_16x16x32_bf16 a[56:59], a[168:171], v[80:83], a[56:59]\n"
    "s_waitcnt lgkmcnt(7)\n"
    "ds_read_b64_tr_b16 a[130:131], v240 offset:49408\n"
    "v_fma_f32 v220, v220, v116, v124\n"
    "v_mfma_f32_16x16x32_bf16 a[56:59], a[172:175], v[88:91], a[56:59]\n"
    "s_mov_b64 s[92:93], s[18:19]\n"
    "s_and_b32 s93, s93, 0xffff\n"
    "s_mov_b32 s94, 1605632\n"
    "s_mov_b32 s59, 802816\n"
    "s_lshl_b32 m0, s50, 10\n"
    "s_add_u32 m0, m0, 147456\n"
    "ds_read_b64_tr_b16 a[132:133], v240 offset:51200\n"
    "v_fma_f32 v221, v221, v117, v125\n"
    "v_mfma_f32_16x16x32_bf16 a[60:63], a[168:171], v[192:195], a[60:63]\n"
    "buffer_load_dwordx4 v252, s[92:95], s59 offen nt lds\n"
    "v_mfma_f32_16x16x32_bf16 a[60:63], a[172:175], v[200:203], a[60:63]\n"
    "s_mov_b32 s68, 2097408\n"
    "s_add_u32 m0, s69, 16384\n"
    "ds_read_b64_tr_b16 a[134:135], v240 offset:51456\n"
    "ds_read_b64_tr_b16 a[136:137], v241 offset:49152\n"
    "v_mfma_f32_16x16x32_bf16 a[64:67], a[176:179], v[80:83], a[64:67]\n"
    "s_waitcnt lgkmcnt(7)\n"
    "buffer_load_dwordx4 v246, s[60:63], s68 offen offset:0 nt lds\n"
    "v_mfma_f32_16x16x32_bf16 a[64:67], a[180:183], v[88:91], a[64:67]\n"
    "s_mov_b64 s[92:93], s[20:21]\n"
    "s_and_b32 s93, s93, 0xffff\n"
    "s_mov_b32 s94, 1605632\n"
    "s_mov_b32 s59, 802816\n"
    "s_lshl_b32 m0, s50, 10\n"
    "s_add_u32 m0, m0, 151552\n"
    "ds_read_b64_tr_b16 a[138:139], v241 offset:49408\n"
    "v_cvt_pk_bf16_f32 v218, v220, v221\n"
    "v_mfma_f32_16x16x32_bf16 a[68:71], a[176:179], v[192:195], a[68:71]\n"
    "buffer_load_dwordx4 v252, s[92:95], s59 offen nt lds\n"
    "v_mfma_f32_16x16x32_bf16 a[68:71], a[180:183], v[200:203], a[68:71]\n"
    "s_add_u32 m0, s69, 17280\n"
    "ds_read_b64_tr_b16 a[140:141], v241 offset:51200\n"
    "v_fma_f32 v222, v222, v118, v126\n"
    "v_mfma_f32_16x16x32_bf16 a[72:75], a[184:187], v[80:83], a[72:75]\n"
    "s_waitcnt lgkmcnt(5)\n"
    "buffer_load_dwordx4 v246, s[60:63], s68 offen offset:128 nt lds\n"
    "v_fma_f32 v223, v223, v119, v127\n"
    "v_mfma_f32_16x16x32_bf16 a[72:75], a[188:191], v[88:91], a[72:75]\n"
    "s_mov_b64 s[92:93], s[22:23]\n"
    "s_and_b32 s93, s93, 0xffff\n"
    "s_mov_b32 s94, 1605632\n"
    "s_mov_b32 s59, 802816\n"
    "s_lshl_b32 m0, s50, 10\n"
    "s_add_u32 m0, m0, 159744\n"
    "ds_read_b64_tr_b16 a[142:143], v241 offset:51456\n"
    "ds_read_b64_tr_b16 a[144:145], v240 offset:49664\n"
    "v_mfma_f32_16x16x32_bf16 a[76:79], a[184:187], v[192:195], a[76:79]\n"
    "buffer_load_dwordx4 v253, s[92:95], s59 offen nt lds\n"
    "v_cvt_pk_bf16_f32 v219, v222, v223\n"
    "v_mfma_f32_16x16x32_bf16 a[76:79], a[188:191], v[200:203], a[76:79]\n"
    "s_mov_b32 s68, 2359552\n"
    "ds_read_b64_tr_b16 a[146:147], v240 offset:49920\n"
    "ds_read_b64_tr_b16 a[148:149], v240 offset:51712\n"
    "v_mfma_f32_16x16x32_bf16 a[16:19], a[128:131], v[96:99], a[16:19]\n"
    "s_waitcnt lgkmcnt(7)\n"
    "s_add_u32 m0, s69, 18432\n"
    "ds_read_b64_tr_b16 a[150:151], v240 offset:51968\n"
    "ds_read_b64_tr_b16 a[152:153], v241 offset:49664\n"
    "v_mfma_f32_16x16x32_bf16 a[16:19], a[132:135], v[104:107], a[16:19]\n"
    "buffer_load_dwordx4 v246, s[60:63], s68 offen offset:0 nt lds\n"
    "v_mfma_f32_16x16x32_bf16 a[20:23], a[128:131], v[208:211], a[20:23]\n"
    "s_waitcnt lgkmcnt(5)\n"
    "ds_read_b64_tr_b16 a[154:155], v241 offset:49920\n"
    "ds_read_b64_tr_b16 a[156:157], v241 offset:51712\n"
    "v_mfma_f32_16x16x32_bf16 a[20:23], a[132:135], v[216:219], a[20:23]\n"
    "s_add_u32 m0, s69, 19328\n"
    "ds_read_b64_tr_b16 a[158:159], v241 offset:51968\n"
    "ds_read_b64_tr_b16 a[160:161], v240 offset:50176\n"
    "v_mfma_f32_16x16x32_bf16 a[24:27], a[136:139], v[96:99], a[24:27]\n"
    "buffer_load_dwordx4 v246, s[60:63], s68 offen offset:128 nt lds\n"
    "v_mfma_f32_16x16x32_bf16 a[24:27], a[140:143], v[104:107], a[24:27]\n"
    "s_nop 0\n"
    "ds_read_b64_tr_b16 a[162:163], v240 offset:50432\n"
    "ds_read_b64_tr_b16 a[164:165], v240 offset:52224\n"
    "v_mfma_f32_16x16x32_bf16 a[28:31], a[136:139], v[208:211], a[28:31]\n"
    "s_nop 0\n"
    "ds_read_b64_tr_b16 a[166:167], v240 offset:52480\n"
    "ds_read_b64_tr_b16 a[168:169], v241 offset:50176\n"
    "v_mfma_f32_16x16x32_bf16 a[28:31], a[140:143], v[216:219], a[28:31]\n"
    "s_waitcnt lgkmcnt(9)\n"
    "ds_read_b64_tr_b16 a[170:171], v241 offset:50432\n"
    "ds_read_b64_tr_b16 a[172:173], v241 offset:52224\n"
    "v_mfma_f32_16x16x32_bf16 a[32:35], a[144:147], v[96:99], a[32:35]\n"
    "s_mov_b32 s68, 2621696\n"
    "ds_read_b64_tr_b16 a[174:175], v241 offset:52480\n"
    "ds_read_b64_tr_b16 a[176:177], v240 offset:50688\n"
    "v_mfma_f32_16x16x32_bf16 a[32:35], a[148:151], v[104:107], a[32:35]\n"
    "s_add_u32 m0, s69, 98304\n"
    "ds_read_b64_tr_b16 a[178:179], v240 offset:50944\n"
    "ds_read_b64_tr_b16 a[180:181], v240 offset:52736\n"
    "v_mfma_f32_16x16x32_bf16 a[36:39], a[144:147], v[208:211], a[36:39]\n"
    "buffer_load_dwordx4 v246, s[60:63], s68 offen offset:0 nt lds\n"
    "v_mfma_f32_16x16x32_bf16 a[36:39], a[148:151], v[216:219], a[36:39]\n"
    "s_waitcnt lgkmcnt(13)\n"
    "ds_read_b64_tr_b16 a[182:183], v240 offset:52992\n"
    "ds_read_b64_tr_b16 a[184:185], v241 offset:50688\n"
    "v_mfma_f32_16x16x32_bf16 a[40:43], a[152:155], v[96:99], a[40:43]\n"
    "s_waitcnt lgkmcnt(13)\n"
    "ds_read_b64_tr_b16 a[186:187], v241 offset:50944\n"
    "ds_read_b64_tr_b16 a[188:189], v241 offset:52736\n"
    "v_mfma_f32_16x16x32_bf16 a[40:43], a[156:159], v[104:107], a[40:43]\n"
    "s_nop 0\n"
    "ds_read_b64_tr_b16 a[190:191], v241 offset:52992\n"
    "ds_read_b64_tr_b16 a[128:129], v240 offset:32768\n"
    "v_mfma_f32_16x16x32_bf16 a[44:47], a[152:155], v[208:211], a[44:47]\n"
    "s_waitcnt lgkmcnt(13)\n"
    "ds_read_b64_tr_b16 a[130:131], v240 offset:33024\n"
    "ds_read_b64_tr_b16 a[132:133], v240 offset:34816\n"
    "v_mfma_f32_16x16x32_bf16 a[44:47], a[156:159], v[216:219], a[44:47]\n"
    "s_add_u32 m0, s69, 99200\n"
    "ds_read_b64_tr_b16 a[134:135], v240 offset:35072\n"
    "v_accvgpr_read_b32 v220, a240\n"
    "v_mfma_f32_16x16x32_bf16 a[48:51], a[160:163], v[96:99], a[48:51]\n"
    "buffer_load_dwordx4 v246, s[60:63], s68 offen offset:128 nt lds\n"
    "v_mfma_f32_16x16x32_bf16 a[48:51], a[164:167], v[104:107], a[48:51]\n"
    "s_nop 0\n"
    "ds_read_b64_tr_b16 a[136:137], v241 offset:32768\n"
    "ds_read_b64_tr_b16 a[138:139], v241 offset:33024\n"
    "v_mfma_f32_16x16x32_bf16 a[52:55], a[160:163], v[208:211], a[52:55]\n"
    "s_nop 0\n"
    "ds_read_b64_tr_b16 a[140:141], v241 offset:34816\n"
    "ds_read_b64_tr_b16 a[142:143], v241 offset:35072\n"
    "v_mfma_f32_16x16x32_bf16 a[52:55], a[164:167], v[216:219], a[52:55]\n"
    "s_waitcnt lgkmcnt(14)\n"
    "v_accvgpr_read_b32 v212, a241\n"
    "v_add_u32 v221, 0x18000, v220\n"
    "v_mfma_f32_16x16x32_bf16 a[56:59], a[168:171], v[96:99], a[56:59]\n"
    "s_mov_b32 s68, 2883840\n"
    "ds_read_b64_tr_b16 a[144:145], v240 offset:33280\n"
    "ds_read_b64_tr_b16 a[146:147], v240 offset:33536\n"
    "v_mfma_f32_16x16x32_bf16 a[56:59], a[172:175], v[104:107], a[56:59]\n"
    "s_add_u32 m0, s69, 100352\n"
    "ds_read_b64_tr_b16 a[148:149], v240 offset:35328\n"
    "ds_read_b64_tr_b16 a[150:151], v240 offset:35584\n"
    "v_mfma_f32_16x16x32_bf16 a[60:63], a[168:171], v[208:211], a[60:63]\n"
    "buffer_load_dwordx4 v246, s[60:63], s68 offen offset:0 nt lds\n"
    "v_mfma_f32_16x16x32_bf16 a[60:63], a[172:175], v[216:219], a[60:63]\n"
    "s_waitcnt lgkmcnt(14)\n"
    "v_xor_b32 v213, 0x1000, v212\n"
    "v_add_u32 v214, 0x18000, v212\n"
    "v_mfma_f32_16x16x32_bf16 a[64:67], a[176:179], v[96:99], a[64:67]\n"
    "s_waitcnt lgkmcnt(14)\n"
    "ds_read_b64_tr_b16 a[152:153], v241 offset:33280\n"
    "ds_read_b64_tr_b16 a[154:155], v241 offset:33536\n"
    "v_mfma_f32_16x16x32_bf16 a[64:67], a[180:183], v[104:107], a[64:67]\n"
    "s_nop 0\n"
    "ds_read_b64_tr_b16 a[156:157], v241 offset:35328\n"
    "ds_read_b64_tr_b16 a[158:159], v241 offset:35584\n"
    "v_mfma_f32_16x16x32_bf16 a[68:71], a[176:179], v[208:211], a[68:71]\n"
    "v_add_u32 v215, 0x18000, v213\n"
    "v_mfma_f32_16x16x32_bf16 a[68:71], a[180:183], v[216:219], a[68:71]\n"
    "s_waitcnt lgkmcnt(14)\n"
    "s_add_u32 m0, s69, 101248\n"
    "v_mfma_f32_16x16x32_bf16 a[72:75], a[184:187], v[96:99], a[72:75]\n"
    "buffer_load_dwordx4 v246, s[60:63], s68 offen offset:128 nt lds\n"
    "v_mfma_f32_16x16x32_bf16 a[72:75], a[188:191], v[104:107], a[72:75]\n"
    "s_nop 0\n"
    "ds_read_b64_tr_b16 a[160:161], v240 offset:33792\n"
    "ds_read_b64_tr_b16 a[162:163], v240 offset:34048\n"
    "v_mfma_f32_16x16x32_bf16 a[76:79], a[184:187], v[208:211], a[76:79]\n"
    "s_nop 0\n"
    "ds_read_b64_tr_b16 a[164:165], v240 offset:35840\n"
    "ds_read_b64_tr_b16 a[166:167], v240 offset:36096\n"
    "v_mfma_f32_16x16x32_bf16 a[76:79], a[188:191], v[216:219], a[76:79]\n"
    "s_waitcnt lgkmcnt(14)\n"
    "v_mfma_f32_16x16x32_bf16 v[4:7], a[128:131], v[0:3], 0\n"
    "s_waitcnt lgkmcnt(14)\n"
    "ds_read_b64_tr_b16 a[168:169], v241 offset:33792\n"
    "ds_read_b64_tr_b16 a[170:171], v241 offset:34048\n"
    "v_mfma_f32_16x16x32_bf16 v[4:7], a[132:135], v[8:11], v[4:7]\n"
    "s_nop 0\n"
    "ds_read_b64_tr_b16 a[172:173], v241 offset:35840\n"
    "ds_read_b64_tr_b16 a[174:175], v241 offset:36096\n"
    "v_mfma_f32_16x16x32_bf16 v[12:15], a[128:131], v[112:115], 0\n"
    "v_mfma_f32_16x16x32_bf16 v[12:15], a[132:135], v[120:123], v[12:15]\n"
    "s_waitcnt lgkmcnt(14)\n"
    "v_mfma_f32_16x16x32_bf16 v[20:23], a[136:139], v[0:3], 0\n"
    "s_waitcnt lgkmcnt(14)\n"
    "ds_read_b64_tr_b16 a[176:177], v240 offset:34304\n"
    "ds_read_b64_tr_b16 a[178:179], v240 offset:34560\n"
    "v_mfma_f32_16x16x32_bf16 v[20:23], a[140:143], v[8:11], v[20:23]\n"
    "s_nop 0\n"
    "ds_read_b64_tr_b16 a[180:181], v240 offset:36352\n"
    "ds_read_b64_tr_b16 a[182:183], v240 offset:36608\n"
    "v_mfma_f32_16x16x32_bf16 v[28:31], a[136:139], v[112:115], 0\n"
    "v_mfma_f32_16x16x32_bf16 v[28:31], a[140:143], v[120:123], v[28:31]\n"
    "s_waitcnt lgkmcnt(14)\n"
    "v_mfma_f32_16x16x32_bf16 v[36:39], a[144:147], v[0:3], 0\n"
    "s_waitcnt lgkmcnt(14)\n"
    "ds_read_b64_tr_b16 a[184:185], v241 offset:34304\n"
    "ds_read_b64_tr_b16 a[186:187], v241 offset:34560\n"
    "v_mfma_f32_16x16x32_bf16 v[36:39], a[148:151], v[8:11], v[36:39]\n"
    "s_nop 0\n"
    "ds_read_b64_tr_b16 a[188:189], v241 offset:36352\n"
    "ds_read_b64_tr_b16 a[190:191], v241 offset:36608\n"
    "v_mfma_f32_16x16x32_bf16 v[44:47], a[144:147], v[112:115], 0\n"
    "v_mfma_f32_16x16x32_bf16 v[44:47], a[148:151], v[120:123], v[44:47]\n"
    "s_waitcnt lgkmcnt(14)\n"
    "v_mfma_f32_16x16x32_bf16 v[52:55], a[152:155], v[0:3], 0\n"
    "s_waitcnt lgkmcnt(14)\n"
    "v_mfma_f32_16x16x32_bf16 v[52:55], a[156:159], v[8:11], v[52:55]\n"
    "v_mfma_f32_16x16x32_bf16 v[60:63], a[152:155], v[112:115], 0\n"
    "s_waitcnt lgkmcnt(12)\n"
    "s_waitcnt vmcnt(18)\n"
    "v_mfma_f32_16x16x32_bf16 v[60:63], a[156:159], v[120:123], v[60:63]\n"
    "s_barrier\n"
    "ds_read_b64_tr_b16 a[128:129], v240 offset:0\n"
    "ds_read_b64_tr_b16 a[130:131], v240 offset:256\n"
    "v_mfma_f32_16x16x32_bf16 v[68:71], a[160:163], v[0:3], 0\n"
    "s_mov_b32 s68, 3145984\n"
    "ds_read_b64_tr_b16 a[132:133], v240 offset:2048\n"
    "ds_read_b64_tr_b16 a[134:135], v240 offset:2304\n"
    "v_mfma_f32_16x16x32_bf16 v[68:71], a[164:167], v[8:11], v[68:71]\n"
    "s_add_u32 m0, s69, 114688\n"
    "v_accvgpr_read_b32 v224, a16\n"
    "v_accvgpr_read_b32 v225, a17\n"
    "v_mfma_f32_16x16x32_bf16 v[76:79], a[160:163], v[112:115], 0\n"
    "buffer_load_dwordx4 v246, s[60:63], s68 offen offset:0 nt lds\n"
    "v_mfma_f32_16x16x32_bf16 v[76:79], a[164:167], v[120:123], v[76:79]\n"
    "s_waitcnt lgkmcnt(14)\n"
    "v_accvgpr_read_b32 v226, a18\n"
    "v_accvgpr_read_b32 v227, a19\n"
    "v_mfma_f32_16x16x32_bf16 v[84:87], a[168:171], v[0:3], 0\n"
    "s_waitcnt lgkmcnt(12)\n"
    "v_cvt_pk_bf16_f32 v224, v224, v225\n"
    "v_cvt_pk_bf16_f32 v225, v226, v227\n"
    "v_mfma_f32_16x16x32_bf16 v[84:87], a[172:175], v[8:11], v[84:87]\n"
    "ds_write_b64 v221, v[224:225] offset:0\n"
    "v_mfma_f32_16x16x32_bf16 v[92:95], a[168:171], v[112:115], 0\n"
    "s_waitcnt lgkmcnt(9)\n"
    "ds_read_b64_tr_b16 a[136:137], v241 offset:0\n"
    "ds_read_b64_tr_b16 a[138:139], v241 offset:256\n"
    "v_mfma_f32_16x16x32_bf16 v[92:95], a[172:175], v[120:123], v[92:95]\n"
    "s_add_u32 m0, s69, 115584\n"
    "ds_read_b64_tr_b16 a[140:141], v241 offset:2048\n"
    "ds_read_b64_tr_b16 a[142:143], v241 offset:2304\n"
    "v_mfma_f32_16x16x32_bf16 v[100:103], a[176:179], v[0:3], 0\n"
    "buffer_load_dwordx4 v246, s[60:63], s68 offen offset:128 nt lds\n"
    "v_mfma_f32_16x16x32_bf16 v[100:103], a[180:183], v[8:11], v[100:103]\n"
    "v_accvgpr_read_b32 v228, a20\n"
    "v_accvgpr_read_b32 v229, a21\n"
    "v_mfma_f32_16x16x32_bf16 v[108:111], a[176:179], v[112:115], 0\n"
    "v_accvgpr_read_b32 v230, a22\n"
    "v_accvgpr_read_b32 v231, a23\n"
    "v_mfma_f32_16x16x32_bf16 v[108:111], a[180:183], v[120:123], v[108:111]\n"
    "s_waitcnt lgkmcnt(9)\n"
    "v_cvt_pk_bf16_f32 v228, v228, v229\n"
    "v_cvt_pk_bf16_f32 v229, v230, v231\n"
    "v_mfma_f32_16x16x32_bf16 v[116:119], a[184:187], v[0:3], 0\n"
    "s_mov_b32 s68, 3408128\n"
    "ds_write_b64 v221, v[228:229] offset:2048\n"
    "v_mfma_f32_16x16x32_bf16 v[116:119], a[188:191], v[8:11], v[116:119]\n"
    "s_add_u32 m0, s69, 116736\n"
    "ds_read_b64_tr_b16 a[144:145], v240 offset:512\n"
    "ds_read_b64_tr_b16 a[146:147], v240 offset:768\n"
    "v_mfma_f32_16x16x32_bf16 v[124:127], a[184:187], v[112:115], 0\n"
    "buffer_load_dwordx4 v246, s[60:63], s68 offen offset:0 nt lds\n"
    "v_mfma_f32_16x16x32_bf16 v[124:127], a[188:191], v[120:123], v[124:127]\n"
    "s_waitcnt lgkmcnt(10)\n"
    "ds_read_b64_tr_b16 a[148:149], v240 offset:2560\n"
    "ds_read_b64_tr_b16 a[150:151], v240 offset:2816\n"
    "v_mfma_f32_16x16x32_bf16 v[4:7], a[128:131], v[16:19], v[4:7]\n"
    "s_waitcnt lgkmcnt(10)\n"
    "ds_read_b64_tr_b16 a[152:153], v241 offset:512\n"
    "ds_read_b64_tr_b16 a[154:155], v241 offset:768\n"
    "v_mfma_f32_16x16x32_bf16 v[4:7], a[132:135], v[24:27], v[4:7]\n"
    "s_nop 0\n"
    "ds_read_b64_tr_b16 a[156:157], v241 offset:2560\n"
    "ds_read_b64_tr_b16 a[158:159], v241 offset:2816\n"
    "v_mfma_f32_16x16x32_bf16 v[12:15], a[128:131], v[128:131], v[12:15]\n"
    "s_waitcnt lgkmcnt(9)\n"
    "v_accvgpr_read_b32 v232, a24\n"
    "v_accvgpr_read_b32 v233, a25\n"
    "v_mfma_f32_16x16x32_bf16 v[12:15], a[132:135], v[136:139], v[12:15]\n"
    "s_add_u32 m0, s69, 117632\n"
    "v_accvgpr_read_b32 v234, a26\n"
    "v_accvgpr_read_b32 v235, a27\n"
    "v_mfma_f32_16x16x32_bf16 v[20:23], a[136:139], v[16:19], v[20:23]\n"
    "buffer_load_dwordx4 v246, s[60:63], s68 offen offset:128 nt lds\n"
    "v_mfma_f32_16x16x32_bf16 v[20:23], a[140:143], v[24:27], v[20:23]\n"
    "v_cvt_pk_bf16_f32 v232, v232, v233\n"
    "v_cvt_pk_bf16_f32 v233, v234, v235\n"
    "v_mfma_f32_16x16x32_bf16 v[28:31], a[136:139], v[128:131], v[28:31]\n"
    "ds_write_b64 v221, v[232:233] offset:512\n"
    "v_mfma_f32_16x16x32_bf16 v[28:31], a[140:143], v[136:139], v[28:31]\n"
    "s_waitcnt lgkmcnt(7)\n"
    "ds_read_b64_tr_b16 a[160:161], v240 offset:1024\n"
    "ds_read_b64_tr_b16 a[162:163], v240 offset:1280\n"
    "v_mfma_f32_16x16x32_bf16 v[36:39], a[144:147], v[16:19], v[36:39]\n"
    "s_waitcnt lgkmcnt(7)\n"
    "ds_read_b64_tr_b16 a[164:165], v240 offset:3072\n"
    "ds_read_b64_tr_b16 a[166:167], v240 offset:3328\n"
    "v_mfma_f32_16x16x32_bf16 v[36:39], a[148:151], v[24:27], v[36:39]\n"
    "s_nop 0\n"
    "ds_read_b64_tr_b16 a[168:169], v241 offset:1024\n"
    "ds_read_b64_tr_b16 a[170:171], v241 offset:1280\n"
    "v_mfma_f32_16x16x32_bf16 v[44:47], a[144:147], v[128:131], v[44:47]\n"
    "s_nop 0\n"
    "ds_read_b64_tr_b16 a[172:173], v241 offset:3072\n"
    "ds_read_b64_tr_b16 a[174:175], v241 offset:3328\n"
    "v_mfma_f32_16x16x32_bf16 v[44:47], a[148:151], v[136:139], v[44:47]\n"
    "s_waitcnt lgkmcnt(11)\n"
    "v_accvgpr_read_b32 v236, a28\n"
    "v_accvgpr_read_b32 v237, a29\n"
    "v_mfma_f32_16x16x32_bf16 v[52:55], a[152:155], v[16:19], v[52:55]\n"
    "s_waitcnt lgkmcnt(9)\n"
    "v_accvgpr_read_b32 v238, a30\n"
    "v_accvgpr_read_b32 v239, a31\n"
    "v_mfma_f32_16x16x32_bf16 v[52:55], a[156:159], v[24:27], v[52:55]\n"
    "v_cvt_pk_bf16_f32 v236, v236, v237\n"
    "v_cvt_pk_bf16_f32 v237, v238, v239\n"
    "v_mfma_f32_16x16x32_bf16 v[60:63], a[152:155], v[128:131], v[60:63]\n"
    "ds_write_b64 v221, v[236:237] offset:2560\n"
    "v_mfma_f32_16x16x32_bf16 v[60:63], a[156:159], v[136:139], v[60:63]\n"
    "s_waitcnt lgkmcnt(7)\n"
    "ds_read_b64_tr_b16 a[176:177], v240 offset:1536\n"
    "ds_read_b64_tr_b16 a[178:179], v240 offset:1792\n"
    "v_mfma_f32_16x16x32_bf16 v[68:71], a[160:163], v[16:19], v[68:71]\n"
    "s_waitcnt lgkmcnt(7)\n"
    "ds_read_b64_tr_b16 a[180:181], v240 offset:3584\n"
    "ds_read_b64_tr_b16 a[182:183], v240 offset:3840\n"
    "v_mfma_f32_16x16x32_bf16 v[68:71], a[164:167], v[24:27], v[68:71]\n"
    "v_accvgpr_read_b32 v224, a32\n"
    "v_accvgpr_read_b32 v225, a33\n"
    "v_mfma_f32_16x16x32_bf16 v[76:79], a[160:163], v[128:131], v[76:79]\n"
    "v_accvgpr_read_b32 v226, a34\n"
    "v_accvgpr_read_b32 v227, a35\n"
    "v_mfma_f32_16x16x32_bf16 v[76:79], a[164:167], v[136:139], v[76:79]\n"
    "s_waitcnt lgkmcnt(7)\n"
    "v_cvt_pk_bf16_f32 v224, v224, v225\n"
    "v_cvt_pk_bf16_f32 v225, v226, v227\n"
    "v_mfma_f32_16x16x32_bf16 v[84:87], a[168:171], v[16:19], v[84:87]\n"
    "s_waitcnt lgkmcnt(5)\n"
    "ds_write_b64 v221, v[224:225] offset:1024\n"
    "v_mfma_f32_16x16x32_bf16 v[84:87], a[172:175], v[24:27], v[84:87]\n"
    "s_nop 0\n"
    "ds_read_b64_tr_b16 a[184:185], v241 offset:1536\n"
    "ds_read_b64_tr_b16 a[186:187], v241 offset:1792\n"
    "ds_read_b64_tr_b16 a[188:189], v241 offset:3584\n"
    "ds_read_b64_tr_b16 a[190:191], v241 offset:3840\n"
    "v_mfma_f32_16x16x32_bf16 v[92:95], a[168:171], v[128:131], v[92:95]\n"
    "s_nop 0\n"
    "ds_read_b64_tr_b16 a[128:129], v242 offset:0\n"
    "ds_read_b64_tr_b16 a[130:131], v242 offset:256\n"
    "v_mfma_f32_16x16x32_bf16 v[92:95], a[172:175], v[136:139], v[92:95]\n"
    "s_waitcnt lgkmcnt(9)\n"
    "ds_read_b64_tr_b16 a[132:133], v242 offset:2048\n"
    "ds_read_b64_tr_b16 a[134:135], v242 offset:2304\n"
    "v_mfma_f32_16x16x32_bf16 v[100:103], a[176:179], v[16:19], v[100:103]\n"
    "s_waitcnt lgkmcnt(9)\n"
    "v_accvgpr_read_b32 v228, a36\n"
    "v_accvgpr_read_b32 v229, a37\n"
    "v_mfma_f32_16x16x32_bf16 v[100:103], a[180:183], v[24:27], v[100:103]\n"
    "v_accvgpr_read_b32 v230, a38\n"
    "v_accvgpr_read_b32 v231, a39\n"
    "v_mfma_f32_16x16x32_bf16 v[108:111], a[176:179], v[128:131], v[108:111]\n"
    "v_cvt_pk_bf16_f32 v228, v228, v229\n"
    "v_cvt_pk_bf16_f32 v229, v230, v231\n"
    "v_mfma_f32_16x16x32_bf16 v[108:111], a[180:183], v[136:139], v[108:111]\n"
    "s_waitcnt lgkmcnt(6)\n"
    "ds_write_b64 v221, v[228:229] offset:3072\n"
    "v_mfma_f32_16x16x32_bf16 v[116:119], a[184:187], v[16:19], v[116:119]\n"
    "s_waitcnt lgkmcnt(5)\n"
    "ds_read_b64_tr_b16 a[136:137], v243 offset:0\n"
    "ds_read_b64_tr_b16 a[138:139], v243 offset:256\n"
    "v_mfma_f32_16x16x32_bf16 v[116:119], a[188:191], v[24:27], v[116:119]\n"
    "s_nop 0\n"
    "ds_read_b64_tr_b16 a[140:141], v243 offset:2048\n"
    "ds_read_b64_tr_b16 a[142:143], v243 offset:2304\n"
    "v_mfma_f32_16x16x32_bf16 v[124:127], a[184:187], v[128:131], v[124:127]\n"
    "s_nop 0\n"
    "ds_read_b64_tr_b16 a[144:145], v242 offset:512\n"
    "ds_read_b64_tr_b16 a[146:147], v242 offset:768\n"
    "v_mfma_f32_16x16x32_bf16 v[124:127], a[188:191], v[136:139], v[124:127]\n"
    "s_waitcnt lgkmcnt(9)\n"
    "ds_read_b64_tr_b16 a[148:149], v242 offset:2560\n"
    "ds_read_b64_tr_b16 a[150:151], v242 offset:2816\n"
    "v_mfma_f32_16x16x32_bf16 v[4:7], a[128:131], v[32:35], v[4:7]\n"
    "s_waitcnt lgkmcnt(9)\n"
    "v_accvgpr_read_b32 v232, a40\n"
    "v_accvgpr_read_b32 v233, a41\n"
    "v_mfma_f32_16x16x32_bf16 v[4:7], a[132:135], v[40:43], v[4:7]\n"
    "v_accvgpr_read_b32 v234, a42\n"
    "v_accvgpr_read_b32 v235, a43\n"
    "v_mfma_f32_16x16x32_bf16 v[12:15], a[128:131], v[144:147], v[12:15]\n"
    "v_cvt_pk_bf16_f32 v232, v232, v233\n"
    "v_cvt_pk_bf16_f32 v233, v234, v235\n"
    "v_mfma_f32_16x16x32_bf16 v[12:15], a[132:135], v[152:155], v[12:15]\n"
    "s_waitcnt lgkmcnt(6)\n"
    "ds_write_b64 v221, v[232:233] offset:1536\n"
    "v_mfma_f32_16x16x32_bf16 v[20:23], a[136:139], v[32:35], v[20:23]\n"
    "s_waitcnt lgkmcnt(5)\n"
    "ds_read_b64_tr_b16 a[152:153], v243 offset:512\n"
    "ds_read_b64_tr_b16 a[154:155], v243 offset:768\n"
    "v_mfma_f32_16x16x32_bf16 v[20:23], a[140:143], v[40:43], v[20:23]\n"
    "s_nop 0\n"
    "ds_read_b64_tr_b16 a[156:157], v243 offset:2560\n"
    "ds_read_b64_tr_b16 a[158:159], v243 offset:2816\n"
    "v_mfma_f32_16x16x32_bf16 v[28:31], a[136:139], v[144:147], v[28:31]\n"
    "s_nop 0\n"
    "ds_read_b64_tr_b16 a[160:161], v242 offset:1024\n"
    "ds_read_b64_tr_b16 a[162:163], v242 offset:1280\n"
    "v_mfma_f32_16x16x32_bf16 v[28:31], a[140:143], v[152:155], v[28:31]\n"
    "s_waitcnt lgkmcnt(9)\n"
    "ds_read_b64_tr_b16 a[164:165], v242 offset:3072\n"
    "ds_read_b64_tr_b16 a[166:167], v242 offset:3328\n"
    "v_mfma_f32_16x16x32_bf16 v[36:39], a[144:147], v[32:35], v[36:39]\n"
    "s_waitcnt lgkmcnt(9)\n"
    "v_accvgpr_read_b32 v236, a44\n"
    "v_accvgpr_read_b32 v237, a45\n"
    "v_mfma_f32_16x16x32_bf16 v[36:39], a[148:151], v[40:43], v[36:39]\n"
    "v_accvgpr_read_b32 v238, a46\n"
    "v_accvgpr_read_b32 v239, a47\n"
    "v_cvt_pk_bf16_f32 v236, v236, v237\n"
    "v_cvt_pk_bf16_f32 v237, v238, v239\n"
    "v_mfma_f32_16x16x32_bf16 v[44:47], a[144:147], v[144:147], v[44:47]\n"
    "ds_write_b64 v221, v[236:237] offset:3584\n"
    "v_mfma_f32_16x16x32_bf16 v[44:47], a[148:151], v[152:155], v[44:47]\n"
    "s_waitcnt lgkmcnt(7)\n"
    "ds_read_b64_tr_b16 a[168:169], v243 offset:1024\n"
    "ds_read_b64_tr_b16 a[170:171], v243 offset:1280\n"
    "v_mfma_f32_16x16x32_bf16 v[52:55], a[152:155], v[32:35], v[52:55]\n"
    "s_waitcnt lgkmcnt(5)\n"
    "ds_read_b64_tr_b16 a[172:173], v243 offset:3072\n"
    "ds_read_b64_tr_b16 a[174:175], v243 offset:3328\n"
    "v_mfma_f32_16x16x32_bf16 v[52:55], a[156:159], v[40:43], v[52:55]\n"
    "s_waitcnt vmcnt(10)\n"
    "ds_read_b64_tr_b16 a[176:177], v242 offset:1536\n"
    "ds_read_b64_tr_b16 a[178:179], v242 offset:1792\n"
    "v_mfma_f32_16x16x32_bf16 v[60:63], a[152:155], v[144:147], v[60:63]\n"
    "s_waitcnt vmcnt(8)\n"
    "ds_read_b64_tr_b16 a[180:181], v242 offset:3584\n"
    "ds_read_b64_tr_b16 a[182:183], v242 offset:3840\n"
    "v_mfma_f32_16x16x32_bf16 v[60:63], a[156:159], v[152:155], v[60:63]\n"
    "s_barrier\n"
    "ds_read_b64_tr_b16 a[184:185], v243 offset:1536\n"
    "ds_read_b64_tr_b16 a[186:187], v243 offset:1792\n"
    "v_mfma_f32_16x16x32_bf16 v[68:71], a[160:163], v[32:35], v[68:71]\n"
    "s_waitcnt lgkmcnt(11)\n"
    "s_mov_b32 s44, 0\n"
    "ds_read_b64_tr_b16 a[188:189], v243 offset:3584\n"
    "ds_read_b64_tr_b16 a[190:191], v243 offset:3840\n"
    "v_mfma_f32_16x16x32_bf16 v[68:71], a[164:167], v[40:43], v[68:71]\n"
    "s_add_u32 m0, s47, 49152\n"
    "v_accvgpr_read_b32 v224, a48\n"
    "v_accvgpr_read_b32 v225, a49\n"
    "v_mfma_f32_16x16x32_bf16 v[76:79], a[160:163], v[144:147], v[76:79]\n"
    "buffer_load_dwordx4 v254, s[36:39], s44 offen offset:0 nt lds\n"
    "v_mfma_f32_16x16x32_bf16 v[76:79], a[164:167], v[152:155], v[76:79]\n"
    "s_waitcnt lgkmcnt(10)\n"
    "v_accvgpr_read_b32 v226, a50\n"
    "v_accvgpr_read_b32 v227, a51\n"
    "v_mfma_f32_16x16x32_bf16 v[84:87], a[168:171], v[32:35], v[84:87]\n"
    "s_waitcnt lgkmcnt(8)\n"
    "v_cvt_pk_bf16_f32 v224, v224, v225\n"
    "v_cvt_pk_bf16_f32 v225, v226, v227\n"
    "v_mfma_f32_16x16x32_bf16 v[84:87], a[172:175], v[40:43], v[84:87]\n"
    "ds_write_b64 v220, v[224:225] offset:0\n"
    "v_mfma_f32_16x16x32_bf16 v[92:95], a[168:171], v[144:147], v[92:95]\n"
    "s_waitcnt lgkmcnt(5)\n"
    "ds_read_b64_tr_b16 a[128:129], v242 offset:16384\n"
    "ds_read_b64_tr_b16 a[130:131], v242 offset:16640\n"
    "v_mfma_f32_16x16x32_bf16 v[92:95], a[172:175], v[152:155], v[92:95]\n"
    "s_add_u32 m0, s47, 51072\n"
    "ds_read_b64_tr_b16 a[132:133], v242 offset:18432\n"
    "ds_read_b64_tr_b16 a[134:135], v242 offset:18688\n"
    "v_mfma_f32_16x16x32_bf16 v[100:103], a[176:179], v[32:35], v[100:103]\n"
    "buffer_load_dwordx4 v254, s[36:39], s44 offen offset:128 nt lds\n"
    "v_mfma_f32_16x16x32_bf16 v[100:103], a[180:183], v[40:43], v[100:103]\n"
    "s_nop 0\n"
    "ds_read_b64_tr_b16 a[0:1], v255 offset:0\n"
    "ds_read_b64_tr_b16 a[2:3], v255 offset:2048\n"
    "v_mfma_f32_16x16x32_bf16 v[108:111], a[176:179], v[144:147], v[108:111]\n"
    "s_nop 0\n"
    "ds_read_b64_tr_b16 a[4:5], v255 offset:4096\n"
    "ds_read_b64_tr_b16 a[6:7], v255 offset:6144\n"
    "v_mfma_f32_16x16x32_bf16 v[108:111], a[180:183], v[152:155], v[108:111]\n"
    "s_waitcnt lgkmcnt(11)\n"
    "ds_read_b64_tr_b16 a[8:9], v255 offset:12288\n"
    "ds_read_b64_tr_b16 a[10:11], v255 offset:14336\n"
    "v_mfma_f32_16x16x32_bf16 v[116:119], a[184:187], v[32:35], v[116:119]\n"
    "s_waitcnt lgkmcnt(11)\n"
    "ds_read_b64_tr_b16 a[136:137], v243 offset:16384\n"
    "ds_read_b64_tr_b16 a[138:139], v243 offset:16640\n"
    "v_mfma_f32_16x16x32_bf16 v[116:119], a[188:191], v[40:43], v[116:119]\n"
    "s_add_u32 m0, s47, 52992\n"
    "ds_read_b64_tr_b16 a[140:141], v243 offset:18432\n"
    "ds_read_b64_tr_b16 a[142:143], v243 offset:18688\n"
    "v_mfma_f32_16x16x32_bf16 v[124:127], a[184:187], v[144:147], v[124:127]\n"
    "buffer_load_dwordx4 v254, s[36:39], s44 offen offset:256 nt lds\n"
    "v_mfma_f32_16x16x32_bf16 v[124:127], a[188:191], v[152:155], v[124:127]\n"
    "s_waitcnt lgkmcnt(12)\n"
    "v_accvgpr_read_b32 v228, a52\n"
    "v_accvgpr_read_b32 v229, a53\n"
    "v_accvgpr_read_b32 v230, a54\n"
    "v_accvgpr_read_b32 v231, a55\n"
    "v_cvt_pk_bf16_f32 v228, v228, v229\n"
    "v_cvt_pk_bf16_f32 v229, v230, v231\n"
    "v_mfma_f32_16x16x32_bf16 v[4:7], a[128:131], v[48:51], v[4:7]\n"
    "s_waitcnt lgkmcnt(10)\n"
    "ds_write_b64 v220, v[228:229] offset:2048\n"
    "v_mfma_f32_16x16x32_bf16 v[4:7], a[132:135], v[56:59], v[4:7]\n"
    "s_nop 0\n"
    "ds_read_b64_tr_b16 a[144:145], v242 offset:16896\n"
    "ds_read_b64_tr_b16 a[146:147], v242 offset:17152\n"
    "ds_read_b64_tr_b16 a[148:149], v242 offset:18944\n"
    "ds_read_b64_tr_b16 a[150:151], v242 offset:19200\n"
    "v_mfma_f32_16x16x32_bf16 v[12:15], a[128:131], v[160:163], v[12:15]\n"
    "s_waitcnt lgkmcnt(5)\n"
    "ds_read_b64_tr_b16 a[152:153], v243 offset:16896\n"
    "ds_read_b64_tr_b16 a[154:155], v243 offset:17152\n"
    "v_mfma_f32_16x16x32_bf16 v[12:15], a[132:135], v[168:171], v[12:15]\n"
    "s_add_u32 m0, s47, 54912\n"
    "ds_read_b64_tr_b16 a[156:157], v243 offset:18944\n"
    "ds_read_b64_tr_b16 a[158:159], v243 offset:19200\n"
    "v_mfma_f32_16x16x32_bf16 v[20:23], a[136:139], v[48:51], v[20:23]\n"
    "buffer_load_dwordx4 v254, s[36:39], s44 offen offset:384 nt lds\n"
    "v_mfma_f32_16x16x32_bf16 v[20:23], a[140:143], v[56:59], v[20:23]\n"
    "v_accvgpr_read_b32 v232, a56\n"
    "v_accvgpr_read_b32 v233, a57\n"
    "v_mfma_f32_16x16x32_bf16 v[28:31], a[136:139], v[160:163], v[28:31]\n"
    "s_waitcnt lgkmcnt(4)\n"
    "v_accvgpr_read_b32 v234, a58\n"
    "v_accvgpr_read_b32 v235, a59\n"
    "v_cvt_pk_bf16_f32 v232, v232, v233\n"
    "v_cvt_pk_bf16_f32 v233, v234, v235\n"
    "v_mfma_f32_16x16x32_bf16 v[28:31], a[140:143], v[168:171], v[28:31]\n"
    "s_add_u32 s59, s45, 48241664\n"
    "ds_write_b64 v220, v[232:233] offset:512\n"
    "v_mfma_f32_16x16x32_bf16 v[36:39], a[144:147], v[48:51], v[36:39]\n"
    "buffer_store_dwordx4 a[0:3], v248, s[64:67], s59 offen sc1\n"
    "v_mfma_f32_16x16x32_bf16 v[36:39], a[148:151], v[56:59], v[36:39]\n"
    "s_nop 0\n"
    "ds_read_b64_tr_b16 a[160:161], v242 offset:17408\n"
    "ds_read_b64_tr_b16 a[162:163], v242 offset:17664\n"
    "ds_read_b64_tr_b16 a[164:165], v242 offset:19456\n"
    "ds_read_b64_tr_b16 a[166:167], v242 offset:19712\n"
    "v_mfma_f32_16x16x32_bf16 v[44:47], a[144:147], v[160:163], v[44:47]\n"
    "s_nop 0\n"
    "ds_read_b64_tr_b16 a[168:169], v243 offset:17408\n"
    "ds_read_b64_tr_b16 a[170:171], v243 offset:17664\n"
    "v_mfma_f32_16x16x32_bf16 v[44:47], a[148:151], v[168:171], v[44:47]\n"
    "s_waitcnt lgkmcnt(9)\n"
    "ds_read_b64_tr_b16 a[172:173], v243 offset:19456\n"
    "ds_read_b64_tr_b16 a[174:175], v243 offset:19712\n"
    "v_mfma_f32_16x16x32_bf16 v[52:55], a[152:155], v[48:51], v[52:55]\n"
    "s_waitcnt lgkmcnt(9)\n"
    "v_accvgpr_read_b32 v236, a60\n"
    "v_accvgpr_read_b32 v237, a61\n"
    "v_mfma_f32_16x16x32_bf16 v[52:55], a[156:159], v[56:59], v[52:55]\n"
    "v_accvgpr_read_b32 v238, a62\n"
    "v_accvgpr_read_b32 v239, a63\n"
    "v_cvt_pk_bf16_f32 v236, v236, v237\n"
    "v_cvt_pk_bf16_f32 v237, v238, v239\n"
    "v_mfma_f32_16x16x32_bf16 v[60:63], a[152:155], v[160:163], v[60:63]\n"
    "s_waitcnt lgkmcnt(4)\n"
    "ds_write_b64 v220, v[236:237] offset:2560\n"
    "v_mfma_f32_16x16x32_bf16 v[60:63], a[156:159], v[168:171], v[60:63]\n"
    "s_add_u32 s59, s45, 48256000\n"
    "ds_read_b64_tr_b16 a[176:177], v242 offset:17920\n"
    "ds_read_b64_tr_b16 a[178:179], v242 offset:18176\n"
    "v_mfma_f32_16x16x32_bf16 v[68:71], a[160:163], v[48:51], v[68:71]\n"
    "buffer_store_dwordx4 a[4:7], v248, s[64:67], s59 offen sc1\n"
    "v_mfma_f32_16x16x32_bf16 v[68:71], a[164:167], v[56:59], v[68:71]\n"
    "s_nop 0\n"
    "ds_read_b64_tr_b16 a[180:181], v242 offset:19968\n"
    "ds_read_b64_tr_b16 a[182:183], v242 offset:20224\n"
    "v_mfma_f32_16x16x32_bf16 v[76:79], a[160:163], v[160:163], v[76:79]\n"
    "v_accvgpr_read_b32 v224, a64\n"
    "v_accvgpr_read_b32 v225, a65\n"
    "v_mfma_f32_16x16x32_bf16 v[76:79], a[164:167], v[168:171], v[76:79]\n"
    "s_waitcnt lgkmcnt(7)\n"
    "v_accvgpr_read_b32 v226, a66\n"
    "v_accvgpr_read_b32 v227, a67\n"
    "v_mfma_f32_16x16x32_bf16 v[84:87], a[168:171], v[48:51], v[84:87]\n"
    "s_waitcnt lgkmcnt(5)\n"
    "v_cvt_pk_bf16_f32 v224, v224, v225\n"
    "v_cvt_pk_bf16_f32 v225, v226, v227\n"
    "v_mfma_f32_16x16x32_bf16 v[84:87], a[172:175], v[56:59], v[84:87]\n"
    "s_nop 0\n"
    "ds_write_b64 v220, v[224:225] offset:1024\n"
    "ds_read_b64_tr_b16 a[184:185], v243 offset:17920\n"
    "ds_read_b64_tr_b16 a[186:187], v243 offset:18176\n"
    "ds_read_b64_tr_b16 a[188:189], v243 offset:19968\n"
    "ds_read_b64_tr_b16 a[190:191], v243 offset:20224\n"
    "v_mfma_f32_16x16x32_bf16 v[92:95], a[168:171], v[160:163], v[92:95]\n"
    "s_nop 0\n"
    "ds_read_b64_tr_b16 a[128:129], v240 offset:16384\n"
    "ds_read_b64_tr_b16 a[130:131], v240 offset:16640\n"
    "v_mfma_f32_16x16x32_bf16 v[92:95], a[172:175], v[168:171], v[92:95]\n"
    "s_waitcnt lgkmcnt(7)\n"
    "ds_read_b64_tr_b16 a[132:133], v240 offset:18432\n"
    "ds_read_b64_tr_b16 a[134:135], v240 offset:18688\n"
    "v_mfma_f32_16x16x32_bf16 v[100:103], a[176:179], v[48:51], v[100:103]\n"
    "s_add_u32 s59, s46, 52019200\n"
    "v_accvgpr_read_b32 v228, a68\n"
    "v_accvgpr_read_b32 v229, a69\n"
    "v_mfma_f32_16x16x32_bf16 v[100:103], a[180:183], v[56:59], v[100:103]\n"
    "buffer_store_dwordx4 a[8:11], v248, s[64:67], s59 offen sc1\n"
    "v_mfma_f32_16x16x32_bf16 v[108:111], a[176:179], v[160:163], v[108:111]\n"
    "v_accvgpr_read_b32 v230, a70\n"
    "v_accvgpr_read_b32 v231, a71\n"
    "v_mfma_f32_16x16x32_bf16 v[108:111], a[180:183], v[168:171], v[108:111]\n"
    "s_waitcnt lgkmcnt(6)\n"
    "v_cvt_pk_bf16_f32 v228, v228, v229\n"
    "v_cvt_pk_bf16_f32 v229, v230, v231\n"
    "v_mfma_f32_16x16x32_bf16 v[116:119], a[184:187], v[48:51], v[116:119]\n"
    "s_waitcnt lgkmcnt(4)\n"
    "ds_write_b64 v220, v[228:229] offset:3072\n"
    "v_mfma_f32_16x16x32_bf16 v[116:119], a[188:191], v[56:59], v[116:119]\n"
    "s_nop 0\n"
    "ds_read_b64_tr_b16 a[136:137], v241 offset:16384\n"
    "ds_read_b64_tr_b16 a[138:139], v241 offset:16640\n"
    "ds_read_b64_tr_b16 a[140:141], v241 offset:18432\n"
    "ds_read_b64_tr_b16 a[142:143], v241 offset:18688\n"
    "v_mfma_f32_16x16x32_bf16 v[124:127], a[184:187], v[160:163], v[124:127]\n"
    "s_nop 0\n"
    "ds_read_b64_tr_b16 a[144:145], v240 offset:16896\n"
    "ds_read_b64_tr_b16 a[146:147], v240 offset:17152\n"
    "v_mfma_f32_16x16x32_bf16 v[124:127], a[188:191], v[168:171], v[124:127]\n"
    "s_waitcnt lgkmcnt(9)\n"
    "ds_read_b64_tr_b16 a[148:149], v240 offset:18944\n"
    "ds_read_b64_tr_b16 a[150:151], v240 offset:19200\n"
    "v_mfma_f32_16x16x32_bf16 v[4:7], a[128:131], v[64:67], v[4:7]\n"
    "s_waitcnt lgkmcnt(9)\n"
    "v_accvgpr_read_b32 v232, a72\n"
    "v_accvgpr_read_b32 v233, a73\n"
    "v_mfma_f32_16x16x32_bf16 v[4:7], a[132:135], v[72:75], v[4:7]\n"
    "v_accvgpr_read_b32 v234, a74\n"
    "v_accvgpr_read_b32 v235, a75\n"
    "v_mfma_f32_16x16x32_bf16 v[12:15], a[128:131], v[176:179], v[12:15]\n"
    "v_cvt_pk_bf16_f32 v232, v232, v233\n"
    "v_cvt_pk_bf16_f32 v233, v234, v235\n"
    "v_mfma_f32_16x16x32_bf16 v[12:15], a[132:135], v[184:187], v[12:15]\n"
    "s_waitcnt lgkmcnt(6)\n"
    "ds_write_b64 v220, v[232:233] offset:1536\n"
    "v_mfma_f32_16x16x32_bf16 v[20:23], a[136:139], v[64:67], v[20:23]\n"
    "s_waitcnt lgkmcnt(5)\n"
    "ds_read_b64_tr_b16 a[152:153], v241 offset:16896\n"
    "ds_read_b64_tr_b16 a[154:155], v241 offset:17152\n"
    "v_mfma_f32_16x16x32_bf16 v[20:23], a[140:143], v[72:75], v[20:23]\n"
    "s_nop 0\n"
    "ds_read_b64_tr_b16 a[156:157], v241 offset:18944\n"
    "ds_read_b64_tr_b16 a[158:159], v241 offset:19200\n"
    "v_mfma_f32_16x16x32_bf16 v[28:31], a[136:139], v[176:179], v[28:31]\n"
    "s_nop 0\n"
    "ds_read_b64_tr_b16 a[160:161], v240 offset:17408\n"
    "ds_read_b64_tr_b16 a[162:163], v240 offset:17664\n"
    "v_mfma_f32_16x16x32_bf16 v[28:31], a[140:143], v[184:187], v[28:31]\n"
    "s_waitcnt lgkmcnt(9)\n"
    "ds_read_b64_tr_b16 a[164:165], v240 offset:19456\n"
    "ds_read_b64_tr_b16 a[166:167], v240 offset:19712\n"
    "v_mfma_f32_16x16x32_bf16 v[36:39], a[144:147], v[64:67], v[36:39]\n"
    "s_waitcnt lgkmcnt(9)\n"
    "v_accvgpr_read_b32 v236, a76\n"
    "v_accvgpr_read_b32 v237, a77\n"
    "v_mfma_f32_16x16x32_bf16 v[36:39], a[148:151], v[72:75], v[36:39]\n"
    "v_accvgpr_read_b32 v238, a78\n"
    "v_accvgpr_read_b32 v239, a79\n"
    "v_cvt_pk_bf16_f32 v236, v236, v237\n"
    "v_cvt_pk_bf16_f32 v237, v238, v239\n"
    "v_mfma_f32_16x16x32_bf16 v[44:47], a[144:147], v[176:179], v[44:47]\n"
    "ds_write_b64 v220, v[236:237] offset:3584\n"
    "v_mfma_f32_16x16x32_bf16 v[44:47], a[148:151], v[184:187], v[44:47]\n"
    "s_waitcnt lgkmcnt(7)\n"
    "ds_read_b64_tr_b16 a[168:169], v241 offset:17408\n"
    "ds_read_b64_tr_b16 a[170:171], v241 offset:17664\n"
    "v_mfma_f32_16x16x32_bf16 v[52:55], a[152:155], v[64:67], v[52:55]\n"
    "s_waitcnt lgkmcnt(7)\n"
    "ds_read_b64_tr_b16 a[172:173], v241 offset:19456\n"
    "ds_read_b64_tr_b16 a[174:175], v241 offset:19712\n"
    "v_mfma_f32_16x16x32_bf16 v[52:55], a[156:159], v[72:75], v[52:55]\n"
    "s_waitcnt lgkmcnt(5)\n"
    "ds_read_b64_tr_b16 a[176:177], v240 offset:17920\n"
    "ds_read_b64_tr_b16 a[178:179], v240 offset:18176\n"
    "v_mfma_f32_16x16x32_bf16 v[60:63], a[152:155], v[176:179], v[60:63]\n"
    "s_waitcnt vmcnt(7)\n"
    "ds_read_b64_tr_b16 a[180:181], v240 offset:19968\n"
    "ds_read_b64_tr_b16 a[182:183], v240 offset:20224\n"
    "v_mfma_f32_16x16x32_bf16 v[60:63], a[156:159], v[184:187], v[60:63]\n"
    "s_barrier\n"
    "ds_read_b64_tr_b16 a[184:185], v241 offset:17920\n"
    "ds_read_b64_tr_b16 a[186:187], v241 offset:18176\n"
    "v_mfma_f32_16x16x32_bf16 v[68:71], a[160:163], v[64:67], v[68:71]\n"
    "s_mov_b32 s44, 8192\n"
    "ds_read_b64_tr_b16 a[188:189], v241 offset:19968\n"
    "ds_read_b64_tr_b16 a[190:191], v241 offset:20224\n"
    "v_mfma_f32_16x16x32_bf16 v[68:71], a[164:167], v[72:75], v[68:71]\n"
    "s_add_u32 m0, s47, 0\n"
    "ds_read_b64_tr_b16 a[128:129], v242 offset:32768\n"
    "ds_read_b64_tr_b16 a[130:131], v242 offset:33024\n"
    "v_mfma_f32_16x16x32_bf16 v[76:79], a[160:163], v[176:179], v[76:79]\n"
    "buffer_load_dwordx4 v254, s[36:39], s44 offen offset:0 nt lds\n"
    "v_mfma_f32_16x16x32_bf16 v[76:79], a[164:167], v[184:187], v[76:79]\n"
    "s_waitcnt lgkmcnt(12)\n"
    "ds_read_b64_tr_b16 a[132:133], v242 offset:34816\n"
    "ds_read_b64_tr_b16 a[134:135], v242 offset:35072\n"
    "v_mfma_f32_16x16x32_bf16 v[84:87], a[168:171], v[64:67], v[84:87]\n"
    "s_waitcnt lgkmcnt(12)\n"
    "ds_read_b64_tr_b16 a[136:137], v243 offset:32768\n"
    "ds_read_b64_tr_b16 a[138:139], v243 offset:33024\n"
    "v_mfma_f32_16x16x32_bf16 v[84:87], a[172:175], v[72:75], v[84:87]\n"
    "s_nop 0\n"
    "ds_read_b64_tr_b16 a[140:141], v243 offset:34816\n"
    "ds_read_b64_tr_b16 a[142:143], v243 offset:35072\n"
    "v_mfma_f32_16x16x32_bf16 v[92:95], a[168:171], v[176:179], v[92:95]\n"
    "s_waitcnt lgkmcnt(12)\n"
    "ds_read_b64_tr_b16 a[144:145], v242 offset:33280\n"
    "ds_read_b64_tr_b16 a[146:147], v242 offset:33536\n"
    "v_mfma_f32_16x16x32_bf16 v[92:95], a[172:175], v[184:187], v[92:95]\n"
    "s_add_u32 m0, s47, 1920\n"
    "ds_read_b64_tr_b16 a[148:149], v242 offset:35328\n"
    "ds_read_b64_tr_b16 a[150:151], v242 offset:35584\n"
    "v_mfma_f32_16x16x32_bf16 v[100:103], a[176:179], v[64:67], v[100:103]\n"
    "buffer_load_dwordx4 v254, s[36:39], s44 offen offset:128 nt lds\n"
    "v_mfma_f32_16x16x32_bf16 v[100:103], a[180:183], v[72:75], v[100:103]\n"
    "s_nop 0\n"
    "ds_read_b64_tr_b16 a[152:153], v243 offset:33280\n"
    "ds_read_b64_tr_b16 a[154:155], v243 offset:33536\n"
    "v_mfma_f32_16x16x32_bf16 v[108:111], a[176:179], v[176:179], v[108:111]\n"
    "s_nop 0\n"
    "ds_read_b64_tr_b16 a[156:157], v243 offset:35328\n"
    "ds_read_b64_tr_b16 a[158:159], v243 offset:35584\n"
    "v_mfma_f32_16x16x32_bf16 v[108:111], a[180:183], v[184:187], v[108:111]\n"
    "s_waitcnt lgkmcnt(14)\n"
    "v_accvgpr_read_b32 v255, a238\n"
    "v_accvgpr_read_b32 v252, a237\n"
    "v_mfma_f32_16x16x32_bf16 v[116:119], a[184:187], v[64:67], v[116:119]\n"
    "s_waitcnt lgkmcnt(14)\n"
    "ds_read_b64_tr_b16 a[160:161], v242 offset:33792\n"
    "ds_read_b64_tr_b16 a[162:163], v242 offset:34048\n"
    "v_mfma_f32_16x16x32_bf16 v[116:119], a[188:191], v[72:75], v[116:119]\n"
    "s_add_u32 m0, s47, 3840\n"
    "ds_read_b64_tr_b16 a[164:165], v242 offset:35840\n"
    "ds_read_b64_tr_b16 a[166:167], v242 offset:36096\n"
    "v_mfma_f32_16x16x32_bf16 v[124:127], a[184:187], v[176:179], v[124:127]\n"
    "buffer_load_dwordx4 v254, s[36:39], s44 offen offset:256 nt lds\n"
    "v_mfma_f32_16x16x32_bf16 v[124:127], a[188:191], v[184:187], v[124:127]\n"
    "s_waitcnt lgkmcnt(14)\n"
    "v_xor_b32 v253, 32, v252\n"
    "v_mov_b32 v228, 0xff800000\n"
    "v_mfma_f32_16x16x32_bf16 v[4:7], a[128:131], v[80:83], v[4:7]\n"
    "s_waitcnt lgkmcnt(14)\n"
    "ds_read_b64_tr_b16 a[168:169], v243 offset:33792\n"
    "ds_read_b64_tr_b16 a[170:171], v243 offset:34048\n"
    "v_mfma_f32_16x16x32_bf16 v[4:7], a[132:135], v[88:91], v[4:7]\n"
    "s_nop 0\n"
    "ds_read_b64_tr_b16 a[172:173], v243 offset:35840\n"
    "ds_read_b64_tr_b16 a[174:175], v243 offset:36096\n"
    "v_mfma_f32_16x16x32_bf16 v[12:15], a[128:131], v[192:195], v[12:15]\n"
    "v_mov_b32 v229, 0xff800000\n"
    "v_mov_b32 v230, 0xff800000\n"
    "v_mfma_f32_16x16x32_bf16 v[12:15], a[132:135], v[200:203], v[12:15]\n"
    "s_waitcnt lgkmcnt(14)\n"
    "v_mov_b32 v231, 0xff800000\n"
    "v_mfma_f32_16x16x32_bf16 v[20:23], a[136:139], v[80:83], v[20:23]\n"
    "s_barrier\n"
    "ds_read_b64_tr_b16 a[176:177], v242 offset:34304\n"
    "ds_read_b64_tr_b16 a[178:179], v242 offset:34560\n"
    "v_mfma_f32_16x16x32_bf16 v[20:23], a[140:143], v[88:91], v[20:23]\n"
    "s_add_u32 m0, s47, 5760\n"
    "ds_read_b64_tr_b16 a[180:181], v242 offset:36352\n"
    "ds_read_b64_tr_b16 a[182:183], v242 offset:36608\n"
    "v_mfma_f32_16x16x32_bf16 v[28:31], a[136:139], v[192:195], v[28:31]\n"
    "buffer_load_dwordx4 v254, s[36:39], s44 offen offset:384 nt lds\n"
    "v_mfma_f32_16x16x32_bf16 v[28:31], a[140:143], v[200:203], v[28:31]\n"
    "s_waitcnt lgkmcnt(14)\n"
    "v_mfma_f32_16x16x32_bf16 v[36:39], a[144:147], v[80:83], v[36:39]\n"
    "s_waitcnt lgkmcnt(14)\n"
    "ds_read_b64_tr_b16 a[184:185], v243 offset:34304\n"
    "ds_read_b64_tr_b16 a[186:187], v243 offset:34560\n"
    "v_mfma_f32_16x16x32_bf16 v[36:39], a[148:151], v[88:91], v[36:39]\n"
    "s_nop 0\n"
    "ds_read_b64_tr_b16 a[188:189], v243 offset:36352\n"
    "ds_read_b64_tr_b16 a[190:191], v243 offset:36608\n"
    "v_mfma_f32_16x16x32_bf16 v[44:47], a[144:147], v[192:195], v[44:47]\n"
    "s_waitcnt lgkmcnt(14)\n"
    "s_mov_b32 s44, 32768\n"
    "s_add_u32 m0, s47, 16384\n"
    "v_mfma_f32_16x16x32_bf16 v[44:47], a[148:151], v[200:203], v[44:47]\n"
    "buffer_load_dwordx4 v254, s[36:39], s44 offen offset:0 nt lds\n"
    "v_mfma_f32_16x16x32_bf16 v[52:55], a[152:155], v[80:83], v[52:55]\n"
    "s_waitcnt lgkmcnt(14)\n"
    "ds_read_b64_tr_b16 a[128:129], v242 offset:49152\n"
    "ds_read_b64_tr_b16 a[130:131], v242 offset:49408\n"
    "v_mfma_f32_16x16x32_bf16 v[52:55], a[156:159], v[88:91], v[52:55]\n"
    "s_nop 0\n"
    "ds_read_b64_tr_b16 a[132:133], v242 offset:51200\n"
    "ds_read_b64_tr_b16 a[134:135], v242 offset:51456\n"
    "v_mfma_f32_16x16x32_bf16 v[60:63], a[152:155], v[192:195], v[60:63]\n"
    "v_mfma_f32_16x16x32_bf16 v[60:63], a[156:159], v[200:203], v[60:63]\n"
    "s_waitcnt lgkmcnt(14)\n"
    "v_mfma_f32_16x16x32_bf16 v[68:71], a[160:163], v[80:83], v[68:71]\n"
    "s_add_u32 m0, s47, 18304\n"
    "ds_read_b64_tr_b16 a[136:137], v243 offset:49152\n"
    "ds_read_b64_tr_b16 a[138:139], v243 offset:49408\n"
    "v_mfma_f32_16x16x32_bf16 v[68:71], a[164:167], v[88:91], v[68:71]\n"
    "buffer_load_dwordx4 v254, s[36:39], s44 offen offset:128 nt lds\n"
    "v_mfma_f32_16x16x32_bf16 v[76:79], a[160:163], v[192:195], v[76:79]\n"
    "s_nop 0\n"
    "ds_read_b64_tr_b16 a[140:141], v243 offset:51200\n"
    "ds_read_b64_tr_b16 a[142:143], v243 offset:51456\n"
    "v_mfma_f32_16x16x32_bf16 v[76:79], a[164:167], v[200:203], v[76:79]\n"
    "s_waitcnt lgkmcnt(14)\n"
    "v_mfma_f32_16x16x32_bf16 v[84:87], a[168:171], v[80:83], v[84:87]\n"
    "s_waitcnt lgkmcnt(14)\n"
    "ds_read_b64_tr_b16 a[144:145], v242 offset:49664\n"
    "ds_read_b64_tr_b16 a[146:147], v242 offset:49920\n"
    "v_mfma_f32_16x16x32_bf16 v[84:87], a[172:175], v[88:91], v[84:87]\n"
    "s_nop 0\n"
    "ds_read_b64_tr_b16 a[148:149], v242 offset:51712\n"
    "ds_read_b64_tr_b16 a[150:151], v242 offset:51968\n"
    "v_mfma_f32_16x16x32_bf16 v[92:95], a[168:171], v[192:195], v[92:95]\n"
    "s_waitcnt lgkmcnt(14)\n"
    "s_add_u32 m0, s47, 20224\n"
    "v_mfma_f32_16x16x32_bf16 v[92:95], a[172:175], v[200:203], v[92:95]\n"
    "buffer_load_dwordx4 v254, s[36:39], s44 offen offset:256 nt lds\n"
    "v_mfma_f32_16x16x32_bf16 v[100:103], a[176:179], v[80:83], v[100:103]\n"
    "s_waitcnt lgkmcnt(14)\n"
    "ds_read_b64_tr_b16 a[152:153], v243 offset:49664\n"
    "ds_read_b64_tr_b16 a[154:155], v243 offset:49920\n"
    "v_mfma_f32_16x16x32_bf16 v[100:103], a[180:183], v[88:91], v[100:103]\n"
    "s_nop 0\n"
    "ds_read_b64_tr_b16 a[156:157], v243 offset:51712\n"
    "ds_read_b64_tr_b16 a[158:159], v243 offset:51968\n"
    "v_mfma_f32_16x16x32_bf16 v[108:111], a[176:179], v[192:195], v[108:111]\n"
    "v_mfma_f32_16x16x32_bf16 v[108:111], a[180:183], v[200:203], v[108:111]\n"
    "s_waitcnt lgkmcnt(14)\n"
    "v_mfma_f32_16x16x32_bf16 v[116:119], a[184:187], v[80:83], v[116:119]\n"
    "s_add_u32 m0, s47, 22144\n"
    "ds_read_b64_tr_b16 a[160:161], v242 offset:50176\n"
    "ds_read_b64_tr_b16 a[162:163], v242 offset:50432\n"
    "v_mfma_f32_16x16x32_bf16 v[116:119], a[188:191], v[88:91], v[116:119]\n"
    "buffer_load_dwordx4 v254, s[36:39], s44 offen offset:384 nt lds\n"
    "v_mfma_f32_16x16x32_bf16 v[124:127], a[184:187], v[192:195], v[124:127]\n"
    "s_nop 0\n"
    "ds_read_b64_tr_b16 a[164:165], v242 offset:52224\n"
    "ds_read_b64_tr_b16 a[166:167], v242 offset:52480\n"
    "v_mfma_f32_16x16x32_bf16 v[124:127], a[188:191], v[200:203], v[124:127]\n"
    "s_waitcnt lgkmcnt(14)\n"
    "v_mfma_f32_16x16x32_bf16 v[4:7], a[128:131], v[96:99], v[4:7]\n"
    "s_waitcnt lgkmcnt(14)\n"
    "ds_read_b64_tr_b16 a[168:169], v243 offset:50176\n"
    "ds_read_b64_tr_b16 a[170:171], v243 offset:50432\n"
    "v_mfma_f32_16x16x32_bf16 v[4:7], a[132:135], v[104:107], v[4:7]\n"
    "s_nop 0\n"
    "ds_read_b64_tr_b16 a[172:173], v243 offset:52224\n"
    "ds_read_b64_tr_b16 a[174:175], v243 offset:52480\n"
    "v_mfma_f32_16x16x32_bf16 v[12:15], a[128:131], v[208:211], v[12:15]\n"
    "v_mfma_f32_16x16x32_bf16 v[12:15], a[132:135], v[216:219], v[12:15]\n"
    "s_waitcnt lgkmcnt(14)\n"
    "v_mfma_f32_16x16x32_bf16 v[20:23], a[136:139], v[96:99], v[20:23]\n"
    "s_waitcnt lgkmcnt(14)\n"
    "ds_read_b64_tr_b16 a[176:177], v242 offset:50688\n"
    "ds_read_b64_tr_b16 a[178:179], v242 offset:50944\n"
    "v_mfma_f32_16x16x32_bf16 v[20:23], a[140:143], v[104:107], v[20:23]\n"
    "s_nop 0\n"
    "ds_read_b64_tr_b16 a[180:181], v242 offset:52736\n"
    "ds_read_b64_tr_b16 a[182:183], v242 offset:52992\n"
    "v_mfma_f32_16x16x32_bf16 v[28:31], a[136:139], v[208:211], v[28:31]\n"
    "v_mfma_f32_16x16x32_bf16 v[28:31], a[140:143], v[216:219], v[28:31]\n"
    "s_waitcnt lgkmcnt(14)\n"
    "v_cvt_pk_bf16_f32 v4, v4, v5\n"
    "v_cvt_pk_bf16_f32 v5, v6, v7\n"
    "v_mfma_f32_16x16x32_bf16 v[36:39], a[144:147], v[96:99], v[36:39]\n"
    "s_waitcnt lgkmcnt(14)\n"
    "ds_write_b64 v220, v[4:5] offset:32768\n"
    "v_mfma_f32_16x16x32_bf16 v[36:39], a[148:151], v[104:107], v[36:39]\n"
    "v_cvt_pk_bf16_f32 v12, v12, v13\n"
    "v_cvt_pk_bf16_f32 v13, v14, v15\n"
    "v_mfma_f32_16x16x32_bf16 v[44:47], a[144:147], v[208:211], v[44:47]\n"
    "ds_write_b64 v220, v[12:13] offset:34816\n"
    "v_mfma_f32_16x16x32_bf16 v[44:47], a[148:151], v[216:219], v[44:47]\n"
    "s_waitcnt lgkmcnt(14)\n"
    "v_cvt_pk_bf16_f32 v20, v20, v21\n"
    "v_cvt_pk_bf16_f32 v21, v22, v23\n"
    "v_mfma_f32_16x16x32_bf16 v[52:55], a[152:155], v[96:99], v[52:55]\n"
    "s_waitcnt lgkmcnt(14)\n"
    "ds_write_b64 v220, v[20:21] offset:33280\n"
    "v_mfma_f32_16x16x32_bf16 v[52:55], a[156:159], v[104:107], v[52:55]\n"
    "v_cvt_pk_bf16_f32 v28, v28, v29\n"
    "v_cvt_pk_bf16_f32 v29, v30, v31\n"
    "v_mfma_f32_16x16x32_bf16 v[60:63], a[152:155], v[208:211], v[60:63]\n"
    "ds_write_b64 v220, v[28:29] offset:35328\n"
    "v_mfma_f32_16x16x32_bf16 v[60:63], a[156:159], v[216:219], v[60:63]\n"
    "s_waitcnt lgkmcnt(14)\n"
    "v_cvt_pk_bf16_f32 v36, v36, v37\n"
    "v_cvt_pk_bf16_f32 v37, v38, v39\n"
    "v_mfma_f32_16x16x32_bf16 v[68:71], a[160:163], v[96:99], v[68:71]\n"
    "s_waitcnt lgkmcnt(12)\n"
    "ds_write_b64 v220, v[36:37] offset:33792\n"
    "v_mfma_f32_16x16x32_bf16 v[68:71], a[164:167], v[104:107], v[68:71]\n"
    "s_nop 0\n"
    "ds_read_b64_tr_b16 a[184:185], v243 offset:50688\n"
    "ds_read_b64_tr_b16 a[186:187], v243 offset:50944\n"
    "v_mfma_f32_16x16x32_bf16 v[76:79], a[160:163], v[208:211], v[76:79]\n"
    "s_nop 0\n"
    "ds_read_b64_tr_b16 a[188:189], v243 offset:52736\n"
    "ds_read_b64_tr_b16 a[190:191], v243 offset:52992\n"
    "v_mfma_f32_16x16x32_bf16 v[76:79], a[164:167], v[216:219], v[76:79]\n"
    "s_waitcnt lgkmcnt(14)\n"
    "v_cvt_pk_bf16_f32 v44, v44, v45\n"
    "v_cvt_pk_bf16_f32 v45, v46, v47\n"
    "v_mfma_f32_16x16x32_bf16 v[84:87], a[168:171], v[96:99], v[84:87]\n"
    "s_waitcnt lgkmcnt(13)\n"
    "ds_write_b64 v220, v[44:45] offset:35840\n"
    "v_mfma_f32_16x16x32_bf16 v[84:87], a[172:175], v[104:107], v[84:87]\n"
    "v_cvt_pk_bf16_f32 v52, v52, v53\n"
    "v_cvt_pk_bf16_f32 v53, v54, v55\n"
    "v_mfma_f32_16x16x32_bf16 v[92:95], a[168:171], v[208:211], v[92:95]\n"
    "ds_write_b64 v220, v[52:53] offset:34304\n"
    "v_mfma_f32_16x16x32_bf16 v[92:95], a[172:175], v[216:219], v[92:95]\n"
    "s_waitcnt lgkmcnt(13)\n"
    "v_cvt_pk_bf16_f32 v60, v60, v61\n"
    "v_cvt_pk_bf16_f32 v61, v62, v63\n"
    "v_mfma_f32_16x16x32_bf16 v[100:103], a[176:179], v[96:99], v[100:103]\n"
    "s_waitcnt lgkmcnt(11)\n"
    "ds_write_b64 v220, v[60:61] offset:36352\n"
    "v_mfma_f32_16x16x32_bf16 v[100:103], a[180:183], v[104:107], v[100:103]\n"
    "v_cvt_pk_bf16_f32 v68, v68, v69\n"
    "v_cvt_pk_bf16_f32 v69, v70, v71\n"
    "v_mfma_f32_16x16x32_bf16 v[108:111], a[176:179], v[208:211], v[108:111]\n"
    "ds_write_b64 v220, v[68:69] offset:49152\n"
    "v_mfma_f32_16x16x32_bf16 v[108:111], a[180:183], v[216:219], v[108:111]\n"
    "s_waitcnt lgkmcnt(6)\n"
    "v_cvt_pk_bf16_f32 v76, v76, v77\n"
    "v_cvt_pk_bf16_f32 v77, v78, v79\n"
    "v_mfma_f32_16x16x32_bf16 v[116:119], a[184:187], v[96:99], v[116:119]\n"
    "s_waitcnt lgkmcnt(4)\n"
    "ds_write_b64 v220, v[76:77] offset:51200\n"
    "v_mfma_f32_16x16x32_bf16 v[116:119], a[188:191], v[104:107], v[116:119]\n"
    "v_mfma_f32_16x16x32_bf16 v[124:127], a[184:187], v[208:211], v[124:127]\n"
    "v_mfma_f32_16x16x32_bf16 v[124:127], a[188:191], v[216:219], v[124:127]\n"
    "s_waitcnt vmcnt(4)\n"
    "s_barrier\n"
    "ds_read_b128 v[128:131], v251 offset:49152\n"
    "ds_read_b128 v[132:135], v251 offset:49664\n"
    "ds_read_b128 v[136:139], v251 offset:51200\n"
    "ds_read_b128 v[140:143], v251 offset:51712\n"
    "ds_read_b128 v[160:163], v251 offset:53248\n"
    "ds_read_b128 v[164:167], v251 offset:53760\n"
    "ds_read_b128 v[168:171], v251 offset:55296\n"
    "ds_read_b128 v[172:175], v251 offset:55808\n"
    "v_cvt_pk_bf16_f32 v84, v84, v85\n"
    "v_cvt_pk_bf16_f32 v85, v86, v87\n"
    "v_cvt_pk_bf16_f32 v92, v92, v93\n"
    "v_cvt_pk_bf16_f32 v93, v94, v95\n"
    "v_cvt_pk_bf16_f32 v100, v100, v101\n"
    "v_cvt_pk_bf16_f32 v101, v102, v103\n"
    "v_cvt_pk_bf16_f32 v108, v108, v109\n"
    "v_cvt_pk_bf16_f32 v109, v110, v111\n"
    "v_cvt_pk_bf16_f32 v116, v116, v117\n"
    "v_cvt_pk_bf16_f32 v117, v118, v119\n"
    "v_cvt_pk_bf16_f32 v124, v124, v125\n"
    "v_cvt_pk_bf16_f32 v125, v126, v127\n"
    "ds_write_b64 v220, v[84:85] offset:49664\n"
    "s_mov_b32 s45, 0\n"
    "s_add_u32 m0, s48, 32768\n"
    "s_nop 0\n"
    "buffer_load_dwordx4 v255, s[40:43], s45 offen offset:0 nt lds\n"
    "ds_write_b64 v220, v[92:93] offset:51712\n"
    "s_add_u32 m0, s48, 33664\n"
    "s_nop 0\n"
    "buffer_load_dwordx4 v255, s[40:43], s45 offen offset:128 nt lds\n"
    "ds_write_b64 v220, v[100:101] offset:50176\n"
    "s_mov_b32 s46, 4096\n"
    "s_add_u32 m0, s48, 34816\n"
    "s_nop 0\n"
    "buffer_load_dwordx4 v255, s[40:43], s46 offen offset:0 nt lds\n"
    "ds_write_b64 v220, v[108:109] offset:52224\n"
    "s_add_u32 m0, s48, 35712\n"
    "s_nop 0\n"
    "buffer_load_dwordx4 v255, s[40:43], s46 offen offset:128 nt lds\n"
    "ds_write_b64 v220, v[116:117] offset:50688\n"
    "s_mov_b32 s45, 256\n"
    "s_add_u32 m0, s48, 49152\n"
    "s_nop 0\n"
    "buffer_load_dwordx4 v255, s[40:43], s45 offen offset:0 nt lds\n"
    "ds_write_b64 v220, v[124:125] offset:52736\n"
    "s_add_u32 m0, s48, 50048\n"
    "s_nop 0\n"
    "buffer_load_dwordx4 v255, s[40:43], s45 offen offset:128 nt lds\n"
    "s_mov_b32 s46, 4352\n"
    "s_add_u32 m0, s48, 51200\n"
    "s_nop 0\n"
    "buffer_load_dwordx4 v255, s[40:43], s46 offen offset:0 nt lds\n"
    "s_add_u32 m0, s48, 52096\n"
    "s_nop 0\n"
    "buffer_load_dwordx4 v255, s[40:43], s46 offen offset:128 nt lds\n"
    "s_waitcnt lgkmcnt(0)\n"
    "s_barrier\n"
    "ds_read_b128 a[0:3], v215 offset:0\n"
    "ds_read_b128 a[4:7], v215 offset:1024\n"
    "ds_read_b128 a[8:11], v213 offset:0\n"
    "ds_read_b128 a[12:15], v213 offset:1024\n"
    "ds_read_b128 a[16:19], v213 offset:32768\n"
    "ds_read_b128 a[20:23], v213 offset:33792\n"
    "ds_read_b128 a[24:27], v213 offset:49152\n"
    "ds_read_b128 a[28:31], v213 offset:50176\n"
    "ds_read_b128 a[32:35], v215 offset:2048\n"
    "ds_read_b128 a[36:39], v215 offset:3072\n"
    "s_waitcnt lgkmcnt(9)\n"
    "v_mfma_f32_16x16x32_bf16 v[144:147], v[128:131], a[0:3], 0\n"
    "s_waitcnt lgkmcnt(8)\n"
    "ds_read_b128 a[40:43], v213 offset:2048\n"
    "v_mfma_f32_16x16x32_bf16 v[144:147], v[132:135], a[4:7], v[144:147]\n"
    "s_waitcnt lgkmcnt(8)\n"
    "ds_read_b128 a[44:47], v213 offset:3072\n"
    "v_mfma_f32_16x16x32_bf16 v[144:147], v[136:139], a[8:11], v[144:147]\n"
    "s_waitcnt lgkmcnt(8)\n"
    "ds_read_b128 a[48:51], v213 offset:34816\n"
    "v_mfma_f32_16x16x32_bf16 v[144:147], v[140:143], a[12:15], v[144:147]\n"
    "s_waitcnt lgkmcnt(8)\n"
    "ds_read_b128 a[52:55], v213 offset:35840\n"
    "v_mfma_f32_16x16x32_bf16 v[144:147], v[160:163], a[16:19], v[144:147]\n"
    "s_waitcnt lgkmcnt(8)\n"
    "ds_read_b128 a[56:59], v213 offset:51200\n"
    "v_mfma_f32_16x16x32_bf16 v[144:147], v[164:167], a[20:23], v[144:147]\n"
    "s_waitcnt lgkmcnt(8)\n"
    "ds_read_b128 a[60:63], v213 offset:52224\n"
    "v_mfma_f32_16x16x32_bf16 v[144:147], v[168:171], a[24:27], v[144:147]\n"
    "s_waitcnt lgkmcnt(8)\n"
    "ds_read_b128 a[64:67], v214 offset:0\n"
    "v_mfma_f32_16x16x32_bf16 v[144:147], v[172:175], a[28:31], v[144:147]\n"
    "s_waitcnt lgkmcnt(8)\n"
    "ds_read_b128 a[68:71], v214 offset:1024\n"
    "v_mfma_f32_16x16x32_bf16 v[148:151], v[128:131], a[32:35], 0\n"
    "s_waitcnt lgkmcnt(8)\n"
    "ds_read_b128 a[72:75], v212 offset:0\n"
    "v_mfma_f32_16x16x32_bf16 v[148:151], v[132:135], a[36:39], v[148:151]\n"
    "s_waitcnt lgkmcnt(8)\n"
    "ds_read_b128 a[76:79], v212 offset:1024\n"
    "v_mfma_f32_16x16x32_bf16 v[148:151], v[136:139], a[40:43], v[148:151]\n"
    "s_waitcnt lgkmcnt(8)\n"
    "ds_read_b128 a[80:83], v212 offset:32768\n"
    "v_mfma_f32_16x16x32_bf16 v[148:151], v[140:143], a[44:47], v[148:151]\n"
    "s_waitcnt lgkmcnt(8)\n"
    "ds_read_b128 a[84:87], v212 offset:33792\n"
    "v_mfma_f32_16x16x32_bf16 v[148:151], v[160:163], a[48:51], v[148:151]\n"
    "s_waitcnt lgkmcnt(8)\n"
    "ds_read_b128 a[88:91], v212 offset:49152\n"
    "v_mfma_f32_16x16x32_bf16 v[148:151], v[164:167], a[52:55], v[148:151]\n"
    "s_waitcnt lgkmcnt(8)\n"
    "ds_read_b128 a[92:95], v212 offset:50176\n"
    "v_max3_f32 v228, v228, v144, v145\n"
    "v_mfma_f32_16x16x32_bf16 v[148:151], v[168:171], a[56:59], v[148:151]\n"
    "s_waitcnt lgkmcnt(8)\n"
    "ds_read_b128 a[96:99], v214 offset:2048\n"
    "v_max3_f32 v228, v228, v146, v147\n"
    "v_mfma_f32_16x16x32_bf16 v[148:151], v[172:175], a[60:63], v[148:151]\n"
    "s_waitcnt lgkmcnt(8)\n"
    "ds_read_b128 a[100:103], v214 offset:3072\n"
    "v_mfma_f32_16x16x32_bf16 v[152:155], v[128:131], a[64:67], 0\n"
    "s_waitcnt lgkmcnt(8)\n"
    "ds_read_b128 a[104:107], v212 offset:2048\n"
    "v_mfma_f32_16x16x32_bf16 v[152:155], v[132:135], a[68:71], v[152:155]\n"
    "s_waitcnt lgkmcnt(8)\n"
    "ds_read_b128 a[108:111], v212 offset:3072\n"
    "v_mfma_f32_16x16x32_bf16 v[152:155], v[136:139], a[72:75], v[152:155]\n"
    "s_waitcnt lgkmcnt(8)\n"
    "ds_read_b128 a[112:115], v212 offset:34816\n"
    "v_mfma_f32_16x16x32_bf16 v[152:155], v[140:143], a[76:79], v[152:155]\n"
    "s_waitcnt lgkmcnt(8)\n"
    "ds_read_b128 a[116:119], v212 offset:35840\n"
    "v_mfma_f32_16x16x32_bf16 v[152:155], v[160:163], a[80:83], v[152:155]\n"
    "s_waitcnt lgkmcnt(8)\n"
    "ds_read_b128 a[120:123], v212 offset:51200\n"
    "v_mfma_f32_16x16x32_bf16 v[152:155], v[164:167], a[84:87], v[152:155]\n"
    "s_waitcnt lgkmcnt(8)\n"
    "ds_read_b128 a[124:127], v212 offset:52224\n"
    "v_max3_f32 v229, v229, v148, v149\n"
    "v_mfma_f32_16x16x32_bf16 v[152:155], v[168:171], a[88:91], v[152:155]\n"
    "s_waitcnt lgkmcnt(8)\n"
    "ds_read_b128 v[176:179], v251 offset:0\n"
    "v_max3_f32 v229, v229, v150, v151\n"
    "v_mfma_f32_16x16x32_bf16 v[152:155], v[172:175], a[92:95], v[152:155]\n"
    "s_waitcnt lgkmcnt(8)\n"
    "ds_read_b128 v[180:183], v251 offset:512\n"
    "v_mfma_f32_16x16x32_bf16 v[156:159], v[128:131], a[96:99], 0\n"
    "s_waitcnt lgkmcnt(8)\n"
    "ds_read_b128 v[184:187], v251 offset:2048\n"
    "v_accvgpr_read_b32 v232, a232\n"
    "v_mfma_f32_16x16x32_bf16 v[156:159], v[132:135], a[100:103], v[156:159]\n"
    "s_waitcnt lgkmcnt(8)\n"
    "ds_read_b128 v[188:191], v251 offset:2560\n"
    "v_accvgpr_read_b32 v238, a233\n"
    "v_mfma_f32_16x16x32_bf16 v[156:159], v[136:139], a[104:107], v[156:159]\n"
    "s_waitcnt lgkmcnt(8)\n"
    "ds_read_b128 v[192:195], v251 offset:4096\n"
    "v_accvgpr_read_b32 v239, a234\n"
    "v_mfma_f32_16x16x32_bf16 v[156:159], v[140:143], a[108:111], v[156:159]\n"
    "s_waitcnt lgkmcnt(8)\n"
    "ds_read_b128 v[196:199], v251 offset:4608\n"
    "v_accvgpr_read_b32 v240, a235\n"
    "v_mfma_f32_16x16x32_bf16 v[156:159], v[160:163], a[112:115], v[156:159]\n"
    "s_waitcnt lgkmcnt(8)\n"
    "ds_read_b128 v[200:203], v251 offset:6144\n"
    "v_accvgpr_read_b32 v246, a236\n"
    "v_mfma_f32_16x16x32_bf16 v[156:159], v[164:167], a[116:119], v[156:159]\n"
    "s_waitcnt lgkmcnt(8)\n"
    "ds_read_b128 v[204:207], v251 offset:6656\n"
    "v_max3_f32 v230, v230, v152, v153\n"
    "v_mfma_f32_16x16x32_bf16 v[156:159], v[168:171], a[120:123], v[156:159]\n"
    "s_waitcnt lgkmcnt(8)\n"
    "v_max3_f32 v230, v230, v154, v155\n"
    "v_xor_b32 v233, 32, v232\n"
    "v_mfma_f32_16x16x32_bf16 v[156:159], v[172:175], a[124:127], v[156:159]\n"
    "s_waitcnt lgkmcnt(4)\n"
    "v_add_u32 v234, 0x10000, v232\n"
    "v_add_u32 v235, 0x10000, v233\n"
    "v_mfma_f32_16x16x32_bf16 v[208:211], v[176:179], a[0:3], 0\n"
    "s_waitcnt vmcnt(8)\n"
    "v_add_u32 v236, 0x20000, v232\n"
    "v_add_u32 v237, 0x20000, v233\n"
    "v_mfma_f32_16x16x32_bf16 v[208:211], v[180:183], a[4:7], v[208:211]\n"
    "s_barrier\n"
    "v_mov_b32 v241, 0x3f80\n"
    "v_xor_b32 v242, 0x4000, v240\n"
    "v_mfma_f32_16x16x32_bf16 v[208:211], v[184:187], a[8:11], v[208:211]\n"
    "s_mov_b32 s44, 40960\n"
    "v_accvgpr_read_b32 v248, a250\n"
    "v_accvgpr_read_b32 v249, a251\n"
    "v_mfma_f32_16x16x32_bf16 v[208:211], v[188:191], a[12:15], v[208:211]\n"
    "s_waitcnt lgkmcnt(0)\n"
    "s_add_u32 m0, s47, 32768\n"
    "v_accvgpr_read_b32 v244, a252\n"
    "v_mov_b32 v245, 0\n"
    "v_mfma_f32_16x16x32_bf16 v[208:211], v[192:195], a[16:19], v[208:211]\n"
    "buffer_load_dwordx4 v254, s[36:39], s44 offen offset:0 nt lds\n"
    "v_mfma_f32_16x16x32_bf16 v[208:211], v[196:199], a[20:23], v[208:211]\n"
    "s_add_u32 m0, s47, 34688\n"
    "v_max3_f32 v231, v231, v156, v157\n"
    "v_max3_f32 v231, v231, v158, v159\n"
    "v_mfma_f32_16x16x32_bf16 v[208:211], v[200:203], a[24:27], v[208:211]\n"
    "buffer_load_dwordx4 v254, s[36:39], s44 offen offset:128 nt lds\n"
    "v_mfma_f32_16x16x32_bf16 v[208:211], v[204:207], a[28:31], v[208:211]\n"
    "s_add_u32 m0, s47, 36608\n"
    "v_mfma_f32_16x16x32_bf16 v[212:215], v[176:179], a[32:35], 0\n"
    "buffer_load_dwordx4 v254, s[36:39], s44 offen offset:256 nt lds\n"
    "v_mfma_f32_16x16x32_bf16 v[212:215], v[180:183], a[36:39], v[212:215]\n"
    "s_add_u32 m0, s47, 38528\n"
    "v_mfma_f32_16x16x32_bf16 v[212:215], v[184:187], a[40:43], v[212:215]\n"
    "buffer_load_dwordx4 v254, s[36:39], s44 offen offset:384 nt lds\n"
    "v_mfma_f32_16x16x32_bf16 v[212:215], v[188:191], a[44:47], v[212:215]\n"
    "s_mov_b32 s44, 65536\n"
    "s_add_u32 m0, s47, 49152\n"
    "v_mfma_f32_16x16x32_bf16 v[212:215], v[192:195], a[48:51], v[212:215]\n"
    "buffer_load_dwordx4 v254, s[36:39], s44 offen offset:0 nt lds\n"
    "v_mfma_f32_16x16x32_bf16 v[212:215], v[196:199], a[52:55], v[212:215]\n"
    "s_add_u32 m0, s47, 51072\n"
    "v_max3_f32 v228, v228, v208, v209\n"
    "v_max3_f32 v228, v228, v210, v211\n"
    "v_mfma_f32_16x16x32_bf16 v[212:215], v[200:203], a[56:59], v[212:215]\n"
    "buffer_load_dwordx4 v254, s[36:39], s44 offen offset:128 nt lds\n"
    "v_mfma_f32_16x16x32_bf16 v[212:215], v[204:207], a[60:63], v[212:215]\n"
    "s_add_u32 m0, s47, 52992\n"
    "v_mfma_f32_16x16x32_bf16 v[216:219], v[176:179], a[64:67], 0\n"
    "buffer_load_dwordx4 v254, s[36:39], s44 offen offset:256 nt lds\n"
    "v_mfma_f32_16x16x32_bf16 v[216:219], v[180:183], a[68:71], v[216:219]\n"
    "s_add_u32 m0, s47, 54912\n"
    "v_mfma_f32_16x16x32_bf16 v[216:219], v[184:187], a[72:75], v[216:219]\n"
    "buffer_load_dwordx4 v254, s[36:39], s44 offen offset:384 nt lds\n"
    "v_mfma_f32_16x16x32_bf16 v[216:219], v[188:191], a[76:79], v[216:219]\n"
    "v_mfma_f32_16x16x32_bf16 v[216:219], v[192:195], a[80:83], v[216:219]\n"
    "v_mfma_f32_16x16x32_bf16 v[216:219], v[196:199], a[84:87], v[216:219]\n"
    "v_max3_f32 v229, v229, v212, v213\n"
    "v_max3_f32 v229, v229, v214, v215\n"
    "v_mfma_f32_16x16x32_bf16 v[216:219], v[200:203], a[88:91], v[216:219]\n"
    "v_mfma_f32_16x16x32_bf16 v[216:219], v[204:207], a[92:95], v[216:219]\n"
    "v_mfma_f32_16x16x32_bf16 v[220:223], v[176:179], a[96:99], 0\n"
    "ds_read_b128 v[176:179], v251 offset:16384\n"
    "v_mfma_f32_16x16x32_bf16 v[220:223], v[180:183], a[100:103], v[220:223]\n"
    "ds_read_b128 v[180:183], v251 offset:16896\n"
    "v_mfma_f32_16x16x32_bf16 v[220:223], v[184:187], a[104:107], v[220:223]\n"
    "ds_read_b128 v[184:187], v251 offset:18432\n"
    "v_mfma_f32_16x16x32_bf16 v[220:223], v[188:191], a[108:111], v[220:223]\n"
    "ds_read_b128 v[188:191], v251 offset:18944\n"
    "v_mfma_f32_16x16x32_bf16 v[220:223], v[192:195], a[112:115], v[220:223]\n"
    "ds_read_b128 v[192:195], v251 offset:20480\n"
    "v_mfma_f32_16x16x32_bf16 v[220:223], v[196:199], a[116:119], v[220:223]\n"
    "ds_read_b128 v[196:199], v251 offset:20992\n"
    "v_max3_f32 v230, v230, v216, v217\n"
    "v_mfma_f32_16x16x32_bf16 v[220:223], v[200:203], a[120:123], v[220:223]\n"
    "ds_read_b128 v[200:203], v251 offset:22528\n"
    "v_max3_f32 v230, v230, v218, v219\n"
    "v_mfma_f32_16x16x32_bf16 v[220:223], v[204:207], a[124:127], v[220:223]\n"
    "ds_read_b128 v[204:207], v251 offset:23040\n"
    "s_mov_b32 s45, 32768\n"
    "s_add_u32 m0, s48, 0\n"
    "s_nop 0\n"
    "buffer_load_dwordx4 v255, s[40:43], s45 offen offset:0 nt lds\n"
    "s_add_u32 m0, s48, 896\n"
    "s_nop 0\n"
    "buffer_load_dwordx4 v255, s[40:43], s45 offen offset:128 nt lds\n"
    "v_max3_f32 v231, v231, v220, v221\n"
    "v_max3_f32 v231, v231, v222, v223\n"
    "s_nop 1\n"
    "v_permlane32_swap_b32 v228, v230\n"
    "v_permlane32_swap_b32 v229, v231\n"
    "v_max_f32 v228, v228, v230\n"
    "v_max_f32 v229, v229, v231\n"
    "s_nop 1\n"
    "v_permlane16_swap_b32 v228, v229\n"
    "v_max_f32 v228, v228, v229\n"
    "v_lshrrev_b32 v128, 4, v250\n"
    "v_lshl_add_u32 v128, v128, 2, v246\n"
    "ds_write_b32 v128, v228\n"
    "v_xor_b32 v129, 0x100, v246\n"
    "s_mov_b32 s46, 36864\n"
    "s_add_u32 m0, s48, 2048\n"
    "s_nop 0\n"
    "buffer_load_dwordx4 v255, s[40:43], s46 offen offset:0 nt lds\n"
    "s_waitcnt vmcnt(15) lgkmcnt(0)\n"
    "s_barrier\n"
    "ds_read_b128 v[228:231], v246\n"
    "ds_read_b128 v[132:135], v129\n"
    "s_add_u32 m0, s48, 2944\n"
    "s_nop 0\n"
    "buffer_load_dwordx4 v255, s[40:43], s46 offen offset:128 nt lds\n"
    "s_waitcnt lgkmcnt(0)\n"
    "v_max_f32 v160, v228, v134\n"
    "v_sub_f32 v161, 0xc2cc0000, v160\n"
    "v_sub_f32 v160, 0xc2cc0000, v160\n"
    "v_mov_b64 v[162:163], v[160:161]\n"
    "v_max_f32 v164, v229, v135\n"
    "v_sub_f32 v165, 0xc2cc0000, v164\n"
    "v_sub_f32 v164, 0xc2cc0000, v164\n"
    "v_mov_b64 v[166:167], v[164:165]\n"
    "v_max_f32 v168, v230, v132\n"
    "v_sub_f32 v169, 0xc2cc0000, v168\n"
    "v_sub_f32 v168, 0xc2cc0000, v168\n"
    "v_mov_b64 v[170:171], v[168:169]\n"
    "v_max_f32 v172, v231, v133\n"
    "v_sub_f32 v173, 0xc2cc0000, v172\n"
    "v_sub_f32 v172, 0xc2cc0000, v172\n"
    "v_mov_b64 v[174:175], v[172:173]\n"
    "v_mov_b32 v228, 0xc2cc0000\n"
    "v_mov_b32 v229, 0xc2cc0000\n"
    "v_mov_b32 v230, 0xc2cc0000\n"
    "v_mov_b32 v231, 0xc2cc0000\n"
    "v_pk_add_f32 v[144:145], v[144:145], v[160:161]\n"
    "v_pk_add_f32 v[146:147], v[146:147], v[160:161]\n"
    "v_pk_add_f32 v[208:209], v[208:209], v[160:161]\n"
    "v_pk_add_f32 v[210:211], v[210:211], v[160:161]\n"
    "v_exp_f32 v144, v144\n"
    "v_exp_f32 v145, v145\n"
    "v_exp_f32 v146, v146\n"
    "v_exp_f32 v147, v147\n"
    "v_exp_f32 v208, v208\n"
    "v_exp_f32 v209, v209\n"
    "v_exp_f32 v210, v210\n"
    "v_exp_f32 v211, v211\n"
    "v_cvt_pk_bf16_f32 v128, v144, v145\n"
    "v_cvt_pk_bf16_f32 v129, v146, v147\n"
    "v_cvt_pk_bf16_f32 v130, v208, v209\n"
    "v_cvt_pk_bf16_f32 v131, v210, v211\n"
    "v_pk_add_f32 v[144:145], v[144:145], v[146:147]\n"
    "v_pk_add_f32 v[144:145], v[144:145], v[208:209]\n"
    "v_pk_add_f32 v[144:145], v[144:145], v[210:211]\n"
    "v_add_f32 v224, v144, v145\n"
    "v_pk_add_f32 v[148:149], v[148:149], v[164:165]\n"
    "v_pk_add_f32 v[150:151], v[150:151], v[164:165]\n"
    "v_pk_add_f32 v[212:213], v[212:213], v[164:165]\n"
    "v_pk_add_f32 v[214:215], v[214:215], v[164:165]\n"
    "v_exp_f32 v148, v148\n"
    "v_exp_f32 v149, v149\n"
    "v_exp_f32 v150, v150\n"
    "v_exp_f32 v151, v151\n"
    "v_exp_f32 v212, v212\n"
    "v_exp_f32 v213, v213\n"
    "v_exp_f32 v214, v214\n"
    "v_exp_f32 v215, v215\n"
    "v_cvt_pk_bf16_f32 v132, v148, v149\n"
    "v_cvt_pk_bf16_f32 v133, v150, v151\n"
    "v_cvt_pk_bf16_f32 v134, v212, v213\n"
    "v_cvt_pk_bf16_f32 v135, v214, v215\n"
    "v_pk_add_f32 v[148:149], v[148:149], v[150:151]\n"
    "v_pk_add_f32 v[148:149], v[148:149], v[212:213]\n"
    "v_pk_add_f32 v[148:149], v[148:149], v[214:215]\n"
    "v_add_f32 v225, v148, v149\n"
    "v_pk_add_f32 v[152:153], v[152:153], v[168:169]\n"
    "v_pk_add_f32 v[154:155], v[154:155], v[168:169]\n"
    "v_pk_add_f32 v[216:217], v[216:217], v[168:169]\n"
    "v_pk_add_f32 v[218:219], v[218:219], v[168:169]\n"
    "v_exp_f32 v152, v152\n"
    "v_exp_f32 v153, v153\n"
    "v_exp_f32 v154, v154\n"
    "v_exp_f32 v155, v155\n"
    "v_exp_f32 v216, v216\n"
    "v_exp_f32 v217, v217\n"
    "v_exp_f32 v218, v218\n"
    "v_exp_f32 v219, v219\n"
    "v_cvt_pk_bf16_f32 v136, v152, v153\n"
    "v_cvt_pk_bf16_f32 v137, v154, v155\n"
    "v_cvt_pk_bf16_f32 v138, v216, v217\n"
    "v_cvt_pk_bf16_f32 v139, v218, v219\n"
    "v_pk_add_f32 v[152:153], v[152:153], v[154:155]\n"
    "v_pk_add_f32 v[152:153], v[152:153], v[216:217]\n"
    "v_pk_add_f32 v[152:153], v[152:153], v[218:219]\n"
    "v_add_f32 v226, v152, v153\n"
    "ds_read_b64_tr_b16 v[208:209], v252 offset:32768\n"
    "ds_read_b64_tr_b16 v[210:211], v252 offset:36864\n"
    "ds_read_b64_tr_b16 v[212:213], v253 offset:32768\n"
    "ds_read_b64_tr_b16 v[214:215], v253 offset:36864\n"
    "v_pk_add_f32 v[156:157], v[156:157], v[172:173]\n"
    "v_pk_add_f32 v[158:159], v[158:159], v[172:173]\n"
    "v_pk_add_f32 v[220:221], v[220:221], v[172:173]\n"
    "v_pk_add_f32 v[222:223], v[222:223], v[172:173]\n"
    "v_exp_f32 v156, v156\n"
    "v_exp_f32 v157, v157\n"
    "v_exp_f32 v158, v158\n"
    "v_exp_f32 v159, v159\n"
    "v_exp_f32 v220, v220\n"
    "v_exp_f32 v221, v221\n"
    "v_cvt_pk_bf16_f32 v140, v156, v157\n"
    "v_cvt_pk_bf16_f32 v141, v158, v159\n"
    "v_pk_add_f32 v[156:157], v[156:157], v[158:159]\n"
    "v_add_f32 v227, v156, v157\n"
    "L_body1_%=:\n"
    "s_mov_b32 s44, 73728\n"
    "s_mov_b32 s45, 33024\n"
    "s_mov_b32 s46, 37120\n"
    "s_mov_b32 s49, 15\n"
    "v_mfma_f32_16x16x32_bf16 v[216:219], v[176:179], a[0:3], v[160:163]\n"
    "v_exp_f32 v222, v222\n"
    "v_mfma_f32_16x16x32_bf16 v[216:219], v[180:183], a[4:7], v[216:219]\n"
    "s_add_u32 m0, s47, 0\n"
    "v_exp_f32 v223, v223\n"
    "v_mfma_f32_16x16x32_bf16 v[216:219], v[184:187], a[8:11], v[216:219]\n"
    "buffer_load_dwordx4 v254, s[36:39], s44 offen offset:0 nt lds\n"
    "v_mfma_f32_16x16x32_bf16 v[216:219], v[188:191], a[12:15], v[216:219]\n"
    "s_waitcnt lgkmcnt(4)\n"
    "v_add_f32 v227, v227, v220\n"
    "v_add_f32 v227, v227, v221\n"
    "v_mfma_f32_16x16x32_bf16 v[216:219], v[192:195], a[16:19], v[216:219]\n"
    "v_cvt_pk_bf16_f32 v142, v220, v221\n"
    "v_add_f32 v227, v227, v222\n"
    "v_mfma_f32_16x16x32_bf16 v[216:219], v[196:199], a[20:23], v[216:219]\n"
    "s_add_u32 m0, s47, 1920\n"
    "v_add_f32 v227, v227, v223\n"
    "v_cvt_pk_bf16_f32 v143, v222, v223\n"
    "v_mfma_f32_16x16x32_bf16 v[216:219], v[200:203], a[24:27], v[216:219]\n"
    "buffer_load_dwordx4 v254, s[36:39], s44 offen offset:128 nt lds\n"
    "v_mfma_f32_16x16x32_bf16 v[216:219], v[204:207], a[28:31], v[216:219]\n"
    "s_waitcnt lgkmcnt(0)\n"
    "v_mfma_f32_16x16x32_bf16 a[136:139], v[212:215], v[128:131], 0\n"
    "v_mfma_f32_16x16x32_bf16 a[128:131], v[208:211], v[128:131], 0\n"
    "s_add_u32 m0, s47, 3840\n"
    "v_mfma_f32_16x16x32_bf16 a[132:135], v[208:211], v[132:135], 0\n"
    "buffer_load_dwordx4 v254, s[36:39], s44 offen offset:256 nt lds\n"
    "v_mfma_f32_16x16x32_bf16 a[140:143], v[212:215], v[132:135], 0\n"
    "v_mfma_f32_16x16x32_bf16 v[8:11], v[212:215], v[136:139], 0\n"
    "v_exp_f32 v216, v216\n"
    "v_mfma_f32_16x16x32_bf16 v[0:3], v[208:211], v[136:139], 0\n"
    "v_exp_f32 v217, v217\n"
    "v_mfma_f32_16x16x32_bf16 v[4:7], v[208:211], v[140:143], 0\n"
    "s_nop 0\n"
    "ds_read_b64_tr_b16 v[208:209], v252 offset:33280\n"
    "ds_read_b64_tr_b16 v[210:211], v252 offset:37376\n"
    "v_mfma_f32_16x16x32_bf16 v[12:15], v[212:215], v[140:143], 0\n"
    "s_nop 0\n"
    "ds_read_b64_tr_b16 v[212:213], v253 offset:33280\n"
    "ds_read_b64_tr_b16 v[214:215], v253 offset:37376\n"
    "v_mfma_f32_16x16x32_bf16 v[220:223], v[176:179], a[32:35], v[164:167]\n"
    "v_exp_f32 v218, v218\n"
    "v_mfma_f32_16x16x32_bf16 v[220:223], v[180:183], a[36:39], v[220:223]\n"
    "s_add_u32 m0, s47, 5760\n"
    "v_exp_f32 v219, v219\n"
    "v_mfma_f32_16x16x32_bf16 v[220:223], v[184:187], a[40:43], v[220:223]\n"
    "buffer_load_dwordx4 v254, s[36:39], s44 offen offset:384 nt lds\n"
    "v_mfma_f32_16x16x32_bf16 v[220:223], v[188:191], a[44:47], v[220:223]\n"
    "v_add_f32 v224, v224, v216\n"
    "v_add_f32 v224, v224, v217\n"
    "v_mfma_f32_16x16x32_bf16 v[220:223], v[192:195], a[48:51], v[220:223]\n"
    "v_cvt_pk_bf16_f32 v144, v216, v217\n"
    "v_add_f32 v224, v224, v218\n"
    "v_mfma_f32_16x16x32_bf16 v[220:223], v[196:199], a[52:55], v[220:223]\n"
    "v_add_f32 v224, v224, v219\n"
    "v_cvt_pk_bf16_f32 v145, v218, v219\n"
    "v_mfma_f32_16x16x32_bf16 v[220:223], v[200:203], a[56:59], v[220:223]\n"
    "global_load_dword v247, v[248:249], off sc1\n"
    "v_mfma_f32_16x16x32_bf16 v[220:223], v[204:207], a[60:63], v[220:223]\n"
    "s_waitcnt lgkmcnt(0)\n"
    "v_mfma_f32_16x16x32_bf16 a[152:155], v[212:215], v[128:131], 0\n"
    "v_lshl_add_u64 v[248:249], v[248:249], 0, v[244:245]\n"
    "v_mfma_f32_16x16x32_bf16 a[144:147], v[208:211], v[128:131], 0\n"
    "s_add_u32 m0, s48, 16384\n"
    "v_mfma_f32_16x16x32_bf16 a[148:151], v[208:211], v[132:135], 0\n"
    "buffer_load_dwordx4 v255, s[40:43], s45 offen offset:0 nt lds\n"
    "v_mfma_f32_16x16x32_bf16 a[156:159], v[212:215], v[132:135], 0\n"
    "v_mfma_f32_16x16x32_bf16 v[24:27], v[212:215], v[136:139], 0\n"
    "v_exp_f32 v220, v220\n"
    "v_mfma_f32_16x16x32_bf16 v[16:19], v[208:211], v[136:139], 0\n"
    "v_exp_f32 v221, v221\n"
    "v_mfma_f32_16x16x32_bf16 v[20:23], v[208:211], v[140:143], 0\n"
    "s_nop 0\n"
    "ds_read_b64_tr_b16 v[208:209], v252 offset:33792\n"
    "ds_read_b64_tr_b16 v[210:211], v252 offset:37888\n"
    "v_mfma_f32_16x16x32_bf16 v[28:31], v[212:215], v[140:143], 0\n"
    "s_nop 0\n"
    "ds_read_b64_tr_b16 v[212:213], v253 offset:33792\n"
    "ds_read_b64_tr_b16 v[214:215], v253 offset:37888\n"
    "v_mfma_f32_16x16x32_bf16 v[216:219], v[176:179], a[64:67], v[168:171]\n"
    "v_exp_f32 v222, v222\n"
    "v_mfma_f32_16x16x32_bf16 v[216:219], v[180:183], a[68:71], v[216:219]\n"
    "s_add_u32 m0, s48, 17280\n"
    "v_exp_f32 v223, v223\n"
    "v_mfma_f32_16x16x32_bf16 v[216:219], v[184:187], a[72:75], v[216:219]\n"
    "buffer_load_dwordx4 v255, s[40:43], s45 offen offset:128 nt lds\n"
    "v_mfma_f32_16x16x32_bf16 v[216:219], v[188:191], a[76:79], v[216:219]\n"
    "v_add_f32 v225, v225, v220\n"
    "v_add_f32 v225, v225, v221\n"
    "v_mfma_f32_16x16x32_bf16 v[216:219], v[192:195], a[80:83], v[216:219]\n"
    "v_cvt_pk_bf16_f32 v148, v220, v221\n"
    "v_add_f32 v225, v225, v222\n"
    "v_mfma_f32_16x16x32_bf16 v[216:219], v[196:199], a[84:87], v[216:219]\n"
    "s_add_u32 m0, s48, 18432\n"
    "v_add_f32 v225, v225, v223\n"
    "v_cvt_pk_bf16_f32 v149, v222, v223\n"
    "v_mfma_f32_16x16x32_bf16 v[216:219], v[200:203], a[88:91], v[216:219]\n"
    "buffer_load_dwordx4 v255, s[40:43], s46 offen offset:0 nt lds\n"
    "v_mfma_f32_16x16x32_bf16 v[216:219], v[204:207], a[92:95], v[216:219]\n"
    "s_waitcnt lgkmcnt(0)\n"
    "v_mfma_f32_16x16x32_bf16 a[168:171], v[212:215], v[128:131], 0\n"
    "s_mov_b64 s[60:61], s[16:17]\n"
    "s_and_b32 s61, s61, 0xffff\n"
    "s_mov_b32 s62, 3670016\n"
    "v_mfma_f32_16x16x32_bf16 a[160:163], v[208:211], v[128:131], 0\n"
    "s_add_u32 m0, s48, 19328\n"
    "v_mfma_f32_16x16x32_bf16 a[164:167], v[208:211], v[132:135], 0\n"
    "buffer_load_dwordx4 v255, s[40:43], s46 offen offset:128 nt lds\n"
    "v_mfma_f32_16x16x32_bf16 a[172:175], v[212:215], v[132:135], 0\n"
    "v_mfma_f32_16x16x32_bf16 v[40:43], v[212:215], v[136:139], 0\n"
    "v_exp_f32 v216, v216\n"
    "v_mfma_f32_16x16x32_bf16 v[32:35], v[208:211], v[136:139], 0\n"
    "v_exp_f32 v217, v217\n"
    "v_mfma_f32_16x16x32_bf16 v[36:39], v[208:211], v[140:143], 0\n"
    "s_nop 0\n"
    "ds_read_b64_tr_b16 v[208:209], v252 offset:34304\n"
    "ds_read_b64_tr_b16 v[210:211], v252 offset:38400\n"
    "v_mfma_f32_16x16x32_bf16 v[44:47], v[212:215], v[140:143], 0\n"
    "s_nop 0\n"
    "ds_read_b64_tr_b16 v[212:213], v253 offset:34304\n"
    "ds_read_b64_tr_b16 v[214:215], v253 offset:38400\n"
    "v_mfma_f32_16x16x32_bf16 v[220:223], v[176:179], a[96:99], v[172:175]\n"
    "s_waitcnt vmcnt(17)\n"
    "v_exp_f32 v218, v218\n"
    "v_mfma_f32_16x16x32_bf16 v[220:223], v[180:183], a[100:103], v[220:223]\n"
    "s_barrier\n"
    "v_exp_f32 v219, v219\n"
    "v_mfma_f32_16x16x32_bf16 v[220:223], v[184:187], a[104:107], v[220:223]\n"
    "s_add_u32 s44, s44, 24576\n"
    "ds_read_b128 v[176:179], v251 offset:32768\n"
    "v_add_f32 v226, v226, v216\n"
    "v_mfma_f32_16x16x32_bf16 v[220:223], v[188:191], a[108:111], v[220:223]\n"
    "s_add_u32 s45, s45, 32512\n"
    "ds_read_b128 v[180:183], v251 offset:33280\n"
    "v_add_f32 v226, v226, v217\n"
    "v_mfma_f32_16x16x32_bf16 v[220:223], v[192:195], a[112:115], v[220:223]\n"
    "s_add_u32 s46, s46, 32512\n"
    "ds_read_b128 v[184:187], v251 offset:34816\n"
    "v_cvt_pk_bf16_f32 v152, v216, v217\n"
    "v_mfma_f32_16x16x32_bf16 v[220:223], v[196:199], a[116:119], v[220:223]\n"
    "ds_read_b128 v[188:191], v251 offset:35328\n"
    "v_add_f32 v226, v226, v218\n"
    "v_mfma_f32_16x16x32_bf16 v[220:223], v[200:203], a[120:123], v[220:223]\n"
    "ds_read_b128 v[192:195], v251 offset:36864\n"
    "v_add_f32 v226, v226, v219\n"
    "v_mfma_f32_16x16x32_bf16 v[220:223], v[204:207], a[124:127], v[220:223]\n"
    "s_waitcnt lgkmcnt(5)\n"
    "ds_read_b128 v[196:199], v251 offset:37376\n"
    "v_cvt_pk_bf16_f32 v153, v218, v219\n"
    "v_mfma_f32_16x16x32_bf16 a[184:187], v[212:215], v[128:131], 0\n"
    "ds_read_b128 v[200:203], v251 offset:38912\n"
    "v_mfma_f32_16x16x32_bf16 a[176:179], v[208:211], v[128:131], 0\n"
    "ds_read_b128 v[204:207], v251 offset:39424\n"
    "v_mfma_f32_16x16x32_bf16 a[180:183], v[208:211], v[132:135], 0\n"
    "s_waitcnt vmcnt(13) lgkmcnt(8)\n"
    "s_barrier\n"
    "v_mfma_f32_16x16x32_bf16 a[188:191], v[212:215], v[132:135], 0\n"
    "v_mfma_f32_16x16x32_bf16 v[56:59], v[212:215], v[136:139], 0\n"
    "v_exp_f32 v220, v220\n"
    "v_mfma_f32_16x16x32_bf16 v[48:51], v[208:211], v[136:139], 0\n"
    "v_exp_f32 v221, v221\n"
    "v_mfma_f32_16x16x32_bf16 v[52:55], v[208:211], v[140:143], 0\n"
    "s_nop 0\n"
    "ds_read_b64_tr_b16 v[208:209], v252 offset:49152\n"
    "ds_read_b64_tr_b16 v[210:211], v252 offset:53248\n"
    "v_mfma_f32_16x16x32_bf16 v[60:63], v[212:215], v[140:143], 0\n"
    "s_waitcnt lgkmcnt(6)\n"
    "ds_read_b64_tr_b16 v[212:213], v253 offset:49152\n"
    "ds_read_b64_tr_b16 v[214:215], v253 offset:53248\n"
    "v_mfma_f32_16x16x32_bf16 v[216:219], v[176:179], a[0:3], v[160:163]\n"
    "v_exp_f32 v222, v222\n"
    "v_mfma_f32_16x16x32_bf16 v[216:219], v[180:183], a[4:7], v[216:219]\n"
    "s_add_u32 m0, s47, 16384\n"
    "v_exp_f32 v223, v223\n"
    "v_mfma_f32_16x16x32_bf16 v[216:219], v[184:187], a[8:11], v[216:219]\n"
    "buffer_load_dwordx4 v254, s[36:39], s44 offen offset:0 nt lds\n"
    "v_mfma_f32_16x16x32_bf16 v[216:219], v[188:191], a[12:15], v[216:219]\n"
    "s_waitcnt lgkmcnt(4)\n"
    "v_add_f32 v227, v227, v220\n"
    "v_add_f32 v227, v227, v221\n"
    "v_mfma_f32_16x16x32_bf16 v[216:219], v[192:195], a[16:19], v[216:219]\n"
    "v_cvt_pk_bf16_f32 v156, v220, v221\n"
    "v_add_f32 v227, v227, v222\n"
    "v_mfma_f32_16x16x32_bf16 v[216:219], v[196:199], a[20:23], v[216:219]\n"
    "s_add_u32 m0, s47, 18304\n"
    "v_add_f32 v227, v227, v223\n"
    "v_cvt_pk_bf16_f32 v157, v222, v223\n"
    "v_mfma_f32_16x16x32_bf16 v[216:219], v[200:203], a[24:27], v[216:219]\n"
    "buffer_load_dwordx4 v254, s[36:39], s44 offen offset:128 nt lds\n"
    "v_mfma_f32_16x16x32_bf16 v[216:219], v[204:207], a[28:31], v[216:219]\n"
    "s_waitcnt lgkmcnt(0)\n"
    "v_mfma_f32_16x16x32_bf16 a[200:203], v[212:215], v[128:131], 0\n"
    "v_mfma_f32_16x16x32_bf16 a[192:195], v[208:211], v[128:131], 0\n"
    "s_add_u32 m0, s47, 20224\n"
    "v_mfma_f32_16x16x32_bf16 a[196:199], v[208:211], v[132:135], 0\n"
    "buffer_load_dwordx4 v254, s[36:39], s44 offen offset:256 nt lds\n"
    "v_mfma_f32_16x16x32_bf16 a[204:207], v[212:215], v[132:135], 0\n"
    "v_mfma_f32_16x16x32_bf16 v[72:75], v[212:215], v[136:139], 0\n"
    "v_exp_f32 v216, v216\n"
    "v_mfma_f32_16x16x32_bf16 v[64:67], v[208:211], v[136:139], 0\n"
    "v_exp_f32 v217, v217\n"
    "v_mfma_f32_16x16x32_bf16 v[68:71], v[208:211], v[140:143], 0\n"
    "s_nop 0\n"
    "ds_read_b64_tr_b16 v[208:209], v252 offset:49664\n"
    "ds_read_b64_tr_b16 v[210:211], v252 offset:53760\n"
    "v_mfma_f32_16x16x32_bf16 v[76:79], v[212:215], v[140:143], 0\n"
    "s_nop 0\n"
    "ds_read_b64_tr_b16 v[212:213], v253 offset:49664\n"
    "ds_read_b64_tr_b16 v[214:215], v253 offset:53760\n"
    "v_mfma_f32_16x16x32_bf16 v[220:223], v[176:179], a[32:35], v[164:167]\n"
    "v_exp_f32 v218, v218\n"
    "v_mfma_f32_16x16x32_bf16 v[220:223], v[180:183], a[36:39], v[220:223]\n"
    "s_add_u32 m0, s47, 22144\n"
    "v_exp_f32 v219, v219\n"
    "v_mfma_f32_16x16x32_bf16 v[220:223], v[184:187], a[40:43], v[220:223]\n"
    "buffer_load_dwordx4 v254, s[36:39], s44 offen offset:384 nt lds\n"
    "v_mfma_f32_16x16x32_bf16 v[220:223], v[188:191], a[44:47], v[220:223]\n"
    "v_add_f32 v224, v224, v216\n"
    "v_add_f32 v224, v224, v217\n"
    "v_mfma_f32_16x16x32_bf16 v[220:223], v[192:195], a[48:51], v[220:223]\n"
    "v_cvt_pk_bf16_f32 v146, v216, v217\n"
    "v_add_f32 v224, v224, v218\n"
    "v_mfma_f32_16x16x32_bf16 v[220:223], v[196:199], a[52:55], v[220:223]\n"
    "v_add_f32 v224, v224, v219\n"
    "v_cvt_pk_bf16_f32 v147, v218, v219\n"
    "v_mfma_f32_16x16x32_bf16 v[220:223], v[200:203], a[56:59], v[220:223]\n"
    "s_lshl_b32 s54, %1, 3\n"
    "s_add_u32 s54, s54, 49152\n"
    "s_lshl_b32 s55, s50, 29\n"
    "v_mfma_f32_16x16x32_bf16 v[220:223], v[204:207], a[60:63], v[220:223]\n"
    "s_waitcnt lgkmcnt(0)\n"
    "s_add_u32 s54, s54, s55\n"
    "v_mov_b32 v229, s53\n"
    "v_mov_b32 v228, s5\n"
    "v_mfma_f32_16x16x32_bf16 a[216:219], v[212:215], v[128:131], 0\n"
    "s_mov_b64 exec, 1\n"
    "buffer_store_dwordx2 v[228:229], off, s[64:67], s54 sc1\n"
    "s_mov_b64 exec, -1\n"
    "v_mfma_f32_16x16x32_bf16 a[208:211], v[208:211], v[128:131], 0\n"
    "s_add_u32 m0, s48, 32768\n"
    "v_mfma_f32_16x16x32_bf16 a[212:215], v[208:211], v[132:135], 0\n"
    "buffer_load_dwordx4 v255, s[40:43], s45 offen offset:0 nt lds\n"
    "v_mfma_f32_16x16x32_bf16 a[220:223], v[212:215], v[132:135], 0\n"
    "v_mfma_f32_16x16x32_bf16 v[88:91], v[212:215], v[136:139], 0\n"
    "v_exp_f32 v220, v220\n"
    "v_mfma_f32_16x16x32_bf16 v[80:83], v[208:211], v[136:139], 0\n"
    "v_exp_f32 v221, v221\n"
    "v_mfma_f32_16x16x32_bf16 v[84:87], v[208:211], v[140:143], 0\n"
    "s_nop 0\n"
    "ds_read_b64_tr_b16 v[208:209], v252 offset:50176\n"
    "ds_read_b64_tr_b16 v[210:211], v252 offset:54272\n"
    "v_mfma_f32_16x16x32_bf16 v[92:95], v[212:215], v[140:143], 0\n"
    "s_nop 0\n"
    "ds_read_b64_tr_b16 v[212:213], v253 offset:50176\n"
    "ds_read_b64_tr_b16 v[214:215], v253 offset:54272\n"
    "v_mfma_f32_16x16x32_bf16 v[216:219], v[176:179], a[64:67], v[168:171]\n"
    "v_exp_f32 v222, v222\n"
    "v_mfma_f32_16x16x32_bf16 v[216:219], v[180:183], a[68:71], v[216:219]\n"
    "s_add_u32 m0, s48, 33664\n"
    "v_exp_f32 v223, v223\n"
    "v_mfma_f32_16x16x32_bf16 v[216:219], v[184:187], a[72:75], v[216:219]\n"
    "buffer_load_dwordx4 v255, s[40:43], s45 offen offset:128 nt lds\n"
    "v_mfma_f32_16x16x32_bf16 v[216:219], v[188:191], a[76:79], v[216:219]\n"
    "v_add_f32 v225, v225, v220\n"
    "v_add_f32 v225, v225, v221\n"
    "v_mfma_f32_16x16x32_bf16 v[216:219], v[192:195], a[80:83], v[216:219]\n"
    "v_cvt_pk_bf16_f32 v150, v220, v221\n"
    "v_add_f32 v225, v225, v222\n"
    "v_mfma_f32_16x16x32_bf16 v[216:219], v[196:199], a[84:87], v[216:219]\n"
    "s_add_u32 m0, s48, 34816\n"
    "v_add_f32 v225, v225, v223\n"
    "v_cvt_pk_bf16_f32 v151, v222, v223\n"
    "v_mfma_f32_16x16x32_bf16 v[216:219], v[200:203], a[88:91], v[216:219]\n"
    "buffer_load_dwordx4 v255, s[40:43], s46 offen offset:0 nt lds\n"
    "v_mfma_f32_16x16x32_bf16 v[216:219], v[204:207], a[92:95], v[216:219]\n"
    "s_waitcnt lgkmcnt(0)\n"
    "v_mfma_f32_16x16x32_bf16 a[232:235], v[212:215], v[128:131], 0\n"
    "v_mfma_f32_16x16x32_bf16 a[224:227], v[208:211], v[128:131], 0\n"
    "s_add_u32 m0, s48, 35712\n"
    "v_mfma_f32_16x16x32_bf16 a[228:231], v[208:211], v[132:135], 0\n"
    "buffer_load_dwordx4 v255, s[40:43], s46 offen offset:128 nt lds\n"
    "v_mfma_f32_16x16x32_bf16 a[236:239], v[212:215], v[132:135], 0\n"
    "v_mfma_f32_16x16x32_bf16 v[104:107], v[212:215], v[136:139], 0\n"
    "v_exp_f32 v216, v216\n"
    "v_mfma_f32_16x16x32_bf16 v[96:99], v[208:211], v[136:139], 0\n"
    "v_exp_f32 v217, v217\n"
    "v_mfma_f32_16x16x32_bf16 v[100:103], v[208:211], v[140:143], 0\n"
    "s_nop 0\n"
    "ds_read_b64_tr_b16 v[208:209], v252 offset:50688\n"
    "ds_read_b64_tr_b16 v[210:211], v252 offset:54784\n"
    "v_mfma_f32_16x16x32_bf16 v[108:111], v[212:215], v[140:143], 0\n"
    "s_nop 0\n"
    "ds_read_b64_tr_b16 v[212:213], v253 offset:50688\n"
    "ds_read_b64_tr_b16 v[214:215], v253 offset:54784\n"
    "v_mfma_f32_16x16x32_bf16 v[220:223], v[176:179], a[96:99], v[172:175]\n"
    "v_exp_f32 v218, v218\n"
    "v_mfma_f32_16x16x32_bf16 v[220:223], v[180:183], a[100:103], v[220:223]\n"
    "v_exp_f32 v219, v219\n"
    "v_mfma_f32_16x16x32_bf16 v[220:223], v[184:187], a[104:107], v[220:223]\n"
    "s_add_u32 s44, s44, 8192\n"
    "ds_read_b128 v[176:179], v251 offset:49152\n"
    "v_add_f32 v226, v226, v216\n"
    "v_mfma_f32_16x16x32_bf16 v[220:223], v[188:191], a[108:111], v[220:223]\n"
    "s_add_u32 s45, s45, 256\n"
    "ds_read_b128 v[180:183], v251 offset:49664\n"
    "v_add_f32 v226, v226, v217\n"
    "v_mfma_f32_16x16x32_bf16 v[220:223], v[192:195], a[112:115], v[220:223]\n"
    "s_add_u32 s46, s46, 256\n"
    "ds_read_b128 v[184:187], v251 offset:51200\n"
    "v_cvt_pk_bf16_f32 v154, v216, v217\n"
    "v_mfma_f32_16x16x32_bf16 v[220:223], v[196:199], a[116:119], v[220:223]\n"
    "ds_read_b128 v[188:191], v251 offset:51712\n"
    "v_add_f32 v226, v226, v218\n"
    "v_mfma_f32_16x16x32_bf16 v[220:223], v[200:203], a[120:123], v[220:223]\n"
    "ds_read_b128 v[192:195], v251 offset:53248\n"
    "v_add_f32 v226, v226, v219\n"
    "v_mfma_f32_16x16x32_bf16 v[220:223], v[204:207], a[124:127], v[220:223]\n"
    "s_waitcnt lgkmcnt(5)\n"
    "ds_read_b128 v[196:199], v251 offset:53760\n"
    "v_cvt_pk_bf16_f32 v155, v218, v219\n"
    "v_mfma_f32_16x16x32_bf16 a[248:251], v[212:215], v[128:131], 0\n"
    "ds_read_b128 v[200:203], v251 offset:55296\n"
    "v_mfma_f32_16x16x32_bf16 a[240:243], v[208:211], v[128:131], 0\n"
    "ds_read_b128 v[204:207], v251 offset:55808\n"
    "v_mfma_f32_16x16x32_bf16 a[244:247], v[208:211], v[132:135], 0\n"
    "s_waitcnt vmcnt(14) lgkmcnt(8)\n"
    "s_barrier\n"
    "v_mfma_f32_16x16x32_bf16 a[252:255], v[212:215], v[132:135], 0\n"
    "v_mfma_f32_16x16x32_bf16 v[120:123], v[212:215], v[136:139], 0\n"
    "v_exp_f32 v220, v220\n"
    "v_mfma_f32_16x16x32_bf16 v[112:115], v[208:211], v[136:139], 0\n"
    "v_exp_f32 v221, v221\n"
    "v_mfma_f32_16x16x32_bf16 v[116:119], v[208:211], v[140:143], 0\n"
    "s_nop 0\n"
    "ds_read_b64_tr_b16 v[208:209], v252 offset:0\n"
    "ds_read_b64_tr_b16 v[210:211], v252 offset:4096\n"
    "v_mfma_f32_16x16x32_bf16 v[124:127], v[212:215], v[140:143], 0\n"
    "s_waitcnt lgkmcnt(6)\n"
    "ds_read_b64_tr_b16 v[212:213], v253 offset:0\n"
    "ds_read_b64_tr_b16 v[214:215], v253 offset:4096\n"
    "s_and_b32 s54, s50, 1\n"
    "s_cmp_eq_u32 s54, 1\n"
    "s_cbranch_scc1 L_even1_%=\n"
    "L_even0_%=:\n"
    "v_mfma_f32_16x16x32_bf16 v[216:219], v[176:179], a[0:3], v[160:163]\n"
    "v_exp_f32 v222, v222\n"
    "v_mfma_f32_16x16x32_bf16 v[216:219], v[180:183], a[4:7], v[216:219]\n"
    "s_add_u32 m0, s47, 32768\n"
    "v_exp_f32 v223, v223\n"
    "v_mfma_f32_16x16x32_bf16 v[216:219], v[184:187], a[8:11], v[216:219]\n"
    "buffer_load_dwordx4 v254, s[36:39], s44 offen offset:0 nt lds\n"
    "v_mfma_f32_16x16x32_bf16 v[216:219], v[188:191], a[12:15], v[216:219]\n"
    "s_waitcnt lgkmcnt(4)\n"
    "v_add_f32 v227, v227, v220\n"
    "v_add_f32 v227, v227, v221\n"
    "v_mfma_f32_16x16x32_bf16 v[216:219], v[192:195], a[16:19], v[216:219]\n"
    "v_cvt_pk_bf16_f32 v158, v220, v221\n"
    "v_add_f32 v227, v227, v222\n"
    "v_mfma_f32_16x16x32_bf16 v[216:219], v[196:199], a[20:23], v[216:219]\n"
    "s_add_u32 m0, s47, 34688\n"
    "v_add_f32 v227, v227, v223\n"
    "v_cvt_pk_bf16_f32 v159, v222, v223\n"
    "v_mfma_f32_16x16x32_bf16 v[216:219], v[200:203], a[24:27], v[216:219]\n"
    "buffer_load_dwordx4 v254, s[36:39], s44 offen offset:128 nt lds\n"
    "v_mfma_f32_16x16x32_bf16 v[216:219], v[204:207], a[28:31], v[216:219]\n"
    "s_waitcnt lgkmcnt(0)\n"
    "v_mfma_f32_16x16x32_bf16 a[136:139], v[212:215], v[144:147], a[136:139]\n"
    "v_mfma_f32_16x16x32_bf16 a[128:131], v[208:211], v[144:147], a[128:131]\n"
    "s_add_u32 m0, s47, 36608\n"
    "v_mfma_f32_16x16x32_bf16 a[132:135], v[208:211], v[148:151], a[132:135]\n"
    "buffer_load_dwordx4 v254, s[36:39], s44 offen offset:256 nt lds\n"
    "v_mfma_f32_16x16x32_bf16 a[140:143], v[212:215], v[148:151], a[140:143]\n"
    "v_mfma_f32_16x16x32_bf16 v[8:11], v[212:215], v[152:155], v[8:11]\n"
    "v_exp_f32 v216, v216\n"
    "v_mfma_f32_16x16x32_bf16 v[0:3], v[208:211], v[152:155], v[0:3]\n"
    "v_exp_f32 v217, v217\n"
    "v_mfma_f32_16x16x32_bf16 v[4:7], v[208:211], v[156:159], v[4:7]\n"
    "s_nop 0\n"
    "ds_read_b64_tr_b16 v[208:209], v252 offset:512\n"
    "ds_read_b64_tr_b16 v[210:211], v252 offset:4608\n"
    "v_mfma_f32_16x16x32_bf16 v[12:15], v[212:215], v[156:159], v[12:15]\n"
    "s_nop 0\n"
    "ds_read_b64_tr_b16 v[212:213], v253 offset:512\n"
    "ds_read_b64_tr_b16 v[214:215], v253 offset:4608\n"
    "v_mfma_f32_16x16x32_bf16 v[220:223], v[176:179], a[32:35], v[164:167]\n"
    "v_exp_f32 v218, v218\n"
    "v_mfma_f32_16x16x32_bf16 v[220:223], v[180:183], a[36:39], v[220:223]\n"
    "s_add_u32 m0, s47, 38528\n"
    "v_exp_f32 v219, v219\n"
    "v_mfma_f32_16x16x32_bf16 v[220:223], v[184:187], a[40:43], v[220:223]\n"
    "buffer_load_dwordx4 v254, s[36:39], s44 offen offset:384 nt lds\n"
    "v_mfma_f32_16x16x32_bf16 v[220:223], v[188:191], a[44:47], v[220:223]\n"
    "v_add_f32 v224, v224, v216\n"
    "v_add_f32 v224, v224, v217\n"
    "v_mfma_f32_16x16x32_bf16 v[220:223], v[192:195], a[48:51], v[220:223]\n"
    "v_cvt_pk_bf16_f32 v128, v216, v217\n"
    "v_add_f32 v224, v224, v218\n"
    "v_mfma_f32_16x16x32_bf16 v[220:223], v[196:199], a[52:55], v[220:223]\n"
    "v_add_f32 v224, v224, v219\n"
    "v_cvt_pk_bf16_f32 v129, v218, v219\n"
    "v_mfma_f32_16x16x32_bf16 v[220:223], v[200:203], a[56:59], v[220:223]\n"
    "global_load_dword v247, v[248:249], off sc1\n"
    "v_mfma_f32_16x16x32_bf16 v[220:223], v[204:207], a[60:63], v[220:223]\n"
    "s_waitcnt lgkmcnt(0)\n"
    "v_mfma_f32_16x16x32_bf16 a[152:155], v[212:215], v[144:147], a[152:155]\n"
    "v_lshl_add_u64 v[248:249], v[248:249], 0, v[244:245]\n"
    "v_mfma_f32_16x16x32_bf16 a[144:147], v[208:211], v[144:147], a[144:147]\n"
    "s_add_u32 m0, s48, 49152\n"
    "v_mfma_f32_16x16x32_bf16 a[148:151], v[208:211], v[148:151], a[148:151]\n"
    "buffer_load_dwordx4 v255, s[40:43], s45 offen offset:0 nt lds\n"
    "v_mfma_f32_16x16x32_bf16 a[156:159], v[212:215], v[148:151], a[156:159]\n"
    "v_mfma_f32_16x16x32_bf16 v[24:27], v[212:215], v[152:155], v[24:27]\n"
    "v_exp_f32 v220, v220\n"
    "v_mfma_f32_16x16x32_bf16 v[16:19], v[208:211], v[152:155], v[16:19]\n"
    "v_exp_f32 v221, v221\n"
    "v_mfma_f32_16x16x32_bf16 v[20:23], v[208:211], v[156:159], v[20:23]\n"
    "s_nop 0\n"
    "ds_read_b64_tr_b16 v[208:209], v252 offset:1024\n"
    "ds_read_b64_tr_b16 v[210:211], v252 offset:5120\n"
    "v_mfma_f32_16x16x32_bf16 v[28:31], v[212:215], v[156:159], v[28:31]\n"
    "s_nop 0\n"
    "ds_read_b64_tr_b16 v[212:213], v253 offset:1024\n"
    "ds_read_b64_tr_b16 v[214:215], v253 offset:5120\n"
    "v_mfma_f32_16x16x32_bf16 v[216:219], v[176:179], a[64:67], v[168:171]\n"
    "v_exp_f32 v222, v222\n"
    "v_mfma_f32_16x16x32_bf16 v[216:219], v[180:183], a[68:71], v[216:219]\n"
    "s_add_u32 m0, s48, 50048\n"
    "v_exp_f32 v223, v223\n"
    "v_mfma_f32_16x16x32_bf16 v[216:219], v[184:187], a[72:75], v[216:219]\n"
    "buffer_load_dwordx4 v255, s[40:43], s45 offen offset:128 nt lds\n"
    "v_mfma_f32_16x16x32_bf16 v[216:219], v[188:191], a[76:79], v[216:219]\n"
    "v_add_f32 v225, v225, v220\n"
    "v_add_f32 v225, v225, v221\n"
    "v_mfma_f32_16x16x32_bf16 v[216:219], v[192:195], a[80:83], v[216:219]\n"
    "v_cvt_pk_bf16_f32 v132, v220, v221\n"
    "v_add_f32 v225, v225, v222\n"
    "v_mfma_f32_16x16x32_bf16 v[216:219], v[196:199], a[84:87], v[216:219]\n"
    "s_add_u32 m0, s48, 51200\n"
    "v_add_f32 v225, v225, v223\n"
    "v_cvt_pk_bf16_f32 v133, v222, v223\n"
    "v_mfma_f32_16x16x32_bf16 v[216:219], v[200:203], a[88:91], v[216:219]\n"
    "buffer_load_dwordx4 v255, s[40:43], s46 offen offset:0 nt lds\n"
    "v_mfma_f32_16x16x32_bf16 v[216:219], v[204:207], a[92:95], v[216:219]\n"
    "s_waitcnt lgkmcnt(0)\n"
    "v_mfma_f32_16x16x32_bf16 a[168:171], v[212:215], v[144:147], a[168:171]\n"
    "v_mfma_f32_16x16x32_bf16 a[160:163], v[208:211], v[144:147], a[160:163]\n"
    "s_add_u32 m0, s48, 52096\n"
    "v_mfma_f32_16x16x32_bf16 a[164:167], v[208:211], v[148:151], a[164:167]\n"
    "buffer_load_dwordx4 v255, s[40:43], s46 offen offset:128 nt lds\n"
    "v_mfma_f32_16x16x32_bf16 a[172:175], v[212:215], v[148:151], a[172:175]\n"
    "v_mfma_f32_16x16x32_bf16 v[40:43], v[212:215], v[152:155], v[40:43]\n"
    "v_exp_f32 v216, v216\n"
    "v_mfma_f32_16x16x32_bf16 v[32:35], v[208:211], v[152:155], v[32:35]\n"
    "v_exp_f32 v217, v217\n"
    "v_mfma_f32_16x16x32_bf16 v[36:39], v[208:211], v[156:159], v[36:39]\n"
    "s_nop 0\n"
    "ds_read_b64_tr_b16 v[208:209], v252 offset:1536\n"
    "ds_read_b64_tr_b16 v[210:211], v252 offset:5632\n"
    "v_mfma_f32_16x16x32_bf16 v[44:47], v[212:215], v[156:159], v[44:47]\n"
    "s_nop 0\n"
    "ds_read_b64_tr_b16 v[212:213], v253 offset:1536\n"
    "ds_read_b64_tr_b16 v[214:215], v253 offset:5632\n"
    "v_mfma_f32_16x16x32_bf16 v[220:223], v[176:179], a[96:99], v[172:175]\n"
    "v_exp_f32 v218, v218\n"
    "v_mfma_f32_16x16x32_bf16 v[220:223], v[180:183], a[100:103], v[220:223]\n"
    "v_exp_f32 v219, v219\n"
    "v_mfma_f32_16x16x32_bf16 v[220:223], v[184:187], a[104:107], v[220:223]\n"
    "s_add_u32 s44, s44, 24576\n"
    "ds_read_b128 v[176:179], v251 offset:0\n"
    "v_add_f32 v226, v226, v216\n"
    "v_mfma_f32_16x16x32_bf16 v[220:223], v[188:191], a[108:111], v[220:223]\n"
    "s_add_u32 s45, s45, 32512\n"
    "ds_read_b128 v[180:183], v251 offset:512\n"
    "v_add_f32 v226, v226, v217\n"
    "v_mfma_f32_16x16x32_bf16 v[220:223], v[192:195], a[112:115], v[220:223]\n"
    "s_add_u32 s46, s46, 32512\n"
    "ds_read_b128 v[184:187], v251 offset:2048\n"
    "v_cvt_pk_bf16_f32 v136, v216, v217\n"
    "v_mfma_f32_16x16x32_bf16 v[220:223], v[196:199], a[116:119], v[220:223]\n"
    "ds_read_b128 v[188:191], v251 offset:2560\n"
    "v_add_f32 v226, v226, v218\n"
    "v_mfma_f32_16x16x32_bf16 v[220:223], v[200:203], a[120:123], v[220:223]\n"
    "ds_read_b128 v[192:195], v251 offset:4096\n"
    "v_add_f32 v226, v226, v219\n"
    "v_mfma_f32_16x16x32_bf16 v[220:223], v[204:207], a[124:127], v[220:223]\n"
    "s_waitcnt lgkmcnt(5)\n"
    "ds_read_b128 v[196:199], v251 offset:4608\n"
    "v_cvt_pk_bf16_f32 v137, v218, v219\n"
    "v_mfma_f32_16x16x32_bf16 a[184:187], v[212:215], v[144:147], a[184:187]\n"
    "ds_read_b128 v[200:203], v251 offset:6144\n"
    "v_mfma_f32_16x16x32_bf16 a[176:179], v[208:211], v[144:147], a[176:179]\n"
    "ds_read_b128 v[204:207], v251 offset:6656\n"
    "v_mfma_f32_16x16x32_bf16 a[180:183], v[208:211], v[148:151], a[180:183]\n"
    "s_waitcnt vmcnt(13) lgkmcnt(8)\n"
    "s_barrier\n"
    "v_mfma_f32_16x16x32_bf16 a[188:191], v[212:215], v[148:151], a[188:191]\n"
    "v_mfma_f32_16x16x32_bf16 v[56:59], v[212:215], v[152:155], v[56:59]\n"
    "v_exp_f32 v220, v220\n"
    "v_mfma_f32_16x16x32_bf16 v[48:51], v[208:211], v[152:155], v[48:51]\n"
    "v_exp_f32 v221, v221\n"
    "v_mfma_f32_16x16x32_bf16 v[52:55], v[208:211], v[156:159], v[52:55]\n"
    "s_nop 0\n"
    "ds_read_b64_tr_b16 v[208:209], v252 offset:16384\n"
    "ds_read_b64_tr_b16 v[210:211], v252 offset:20480\n"
    "v_mfma_f32_16x16x32_bf16 v[60:63], v[212:215], v[156:159], v[60:63]\n"
    "s_waitcnt lgkmcnt(6)\n"
    "ds_read_b64_tr_b16 v[212:213], v253 offset:16384\n"
    "ds_read_b64_tr_b16 v[214:215], v253 offset:20480\n"
    "v_mfma_f32_16x16x32_bf16 v[216:219], v[176:179], a[0:3], v[160:163]\n"
    "v_exp_f32 v222, v222\n"
    "v_mfma_f32_16x16x32_bf16 v[216:219], v[180:183], a[4:7], v[216:219]\n"
    "s_add_u32 m0, s47, 49152\n"
    "v_exp_f32 v223, v223\n"
    "v_mfma_f32_16x16x32_bf16 v[216:219], v[184:187], a[8:11], v[216:219]\n"
    "buffer_load_dwordx4 v254, s[36:39], s44 offen offset:0 nt lds\n"
    "v_mfma_f32_16x16x32_bf16 v[216:219], v[188:191], a[12:15], v[216:219]\n"
    "s_waitcnt lgkmcnt(4)\n"
    "v_add_f32 v227, v227, v220\n"
    "v_add_f32 v227, v227, v221\n"
    "v_mfma_f32_16x16x32_bf16 v[216:219], v[192:195], a[16:19], v[216:219]\n"
    "v_cvt_pk_bf16_f32 v140, v220, v221\n"
    "v_add_f32 v227, v227, v222\n"
    "v_mfma_f32_16x16x32_bf16 v[216:219], v[196:199], a[20:23], v[216:219]\n"
    "s_add_u32 m0, s47, 51072\n"
    "v_add_f32 v227, v227, v223\n"
    "v_cvt_pk_bf16_f32 v141, v222, v223\n"
    "v_mfma_f32_16x16x32_bf16 v[216:219], v[200:203], a[24:27], v[216:219]\n"
    "buffer_load_dwordx4 v254, s[36:39], s44 offen offset:128 nt lds\n"
    "v_mfma_f32_16x16x32_bf16 v[216:219], v[204:207], a[28:31], v[216:219]\n"
    "s_waitcnt lgkmcnt(0)\n"
    "v_mfma_f32_16x16x32_bf16 a[200:203], v[212:215], v[144:147], a[200:203]\n"
    "v_mfma_f32_16x16x32_bf16 a[192:195], v[208:211], v[144:147], a[192:195]\n"
    "s_add_u32 m0, s47, 52992\n"
    "v_mfma_f32_16x16x32_bf16 a[196:199], v[208:211], v[148:151], a[196:199]\n"
    "buffer_load_dwordx4 v254, s[36:39], s44 offen offset:256 nt lds\n"
    "v_mfma_f32_16x16x32_bf16 a[204:207], v[212:215], v[148:151], a[204:207]\n"
    "v_mfma_f32_16x16x32_bf16 v[72:75], v[212:215], v[152:155], v[72:75]\n"
    "v_exp_f32 v216, v216\n"
    "v_mfma_f32_16x16x32_bf16 v[64:67], v[208:211], v[152:155], v[64:67]\n"
    "v_exp_f32 v217, v217\n"
    "v_mfma_f32_16x16x32_bf16 v[68:71], v[208:211], v[156:159], v[68:71]\n"
    "s_nop 0\n"
    "ds_read_b64_tr_b16 v[208:209], v252 offset:16896\n"
    "ds_read_b64_tr_b16 v[210:211], v252 offset:20992\n"
    "v_mfma_f32_16x16x32_bf16 v[76:79], v[212:215], v[156:159], v[76:79]\n"
    "s_nop 0\n"
    "ds_read_b64_tr_b16 v[212:213], v253 offset:16896\n"
    "ds_read_b64_tr_b16 v[214:215], v253 offset:20992\n"
    "v_mfma_f32_16x16x32_bf16 v[220:223], v[176:179], a[32:35], v[164:167]\n"
    "v_exp_f32 v218, v218\n"
    "v_mfma_f32_16x16x32_bf16 v[220:223], v[180:183], a[36:39], v[220:223]\n"
    "s_add_u32 m0, s47, 54912\n"
    "v_exp_f32 v219, v219\n"
    "v_mfma_f32_16x16x32_bf16 v[220:223], v[184:187], a[40:43], v[220:223]\n"
    "buffer_load_dwordx4 v254, s[36:39], s44 offen offset:384 nt lds\n"
    "v_mfma_f32_16x16x32_bf16 v[220:223], v[188:191], a[44:47], v[220:223]\n"
    "v_add_f32 v224, v224, v216\n"
    "v_add_f32 v224, v224, v217\n"
    "v_mfma_f32_16x16x32_bf16 v[220:223], v[192:195], a[48:51], v[220:223]\n"
    "v_cvt_pk_bf16_f32 v130, v216, v217\n"
    "v_add_f32 v224, v224, v218\n"
    "v_mfma_f32_16x16x32_bf16 v[220:223], v[196:199], a[52:55], v[220:223]\n"
    "v_add_f32 v224, v224, v219\n"
    "v_cvt_pk_bf16_f32 v131, v218, v219\n"
    "v_mfma_f32_16x16x32_bf16 v[220:223], v[200:203], a[56:59], v[220:223]\n"
    "v_mfma_f32_16x16x32_bf16 v[220:223], v[204:207], a[60:63], v[220:223]\n"
    "s_waitcnt lgkmcnt(0)\n"
    "v_mfma_f32_16x16x32_bf16 a[216:219], v[212:215], v[144:147], a[216:219]\n"
    "v_mfma_f32_16x16x32_bf16 a[208:211], v[208:211], v[144:147], a[208:211]\n"
    "s_add_u32 m0, s48, 0\n"
    "v_mfma_f32_16x16x32_bf16 a[212:215], v[208:211], v[148:151], a[212:215]\n"
    "buffer_load_dwordx4 v255, s[40:43], s45 offen offset:0 nt lds\n"
    "v_mfma_f32_16x16x32_bf16 a[220:223], v[212:215], v[148:151], a[220:223]\n"
    "v_mfma_f32_16x16x32_bf16 v[88:91], v[212:215], v[152:155], v[88:91]\n"
    "v_exp_f32 v220, v220\n"
    "v_mfma_f32_16x16x32_bf16 v[80:83], v[208:211], v[152:155], v[80:83]\n"
    "v_exp_f32 v221, v221\n"
    "v_mfma_f32_16x16x32_bf16 v[84:87], v[208:211], v[156:159], v[84:87]\n"
    "s_nop 0\n"
    "ds_read_b64_tr_b16 v[208:209], v252 offset:17408\n"
    "ds_read_b64_tr_b16 v[210:211], v252 offset:21504\n"
    "v_mfma_f32_16x16x32_bf16 v[92:95], v[212:215], v[156:159], v[92:95]\n"
    "s_nop 0\n"
    "ds_read_b64_tr_b16 v[212:213], v253 offset:17408\n"
    "ds_read_b64_tr_b16 v[214:215], v253 offset:21504\n"
    "v_mfma_f32_16x16x32_bf16 v[216:219], v[176:179], a[64:67], v[168:171]\n"
    "v_exp_f32 v222, v222\n"
    "v_mfma_f32_16x16x32_bf16 v[216:219], v[180:183], a[68:71], v[216:219]\n"
    "s_add_u32 m0, s48, 896\n"
    "v_exp_f32 v223, v223\n"
    "v_mfma_f32_16x16x32_bf16 v[216:219], v[184:187], a[72:75], v[216:219]\n"
    "buffer_load_dwordx4 v255, s[40:43], s45 offen offset:128 nt lds\n"
    "v_mfma_f32_16x16x32_bf16 v[216:219], v[188:191], a[76:79], v[216:219]\n"
    "v_add_f32 v225, v225, v220\n"
    "v_add_f32 v225, v225, v221\n"
    "v_mfma_f32_16x16x32_bf16 v[216:219], v[192:195], a[80:83], v[216:219]\n"
    "v_cvt_pk_bf16_f32 v134, v220, v221\n"
    "v_add_f32 v225, v225, v222\n"
    "v_mfma_f32_16x16x32_bf16 v[216:219], v[196:199], a[84:87], v[216:219]\n"
    "s_add_u32 m0, s48, 2048\n"
    "v_add_f32 v225, v225, v223\n"
    "v_cvt_pk_bf16_f32 v135, v222, v223\n"
    "v_mfma_f32_16x16x32_bf16 v[216:219], v[200:203], a[88:91], v[216:219]\n"
    "buffer_load_dwordx4 v255, s[40:43], s46 offen offset:0 nt lds\n"
    "v_mfma_f32_16x16x32_bf16 v[216:219], v[204:207], a[92:95], v[216:219]\n"
    "s_waitcnt lgkmcnt(0)\n"
    "v_mfma_f32_16x16x32_bf16 a[232:235], v[212:215], v[144:147], a[232:235]\n"
    "v_mfma_f32_16x16x32_bf16 a[224:227], v[208:211], v[144:147], a[224:227]\n"
    "s_add_u32 m0, s48, 2944\n"
    "v_mfma_f32_16x16x32_bf16 a[228:231], v[208:211], v[148:151], a[228:231]\n"
    "buffer_load_dwordx4 v255, s[40:43], s46 offen offset:128 nt lds\n"
    "v_mfma_f32_16x16x32_bf16 a[236:239], v[212:215], v[148:151], a[236:239]\n"
    "v_mfma_f32_16x16x32_bf16 v[104:107], v[212:215], v[152:155], v[104:107]\n"
    "v_exp_f32 v216, v216\n"
    "v_mfma_f32_16x16x32_bf16 v[96:99], v[208:211], v[152:155], v[96:99]\n"
    "v_exp_f32 v217, v217\n"
    "v_mfma_f32_16x16x32_bf16 v[100:103], v[208:211], v[156:159], v[100:103]\n"
    "s_nop 0\n"
    "ds_read_b64_tr_b16 v[208:209], v252 offset:17920\n"
    "ds_read_b64_tr_b16 v[210:211], v252 offset:22016\n"
    "v_mfma_f32_16x16x32_bf16 v[108:111], v[212:215], v[156:159], v[108:111]\n"
    "s_nop 0\n"
    "ds_read_b64_tr_b16 v[212:213], v253 offset:17920\n"
    "ds_read_b64_tr_b16 v[214:215], v253 offset:22016\n"
    "v_mfma_f32_16x16x32_bf16 v[220:223], v[176:179], a[96:99], v[172:175]\n"
    "v_exp_f32 v218, v218\n"
    "v_mfma_f32_16x16x32_bf16 v[220:223], v[180:183], a[100:103], v[220:223]\n"
    "v_exp_f32 v219, v219\n"
    "v_mfma_f32_16x16x32_bf16 v[220:223], v[184:187], a[104:107], v[220:223]\n"
    "s_add_u32 s44, s44, 8192\n"
    "ds_read_b128 v[176:179], v251 offset:16384\n"
    "v_add_f32 v226, v226, v216\n"
    "v_mfma_f32_16x16x32_bf16 v[220:223], v[188:191], a[108:111], v[220:223]\n"
    "s_add_u32 s45, s45, 256\n"
    "ds_read_b128 v[180:183], v251 offset:16896\n"
    "v_add_f32 v226, v226, v217\n"
    "v_mfma_f32_16x16x32_bf16 v[220:223], v[192:195], a[112:115], v[220:223]\n"
    "s_add_u32 s46, s46, 256\n"
    "ds_read_b128 v[184:187], v251 offset:18432\n"
    "v_cvt_pk_bf16_f32 v138, v216, v217\n"
    "v_mfma_f32_16x16x32_bf16 v[220:223], v[196:199], a[116:119], v[220:223]\n"
    "ds_read_b128 v[188:191], v251 offset:18944\n"
    "v_add_f32 v226, v226, v218\n"
    "v_mfma_f32_16x16x32_bf16 v[220:223], v[200:203], a[120:123], v[220:223]\n"
    "ds_read_b128 v[192:195], v251 offset:20480\n"
    "v_add_f32 v226, v226, v219\n"
    "v_mfma_f32_16x16x32_bf16 v[220:223], v[204:207], a[124:127], v[220:223]\n"
    "s_waitcnt lgkmcnt(5)\n"
    "ds_read_b128 v[196:199], v251 offset:20992\n"
    "v_cvt_pk_bf16_f32 v139, v218, v219\n"
    "v_mfma_f32_16x16x32_bf16 a[248:251], v[212:215], v[144:147], a[248:251]\n"
    "ds_read_b128 v[200:203], v251 offset:22528\n"
    "v_mfma_f32_16x16x32_bf16 a[240:243], v[208:211], v[144:147], a[240:243]\n"
    "ds_read_b128 v[204:207], v251 offset:23040\n"
    "v_mfma_f32_16x16x32_bf16 a[244:247], v[208:211], v[148:151], a[244:247]\n"
    "s_waitcnt vmcnt(13) lgkmcnt(8)\n"
    "s_barrier\n"
    "v_mfma_f32_16x16x32_bf16 a[252:255], v[212:215], v[148:151], a[252:255]\n"
    "v_mfma_f32_16x16x32_bf16 v[120:123], v[212:215], v[152:155], v[120:123]\n"
    "v_exp_f32 v220, v220\n"
    "v_mfma_f32_16x16x32_bf16 v[112:115], v[208:211], v[152:155], v[112:115]\n"
    "v_exp_f32 v221, v221\n"
    "v_mfma_f32_16x16x32_bf16 v[116:119], v[208:211], v[156:159], v[116:119]\n"
    "s_nop 0\n"
    "ds_read_b64_tr_b16 v[208:209], v252 offset:32768\n"
    "ds_read_b64_tr_b16 v[210:211], v252 offset:36864\n"
    "v_mfma_f32_16x16x32_bf16 v[124:127], v[212:215], v[156:159], v[124:127]\n"
    "s_waitcnt lgkmcnt(6)\n"
    "ds_read_b64_tr_b16 v[212:213], v253 offset:32768\n"
    "ds_read_b64_tr_b16 v[214:215], v253 offset:36864\n"
    "v_mfma_f32_16x16x32_bf16 v[216:219], v[176:179], a[0:3], v[160:163]\n"
    "v_exp_f32 v222, v222\n"
    "v_mfma_f32_16x16x32_bf16 v[216:219], v[180:183], a[4:7], v[216:219]\n"
    "s_add_u32 m0, s47, 0\n"
    "v_exp_f32 v223, v223\n"
    "v_mfma_f32_16x16x32_bf16 v[216:219], v[184:187], a[8:11], v[216:219]\n"
    "buffer_load_dwordx4 v254, s[36:39], s44 offen offset:0 nt lds\n"
    "v_mfma_f32_16x16x32_bf16 v[216:219], v[188:191], a[12:15], v[216:219]\n"
    "s_waitcnt lgkmcnt(4)\n"
    "v_add_f32 v227, v227, v220\n"
    "v_add_f32 v227, v227, v221\n"
    "v_mfma_f32_16x16x32_bf16 v[216:219], v[192:195], a[16:19], v[216:219]\n"
    "v_cvt_pk_bf16_f32 v142, v220, v221\n"
    "v_add_f32 v227, v227, v222\n"
    "v_mfma_f32_16x16x32_bf16 v[216:219], v[196:199], a[20:23], v[216:219]\n"
    "s_add_u32 m0, s47, 1920\n"
    "v_add_f32 v227, v227, v223\n"
    "v_cvt_pk_bf16_f32 v143, v222, v223\n"
    "v_mfma_f32_16x16x32_bf16 v[216:219], v[200:203], a[24:27], v[216:219]\n"
    "buffer_load_dwordx4 v254, s[36:39], s44 offen offset:128 nt lds\n"
    "v_mfma_f32_16x16x32_bf16 v[216:219], v[204:207], a[28:31], v[216:219]\n"
    "s_waitcnt lgkmcnt(0)\n"
    "v_mfma_f32_16x16x32_bf16 a[136:139], v[212:215], v[128:131], a[136:139]\n"
    "v_mfma_f32_16x16x32_bf16 a[128:131], v[208:211], v[128:131], a[128:131]\n"
    "s_add_u32 m0, s47, 3840\n"
    "v_mfma_f32_16x16x32_bf16 a[132:135], v[208:211], v[132:135], a[132:135]\n"
    "buffer_load_dwordx4 v254, s[36:39], s44 offen offset:256 nt lds\n"
    "v_mfma_f32_16x16x32_bf16 a[140:143], v[212:215], v[132:135], a[140:143]\n"
    "v_mfma_f32_16x16x32_bf16 v[8:11], v[212:215], v[136:139], v[8:11]\n"
    "v_exp_f32 v216, v216\n"
    "v_mfma_f32_16x16x32_bf16 v[0:3], v[208:211], v[136:139], v[0:3]\n"
    "v_exp_f32 v217, v217\n"
    "v_mfma_f32_16x16x32_bf16 v[4:7], v[208:211], v[140:143], v[4:7]\n"
    "s_nop 0\n"
    "ds_read_b64_tr_b16 v[208:209], v252 offset:33280\n"
    "ds_read_b64_tr_b16 v[210:211], v252 offset:37376\n"
    "v_mfma_f32_16x16x32_bf16 v[12:15], v[212:215], v[140:143], v[12:15]\n"
    "s_nop 0\n"
    "ds_read_b64_tr_b16 v[212:213], v253 offset:33280\n"
    "ds_read_b64_tr_b16 v[214:215], v253 offset:37376\n"
    "v_mfma_f32_16x16x32_bf16 v[220:223], v[176:179], a[32:35], v[164:167]\n"
    "v_exp_f32 v218, v218\n"
    "v_mfma_f32_16x16x32_bf16 v[220:223], v[180:183], a[36:39], v[220:223]\n"
    "s_add_u32 m0, s47, 5760\n"
    "v_exp_f32 v219, v219\n"
    "v_mfma_f32_16x16x32_bf16 v[220:223], v[184:187], a[40:43], v[220:223]\n"
    "buffer_load_dwordx4 v254, s[36:39], s44 offen offset:384 nt lds\n"
    "v_mfma_f32_16x16x32_bf16 v[220:223], v[188:191], a[44:47], v[220:223]\n"
    "v_add_f32 v224, v224, v216\n"
    "v_add_f32 v224, v224, v217\n"
    "v_mfma_f32_16x16x32_bf16 v[220:223], v[192:195], a[48:51], v[220:223]\n"
    "v_cvt_pk_bf16_f32 v144, v216, v217\n"
    "v_add_f32 v224, v224, v218\n"
    "v_mfma_f32_16x16x32_bf16 v[220:223], v[196:199], a[52:55], v[220:223]\n"
    "v_add_f32 v224, v224, v219\n"
    "v_cvt_pk_bf16_f32 v145, v218, v219\n"
    "v_mfma_f32_16x16x32_bf16 v[220:223], v[200:203], a[56:59], v[220:223]\n"
    "global_load_dword v247, v[248:249], off sc1\n"
    "v_mfma_f32_16x16x32_bf16 v[220:223], v[204:207], a[60:63], v[220:223]\n"
    "s_waitcnt lgkmcnt(0)\n"
    "v_mfma_f32_16x16x32_bf16 a[152:155], v[212:215], v[128:131], a[152:155]\n"
    "v_lshl_add_u64 v[248:249], v[248:249], 0, v[244:245]\n"
    "v_mfma_f32_16x16x32_bf16 a[144:147], v[208:211], v[128:131], a[144:147]\n"
    "s_add_u32 m0, s48, 16384\n"
    "v_mfma_f32_16x16x32_bf16 a[148:151], v[208:211], v[132:135], a[148:151]\n"
    "buffer_load_dwordx4 v255, s[40:43], s45 offen offset:0 nt lds\n"
    "v_mfma_f32_16x16x32_bf16 a[156:159], v[212:215], v[132:135], a[156:159]\n"
    "v_mfma_f32_16x16x32_bf16 v[24:27], v[212:215], v[136:139], v[24:27]\n"
    "v_exp_f32 v220, v220\n"
    "v_mfma_f32_16x16x32_bf16 v[16:19], v[208:211], v[136:139], v[16:19]\n"
    "v_exp_f32 v221, v221\n"
    "v_mfma_f32_16x16x32_bf16 v[20:23], v[208:211], v[140:143], v[20:23]\n"
    "s_nop 0\n"
    "ds_read_b64_tr_b16 v[208:209], v252 offset:33792\n"
    "ds_read_b64_tr_b16 v[210:211], v252 offset:37888\n"
    "v_mfma_f32_16x16x32_bf16 v[28:31], v[212:215], v[140:143], v[28:31]\n"
    "s_nop 0\n"
    "ds_read_b64_tr_b16 v[212:213], v253 offset:33792\n"
    "ds_read_b64_tr_b16 v[214:215], v253 offset:37888\n"
    "v_mfma_f32_16x16x32_bf16 v[216:219], v[176:179], a[64:67], v[168:171]\n"
    "v_exp_f32 v222, v222\n"
    "v_mfma_f32_16x16x32_bf16 v[216:219], v[180:183], a[68:71], v[216:219]\n"
    "s_add_u32 m0, s48, 17280\n"
    "v_exp_f32 v223, v223\n"
    "v_mfma_f32_16x16x32_bf16 v[216:219], v[184:187], a[72:75], v[216:219]\n"
    "buffer_load_dwordx4 v255, s[40:43], s45 offen offset:128 nt lds\n"
    "v_mfma_f32_16x16x32_bf16 v[216:219], v[188:191], a[76:79], v[216:219]\n"
    "v_add_f32 v225, v225, v220\n"
    "v_add_f32 v225, v225, v221\n"
    "v_mfma_f32_16x16x32_bf16 v[216:219], v[192:195], a[80:83], v[216:219]\n"
    "v_cvt_pk_bf16_f32 v148, v220, v221\n"
    "v_add_f32 v225, v225, v222\n"
    "v_mfma_f32_16x16x32_bf16 v[216:219], v[196:199], a[84:87], v[216:219]\n"
    "s_add_u32 m0, s48, 18432\n"
    "v_add_f32 v225, v225, v223\n"
    "v_cvt_pk_bf16_f32 v149, v222, v223\n"
    "v_mfma_f32_16x16x32_bf16 v[216:219], v[200:203], a[88:91], v[216:219]\n"
    "buffer_load_dwordx4 v255, s[40:43], s46 offen offset:0 nt lds\n"
    "v_mfma_f32_16x16x32_bf16 v[216:219], v[204:207], a[92:95], v[216:219]\n"
    "s_waitcnt lgkmcnt(0)\n"
    "v_mfma_f32_16x16x32_bf16 a[168:171], v[212:215], v[128:131], a[168:171]\n"
    "v_mfma_f32_16x16x32_bf16 a[160:163], v[208:211], v[128:131], a[160:163]\n"
    "s_add_u32 m0, s48, 19328\n"
    "v_mfma_f32_16x16x32_bf16 a[164:167], v[208:211], v[132:135], a[164:167]\n"
    "buffer_load_dwordx4 v255, s[40:43], s46 offen offset:128 nt lds\n"
    "v_mfma_f32_16x16x32_bf16 a[172:175], v[212:215], v[132:135], a[172:175]\n"
    "v_mfma_f32_16x16x32_bf16 v[40:43], v[212:215], v[136:139], v[40:43]\n"
    "v_exp_f32 v216, v216\n"
    "v_mfma_f32_16x16x32_bf16 v[32:35], v[208:211], v[136:139], v[32:35]\n"
    "v_exp_f32 v217, v217\n"
    "v_mfma_f32_16x16x32_bf16 v[36:39], v[208:211], v[140:143], v[36:39]\n"
    "s_nop 0\n"
    "ds_read_b64_tr_b16 v[208:209], v252 offset:34304\n"
    "ds_read_b64_tr_b16 v[210:211], v252 offset:38400\n"
    "v_mfma_f32_16x16x32_bf16 v[44:47], v[212:215], v[140:143], v[44:47]\n"
    "s_nop 0\n"
    "ds_read_b64_tr_b16 v[212:213], v253 offset:34304\n"
    "ds_read_b64_tr_b16 v[214:215], v253 offset:38400\n"
    "v_mfma_f32_16x16x32_bf16 v[220:223], v[176:179], a[96:99], v[172:175]\n"
    "v_exp_f32 v218, v218\n"
    "v_mfma_f32_16x16x32_bf16 v[220:223], v[180:183], a[100:103], v[220:223]\n"
    "v_exp_f32 v219, v219\n"
    "v_mfma_f32_16x16x32_bf16 v[220:223], v[184:187], a[104:107], v[220:223]\n"
    "s_add_u32 s44, s44, 24576\n"
    "ds_read_b128 v[176:179], v251 offset:32768\n"
    "v_add_f32 v226, v226, v216\n"
    "v_mfma_f32_16x16x32_bf16 v[220:223], v[188:191], a[108:111], v[220:223]\n"
    "s_add_u32 s45, s45, 32512\n"
    "ds_read_b128 v[180:183], v251 offset:33280\n"
    "v_add_f32 v226, v226, v217\n"
    "v_mfma_f32_16x16x32_bf16 v[220:223], v[192:195], a[112:115], v[220:223]\n"
    "s_add_u32 s46, s46, 32512\n"
    "ds_read_b128 v[184:187], v251 offset:34816\n"
    "v_cvt_pk_bf16_f32 v152, v216, v217\n"
    "v_mfma_f32_16x16x32_bf16 v[220:223], v[196:199], a[116:119], v[220:223]\n"
    "ds_read_b128 v[188:191], v251 offset:35328\n"
    "v_add_f32 v226, v226, v218\n"
    "v_mfma_f32_16x16x32_bf16 v[220:223], v[200:203], a[120:123], v[220:223]\n"
    "ds_read_b128 v[192:195], v251 offset:36864\n"
    "v_add_f32 v226, v226, v219\n"
    "v_mfma_f32_16x16x32_bf16 v[220:223], v[204:207], a[124:127], v[220:223]\n"
    "s_waitcnt lgkmcnt(5)\n"
    "ds_read_b128 v[196:199], v251 offset:37376\n"
    "v_cvt_pk_bf16_f32 v153, v218, v219\n"
    "v_mfma_f32_16x16x32_bf16 a[184:187], v[212:215], v[128:131], a[184:187]\n"
    "ds_read_b128 v[200:203], v251 offset:38912\n"
    "v_mfma_f32_16x16x32_bf16 a[176:179], v[208:211], v[128:131], a[176:179]\n"
    "ds_read_b128 v[204:207], v251 offset:39424\n"
    "v_mfma_f32_16x16x32_bf16 a[180:183], v[208:211], v[132:135], a[180:183]\n"
    "s_waitcnt vmcnt(13) lgkmcnt(8)\n"
    "s_barrier\n"
    "v_mfma_f32_16x16x32_bf16 a[188:191], v[212:215], v[132:135], a[188:191]\n"
    "v_mfma_f32_16x16x32_bf16 v[56:59], v[212:215], v[136:139], v[56:59]\n"
    "v_exp_f32 v220, v220\n"
    "v_mfma_f32_16x16x32_bf16 v[48:51], v[208:211], v[136:139], v[48:51]\n"
    "v_exp_f32 v221, v221\n"
    "v_mfma_f32_16x16x32_bf16 v[52:55], v[208:211], v[140:143], v[52:55]\n"
    "s_nop 0\n"
    "ds_read_b64_tr_b16 v[208:209], v252 offset:49152\n"
    "ds_read_b64_tr_b16 v[210:211], v252 offset:53248\n"
    "v_mfma_f32_16x16x32_bf16 v[60:63], v[212:215], v[140:143], v[60:63]\n"
    "s_waitcnt lgkmcnt(6)\n"
    "ds_read_b64_tr_b16 v[212:213], v253 offset:49152\n"
    "ds_read_b64_tr_b16 v[214:215], v253 offset:53248\n"
    "v_mfma_f32_16x16x32_bf16 v[216:219], v[176:179], a[0:3], v[160:163]\n"
    "v_exp_f32 v222, v222\n"
    "v_mfma_f32_16x16x32_bf16 v[216:219], v[180:183], a[4:7], v[216:219]\n"
    "s_add_u32 m0, s47, 16384\n"
    "v_exp_f32 v223, v223\n"
    "v_mfma_f32_16x16x32_bf16 v[216:219], v[184:187], a[8:11], v[216:219]\n"
    "buffer_load_dwordx4 v254, s[36:39], s44 offen offset:0 nt lds\n"
    "v_mfma_f32_16x16x32_bf16 v[216:219], v[188:191], a[12:15], v[216:219]\n"
    "s_waitcnt lgkmcnt(4)\n"
    "v_add_f32 v227, v227, v220\n"
    "v_add_f32 v227, v227, v221\n"
    "v_mfma_f32_16x16x32_bf16 v[216:219], v[192:195], a[16:19], v[216:219]\n"
    "v_cvt_pk_bf16_f32 v156, v220, v221\n"
    "v_add_f32 v227, v227, v222\n"
    "v_mfma_f32_16x16x32_bf16 v[216:219], v[196:199], a[20:23], v[216:219]\n"
    "s_add_u32 m0, s47, 18304\n"
    "v_add_f32 v227, v227, v223\n"
    "v_cvt_pk_bf16_f32 v157, v222, v223\n"
    "v_mfma_f32_16x16x32_bf16 v[216:219], v[200:203], a[24:27], v[216:219]\n"
    "buffer_load_dwordx4 v254, s[36:39], s44 offen offset:128 nt lds\n"
    "v_mfma_f32_16x16x32_bf16 v[216:219], v[204:207], a[28:31], v[216:219]\n"
    "s_waitcnt lgkmcnt(0)\n"
    "v_mfma_f32_16x16x32_bf16 a[200:203], v[212:215], v[128:131], a[200:203]\n"
    "v_mfma_f32_16x16x32_bf16 a[192:195], v[208:211], v[128:131], a[192:195]\n"
    "s_add_u32 m0, s47, 20224\n"
    "v_mfma_f32_16x16x32_bf16 a[196:199], v[208:211], v[132:135], a[196:199]\n"
    "buffer_load_dwordx4 v254, s[36:39], s44 offen offset:256 nt lds\n"
    "v_mfma_f32_16x16x32_bf16 a[204:207], v[212:215], v[132:135], a[204:207]\n"
    "v_mfma_f32_16x16x32_bf16 v[72:75], v[212:215], v[136:139], v[72:75]\n"
    "v_exp_f32 v216, v216\n"
    "v_mfma_f32_16x16x32_bf16 v[64:67], v[208:211], v[136:139], v[64:67]\n"
    "v_exp_f32 v217, v217\n"
    "v_mfma_f32_16x16x32_bf16 v[68:71], v[208:211], v[140:143], v[68:71]\n"
    "s_nop 0\n"
    "ds_read_b64_tr_b16 v[208:209], v252 offset:49664\n"
    "ds_read_b64_tr_b16 v[210:211], v252 offset:53760\n"
    "v_mfma_f32_16x16x32_bf16 v[76:79], v[212:215], v[140:143], v[76:79]\n"
    "s_nop 0\n"
    "ds_read_b64_tr_b16 v[212:213], v253 offset:49664\n"
    "ds_read_b64_tr_b16 v[214:215], v253 offset:53760\n"
    "v_mfma_f32_16x16x32_bf16 v[220:223], v[176:179], a[32:35], v[164:167]\n"
    "v_exp_f32 v218, v218\n"
    "v_mfma_f32_16x16x32_bf16 v[220:223], v[180:183], a[36:39], v[220:223]\n"
    "s_add_u32 m0, s47, 22144\n"
    "v_exp_f32 v219, v219\n"
    "v_mfma_f32_16x16x32_bf16 v[220:223], v[184:187], a[40:43], v[220:223]\n"
    "buffer_load_dwordx4 v254, s[36:39], s44 offen offset:384 nt lds\n"
    "v_mfma_f32_16x16x32_bf16 v[220:223], v[188:191], a[44:47], v[220:223]\n"
    "v_add_f32 v224, v224, v216\n"
    "v_add_f32 v224, v224, v217\n"
    "v_mfma_f32_16x16x32_bf16 v[220:223], v[192:195], a[48:51], v[220:223]\n"
    "v_cvt_pk_bf16_f32 v146, v216, v217\n"
    "v_add_f32 v224, v224, v218\n"
    "v_mfma_f32_16x16x32_bf16 v[220:223], v[196:199], a[52:55], v[220:223]\n"
    "v_add_f32 v224, v224, v219\n"
    "v_cvt_pk_bf16_f32 v147, v218, v219\n"
    "v_mfma_f32_16x16x32_bf16 v[220:223], v[200:203], a[56:59], v[220:223]\n"
    "v_mfma_f32_16x16x32_bf16 v[220:223], v[204:207], a[60:63], v[220:223]\n"
    "s_waitcnt lgkmcnt(0)\n"
    "v_mfma_f32_16x16x32_bf16 a[216:219], v[212:215], v[128:131], a[216:219]\n"
    "v_mfma_f32_16x16x32_bf16 a[208:211], v[208:211], v[128:131], a[208:211]\n"
    "s_add_u32 m0, s48, 32768\n"
    "v_mfma_f32_16x16x32_bf16 a[212:215], v[208:211], v[132:135], a[212:215]\n"
    "buffer_load_dwordx4 v255, s[40:43], s45 offen offset:0 nt lds\n"
    "v_mfma_f32_16x16x32_bf16 a[220:223], v[212:215], v[132:135], a[220:223]\n"
    "v_mfma_f32_16x16x32_bf16 v[88:91], v[212:215], v[136:139], v[88:91]\n"
    "v_exp_f32 v220, v220\n"
    "v_mfma_f32_16x16x32_bf16 v[80:83], v[208:211], v[136:139], v[80:83]\n"
    "v_exp_f32 v221, v221\n"
    "v_mfma_f32_16x16x32_bf16 v[84:87], v[208:211], v[140:143], v[84:87]\n"
    "s_nop 0\n"
    "ds_read_b64_tr_b16 v[208:209], v252 offset:50176\n"
    "ds_read_b64_tr_b16 v[210:211], v252 offset:54272\n"
    "v_mfma_f32_16x16x32_bf16 v[92:95], v[212:215], v[140:143], v[92:95]\n"
    "s_nop 0\n"
    "ds_read_b64_tr_b16 v[212:213], v253 offset:50176\n"
    "ds_read_b64_tr_b16 v[214:215], v253 offset:54272\n"
    "v_mfma_f32_16x16x32_bf16 v[216:219], v[176:179], a[64:67], v[168:171]\n"
    "v_exp_f32 v222, v222\n"
    "v_mfma_f32_16x16x32_bf16 v[216:219], v[180:183], a[68:71], v[216:219]\n"
    "s_add_u32 m0, s48, 33664\n"
    "v_exp_f32 v223, v223\n"
    "v_mfma_f32_16x16x32_bf16 v[216:219], v[184:187], a[72:75], v[216:219]\n"
    "buffer_load_dwordx4 v255, s[40:43], s45 offen offset:128 nt lds\n"
    "v_mfma_f32_16x16x32_bf16 v[216:219], v[188:191], a[76:79], v[216:219]\n"
    "v_add_f32 v225, v225, v220\n"
    "v_add_f32 v225, v225, v221\n"
    "v_mfma_f32_16x16x32_bf16 v[216:219], v[192:195], a[80:83], v[216:219]\n"
    "v_cvt_pk_bf16_f32 v150, v220, v221\n"
    "v_add_f32 v225, v225, v222\n"
    "v_mfma_f32_16x16x32_bf16 v[216:219], v[196:199], a[84:87], v[216:219]\n"
    "s_add_u32 m0, s48, 34816\n"
    "v_add_f32 v225, v225, v223\n"
    "v_cvt_pk_bf16_f32 v151, v222, v223\n"
    "v_mfma_f32_16x16x32_bf16 v[216:219], v[200:203], a[88:91], v[216:219]\n"
    "buffer_load_dwordx4 v255, s[40:43], s46 offen offset:0 nt lds\n"
    "v_mfma_f32_16x16x32_bf16 v[216:219], v[204:207], a[92:95], v[216:219]\n"
    "s_waitcnt lgkmcnt(0)\n"
    "v_mfma_f32_16x16x32_bf16 a[232:235], v[212:215], v[128:131], a[232:235]\n"
    "s_sub_u32 s49, s49, 1\n"
    "v_mfma_f32_16x16x32_bf16 a[224:227], v[208:211], v[128:131], a[224:227]\n"
    "s_add_u32 m0, s48, 35712\n"
    "v_mfma_f32_16x16x32_bf16 a[228:231], v[208:211], v[132:135], a[228:231]\n"
    "buffer_load_dwordx4 v255, s[40:43], s46 offen offset:128 nt lds\n"
    "v_mfma_f32_16x16x32_bf16 a[236:239], v[212:215], v[132:135], a[236:239]\n"
    "v_mfma_f32_16x16x32_bf16 v[104:107], v[212:215], v[136:139], v[104:107]\n"
    "v_exp_f32 v216, v216\n"
    "v_mfma_f32_16x16x32_bf16 v[96:99], v[208:211], v[136:139], v[96:99]\n"
    "v_exp_f32 v217, v217\n"
    "v_mfma_f32_16x16x32_bf16 v[100:103], v[208:211], v[140:143], v[100:103]\n"
    "s_nop 0\n"
    "ds_read_b64_tr_b16 v[208:209], v252 offset:50688\n"
    "ds_read_b64_tr_b16 v[210:211], v252 offset:54784\n"
    "v_mfma_f32_16x16x32_bf16 v[108:111], v[212:215], v[140:143], v[108:111]\n"
    "s_nop 0\n"
    "ds_read_b64_tr_b16 v[212:213], v253 offset:50688\n"
    "ds_read_b64_tr_b16 v[214:215], v253 offset:54784\n"
    "v_mfma_f32_16x16x32_bf16 v[220:223], v[176:179], a[96:99], v[172:175]\n"
    "v_exp_f32 v218, v218\n"
    "v_mfma_f32_16x16x32_bf16 v[220:223], v[180:183], a[100:103], v[220:223]\n"
    "v_exp_f32 v219, v219\n"
    "v_mfma_f32_16x16x32_bf16 v[220:223], v[184:187], a[104:107], v[220:223]\n"
    "s_add_u32 s44, s44, 8192\n"
    "ds_read_b128 v[176:179], v251 offset:49152\n"
    "v_add_f32 v226, v226, v216\n"
    "v_mfma_f32_16x16x32_bf16 v[220:223], v[188:191], a[108:111], v[220:223]\n"
    "s_add_u32 s45, s45, 256\n"
    "ds_read_b128 v[180:183], v251 offset:49664\n"
    "v_add_f32 v226, v226, v217\n"
    "v_mfma_f32_16x16x32_bf16 v[220:223], v[192:195], a[112:115], v[220:223]\n"
    "s_add_u32 s46, s46, 256\n"
    "ds_read_b128 v[184:187], v251 offset:51200\n"
    "v_cvt_pk_bf16_f32 v154, v216, v217\n"
    "v_mfma_f32_16x16x32_bf16 v[220:223], v[196:199], a[116:119], v[220:223]\n"
    "ds_read_b128 v[188:191], v251 offset:51712\n"
    "v_add_f32 v226, v226, v218\n"
    "v_mfma_f32_16x16x32_bf16 v[220:223], v[200:203], a[120:123], v[220:223]\n"
    "s_cmp_lg_u32 s49, 0\n"
    "ds_read_b128 v[192:195], v251 offset:53248\n"
    "v_add_f32 v226, v226, v219\n"
    "v_mfma_f32_16x16x32_bf16 v[220:223], v[204:207], a[124:127], v[220:223]\n"
    "s_waitcnt lgkmcnt(5)\n"
    "ds_read_b128 v[196:199], v251 offset:53760\n"
    "v_cvt_pk_bf16_f32 v155, v218, v219\n"
    "v_mfma_f32_16x16x32_bf16 a[248:251], v[212:215], v[128:131], a[248:251]\n"
    "ds_read_b128 v[200:203], v251 offset:55296\n"
    "v_mfma_f32_16x16x32_bf16 a[240:243], v[208:211], v[128:131], a[240:243]\n"
    "ds_read_b128 v[204:207], v251 offset:55808\n"
    "v_mfma_f32_16x16x32_bf16 a[244:247], v[208:211], v[132:135], a[244:247]\n"
    "s_waitcnt vmcnt(13) lgkmcnt(8)\n"
    "s_barrier\n"
    "v_mfma_f32_16x16x32_bf16 a[252:255], v[212:215], v[132:135], a[252:255]\n"
    "v_mfma_f32_16x16x32_bf16 v[120:123], v[212:215], v[136:139], v[120:123]\n"
    "v_exp_f32 v220, v220\n"
    "v_mfma_f32_16x16x32_bf16 v[112:115], v[208:211], v[136:139], v[112:115]\n"
    "v_exp_f32 v221, v221\n"
    "v_mfma_f32_16x16x32_bf16 v[116:119], v[208:211], v[140:143], v[116:119]\n"
    "s_nop 0\n"
    "ds_read_b64_tr_b16 v[208:209], v252 offset:0\n"
    "ds_read_b64_tr_b16 v[210:211], v252 offset:4096\n"
    "v_mfma_f32_16x16x32_bf16 v[124:127], v[212:215], v[140:143], v[124:127]\n"
    "s_waitcnt lgkmcnt(6)\n"
    "ds_read_b64_tr_b16 v[212:213], v253 offset:0\n"
    "ds_read_b64_tr_b16 v[214:215], v253 offset:4096\n"
    "s_cbranch_scc1 L_even0_%=\n"
    "s_branch L_tail_%=\n"
    "L_even1_%=:\n"
    "v_mfma_f32_16x16x32_bf16 v[216:219], v[176:179], a[0:3], v[160:163]\n"
    "v_exp_f32 v222, v222\n"
    "v_mfma_f32_16x16x32_bf16 v[216:219], v[180:183], a[4:7], v[216:219]\n"
    "s_add_u32 m0, s47, 32768\n"
    "v_exp_f32 v223, v223\n"
    "v_mfma_f32_16x16x32_bf16 v[216:219], v[184:187], a[8:11], v[216:219]\n"
    "buffer_load_dwordx4 v254, s[36:39], s44 offen offset:0 nt lds\n"
    "v_mfma_f32_16x16x32_bf16 v[216:219], v[188:191], a[12:15], v[216:219]\n"
    "s_waitcnt lgkmcnt(4)\n"
    "v_add_f32 v227, v227, v220\n"
    "v_add_f32 v227, v227, v221\n"
    "v_mfma_f32_16x16x32_bf16 v[216:219], v[192:195], a[16:19], v[216:219]\n"
    "v_cvt_pk_bf16_f32 v158, v220, v221\n"
    "v_add_f32 v227, v227, v222\n"
    "v_mfma_f32_16x16x32_bf16 v[216:219], v[196:199], a[20:23], v[216:219]\n"
    "s_add_u32 m0, s47, 34688\n"
    "v_add_f32 v227, v227, v223\n"
    "v_cvt_pk_bf16_f32 v159, v222, v223\n"
    "v_mfma_f32_16x16x32_bf16 v[216:219], v[200:203], a[24:27], v[216:219]\n"
    "buffer_load_dwordx4 v254, s[36:39], s44 offen offset:128 nt lds\n"
    "v_mfma_f32_16x16x32_bf16 v[216:219], v[204:207], a[28:31], v[216:219]\n"
    "s_waitcnt lgkmcnt(0)\n"
    "v_mfma_f32_16x16x32_bf16 a[136:139], v[212:215], v[144:147], a[136:139]\n"
    "v_mfma_f32_16x16x32_bf16 a[128:131], v[208:211], v[144:147], a[128:131]\n"
    "s_add_u32 m0, s47, 36608\n"
    "v_mfma_f32_16x16x32_bf16 a[132:135], v[208:211], v[148:151], a[132:135]\n"
    "buffer_load_dwordx4 v254, s[36:39], s44 offen offset:256 nt lds\n"
    "v_mfma_f32_16x16x32_bf16 a[140:143], v[212:215], v[148:151], a[140:143]\n"
    "v_mfma_f32_16x16x32_bf16 v[8:11], v[212:215], v[152:155], v[8:11]\n"
    "v_exp_f32 v216, v216\n"
    "v_mfma_f32_16x16x32_bf16 v[0:3], v[208:211], v[152:155], v[0:3]\n"
    "v_exp_f32 v217, v217\n"
    "v_mfma_f32_16x16x32_bf16 v[4:7], v[208:211], v[156:159], v[4:7]\n"
    "s_nop 0\n"
    "ds_read_b64_tr_b16 v[208:209], v252 offset:512\n"
    "ds_read_b64_tr_b16 v[210:211], v252 offset:4608\n"
    "v_mfma_f32_16x16x32_bf16 v[12:15], v[212:215], v[156:159], v[12:15]\n"
    "s_nop 0\n"
    "ds_read_b64_tr_b16 v[212:213], v253 offset:512\n"
    "ds_read_b64_tr_b16 v[214:215], v253 offset:4608\n"
    "v_mfma_f32_16x16x32_bf16 v[220:223], v[176:179], a[32:35], v[164:167]\n"
    "v_exp_f32 v218, v218\n"
    "v_mfma_f32_16x16x32_bf16 v[220:223], v[180:183], a[36:39], v[220:223]\n"
    "s_add_u32 m0, s47, 38528\n"
    "v_exp_f32 v219, v219\n"
    "v_mfma_f32_16x16x32_bf16 v[220:223], v[184:187], a[40:43], v[220:223]\n"
    "buffer_load_dwordx4 v254, s[36:39], s44 offen offset:384 nt lds\n"
    "v_mfma_f32_16x16x32_bf16 v[220:223], v[188:191], a[44:47], v[220:223]\n"
    "v_add_f32 v224, v224, v216\n"
    "v_add_f32 v224, v224, v217\n"
    "v_mfma_f32_16x16x32_bf16 v[220:223], v[192:195], a[48:51], v[220:223]\n"
    "v_cvt_pk_bf16_f32 v128, v216, v217\n"
    "v_add_f32 v224, v224, v218\n"
    "v_mfma_f32_16x16x32_bf16 v[220:223], v[196:199], a[52:55], v[220:223]\n"
    "v_add_f32 v224, v224, v219\n"
    "v_cvt_pk_bf16_f32 v129, v218, v219\n"
    "v_mfma_f32_16x16x32_bf16 v[220:223], v[200:203], a[56:59], v[220:223]\n"
    "global_load_dword v247, v[248:249], off sc1\n"
    "v_mfma_f32_16x16x32_bf16 v[220:223], v[204:207], a[60:63], v[220:223]\n"
    "s_waitcnt lgkmcnt(0)\n"
    "v_mfma_f32_16x16x32_bf16 a[152:155], v[212:215], v[144:147], a[152:155]\n"
    "v_lshl_add_u64 v[248:249], v[248:249], 0, v[244:245]\n"
    "v_mfma_f32_16x16x32_bf16 a[144:147], v[208:211], v[144:147], a[144:147]\n"
    "s_add_u32 m0, s48, 49152\n"
    "v_mfma_f32_16x16x32_bf16 a[148:151], v[208:211], v[148:151], a[148:151]\n"
    "buffer_load_dwordx4 v255, s[40:43], s45 offen offset:0 nt lds\n"
    "v_mfma_f32_16x16x32_bf16 a[156:159], v[212:215], v[148:151], a[156:159]\n"
    "v_mfma_f32_16x16x32_bf16 v[24:27], v[212:215], v[152:155], v[24:27]\n"
    "v_exp_f32 v220, v220\n"
    "v_mfma_f32_16x16x32_bf16 v[16:19], v[208:211], v[152:155], v[16:19]\n"
    "v_exp_f32 v221, v221\n"
    "v_mfma_f32_16x16x32_bf16 v[20:23], v[208:211], v[156:159], v[20:23]\n"
    "s_nop 0\n"
    "ds_read_b64_tr_b16 v[208:209], v252 offset:1024\n"
    "ds_read_b64_tr_b16 v[210:211], v252 offset:5120\n"
    "v_mfma_f32_16x16x32_bf16 v[28:31], v[212:215], v[156:159], v[28:31]\n"
    "s_nop 0\n"
    "ds_read_b64_tr_b16 v[212:213], v253 offset:1024\n"
    "ds_read_b64_tr_b16 v[214:215], v253 offset:5120\n"
    "v_mfma_f32_16x16x32_bf16 v[216:219], v[176:179], a[64:67], v[168:171]\n"
    "v_exp_f32 v222, v222\n"
    "v_mfma_f32_16x16x32_bf16 v[216:219], v[180:183], a[68:71], v[216:219]\n"
    "s_add_u32 m0, s48, 50048\n"
    "v_exp_f32 v223, v223\n"
    "v_mfma_f32_16x16x32_bf16 v[216:219], v[184:187], a[72:75], v[216:219]\n"
    "buffer_load_dwordx4 v255, s[40:43], s45 offen offset:128 nt lds\n"
    "v_mfma_f32_16x16x32_bf16 v[216:219], v[188:191], a[76:79], v[216:219]\n"
    "v_add_f32 v225, v225, v220\n"
    "v_add_f32 v225, v225, v221\n"
    "v_mfma_f32_16x16x32_bf16 v[216:219], v[192:195], a[80:83], v[216:219]\n"
    "v_cvt_pk_bf16_f32 v132, v220, v221\n"
    "v_add_f32 v225, v225, v222\n"
    "v_mfma_f32_16x16x32_bf16 v[216:219], v[196:199], a[84:87], v[216:219]\n"
    "s_add_u32 m0, s48, 51200\n"
    "v_add_f32 v225, v225, v223\n"
    "v_cvt_pk_bf16_f32 v133, v222, v223\n"
    "v_mfma_f32_16x16x32_bf16 v[216:219], v[200:203], a[88:91], v[216:219]\n"
    "buffer_load_dwordx4 v255, s[40:43], s46 offen offset:0 nt lds\n"
    "v_mfma_f32_16x16x32_bf16 v[216:219], v[204:207], a[92:95], v[216:219]\n"
    "s_waitcnt lgkmcnt(0)\n"
    "v_mfma_f32_16x16x32_bf16 a[168:171], v[212:215], v[144:147], a[168:171]\n"
    "v_mfma_f32_16x16x32_bf16 a[160:163], v[208:211], v[144:147], a[160:163]\n"
    "s_add_u32 m0, s48, 52096\n"
    "v_mfma_f32_16x16x32_bf16 a[164:167], v[208:211], v[148:151], a[164:167]\n"
    "buffer_load_dwordx4 v255, s[40:43], s46 offen offset:128 nt lds\n"
    "v_mfma_f32_16x16x32_bf16 a[172:175], v[212:215], v[148:151], a[172:175]\n"
    "v_mfma_f32_16x16x32_bf16 v[40:43], v[212:215], v[152:155], v[40:43]\n"
    "v_exp_f32 v216, v216\n"
    "v_mfma_f32_16x16x32_bf16 v[32:35], v[208:211], v[152:155], v[32:35]\n"
    "v_exp_f32 v217, v217\n"
    "v_mfma_f32_16x16x32_bf16 v[36:39], v[208:211], v[156:159], v[36:39]\n"
    "s_nop 0\n"
    "ds_read_b64_tr_b16 v[208:209], v252 offset:1536\n"
    "ds_read_b64_tr_b16 v[210:211], v252 offset:5632\n"
    "v_mfma_f32_16x16x32_bf16 v[44:47], v[212:215], v[156:159], v[44:47]\n"
    "s_nop 0\n"
    "ds_read_b64_tr_b16 v[212:213], v253 offset:1536\n"
    "ds_read_b64_tr_b16 v[214:215], v253 offset:5632\n"
    "v_mfma_f32_16x16x32_bf16 v[220:223], v[176:179], a[96:99], v[172:175]\n"
    "v_exp_f32 v218, v218\n"
    "v_mfma_f32_16x16x32_bf16 v[220:223], v[180:183], a[100:103], v[220:223]\n"
    "v_exp_f32 v219, v219\n"
    "v_mfma_f32_16x16x32_bf16 v[220:223], v[184:187], a[104:107], v[220:223]\n"
    "s_add_u32 s44, s44, 24576\n"
    "ds_read_b128 v[176:179], v251 offset:0\n"
    "v_add_f32 v226, v226, v216\n"
    "v_mfma_f32_16x16x32_bf16 v[220:223], v[188:191], a[108:111], v[220:223]\n"
    "s_add_u32 s45, s45, 32512\n"
    "ds_read_b128 v[180:183], v251 offset:512\n"
    "v_add_f32 v226, v226, v217\n"
    "v_mfma_f32_16x16x32_bf16 v[220:223], v[192:195], a[112:115], v[220:223]\n"
    "s_add_u32 s46, s46, 32512\n"
    "ds_read_b128 v[184:187], v251 offset:2048\n"
    "v_cvt_pk_bf16_f32 v136, v216, v217\n"
    "v_mfma_f32_16x16x32_bf16 v[220:223], v[196:199], a[116:119], v[220:223]\n"
    "s_waitcnt lgkmcnt(3)\n"
    "ds_read_b128 v[188:191], v251 offset:2560\n"
    "v_add_f32 v226, v226, v218\n"
    "v_mfma_f32_16x16x32_bf16 v[220:223], v[200:203], a[120:123], v[220:223]\n"
    "ds_read_b128 v[192:195], v251 offset:4096\n"
    "v_add_f32 v226, v226, v219\n"
    "v_mfma_f32_16x16x32_bf16 v[220:223], v[204:207], a[124:127], v[220:223]\n"
    "s_waitcnt vmcnt(13) lgkmcnt(5)\n"
    "ds_read_b128 v[196:199], v251 offset:4608\n"
    "v_cvt_pk_bf16_f32 v137, v218, v219\n"
    "v_mfma_f32_16x16x32_bf16 a[184:187], v[212:215], v[144:147], a[184:187]\n"
    "s_barrier\n"
    "ds_read_b128 v[200:203], v251 offset:6144\n"
    "v_mfma_f32_16x16x32_bf16 a[176:179], v[208:211], v[144:147], a[176:179]\n"
    "ds_read_b128 v[204:207], v251 offset:6656\n"
    "v_mfma_f32_16x16x32_bf16 a[180:183], v[208:211], v[148:151], a[180:183]\n"
    "v_mfma_f32_16x16x32_bf16 a[188:191], v[212:215], v[148:151], a[188:191]\n"
    "v_mfma_f32_16x16x32_bf16 v[56:59], v[212:215], v[152:155], v[56:59]\n"
    "v_exp_f32 v220, v220\n"
    "v_mfma_f32_16x16x32_bf16 v[48:51], v[208:211], v[152:155], v[48:51]\n"
    "v_exp_f32 v221, v221\n"
    "v_mfma_f32_16x16x32_bf16 v[52:55], v[208:211], v[156:159], v[52:55]\n"
    "s_nop 0\n"
    "ds_read_b64_tr_b16 v[208:209], v252 offset:16384\n"
    "ds_read_b64_tr_b16 v[210:211], v252 offset:20480\n"
    "v_mfma_f32_16x16x32_bf16 v[60:63], v[212:215], v[156:159], v[60:63]\n"
    "s_waitcnt lgkmcnt(6)\n"
    "ds_read_b64_tr_b16 v[212:213], v253 offset:16384\n"
    "ds_read_b64_tr_b16 v[214:215], v253 offset:20480\n"
    "v_mfma_f32_16x16x32_bf16 v[216:219], v[176:179], a[0:3], v[160:163]\n"
    "v_exp_f32 v222, v222\n"
    "v_mfma_f32_16x16x32_bf16 v[216:219], v[180:183], a[4:7], v[216:219]\n"
    "s_add_u32 m0, s47, 49152\n"
    "v_exp_f32 v223, v223\n"
    "v_mfma_f32_16x16x32_bf16 v[216:219], v[184:187], a[8:11], v[216:219]\n"
    "buffer_load_dwordx4 v254, s[36:39], s44 offen offset:0 nt lds\n"
    "v_mfma_f32_16x16x32_bf16 v[216:219], v[188:191], a[12:15], v[216:219]\n"
    "s_waitcnt lgkmcnt(4)\n"
    "v_add_f32 v227, v227, v220\n"
    "v_add_f32 v227, v227, v221\n"
    "v_mfma_f32_16x16x32_bf16 v[216:219], v[192:195], a[16:19], v[216:219]\n"
    "v_cvt_pk_bf16_f32 v140, v220, v221\n"
    "v_add_f32 v227, v227, v222\n"
    "v_mfma_f32_16x16x32_bf16 v[216:219], v[196:199], a[20:23], v[216:219]\n"
    "s_add_u32 m0, s47, 51072\n"
    "v_add_f32 v227, v227, v223\n"
    "v_cvt_pk_bf16_f32 v141, v222, v223\n"
    "v_mfma_f32_16x16x32_bf16 v[216:219], v[200:203], a[24:27], v[216:219]\n"
    "buffer_load_dwordx4 v254, s[36:39], s44 offen offset:128 nt lds\n"
    "v_mfma_f32_16x16x32_bf16 v[216:219], v[204:207], a[28:31], v[216:219]\n"
    "s_waitcnt lgkmcnt(0)\n"
    "v_mfma_f32_16x16x32_bf16 a[200:203], v[212:215], v[144:147], a[200:203]\n"
    "v_mfma_f32_16x16x32_bf16 a[192:195], v[208:211], v[144:147], a[192:195]\n"
    "s_add_u32 m0, s47, 52992\n"
    "v_mfma_f32_16x16x32_bf16 a[196:199], v[208:211], v[148:151], a[196:199]\n"
    "buffer_load_dwordx4 v254, s[36:39], s44 offen offset:256 nt lds\n"
    "v_mfma_f32_16x16x32_bf16 a[204:207], v[212:215], v[148:151], a[204:207]\n"
    "v_mfma_f32_16x16x32_bf16 v[72:75], v[212:215], v[152:155], v[72:75]\n"
    "v_exp_f32 v216, v216\n"
    "v_mfma_f32_16x16x32_bf16 v[64:67], v[208:211], v[152:155], v[64:67]\n"
    "v_exp_f32 v217, v217\n"
    "v_mfma_f32_16x16x32_bf16 v[68:71], v[208:211], v[156:159], v[68:71]\n"
    "s_nop 0\n"
    "ds_read_b64_tr_b16 v[208:209], v252 offset:16896\n"
    "ds_read_b64_tr_b16 v[210:211], v252 offset:20992\n"
    "v_mfma_f32_16x16x32_bf16 v[76:79], v[212:215], v[156:159], v[76:79]\n"
    "s_nop 0\n"
    "ds_read_b64_tr_b16 v[212:213], v253 offset:16896\n"
    "ds_read_b64_tr_b16 v[214:215], v253 offset:20992\n"
    "v_mfma_f32_16x16x32_bf16 v[220:223], v[176:179], a[32:35], v[164:167]\n"
    "v_exp_f32 v218, v218\n"
    "v_mfma_f32_16x16x32_bf16 v[220:223], v[180:183], a[36:39], v[220:223]\n"
    "s_add_u32 m0, s47, 54912\n"
    "v_exp_f32 v219, v219\n"
    "v_mfma_f32_16x16x32_bf16 v[220:223], v[184:187], a[40:43], v[220:223]\n"
    "buffer_load_dwordx4 v254, s[36:39], s44 offen offset:384 nt lds\n"
    "v_mfma_f32_16x16x32_bf16 v[220:223], v[188:191], a[44:47], v[220:223]\n"
    "v_add_f32 v224, v224, v216\n"
    "v_add_f32 v224, v224, v217\n"
    "v_mfma_f32_16x16x32_bf16 v[220:223], v[192:195], a[48:51], v[220:223]\n"
    "v_cvt_pk_bf16_f32 v130, v216, v217\n"
    "v_add_f32 v224, v224, v218\n"
    "v_mfma_f32_16x16x32_bf16 v[220:223], v[196:199], a[52:55], v[220:223]\n"
    "v_add_f32 v224, v224, v219\n"
    "v_cvt_pk_bf16_f32 v131, v218, v219\n"
    "v_mfma_f32_16x16x32_bf16 v[220:223], v[200:203], a[56:59], v[220:223]\n"
    "v_mfma_f32_16x16x32_bf16 v[220:223], v[204:207], a[60:63], v[220:223]\n"
    "s_waitcnt lgkmcnt(0)\n"
    "v_mfma_f32_16x16x32_bf16 a[216:219], v[212:215], v[144:147], a[216:219]\n"
    "v_mfma_f32_16x16x32_bf16 a[208:211], v[208:211], v[144:147], a[208:211]\n"
    "s_add_u32 m0, s48, 0\n"
    "v_mfma_f32_16x16x32_bf16 a[212:215], v[208:211], v[148:151], a[212:215]\n"
    "buffer_load_dwordx4 v255, s[40:43], s45 offen offset:0 nt lds\n"
    "v_mfma_f32_16x16x32_bf16 a[220:223], v[212:215], v[148:151], a[220:223]\n"
    "v_mfma_f32_16x16x32_bf16 v[88:91], v[212:215], v[152:155], v[88:91]\n"
    "v_exp_f32 v220, v220\n"
    "v_mfma_f32_16x16x32_bf16 v[80:83], v[208:211], v[152:155], v[80:83]\n"
    "v_exp_f32 v221, v221\n"
    "v_mfma_f32_16x16x32_bf16 v[84:87], v[208:211], v[156:159], v[84:87]\n"
    "s_nop 0\n"
    "ds_read_b64_tr_b16 v[208:209], v252 offset:17408\n"
    "ds_read_b64_tr_b16 v[210:211], v252 offset:21504\n"
    "v_mfma_f32_16x16x32_bf16 v[92:95], v[212:215], v[156:159], v[92:95]\n"
    "s_nop 0\n"
    "ds_read_b64_tr_b16 v[212:213], v253 offset:17408\n"
    "ds_read_b64_tr_b16 v[214:215], v253 offset:21504\n"
    "v_mfma_f32_16x16x32_bf16 v[216:219], v[176:179], a[64:67], v[168:171]\n"
    "v_exp_f32 v222, v222\n"
    "v_mfma_f32_16x16x32_bf16 v[216:219], v[180:183], a[68:71], v[216:219]\n"
    "s_add_u32 m0, s48, 896\n"
    "v_exp_f32 v223, v223\n"
    "v_mfma_f32_16x16x32_bf16 v[216:219], v[184:187], a[72:75], v[216:219]\n"
    "buffer_load_dwordx4 v255, s[40:43], s45 offen offset:128 nt lds\n"
    "v_mfma_f32_16x16x32_bf16 v[216:219], v[188:191], a[76:79], v[216:219]\n"
    "v_add_f32 v225, v225, v220\n"
    "v_add_f32 v225, v225, v221\n"
    "v_mfma_f32_16x16x32_bf16 v[216:219], v[192:195], a[80:83], v[216:219]\n"
    "v_cvt_pk_bf16_f32 v134, v220, v221\n"
    "v_add_f32 v225, v225, v222\n"
    "v_mfma_f32_16x16x32_bf16 v[216:219], v[196:199], a[84:87], v[216:219]\n"
    "s_add_u32 m0, s48, 2048\n"
    "v_add_f32 v225, v225, v223\n"
    "v_cvt_pk_bf16_f32 v135, v222, v223\n"
    "v_mfma_f32_16x16x32_bf16 v[216:219], v[200:203], a[88:91], v[216:219]\n"
    "buffer_load_dwordx4 v255, s[40:43], s46 offen offset:0 nt lds\n"
    "v_mfma_f32_16x16x32_bf16 v[216:219], v[204:207], a[92:95], v[216:219]\n"
    "s_waitcnt lgkmcnt(0)\n"
    "v_mfma_f32_16x16x32_bf16 a[232:235], v[212:215], v[144:147], a[232:235]\n"
    "v_mfma_f32_16x16x32_bf16 a[224:227], v[208:211], v[144:147], a[224:227]\n"
    "s_add_u32 m0, s48, 2944\n"
    "v_mfma_f32_16x16x32_bf16 a[228:231], v[208:211], v[148:151], a[228:231]\n"
    "buffer_load_dwordx4 v255, s[40:43], s46 offen offset:128 nt lds\n"
    "v_mfma_f32_16x16x32_bf16 a[236:239], v[212:215], v[148:151], a[236:239]\n"
    "v_mfma_f32_16x16x32_bf16 v[104:107], v[212:215], v[152:155], v[104:107]\n"
    "v_exp_f32 v216, v216\n"
    "v_mfma_f32_16x16x32_bf16 v[96:99], v[208:211], v[152:155], v[96:99]\n"
    "v_exp_f32 v217, v217\n"
    "v_mfma_f32_16x16x32_bf16 v[100:103], v[208:211], v[156:159], v[100:103]\n"
    "s_nop 0\n"
    "ds_read_b64_tr_b16 v[208:209], v252 offset:17920\n"
    "ds_read_b64_tr_b16 v[210:211], v252 offset:22016\n"
    "v_mfma_f32_16x16x32_bf16 v[108:111], v[212:215], v[156:159], v[108:111]\n"
    "s_nop 0\n"
    "ds_read_b64_tr_b16 v[212:213], v253 offset:17920\n"
    "ds_read_b64_tr_b16 v[214:215], v253 offset:22016\n"
    "v_mfma_f32_16x16x32_bf16 v[220:223], v[176:179], a[96:99], v[172:175]\n"
    "v_exp_f32 v218, v218\n"
    "v_mfma_f32_16x16x32_bf16 v[220:223], v[180:183], a[100:103], v[220:223]\n"
    "v_exp_f32 v219, v219\n"
    "v_mfma_f32_16x16x32_bf16 v[220:223], v[184:187], a[104:107], v[220:223]\n"
    "s_add_u32 s44, s44, 8192\n"
    "ds_read_b128 v[176:179], v251 offset:16384\n"
    "v_add_f32 v226, v226, v216\n"
    "v_mfma_f32_16x16x32_bf16 v[220:223], v[188:191], a[108:111], v[220:223]\n"
    "s_add_u32 s45, s45, 256\n"
    "ds_read_b128 v[180:183], v251 offset:16896\n"
    "v_add_f32 v226, v226, v217\n"
    "v_mfma_f32_16x16x32_bf16 v[220:223], v[192:195], a[112:115], v[220:223]\n"
    "s_add_u32 s46, s46, 256\n"
    "ds_read_b128 v[184:187], v251 offset:18432\n"
    "v_cvt_pk_bf16_f32 v138, v216, v217\n"
    "v_mfma_f32_16x16x32_bf16 v[220:223], v[196:199], a[116:119], v[220:223]\n"
    "s_waitcnt lgkmcnt(3)\n"
    "ds_read_b128 v[188:191], v251 offset:18944\n"
    "v_add_f32 v226, v226, v218\n"
    "v_mfma_f32_16x16x32_bf16 v[220:223], v[200:203], a[120:123], v[220:223]\n"
    "ds_read_b128 v[192:195], v251 offset:20480\n"
    "v_add_f32 v226, v226, v219\n"
    "v_mfma_f32_16x16x32_bf16 v[220:223], v[204:207], a[124:127], v[220:223]\n"
    "s_waitcnt vmcnt(13) lgkmcnt(5)\n"
    "ds_read_b128 v[196:199], v251 offset:20992\n"
    "v_cvt_pk_bf16_f32 v139, v218, v219\n"
    "v_mfma_f32_16x16x32_bf16 a[248:251], v[212:215], v[144:147], a[248:251]\n"
    "s_barrier\n"
    "ds_read_b128 v[200:203], v251 offset:22528\n"
    "v_mfma_f32_16x16x32_bf16 a[240:243], v[208:211], v[144:147], a[240:243]\n"
    "ds_read_b128 v[204:207], v251 offset:23040\n"
    "v_mfma_f32_16x16x32_bf16 a[244:247], v[208:211], v[148:151], a[244:247]\n"
    "v_mfma_f32_16x16x32_bf16 a[252:255], v[212:215], v[148:151], a[252:255]\n"
    "v_mfma_f32_16x16x32_bf16 v[120:123], v[212:215], v[152:155], v[120:123]\n"
    "v_exp_f32 v220, v220\n"
    "v_mfma_f32_16x16x32_bf16 v[112:115], v[208:211], v[152:155], v[112:115]\n"
    "v_exp_f32 v221, v221\n"
    "v_mfma_f32_16x16x32_bf16 v[116:119], v[208:211], v[156:159], v[116:119]\n"
    "s_nop 0\n"
    "ds_read_b64_tr_b16 v[208:209], v252 offset:32768\n"
    "ds_read_b64_tr_b16 v[210:211], v252 offset:36864\n"
    "v_mfma_f32_16x16x32_bf16 v[124:127], v[212:215], v[156:159], v[124:127]\n"
    "s_waitcnt lgkmcnt(6)\n"
    "ds_read_b64_tr_b16 v[212:213], v253 offset:32768\n"
    "ds_read_b64_tr_b16 v[214:215], v253 offset:36864\n"
    "v_mfma_f32_16x16x32_bf16 v[216:219], v[176:179], a[0:3], v[160:163]\n"
    "v_exp_f32 v222, v222\n"
    "v_mfma_f32_16x16x32_bf16 v[216:219], v[180:183], a[4:7], v[216:219]\n"
    "s_add_u32 m0, s47, 0\n"
    "v_exp_f32 v223, v223\n"
    "v_mfma_f32_16x16x32_bf16 v[216:219], v[184:187], a[8:11], v[216:219]\n"
    "buffer_load_dwordx4 v254, s[36:39], s44 offen offset:0 nt lds\n"
    "v_mfma_f32_16x16x32_bf16 v[216:219], v[188:191], a[12:15], v[216:219]\n"
    "s_waitcnt lgkmcnt(4)\n"
    "v_add_f32 v227, v227, v220\n"
    "v_add_f32 v227, v227, v221\n"
    "v_mfma_f32_16x16x32_bf16 v[216:219], v[192:195], a[16:19], v[216:219]\n"
    "v_cvt_pk_bf16_f32 v142, v220, v221\n"
    "v_add_f32 v227, v227, v222\n"
    "v_mfma_f32_16x16x32_bf16 v[216:219], v[196:199], a[20:23], v[216:219]\n"
    "s_add_u32 m0, s47, 1920\n"
    "v_add_f32 v227, v227, v223\n"
    "v_cvt_pk_bf16_f32 v143, v222, v223\n"
    "v_mfma_f32_16x16x32_bf16 v[216:219], v[200:203], a[24:27], v[216:219]\n"
    "buffer_load_dwordx4 v254, s[36:39], s44 offen offset:128 nt lds\n"
    "v_mfma_f32_16x16x32_bf16 v[216:219], v[204:207], a[28:31], v[216:219]\n"
    "s_waitcnt lgkmcnt(0)\n"
    "v_mfma_f32_16x16x32_bf16 a[136:139], v[212:215], v[128:131], a[136:139]\n"
    "v_mfma_f32_16x16x32_bf16 a[128:131], v[208:211], v[128:131], a[128:131]\n"
    "s_add_u32 m0, s47, 3840\n"
    "v_mfma_f32_16x16x32_bf16 a[132:135], v[208:211], v[132:135], a[132:135]\n"
    "buffer_load_dwordx4 v254, s[36:39], s44 offen offset:256 nt lds\n"
    "v_mfma_f32_16x16x32_bf16 a[140:143], v[212:215], v[132:135], a[140:143]\n"
    "v_mfma_f32_16x16x32_bf16 v[8:11], v[212:215], v[136:139], v[8:11]\n"
    "v_exp_f32 v216, v216\n"
    "v_mfma_f32_16x16x32_bf16 v[0:3], v[208:211], v[136:139], v[0:3]\n"
    "v_exp_f32 v217, v217\n"
    "v_mfma_f32_16x16x32_bf16 v[4:7], v[208:211], v[140:143], v[4:7]\n"
    "s_nop 0\n"
    "ds_read_b64_tr_b16 v[208:209], v252 offset:33280\n"
    "ds_read_b64_tr_b16 v[210:211], v252 offset:37376\n"
    "v_mfma_f32_16x16x32_bf16 v[12:15], v[212:215], v[140:143], v[12:15]\n"
    "s_nop 0\n"
    "ds_read_b64_tr_b16 v[212:213], v253 offset:33280\n"
    "ds_read_b64_tr_b16 v[214:215], v253 offset:37376\n"
    "v_mfma_f32_16x16x32_bf16 v[220:223], v[176:179], a[32:35], v[164:167]\n"
    "v_exp_f32 v218, v218\n"
    "v_mfma_f32_16x16x32_bf16 v[220:223], v[180:183], a[36:39], v[220:223]\n"
    "s_add_u32 m0, s47, 5760\n"
    "v_exp_f32 v219, v219\n"
    "v_mfma_f32_16x16x32_bf16 v[220:223], v[184:187], a[40:43], v[220:223]\n"
    "buffer_load_dwordx4 v254, s[36:39], s44 offen offset:384 nt lds\n"
    "v_mfma_f32_16x16x32_bf16 v[220:223], v[188:191], a[44:47], v[220:223]\n"
    "v_add_f32 v224, v224, v216\n"
    "v_add_f32 v224, v224, v217\n"
    "v_mfma_f32_16x16x32_bf16 v[220:223], v[192:195], a[48:51], v[220:223]\n"
    "v_cvt_pk_bf16_f32 v144, v216, v217\n"
    "v_add_f32 v224, v224, v218\n"
    "v_mfma_f32_16x16x32_bf16 v[220:223], v[196:199], a[52:55], v[220:223]\n"
    "v_add_f32 v224, v224, v219\n"
    "v_cvt_pk_bf16_f32 v145, v218, v219\n"
    "v_mfma_f32_16x16x32_bf16 v[220:223], v[200:203], a[56:59], v[220:223]\n"
    "global_load_dword v247, v[248:249], off sc1\n"
    "v_mfma_f32_16x16x32_bf16 v[220:223], v[204:207], a[60:63], v[220:223]\n"
    "s_waitcnt lgkmcnt(0)\n"
    "v_mfma_f32_16x16x32_bf16 a[152:155], v[212:215], v[128:131], a[152:155]\n"
    "v_lshl_add_u64 v[248:249], v[248:249], 0, v[244:245]\n"
    "v_mfma_f32_16x16x32_bf16 a[144:147], v[208:211], v[128:131], a[144:147]\n"
    "s_add_u32 m0, s48, 16384\n"
    "v_mfma_f32_16x16x32_bf16 a[148:151], v[208:211], v[132:135], a[148:151]\n"
    "buffer_load_dwordx4 v255, s[40:43], s45 offen offset:0 nt lds\n"
    "v_mfma_f32_16x16x32_bf16 a[156:159], v[212:215], v[132:135], a[156:159]\n"
    "v_mfma_f32_16x16x32_bf16 v[24:27], v[212:215], v[136:139], v[24:27]\n"
    "v_exp_f32 v220, v220\n"
    "v_mfma_f32_16x16x32_bf16 v[16:19], v[208:211], v[136:139], v[16:19]\n"
    "v_exp_f32 v221, v221\n"
    "v_mfma_f32_16x16x32_bf16 v[20:23], v[208:211], v[140:143], v[20:23]\n"
    "s_nop 0\n"
    "ds_read_b64_tr_b16 v[208:209], v252 offset:33792\n"
    "ds_read_b64_tr_b16 v[210:211], v252 offset:37888\n"
    "v_mfma_f32_16x16x32_bf16 v[28:31], v[212:215], v[140:143], v[28:31]\n"
    "s_nop 0\n"
    "ds_read_b64_tr_b16 v[212:213], v253 offset:33792\n"
    "ds_read_b64_tr_b16 v[214:215], v253 offset:37888\n"
    "v_mfma_f32_16x16x32_bf16 v[216:219], v[176:179], a[64:67], v[168:171]\n"
    "v_exp_f32 v222, v222\n"
    "v_mfma_f32_16x16x32_bf16 v[216:219], v[180:183], a[68:71], v[216:219]\n"
    "s_add_u32 m0, s48, 17280\n"
    "v_exp_f32 v223, v223\n"
    "v_mfma_f32_16x16x32_bf16 v[216:219], v[184:187], a[72:75], v[216:219]\n"
    "buffer_load_dwordx4 v255, s[40:43], s45 offen offset:128 nt lds\n"
    "v_mfma_f32_16x16x32_bf16 v[216:219], v[188:191], a[76:79], v[216:219]\n"
    "v_add_f32 v225, v225, v220\n"
    "v_add_f32 v225, v225, v221\n"
    "v_mfma_f32_16x16x32_bf16 v[216:219], v[192:195], a[80:83], v[216:219]\n"
    "v_cvt_pk_bf16_f32 v148, v220, v221\n"
    "v_add_f32 v225, v225, v222\n"
    "v_mfma_f32_16x16x32_bf16 v[216:219], v[196:199], a[84:87], v[216:219]\n"
    "s_add_u32 m0, s48, 18432\n"
    "v_add_f32 v225, v225, v223\n"
    "v_cvt_pk_bf16_f32 v149, v222, v223\n"
    "v_mfma_f32_16x16x32_bf16 v[216:219], v[200:203], a[88:91], v[216:219]\n"
    "buffer_load_dwordx4 v255, s[40:43], s46 offen offset:0 nt lds\n"
    "v_mfma_f32_16x16x32_bf16 v[216:219], v[204:207], a[92:95], v[216:219]\n"
    "s_waitcnt lgkmcnt(0)\n"
    "v_mfma_f32_16x16x32_bf16 a[168:171], v[212:215], v[128:131], a[168:171]\n"
    "v_mfma_f32_16x16x32_bf16 a[160:163], v[208:211], v[128:131], a[160:163]\n"
    "s_add_u32 m0, s48, 19328\n"
    "v_mfma_f32_16x16x32_bf16 a[164:167], v[208:211], v[132:135], a[164:167]\n"
    "buffer_load_dwordx4 v255, s[40:43], s46 offen offset:128 nt lds\n"
    "v_mfma_f32_16x16x32_bf16 a[172:175], v[212:215], v[132:135], a[172:175]\n"
    "v_mfma_f32_16x16x32_bf16 v[40:43], v[212:215], v[136:139], v[40:43]\n"
    "v_exp_f32 v216, v216\n"
    "v_mfma_f32_16x16x32_bf16 v[32:35], v[208:211], v[136:139], v[32:35]\n"
    "v_exp_f32 v217, v217\n"
    "v_mfma_f32_16x16x32_bf16 v[36:39], v[208:211], v[140:143], v[36:39]\n"
    "s_nop 0\n"
    "ds_read_b64_tr_b16 v[208:209], v252 offset:34304\n"
    "ds_read_b64_tr_b16 v[210:211], v252 offset:38400\n"
    "v_mfma_f32_16x16x32_bf16 v[44:47], v[212:215], v[140:143], v[44:47]\n"
    "s_nop 0\n"
    "ds_read_b64_tr_b16 v[212:213], v253 offset:34304\n"
    "ds_read_b64_tr_b16 v[214:215], v253 offset:38400\n"
    "v_mfma_f32_16x16x32_bf16 v[220:223], v[176:179], a[96:99], v[172:175]\n"
    "v_exp_f32 v218, v218\n"
    "v_mfma_f32_16x16x32_bf16 v[220:223], v[180:183], a[100:103], v[220:223]\n"
    "v_exp_f32 v219, v219\n"
    "v_mfma_f32_16x16x32_bf16 v[220:223], v[184:187], a[104:107], v[220:223]\n"
    "s_add_u32 s44, s44, 24576\n"
    "ds_read_b128 v[176:179], v251 offset:32768\n"
    "v_add_f32 v226, v226, v216\n"
    "v_mfma_f32_16x16x32_bf16 v[220:223], v[188:191], a[108:111], v[220:223]\n"
    "s_add_u32 s45, s45, 32512\n"
    "ds_read_b128 v[180:183], v251 offset:33280\n"
    "v_add_f32 v226, v226, v217\n"
    "v_mfma_f32_16x16x32_bf16 v[220:223], v[192:195], a[112:115], v[220:223]\n"
    "s_add_u32 s46, s46, 32512\n"
    "ds_read_b128 v[184:187], v251 offset:34816\n"
    "v_cvt_pk_bf16_f32 v152, v216, v217\n"
    "v_mfma_f32_16x16x32_bf16 v[220:223], v[196:199], a[116:119], v[220:223]\n"
    "s_waitcnt lgkmcnt(3)\n"
    "ds_read_b128 v[188:191], v251 offset:35328\n"
    "v_add_f32 v226, v226, v218\n"
    "v_mfma_f32_16x16x32_bf16 v[220:223], v[200:203], a[120:123], v[220:223]\n"
    "ds_read_b128 v[192:195], v251 offset:36864\n"
    "v_add_f32 v226, v226, v219\n"
    "v_mfma_f32_16x16x32_bf16 v[220:223], v[204:207], a[124:127], v[220:223]\n"
    "s_waitcnt vmcnt(13) lgkmcnt(5)\n"
    "ds_read_b128 v[196:199], v251 offset:37376\n"
    "v_cvt_pk_bf16_f32 v153, v218, v219\n"
    "v_mfma_f32_16x16x32_bf16 a[184:187], v[212:215], v[128:131], a[184:187]\n"
    "s_barrier\n"
    "ds_read_b128 v[200:203], v251 offset:38912\n"
    "v_mfma_f32_16x16x32_bf16 a[176:179], v[208:211], v[128:131], a[176:179]\n"
    "ds_read_b128 v[204:207], v251 offset:39424\n"
    "v_mfma_f32_16x16x32_bf16 a[180:183], v[208:211], v[132:135], a[180:183]\n"
    "v_mfma_f32_16x16x32_bf16 a[188:191], v[212:215], v[132:135], a[188:191]\n"
    "v_mfma_f32_16x16x32_bf16 v[56:59], v[212:215], v[136:139], v[56:59]\n"
    "v_exp_f32 v220, v220\n"
    "v_mfma_f32_16x16x32_bf16 v[48:51], v[208:211], v[136:139], v[48:51]\n"
    "v_exp_f32 v221, v221\n"
    "v_mfma_f32_16x16x32_bf16 v[52:55], v[208:211], v[140:143], v[52:55]\n"
    "s_nop 0\n"
    "ds_read_b64_tr_b16 v[208:209], v252 offset:49152\n"
    "ds_read_b64_tr_b16 v[210:211], v252 offset:53248\n"
    "v_mfma_f32_16x16x32_bf16 v[60:63], v[212:215], v[140:143], v[60:63]\n"
    "s_waitcnt lgkmcnt(6)\n"
    "ds_read_b64_tr_b16 v[212:213], v253 offset:49152\n"
    "ds_read_b64_tr_b16 v[214:215], v253 offset:53248\n"
    "v_mfma_f32_16x16x32_bf16 v[216:219], v[176:179], a[0:3], v[160:163]\n"
    "v_exp_f32 v222, v222\n"
    "v_mfma_f32_16x16x32_bf16 v[216:219], v[180:183], a[4:7], v[216:219]\n"
    "s_add_u32 m0, s47, 16384\n"
    "v_exp_f32 v223, v223\n"
    "v_mfma_f32_16x16x32_bf16 v[216:219], v[184:187], a[8:11], v[216:219]\n"
    "buffer_load_dwordx4 v254, s[36:39], s44 offen offset:0 nt lds\n"
    "v_mfma_f32_16x16x32_bf16 v[216:219], v[188:191], a[12:15], v[216:219]\n"
    "s_waitcnt lgkmcnt(4)\n"
    "v_add_f32 v227, v227, v220\n"
    "v_add_f32 v227, v227, v221\n"
    "v_mfma_f32_16x16x32_bf16 v[216:219], v[192:195], a[16:19], v[216:219]\n"
    "v_cvt_pk_bf16_f32 v156, v220, v221\n"
    "v_add_f32 v227, v227, v222\n"
    "v_mfma_f32_16x16x32_bf16 v[216:219], v[196:199], a[20:23], v[216:219]\n"
    "s_add_u32 m0, s47, 18304\n"
    "v_add_f32 v227, v227, v223\n"
    "v_cvt_pk_bf16_f32 v157, v222, v223\n"
    "v_mfma_f32_16x16x32_bf16 v[216:219], v[200:203], a[24:27], v[216:219]\n"
    "buffer_load_dwordx4 v254, s[36:39], s44 offen offset:128 nt lds\n"
    "v_mfma_f32_16x16x32_bf16 v[216:219], v[204:207], a[28:31], v[216:219]\n"
    "s_waitcnt lgkmcnt(0)\n"
    "v_mfma_f32_16x16x32_bf16 a[200:203], v[212:215], v[128:131], a[200:203]\n"
    "v_mfma_f32_16x16x32_bf16 a[192:195], v[208:211], v[128:131], a[192:195]\n"
    "s_add_u32 m0, s47, 20224\n"
    "v_mfma_f32_16x16x32_bf16 a[196:199], v[208:211], v[132:135], a[196:199]\n"
    "buffer_load_dwordx4 v254, s[36:39], s44 offen offset:256 nt lds\n"
    "v_mfma_f32_16x16x32_bf16 a[204:207], v[212:215], v[132:135], a[204:207]\n"
    "v_mfma_f32_16x16x32_bf16 v[72:75], v[212:215], v[136:139], v[72:75]\n"
    "v_exp_f32 v216, v216\n"
    "v_mfma_f32_16x16x32_bf16 v[64:67], v[208:211], v[136:139], v[64:67]\n"
    "v_exp_f32 v217, v217\n"
    "v_mfma_f32_16x16x32_bf16 v[68:71], v[208:211], v[140:143], v[68:71]\n"
    "s_nop 0\n"
    "ds_read_b64_tr_b16 v[208:209], v252 offset:49664\n"
    "ds_read_b64_tr_b16 v[210:211], v252 offset:53760\n"
    "v_mfma_f32_16x16x32_bf16 v[76:79], v[212:215], v[140:143], v[76:79]\n"
    "s_nop 0\n"
    "ds_read_b64_tr_b16 v[212:213], v253 offset:49664\n"
    "ds_read_b64_tr_b16 v[214:215], v253 offset:53760\n"
    "v_mfma_f32_16x16x32_bf16 v[220:223], v[176:179], a[32:35], v[164:167]\n"
    "v_exp_f32 v218, v218\n"
    "v_mfma_f32_16x16x32_bf16 v[220:223], v[180:183], a[36:39], v[220:223]\n"
    "s_add_u32 m0, s47, 22144\n"
    "v_exp_f32 v219, v219\n"
    "v_mfma_f32_16x16x32_bf16 v[220:223], v[184:187], a[40:43], v[220:223]\n"
    "buffer_load_dwordx4 v254, s[36:39], s44 offen offset:384 nt lds\n"
    "v_mfma_f32_16x16x32_bf16 v[220:223], v[188:191], a[44:47], v[220:223]\n"
    "v_add_f32 v224, v224, v216\n"
    "v_add_f32 v224, v224, v217\n"
    "v_mfma_f32_16x16x32_bf16 v[220:223], v[192:195], a[48:51], v[220:223]\n"
    "v_cvt_pk_bf16_f32 v146, v216, v217\n"
    "v_add_f32 v224, v224, v218\n"
    "v_mfma_f32_16x16x32_bf16 v[220:223], v[196:199], a[52:55], v[220:223]\n"
    "v_add_f32 v224, v224, v219\n"
    "v_cvt_pk_bf16_f32 v147, v218, v219\n"
    "v_mfma_f32_16x16x32_bf16 v[220:223], v[200:203], a[56:59], v[220:223]\n"
    "v_mfma_f32_16x16x32_bf16 v[220:223], v[204:207], a[60:63], v[220:223]\n"
    "s_waitcnt lgkmcnt(0)\n"
    "v_mfma_f32_16x16x32_bf16 a[216:219], v[212:215], v[128:131], a[216:219]\n"
    "v_mfma_f32_16x16x32_bf16 a[208:211], v[208:211], v[128:131], a[208:211]\n"
    "s_add_u32 m0, s48, 32768\n"
    "v_mfma_f32_16x16x32_bf16 a[212:215], v[208:211], v[132:135], a[212:215]\n"
    "buffer_load_dwordx4 v255, s[40:43], s45 offen offset:0 nt lds\n"
    "v_mfma_f32_16x16x32_bf16 a[220:223], v[212:215], v[132:135], a[220:223]\n"
    "v_mfma_f32_16x16x32_bf16 v[88:91], v[212:215], v[136:139], v[88:91]\n"
    "v_exp_f32 v220, v220\n"
    "v_mfma_f32_16x16x32_bf16 v[80:83], v[208:211], v[136:139], v[80:83]\n"
    "v_exp_f32 v221, v221\n"
    "v_mfma_f32_16x16x32_bf16 v[84:87], v[208:211], v[140:143], v[84:87]\n"
    "s_nop 0\n"
    "ds_read_b64_tr_b16 v[208:209], v252 offset:50176\n"
    "ds_read_b64_tr_b16 v[210:211], v252 offset:54272\n"
    "v_mfma_f32_16x16x32_bf16 v[92:95], v[212:215], v[140:143], v[92:95]\n"
    "s_nop 0\n"
    "ds_read_b64_tr_b16 v[212:213], v253 offset:50176\n"
    "ds_read_b64_tr_b16 v[214:215], v253 offset:54272\n"
    "v_mfma_f32_16x16x32_bf16 v[216:219], v[176:179], a[64:67], v[168:171]\n"
    "v_exp_f32 v222, v222\n"
    "v_mfma_f32_16x16x32_bf16 v[216:219], v[180:183], a[68:71], v[216:219]\n"
    "s_add_u32 m0, s48, 33664\n"
    "v_exp_f32 v223, v223\n"
    "v_mfma_f32_16x16x32_bf16 v[216:219], v[184:187], a[72:75], v[216:219]\n"
    "buffer_load_dwordx4 v255, s[40:43], s45 offen offset:128 nt lds\n"
    "v_mfma_f32_16x16x32_bf16 v[216:219], v[188:191], a[76:79], v[216:219]\n"
    "v_add_f32 v225, v225, v220\n"
    "v_add_f32 v225, v225, v221\n"
    "v_mfma_f32_16x16x32_bf16 v[216:219], v[192:195], a[80:83], v[216:219]\n"
    "v_cvt_pk_bf16_f32 v150, v220, v221\n"
    "v_add_f32 v225, v225, v222\n"
    "v_mfma_f32_16x16x32_bf16 v[216:219], v[196:199], a[84:87], v[216:219]\n"
    "s_add_u32 m0, s48, 34816\n"
    "v_add_f32 v225, v225, v223\n"
    "v_cvt_pk_bf16_f32 v151, v222, v223\n"
    "v_mfma_f32_16x16x32_bf16 v[216:219], v[200:203], a[88:91], v[216:219]\n"
    "buffer_load_dwordx4 v255, s[40:43], s46 offen offset:0 nt lds\n"
    "v_mfma_f32_16x16x32_bf16 v[216:219], v[204:207], a[92:95], v[216:219]\n"
    "s_waitcnt lgkmcnt(0)\n"
    "v_mfma_f32_16x16x32_bf16 a[232:235], v[212:215], v[128:131], a[232:235]\n"
    "s_sub_u32 s49, s49, 1\n"
    "v_mfma_f32_16x16x32_bf16 a[224:227], v[208:211], v[128:131], a[224:227]\n"
    "s_add_u32 m0, s48, 35712\n"
    "v_mfma_f32_16x16x32_bf16 a[228:231], v[208:211], v[132:135], a[228:231]\n"
    "buffer_load_dwordx4 v255, s[40:43], s46 offen offset:128 nt lds\n"
    "v_mfma_f32_16x16x32_bf16 a[236:239], v[212:215], v[132:135], a[236:239]\n"
    "v_mfma_f32_16x16x32_bf16 v[104:107], v[212:215], v[136:139], v[104:107]\n"
    "v_exp_f32 v216, v216\n"
    "v_mfma_f32_16x16x32_bf16 v[96:99], v[208:211], v[136:139], v[96:99]\n"
    "v_exp_f32 v217, v217\n"
    "v_mfma_f32_16x16x32_bf16 v[100:103], v[208:211], v[140:143], v[100:103]\n"
    "s_nop 0\n"
    "ds_read_b64_tr_b16 v[208:209], v252 offset:50688\n"
    "ds_read_b64_tr_b16 v[210:211], v252 offset:54784\n"
    "v_mfma_f32_16x16x32_bf16 v[108:111], v[212:215], v[140:143], v[108:111]\n"
    "s_nop 0\n"
    "ds_read_b64_tr_b16 v[212:213], v253 offset:50688\n"
    "ds_read_b64_tr_b16 v[214:215], v253 offset:54784\n"
    "v_mfma_f32_16x16x32_bf16 v[220:223], v[176:179], a[96:99], v[172:175]\n"
    "v_exp_f32 v218, v218\n"
    "v_mfma_f32_16x16x32_bf16 v[220:223], v[180:183], a[100:103], v[220:223]\n"
    "v_exp_f32 v219, v219\n"
    "v_mfma_f32_16x16x32_bf16 v[220:223], v[184:187], a[104:107], v[220:223]\n"
    "s_add_u32 s44, s44, 8192\n"
    "ds_read_b128 v[176:179], v251 offset:49152\n"
    "v_add_f32 v226, v226, v216\n"
    "v_mfma_f32_16x16x32_bf16 v[220:223], v[188:191], a[108:111], v[220:223]\n"
    "s_add_u32 s45, s45, 256\n"
    "ds_read_b128 v[180:183], v251 offset:49664\n"
    "v_add_f32 v226, v226, v217\n"
    "v_mfma_f32_16x16x32_bf16 v[220:223], v[192:195], a[112:115], v[220:223]\n"
    "s_add_u32 s46, s46, 256\n"
    "ds_read_b128 v[184:187], v251 offset:51200\n"
    "v_cvt_pk_bf16_f32 v154, v216, v217\n"
    "v_mfma_f32_16x16x32_bf16 v[220:223], v[196:199], a[116:119], v[220:223]\n"
    "s_waitcnt lgkmcnt(3)\n"
    "ds_read_b128 v[188:191], v251 offset:51712\n"
    "v_add_f32 v226, v226, v218\n"
    "v_mfma_f32_16x16x32_bf16 v[220:223], v[200:203], a[120:123], v[220:223]\n"
    "s_cmp_lg_u32 s49, 0\n"
    "ds_read_b128 v[192:195], v251 offset:53248\n"
    "v_add_f32 v226, v226, v219\n"
    "v_mfma_f32_16x16x32_bf16 v[220:223], v[204:207], a[124:127], v[220:223]\n"
    "s_waitcnt vmcnt(13) lgkmcnt(5)\n"
    "ds_read_b128 v[196:199], v251 offset:53760\n"
    "v_cvt_pk_bf16_f32 v155, v218, v219\n"
    "v_mfma_f32_16x16x32_bf16 a[248:251], v[212:215], v[128:131], a[248:251]\n"
    "s_barrier\n"
    "ds_read_b128 v[200:203], v251 offset:55296\n"
    "v_mfma_f32_16x16x32_bf16 a[240:243], v[208:211], v[128:131], a[240:243]\n"
    "ds_read_b128 v[204:207], v251 offset:55808\n"
    "v_mfma_f32_16x16x32_bf16 a[244:247], v[208:211], v[132:135], a[244:247]\n"
    "v_mfma_f32_16x16x32_bf16 a[252:255], v[212:215], v[132:135], a[252:255]\n"
    "v_mfma_f32_16x16x32_bf16 v[120:123], v[212:215], v[136:139], v[120:123]\n"
    "v_exp_f32 v220, v220\n"
    "v_mfma_f32_16x16x32_bf16 v[112:115], v[208:211], v[136:139], v[112:115]\n"
    "v_exp_f32 v221, v221\n"
    "v_mfma_f32_16x16x32_bf16 v[116:119], v[208:211], v[140:143], v[116:119]\n"
    "s_nop 0\n"
    "ds_read_b64_tr_b16 v[208:209], v252 offset:0\n"
    "ds_read_b64_tr_b16 v[210:211], v252 offset:4096\n"
    "v_mfma_f32_16x16x32_bf16 v[124:127], v[212:215], v[140:143], v[124:127]\n"
    "s_waitcnt lgkmcnt(6)\n"
    "ds_read_b64_tr_b16 v[212:213], v253 offset:0\n"
    "ds_read_b64_tr_b16 v[214:215], v253 offset:4096\n"
    "s_cbranch_scc1 L_even1_%=\n"
    "L_tail_%=:\n"
    "ds_read_b64_tr_b16 v[176:177], v252 offset:512\n"
    "ds_read_b64_tr_b16 v[178:179], v252 offset:4608\n"
    "s_waitcnt lgkmcnt(4)\n"
    "v_mfma_f32_16x16x32_bf16 a[128:131], v[208:211], v[144:147], a[128:131]\n"
    "s_lshl_b32 s69, s50, 13\n"
    "v_exp_f32 v222, v222\n"
    "v_mfma_f32_16x16x32_bf16 a[132:135], v[208:211], v[148:151], a[132:135]\n"
    "s_waitcnt vmcnt(0)\n"
    "v_exp_f32 v223, v223\n"
    "v_mfma_f32_16x16x32_bf16 v[0:3], v[208:211], v[152:155], v[0:3]\n"
    "s_waitcnt lgkmcnt(2)\n"
    "s_barrier\n"
    "v_cvt_pk_bf16_f32 v158, v220, v221\n"
    "v_cvt_pk_bf16_f32 v159, v222, v223\n"
    "v_mfma_f32_16x16x32_bf16 a[136:139], v[212:215], v[144:147], a[136:139]\n"
    "s_lshl_b32 s57, s50, 14\n"
    "ds_read_b64_tr_b16 v[180:181], v253 offset:512\n"
    "ds_read_b64_tr_b16 v[182:183], v253 offset:4608\n"
    "v_mfma_f32_16x16x32_bf16 a[140:143], v[212:215], v[148:151], a[140:143]\n"
    "s_sub_u32 s57, 49152, s57\n"
    "v_add_f32 v227, v227, v220\n"
    "v_add_f32 v227, v227, v221\n"
    "v_mfma_f32_16x16x32_bf16 v[8:11], v[212:215], v[152:155], v[8:11]\n"
    "buffer_load_dwordx4 v[136:139], v240, s[64:67], s57 offen offset:0 sc1\n"
    "v_add_f32 v227, v227, v222\n"
    "v_mfma_f32_16x16x32_bf16 v[4:7], v[208:211], v[156:159], v[4:7]\n"
    "v_add_f32 v227, v227, v223\n"
    "v_add_f32 v128, v224, v225\n"
    "v_mfma_f32_16x16x32_bf16 v[12:15], v[212:215], v[156:159], v[12:15]\n"
    "s_waitcnt lgkmcnt(2)\n"
    "ds_read_b64_tr_b16 v[184:185], v252 offset:1024\n"
    "v_add_f32 v129, v226, v227\n"
    "v_mfma_f32_16x16x32_bf16 a[144:147], v[176:179], v[144:147], a[144:147]\n"
    "ds_read_b64_tr_b16 v[186:187], v252 offset:5120\n"
    "v_add_f32 v128, v128, v129\n"
    "v_mfma_f32_16x16x32_bf16 a[148:151], v[176:179], v[148:151], a[148:151]\n"
    "s_waitcnt lgkmcnt(2)\n"
    "v_cmp_nge_f32 vcc, 0x58800000, v128\n"
    "v_lshrrev_b32 v129, 4, v250\n"
    "v_mfma_f32_16x16x32_bf16 v[16:19], v[176:179], v[152:155], v[16:19]\n"
    "buffer_load_dwordx4 v[140:143], v240, s[64:67], s57 offen offset:1024 sc1\n"
    "v_lshl_add_u32 v129, v129, 2, v246\n"
    "v_accvgpr_read_b32 v192, a128\n"
    "v_accvgpr_read_b32 v193, a129\n"
    "v_accvgpr_read_b32 v194, a130\n"
    "v_accvgpr_read_b32 v195, a131\n"
    "v_mfma_f32_16x16x32_bf16 v[20:23], v[176:179], v[156:159], v[20:23]\n"
    "s_or_b32 s54, vcc_lo, vcc_hi\n"
    "v_accvgpr_read_b32 v196, a136\n"
    "v_accvgpr_read_b32 v197, a137\n"
    "v_accvgpr_read_b32 v198, a138\n"
    "v_accvgpr_read_b32 v199, a139\n"
    "v_mfma_f32_16x16x32_bf16 a[152:155], v[180:183], v[144:147], a[152:155]\n"
    "s_waitcnt lgkmcnt(0)\n"
    "ds_read_b64_tr_b16 v[188:189], v253 offset:1024\n"
    "ds_read_b64_tr_b16 v[190:191], v253 offset:5120\n"
    "v_permlane32_swap_b32 v224, v226\n"
    "v_cvt_pk_bf16_f32 v192, v192, v193\n"
    "v_cvt_pk_bf16_f32 v193, v194, v195\n"
    "v_cvt_pk_bf16_f32 v194, v196, v197\n"
    "v_cvt_pk_bf16_f32 v195, v198, v199\n"
    "v_mfma_f32_16x16x32_bf16 a[156:159], v[180:183], v[148:151], a[156:159]\n"
    "s_mov_b32 s68, 0\n"
    "ds_write_b128 v240, v[192:195] offset:0\n"
    "v_permlane32_swap_b32 v225, v227\n"
    "v_add_f32 v224, v224, v226\n"
    "v_mfma_f32_16x16x32_bf16 v[24:27], v[180:183], v[152:155], v[24:27]\n"
    "s_add_u32 m0, s69, 98304\n"
    "v_add_f32 v225, v225, v227\n"
    "v_accvgpr_read_b32 v200, a132\n"
    "v_accvgpr_read_b32 v201, a133\n"
    "v_accvgpr_read_b32 v202, a134\n"
    "v_accvgpr_read_b32 v203, a135\n"
    "v_mfma_f32_16x16x32_bf16 v[28:31], v[180:183], v[156:159], v[28:31]\n"
    "buffer_load_dwordx4 v238, s[60:63], s68 offen lds\n"
    "v_permlane16_swap_b32 v224, v225\n"
    "v_accvgpr_read_b32 v204, a140\n"
    "v_accvgpr_read_b32 v205, a141\n"
    "v_accvgpr_read_b32 v206, a142\n"
    "v_accvgpr_read_b32 v207, a143\n"
    "v_mfma_f32_16x16x32_bf16 a[160:163], v[184:187], v[144:147], a[160:163]\n"
    "s_waitcnt lgkmcnt(1)\n"
    "ds_read_b64_tr_b16 v[208:209], v252 offset:1536\n"
    "ds_read_b64_tr_b16 v[210:211], v252 offset:5632\n"
    "v_add_f32 v224, v224, v225\n"
    "v_mov_b32 v128, s54\n"
    "v_cvt_pk_bf16_f32 v200, v200, v201\n"
    "v_cvt_pk_bf16_f32 v201, v202, v203\n"
    "v_cvt_pk_bf16_f32 v202, v204, v205\n"
    "v_cvt_pk_bf16_f32 v203, v206, v207\n"
    "v_mfma_f32_16x16x32_bf16 a[164:167], v[184:187], v[148:151], a[164:167]\n"
    "s_lshl_b32 s54, s50, 2\n"
    "ds_write_b128 v240, v[200:203] offset:1024\n"
    "v_mfma_f32_16x16x32_bf16 v[32:35], v[184:187], v[152:155], v[32:35]\n"
    "s_mov_b32 s68, 7168\n"
    "v_mov_b32 v130, 159744\n"
    "v_accvgpr_read_b32 v192, a144\n"
    "v_accvgpr_read_b32 v193, a145\n"
    "v_accvgpr_read_b32 v194, a146\n"
    "v_accvgpr_read_b32 v195, a147\n"
    "v_mfma_f32_16x16x32_bf16 v[36:39], v[184:187], v[156:159], v[36:39]\n"
    "s_add_u32 m0, s69, 99328\n"
    "v_add_u32 v131, s54, v130\n"
    "ds_write_b32 v129, v224\n"
    "v_accvgpr_read_b32 v196, a152\n"
    "v_accvgpr_read_b32 v197, a153\n"
    "v_accvgpr_read_b32 v198, a154\n"
    "v_accvgpr_read_b32 v199, a155\n"
    "v_mfma_f32_16x16x32_bf16 a[168:171], v[188:191], v[144:147], a[168:171]\n"
    "s_nop 0\n"
    "buffer_load_dwordx4 v238, s[60:63], s68 offen lds\n"
    "ds_read_b64_tr_b16 v[212:213], v253 offset:1536\n"
    "ds_read_b64_tr_b16 v[214:215], v253 offset:5632\n"
    "ds_write_b32 v131, v128 offset:1024\n"
    "v_cvt_pk_bf16_f32 v192, v192, v193\n"
    "v_cvt_pk_bf16_f32 v193, v194, v195\n"
    "v_cvt_pk_bf16_f32 v194, v196, v197\n"
    "v_cvt_pk_bf16_f32 v195, v198, v199\n"
    "v_mfma_f32_16x16x32_bf16 a[172:175], v[188:191], v[148:151], a[172:175]\n"
    "ds_write_b128 v240, v[192:195] offset:2048\n"
    "v_xor_b32 v129, 0x100, v246\n"
    "v_mfma_f32_16x16x32_bf16 v[40:43], v[188:191], v[152:155], v[40:43]\n"
    "v_accvgpr_read_b32 v200, a148\n"
    "v_accvgpr_read_b32 v201, a149\n"
    "v_accvgpr_read_b32 v202, a150\n"
    "v_accvgpr_read_b32 v203, a151\n"
    "v_mfma_f32_16x16x32_bf16 v[44:47], v[188:191], v[156:159], v[44:47]\n"
    "s_waitcnt lgkmcnt(6)\n"
    "v_accvgpr_read_b32 v204, a156\n"
    "v_accvgpr_read_b32 v205, a157\n"
    "v_accvgpr_read_b32 v206, a158\n"
    "v_accvgpr_read_b32 v207, a159\n"
    "v_mfma_f32_16x16x32_bf16 a[176:179], v[208:211], v[144:147], a[176:179]\n"
    "s_waitcnt lgkmcnt(2)\n"
    "ds_read_b64_tr_b16 v[176:177], v252 offset:16384\n"
    "ds_read_b64_tr_b16 v[178:179], v252 offset:20480\n"
    "v_cvt_pk_bf16_f32 v200, v200, v201\n"
    "v_cvt_pk_bf16_f32 v201, v202, v203\n"
    "v_cvt_pk_bf16_f32 v202, v204, v205\n"
    "v_cvt_pk_bf16_f32 v203, v206, v207\n"
    "v_mfma_f32_16x16x32_bf16 a[180:183], v[208:211], v[148:151], a[180:183]\n"
    "s_waitcnt lgkmcnt(3)\n"
    "ds_write_b128 v240, v[200:203] offset:3072\n"
    "v_mfma_f32_16x16x32_bf16 v[48:51], v[208:211], v[152:155], v[48:51]\n"
    "s_barrier\n"
    "s_mov_b32 s68, 14336\n"
    "v_accvgpr_read_b32 v192, a160\n"
    "v_accvgpr_read_b32 v193, a161\n"
    "v_accvgpr_read_b32 v194, a162\n"
    "v_accvgpr_read_b32 v195, a163\n"
    "v_mfma_f32_16x16x32_bf16 v[52:55], v[208:211], v[156:159], v[52:55]\n"
    "s_add_u32 m0, s69, 100352\n"
    "v_accvgpr_read_b32 v196, a168\n"
    "v_accvgpr_read_b32 v197, a169\n"
    "v_accvgpr_read_b32 v198, a170\n"
    "v_accvgpr_read_b32 v199, a171\n"
    "v_mfma_f32_16x16x32_bf16 a[184:187], v[212:215], v[144:147], a[184:187]\n"
    "s_nop 0\n"
    "buffer_load_dwordx4 v238, s[60:63], s68 offen lds\n"
    "ds_read_b64_tr_b16 v[180:181], v253 offset:16384\n"
    "ds_read_b64_tr_b16 v[182:183], v253 offset:20480\n"
    "v_cvt_pk_bf16_f32 v192, v192, v193\n"
    "v_cvt_pk_bf16_f32 v193, v194, v195\n"
    "v_cvt_pk_bf16_f32 v194, v196, v197\n"
    "v_cvt_pk_bf16_f32 v195, v198, v199\n"
    "v_mfma_f32_16x16x32_bf16 a[188:191], v[212:215], v[148:151], a[188:191]\n"
    "s_mov_b32 s68, 21504\n"
    "ds_write_b128 v240, v[192:195] offset:4096\n"
    "ds_read_b64 v[224:225], v246 offset:8\n"
    "ds_read_b64 v[226:227], v129\n"
    "ds_read_b128 v[132:135], v130 offset:1024\n"
    "v_mfma_f32_16x16x32_bf16 v[56:59], v[212:215], v[152:155], v[56:59]\n"
    "s_add_u32 m0, s69, 101376\n"
    "v_accvgpr_read_b32 v200, a164\n"
    "v_accvgpr_read_b32 v201, a165\n"
    "v_accvgpr_read_b32 v202, a166\n"
    "v_accvgpr_read_b32 v203, a167\n"
    "v_mfma_f32_16x16x32_bf16 v[60:63], v[212:215], v[156:159], v[60:63]\n"
    "s_waitcnt lgkmcnt(7)\n"
    "buffer_load_dwordx4 v238, s[60:63], s68 offen lds\n"
    "v_accvgpr_read_b32 v204, a172\n"
    "v_accvgpr_read_b32 v205, a173\n"
    "v_accvgpr_read_b32 v206, a174\n"
    "v_accvgpr_read_b32 v207, a175\n"
    "v_mfma_f32_16x16x32_bf16 a[192:195], v[176:179], v[144:147], a[192:195]\n"
    "s_waitcnt lgkmcnt(4)\n"
    "ds_read_b64_tr_b16 v[184:185], v252 offset:16896\n"
    "ds_read_b64_tr_b16 v[186:187], v252 offset:20992\n"
    "v_cvt_pk_bf16_f32 v200, v200, v201\n"
    "v_cvt_pk_bf16_f32 v201, v202, v203\n"
    "v_cvt_pk_bf16_f32 v202, v204, v205\n"
    "v_cvt_pk_bf16_f32 v203, v206, v207\n"
    "v_mfma_f32_16x16x32_bf16 a[196:199], v[176:179], v[148:151], a[196:199]\n"
    "s_mov_b32 s68, 28672\n"
    "ds_write_b128 v240, v[200:203] offset:5120\n"
    "v_mfma_f32_16x16x32_bf16 v[64:67], v[176:179], v[152:155], v[64:67]\n"
    "s_add_u32 m0, s69, 102400\n"
    "v_accvgpr_read_b32 v192, a176\n"
    "v_accvgpr_read_b32 v193, a177\n"
    "v_accvgpr_read_b32 v194, a178\n"
    "v_accvgpr_read_b32 v195, a179\n"
    "v_mfma_f32_16x16x32_bf16 v[68:71], v[176:179], v[156:159], v[68:71]\n"
    "buffer_load_dwordx4 v238, s[60:63], s68 offen lds\n"
    "v_accvgpr_read_b32 v196, a184\n"
    "v_accvgpr_read_b32 v197, a185\n"
    "v_accvgpr_read_b32 v198, a186\n"
    "v_accvgpr_read_b32 v199, a187\n"
    "v_mfma_f32_16x16x32_bf16 a[200:203], v[180:183], v[144:147], a[200:203]\n"
    "s_nop 0\n"
    "ds_read_b64_tr_b16 v[188:189], v253 offset:16896\n"
    "ds_read_b64_tr_b16 v[190:191], v253 offset:20992\n"
    "v_cvt_pk_bf16_f32 v192, v192, v193\n"
    "v_cvt_pk_bf16_f32 v193, v194, v195\n"
    "v_cvt_pk_bf16_f32 v194, v196, v197\n"
    "v_cvt_pk_bf16_f32 v195, v198, v199\n"
    "v_mfma_f32_16x16x32_bf16 a[204:207], v[180:183], v[148:151], a[204:207]\n"
    "s_waitcnt lgkmcnt(3)\n"
    "ds_write_b128 v240, v[192:195] offset:6144\n"
    "v_mfma_f32_16x16x32_bf16 v[72:75], v[180:183], v[152:155], v[72:75]\n"
    "s_mov_b32 s68, 35840\n"
    "v_accvgpr_read_b32 v200, a180\n"
    "v_accvgpr_read_b32 v201, a181\n"
    "v_accvgpr_read_b32 v202, a182\n"
    "v_accvgpr_read_b32 v203, a183\n"
    "v_mfma_f32_16x16x32_bf16 v[76:79], v[180:183], v[156:159], v[76:79]\n"
    "s_add_u32 m0, s69, 103424\n"
    "v_accvgpr_read_b32 v204, a188\n"
    "v_accvgpr_read_b32 v205, a189\n"
    "v_accvgpr_read_b32 v206, a190\n"
    "v_accvgpr_read_b32 v207, a191\n"
    "v_mfma_f32_16x16x32_bf16 a[208:211], v[184:187], v[144:147], a[208:211]\n"
    "s_nop 0\n"
    "buffer_load_dwordx4 v238, s[60:63], s68 offen lds\n"
    "ds_read_b64_tr_b16 v[208:209], v252 offset:17408\n"
    "ds_read_b64_tr_b16 v[210:211], v252 offset:21504\n"
    "v_add_f32 v216, v224, v226\n"
    "v_cvt_pk_bf16_f32 v200, v200, v201\n"
    "v_cvt_pk_bf16_f32 v201, v202, v203\n"
    "v_cvt_pk_bf16_f32 v202, v204, v205\n"
    "v_cvt_pk_bf16_f32 v203, v206, v207\n"
    "v_mfma_f32_16x16x32_bf16 a[212:215], v[184:187], v[148:151], a[212:215]\n"
    "ds_write_b128 v240, v[200:203] offset:7168\n"
    "v_add_f32 v218, v225, v227\n"
    "v_mfma_f32_16x16x32_bf16 v[80:83], v[184:187], v[152:155], v[80:83]\n"
    "v_or3_b32 v132, v132, v133, v134\n"
    "v_accvgpr_read_b32 v192, a192\n"
    "v_accvgpr_read_b32 v193, a193\n"
    "v_accvgpr_read_b32 v194, a194\n"
    "v_accvgpr_read_b32 v195, a195\n"
    "v_mfma_f32_16x16x32_bf16 v[84:87], v[184:187], v[156:159], v[84:87]\n"
    "s_waitcnt lgkmcnt(4)\n"
    "v_rcp_f32 v216, v216\n"
    "v_accvgpr_read_b32 v196, a200\n"
    "v_accvgpr_read_b32 v197, a201\n"
    "v_accvgpr_read_b32 v198, a202\n"
    "v_accvgpr_read_b32 v199, a203\n"
    "v_mfma_f32_16x16x32_bf16 a[216:219], v[188:191], v[144:147], a[216:219]\n"
    "s_mov_b32 s68, 43008\n"
    "ds_read_b64_tr_b16 v[212:213], v253 offset:17408\n"
    "ds_read_b64_tr_b16 v[214:215], v253 offset:21504\n"
    "v_rcp_f32 v218, v218\n"
    "v_cvt_pk_bf16_f32 v192, v192, v193\n"
    "v_cvt_pk_bf16_f32 v193, v194, v195\n"
    "v_cvt_pk_bf16_f32 v194, v196, v197\n"
    "v_cvt_pk_bf16_f32 v195, v198, v199\n"
    "v_mfma_f32_16x16x32_bf16 a[220:223], v[188:191], v[148:151], a[220:223]\n"
    "s_add_u32 m0, s69, 104448\n"
    "ds_write_b128 v240, v[192:195] offset:8192\n"
    "v_or_b32 v132, v132, v135\n"
    "v_mfma_f32_16x16x32_bf16 v[88:91], v[188:191], v[152:155], v[88:91]\n"
    "buffer_load_dwordx4 v238, s[60:63], s68 offen lds\n"
    "v_cmp_ne_u32 vcc, 0, v132\n"
    "v_accvgpr_read_b32 v200, a196\n"
    "v_accvgpr_read_b32 v201, a197\n"
    "v_accvgpr_read_b32 v202, a198\n"
    "v_accvgpr_read_b32 v203, a199\n"
    "v_mfma_f32_16x16x32_bf16 v[92:95], v[188:191], v[156:159], v[92:95]\n"
    "s_waitcnt lgkmcnt(4)\n"
    "v_accvgpr_read_b32 v204, a204\n"
    "v_accvgpr_read_b32 v205, a205\n"
    "v_accvgpr_read_b32 v206, a206\n"
    "v_accvgpr_read_b32 v207, a207\n"
    "v_mfma_f32_16x16x32_bf16 a[224:227], v[208:211], v[144:147], a[224:227]\n"
    "s_mov_b32 s68, 50176\n"
    "ds_read_b64_tr_b16 v[176:177], v252 offset:17920\n"
    "ds_read_b64_tr_b16 v[178:179], v252 offset:22016\n"
    "v_cvt_pk_bf16_f32 v200, v200, v201\n"
    "v_cvt_pk_bf16_f32 v201, v202, v203\n"
    "v_cvt_pk_bf16_f32 v202, v204, v205\n"
    "v_cvt_pk_bf16_f32 v203, v206, v207\n"
    "v_mfma_f32_16x16x32_bf16 a[228:231], v[208:211], v[148:151], a[228:231]\n"
    "s_add_u32 m0, s69, 105472\n"
    "ds_write_b128 v240, v[200:203] offset:9216\n"
    "ds_read_b64_tr_b16 v[180:181], v253 offset:17920\n"
    "ds_read_b64_tr_b16 v[182:183], v253 offset:22016\n"
    "v_mfma_f32_16x16x32_bf16 v[96:99], v[208:211], v[152:155], v[96:99]\n"
    "buffer_load_dwordx4 v238, s[60:63], s68 offen lds\n"
    "v_accvgpr_read_b32 v192, a208\n"
    "v_accvgpr_read_b32 v193, a209\n"
    "v_accvgpr_read_b32 v194, a210\n"
    "v_accvgpr_read_b32 v195, a211\n"
    "v_mfma_f32_16x16x32_bf16 v[100:103], v[208:211], v[156:159], v[100:103]\n"
    "s_waitcnt lgkmcnt(6)\n"
    "v_accvgpr_read_b32 v196, a216\n"
    "v_accvgpr_read_b32 v197, a217\n"
    "v_accvgpr_read_b32 v198, a218\n"
    "v_accvgpr_read_b32 v199, a219\n"
    "v_mfma_f32_16x16x32_bf16 a[232:235], v[212:215], v[144:147], a[232:235]\n"
    "v_cvt_pk_bf16_f32 v192, v192, v193\n"
    "v_cvt_pk_bf16_f32 v193, v194, v195\n"
    "v_cvt_pk_bf16_f32 v194, v196, v197\n"
    "v_cvt_pk_bf16_f32 v195, v198, v199\n"
    "v_mfma_f32_16x16x32_bf16 a[236:239], v[212:215], v[148:151], a[236:239]\n"
    "ds_write_b128 v240, v[192:195] offset:10240\n"
    "v_accvgpr_read_b32 v200, a212\n"
    "v_accvgpr_read_b32 v201, a213\n"
    "v_accvgpr_read_b32 v202, a214\n"
    "v_accvgpr_read_b32 v203, a215\n"
    "v_mfma_f32_16x16x32_bf16 v[104:107], v[212:215], v[152:155], v[104:107]\n"
    "v_accvgpr_read_b32 v204, a220\n"
    "v_accvgpr_read_b32 v205, a221\n"
    "v_accvgpr_read_b32 v206, a222\n"
    "v_accvgpr_read_b32 v207, a223\n"
    "v_mfma_f32_16x16x32_bf16 v[108:111], v[212:215], v[156:159], v[108:111]\n"
    "s_waitcnt lgkmcnt(4)\n"
    "v_cvt_pk_bf16_f32 v200, v200, v201\n"
    "v_cvt_pk_bf16_f32 v201, v202, v203\n"
    "v_cvt_pk_bf16_f32 v202, v204, v205\n"
    "v_cvt_pk_bf16_f32 v203, v206, v207\n"
    "v_mfma_f32_16x16x32_bf16 a[240:243], v[176:179], v[144:147], a[240:243]\n"
    "s_waitcnt lgkmcnt(1)\n"
    "ds_write_b128 v240, v[200:203] offset:11264\n"
    "v_accvgpr_read_b32 v192, a224\n"
    "v_accvgpr_read_b32 v193, a225\n"
    "v_accvgpr_read_b32 v194, a226\n"
    "v_accvgpr_read_b32 v195, a227\n"
    "v_accvgpr_read_b32 v196, a232\n"
    "v_accvgpr_read_b32 v197, a233\n"
    "v_accvgpr_read_b32 v198, a234\n"
    "v_accvgpr_read_b32 v199, a235\n"
    "v_mfma_f32_16x16x32_bf16 a[248:251], v[180:183], v[144:147], a[248:251]\n"
    "v_cvt_pk_bf16_f32 v192, v192, v193\n"
    "v_cvt_pk_bf16_f32 v193, v194, v195\n"
    "v_cvt_pk_bf16_f32 v194, v196, v197\n"
    "v_cvt_pk_bf16_f32 v195, v198, v199\n"
    "v_mfma_f32_16x16x32_bf16 a[244:247], v[176:179], v[148:151], a[244:247]\n"
    "ds_write_b128 v240, v[192:195] offset:12288\n"
    "v_accvgpr_read_b32 v200, a228\n"
    "v_accvgpr_read_b32 v201, a229\n"
    "v_accvgpr_read_b32 v202, a230\n"
    "v_accvgpr_read_b32 v203, a231\n"
    "v_accvgpr_read_b32 v204, a236\n"
    "v_accvgpr_read_b32 v205, a237\n"
    "v_accvgpr_read_b32 v206, a238\n"
    "v_accvgpr_read_b32 v207, a239\n"
    "v_mfma_f32_16x16x32_bf16 a[252:255], v[180:183], v[148:151], a[252:255]\n"
    "v_cvt_pk_bf16_f32 v200, v200, v201\n"
    "v_cvt_pk_bf16_f32 v201, v202, v203\n"
    "v_cvt_pk_bf16_f32 v202, v204, v205\n"
    "v_cvt_pk_bf16_f32 v203, v206, v207\n"
    "v_mfma_f32_16x16x32_bf16 v[112:115], v[176:179], v[152:155], v[112:115]\n"
    "ds_write_b128 v240, v[200:203] offset:13312\n"
    "v_accvgpr_read_b32 v192, a240\n"
    "v_accvgpr_read_b32 v193, a241\n"
    "v_accvgpr_read_b32 v194, a242\n"
    "v_accvgpr_read_b32 v195, a243\n"
    "v_mfma_f32_16x16x32_bf16 v[120:123], v[180:183], v[152:155], v[120:123]\n"
    "v_accvgpr_read_b32 v196, a248\n"
    "v_accvgpr_read_b32 v197, a249\n"
    "v_accvgpr_read_b32 v198, a250\n"
    "v_accvgpr_read_b32 v199, a251\n"
    "v_mfma_f32_16x16x32_bf16 v[116:119], v[176:179], v[156:159], v[116:119]\n"
    "v_cvt_pk_bf16_f32 v192, v192, v193\n"
    "v_cvt_pk_bf16_f32 v193, v194, v195\n"
    "v_cvt_pk_bf16_f32 v194, v196, v197\n"
    "v_cvt_pk_bf16_f32 v195, v198, v199\n"
    "v_mfma_f32_16x16x32_bf16 v[124:127], v[180:183], v[156:159], v[124:127]\n"
    "ds_write_b128 v240, v[192:195] offset:14336\n"
    "v_accvgpr_read_b32 v200, a244\n"
    "v_accvgpr_read_b32 v201, a245\n"
    "v_accvgpr_read_b32 v202, a246\n"
    "v_accvgpr_read_b32 v203, a247\n"
    "v_accvgpr_read_b32 v204, a252\n"
    "v_accvgpr_read_b32 v205, a253\n"
    "v_accvgpr_read_b32 v206, a254\n"
    "v_accvgpr_read_b32 v207, a255\n"
    "v_cvt_pk_bf16_f32 v200, v200, v201\n"
    "v_cvt_pk_bf16_f32 v201, v202, v203\n"
    "v_cvt_pk_bf16_f32 v202, v204, v205\n"
    "v_cvt_pk_bf16_f32 v203, v206, v207\n"
    "ds_write_b128 v240, v[200:203] offset:15360\n"
    "s_cbranch_vccnz L_cmb_%=_redo\n"
    "L_cmb_%=_back:\n"
    "v_bfe_u32 v214, v250, 2, 2\n"
    "v_lshrrev_b32 v215, 4, v250\n"
    "v_cmp_eq_u32 vcc, v214, v215\n"
    "v_and_b32 v214, 1, v250\n"
    "v_lshlrev_b32 v214, 4, v214\n"
    "v_lshlrev_b32 v214, v214, v241\n"
    "v_cndmask_b32 v214, 0, v214, vcc\n"
    "v_bfe_i32 v215, v250, 1, 1\n"
    "v_and_b32 v211, v214, v215\n"
    "v_xor_b32 v210, v214, v211\n"
    "v_mov_b64 v[208:209], 0\n"
    "v_mov_b64 v[212:213], 0\n"
    "s_waitcnt vmcnt(8)\n"
    "L_gr4_%=:\n"
    "v_xor_b32 v128, s53, v137\n"
    "v_xor_b32 v129, s53, v139\n"
    "v_or_b32 v128, v128, v129\n"
    "v_xor_b32 v129, s53, v141\n"
    "v_or_b32 v128, v128, v129\n"
    "v_xor_b32 v129, s53, v143\n"
    "v_or_b32 v128, v128, v129\n"
    "v_cmp_ne_u32 vcc, 0, v128\n"
    "s_nop 1\n"
    "s_cbranch_vccz L_grd5_%=\n"
    "s_sub_u32 s99, s99, 1\n"
    "s_cbranch_scc1 L_grd5_%=\n"
    "s_nop 0\n"
    "s_lshl_b32 s57, s50, 14\n"
    "s_sub_u32 s57, 49152, s57\n"
    "buffer_load_dwordx4 v[136:139], v240, s[64:67], s57 offen offset:0 sc1\n"
    "buffer_load_dwordx4 v[140:143], v240, s[64:67], s57 offen offset:1024 sc1\n"
    "s_waitcnt vmcnt(0)\n"
    "s_branch L_gr4_%=\n"
    "L_grd5_%=:\n"
    "s_max_i32 s99, s99, 0\n"
    "v_cmp_ne_u32 s[36:37], 0, v136\n"
    "v_cmp_ne_u32 s[38:39], 0, v138\n"
    "v_cmp_ne_u32 s[40:41], 0, v140\n"
    "v_cmp_ne_u32 s[42:43], 0, v142\n"
    "s_bcnt1_i32_b64 s36, s[36:37]\n"
    "s_bcnt1_i32_b64 s38, s[38:39]\n"
    "s_bcnt1_i32_b64 s40, s[40:41]\n"
    "s_bcnt1_i32_b64 s42, s[42:43]\n"
    "s_add_u32 s36, s36, s38\n"
    "s_add_u32 s40, s40, s42\n"
    "s_add_u32 s36, s36, s40\n"
    "s_bfe_i32 s42, %1, 0x10002\n"
    "s_bfe_i32 s43, s52, 0x10003\n"
    "s_cmp_gt_u32 s36, 86\n"
    "s_cselect_b32 s56, 0, -1\n"
    "s_xor_b32 s56, s56, 0x55555555\n"
    "s_xor_b32 s42, s42, s56\n"
    "s_xor_b32 s43, s43, s56\n"
    "s_waitcnt vmcnt(0)\n"
    "s_waitcnt lgkmcnt(0)\n"
    "s_barrier\n"
    "ds_read_b128 a[64:67], v242 offset:0\n"
    "ds_read_b128 a[68:71], v242 offset:1024\n"
    "ds_read_b128 a[72:75], v242 offset:2048\n"
    "ds_read_b128 a[76:79], v242 offset:3072\n"
    "ds_read_b128 a[80:83], v242 offset:4096\n"
    "ds_read_b128 a[84:87], v242 offset:5120\n"
    "s_waitcnt lgkmcnt(5)\n"
    "v_mfma_f32_16x16x32_bf16 v[0:3], v[210:213], a[64:67], v[0:3]\n"
    "s_waitcnt lgkmcnt(4)\n"
    "ds_read_b128 a[88:91], v242 offset:6144\n"
    "v_mfma_f32_16x16x32_bf16 v[8:11], v[208:211], a[64:67], v[8:11]\n"
    "s_mov_b32 s68, 128\n"
    "s_add_u32 m0, s69, 131072\n"
    "ds_read_b128 a[92:95], v242 offset:7168\n"
    "v_mfma_f32_16x16x32_bf16 v[4:7], v[210:213], a[68:71], v[4:7]\n"
    "buffer_load_dwordx4 v238, s[60:63], s68 offen lds\n"
    "v_mfma_f32_16x16x32_bf16 v[12:15], v[208:211], a[68:71], v[12:15]\n"
    "s_waitcnt lgkmcnt(4)\n"
    "ds_read_b128 a[96:99], v242 offset:8192\n"
    "ds_read_b128 a[100:103], v242 offset:9216\n"
    "v_mfma_f32_16x16x32_bf16 v[16:19], v[210:213], a[72:75], v[16:19]\n"
    "s_mov_b32 s68, 7296\n"
    "s_add_u32 m0, s69, 132096\n"
    "ds_read_b128 a[104:107], v242 offset:10240\n"
    "v_mfma_f32_16x16x32_bf16 v[24:27], v[208:211], a[72:75], v[24:27]\n"
    "buffer_load_dwordx4 v238, s[60:63], s68 offen lds\n"
    "v_cvt_pk_bf16_f32 v128, v0, v1\n"
    "v_cvt_pk_bf16_f32 v129, v2, v3\n"
    "v_mfma_f32_16x16x32_bf16 v[20:23], v[210:213], a[76:79], v[20:23]\n"
    "s_waitcnt lgkmcnt(5)\n"
    "ds_read_b128 a[108:111], v242 offset:11264\n"
    "v_cvt_pk_bf16_f32 v130, v8, v9\n"
    "v_cvt_pk_bf16_f32 v131, v10, v11\n"
    "v_mfma_f32_16x16x32_bf16 v[28:31], v[208:211], a[76:79], v[28:31]\n"
    "s_mov_b32 s68, 14464\n"
    "ds_read_b128 a[112:115], v242 offset:12288\n"
    "v_cvt_pk_bf16_f32 v160, v4, v5\n"
    "v_cvt_pk_bf16_f32 v161, v6, v7\n"
    "v_mfma_f32_16x16x32_bf16 v[32:35], v[210:213], a[80:83], v[32:35]\n"
    "s_add_u32 m0, s69, 133120\n"
    "ds_read_b128 a[116:119], v242 offset:13312\n"
    "v_cvt_pk_bf16_f32 v162, v12, v13\n"
    "v_cvt_pk_bf16_f32 v163, v14, v15\n"
    "v_mfma_f32_16x16x32_bf16 v[40:43], v[208:211], a[80:83], v[40:43]\n"
    "buffer_load_dwordx4 v238, s[60:63], s68 offen lds\n"
    "ds_read_b128 a[120:123], v242 offset:14336\n"
    "v_cvt_pk_bf16_f32 v132, v16, v17\n"
    "v_cvt_pk_bf16_f32 v133, v18, v19\n"
    "v_mfma_f32_16x16x32_bf16 v[36:39], v[210:213], a[84:87], v[36:39]\n"
    "s_mov_b32 s68, 21632\n"
    "ds_read_b128 a[124:127], v242 offset:15360\n"
    "v_cvt_pk_bf16_f32 v134, v24, v25\n"
    "v_cvt_pk_bf16_f32 v135, v26, v27\n"
    "v_mfma_f32_16x16x32_bf16 v[44:47], v[208:211], a[84:87], v[44:47]\n"
    "s_waitcnt lgkmcnt(9)\n"
    "s_add_u32 m0, s69, 134144\n"
    "v_cvt_pk_bf16_f32 v164, v20, v21\n"
    "v_cvt_pk_bf16_f32 v165, v22, v23\n"
    "v_mfma_f32_16x16x32_bf16 v[48:51], v[210:213], a[88:91], v[48:51]\n"
    "buffer_load_dwordx4 v238, s[60:63], s68 offen lds\n"
    "v_cvt_pk_bf16_f32 v166, v28, v29\n"
    "v_cvt_pk_bf16_f32 v167, v30, v31\n"
    "v_mfma_f32_16x16x32_bf16 v[56:59], v[208:211], a[88:91], v[56:59]\n"
    "s_waitcnt lgkmcnt(7)\n"
    "v_cvt_pk_bf16_f32 v136, v32, v33\n"
    "v_cvt_pk_bf16_f32 v137, v34, v35\n"
    "v_mfma_f32_16x16x32_bf16 v[52:55], v[210:213], a[92:95], v[52:55]\n"
    "s_mov_b32 s68, 28800\n"
    "v_cvt_pk_bf16_f32 v138, v40, v41\n"
    "v_cvt_pk_bf16_f32 v139, v42, v43\n"
    "v_mfma_f32_16x16x32_bf16 v[60:63], v[208:211], a[92:95], v[60:63]\n"
    "s_add_u32 m0, s69, 135168\n"
    "v_cvt_pk_bf16_f32 v168, v36, v37\n"
    "v_cvt_pk_bf16_f32 v169, v38, v39\n"
    "v_mfma_f32_16x16x32_bf16 v[64:67], v[210:213], a[96:99], v[64:67]\n"
    "buffer_load_dwordx4 v238, s[60:63], s68 offen lds\n"
    "ds_read_b64_tr_b16 a[0:1], v234 offset:32768\n"
    "v_cvt_pk_bf16_f32 v170, v44, v45\n"
    "v_cvt_pk_bf16_f32 v171, v46, v47\n"
    "v_mfma_f32_16x16x32_bf16 v[72:75], v[208:211], a[96:99], v[72:75]\n"
    "s_waitcnt lgkmcnt(6)\n"
    "s_mov_b32 s68, 35968\n"
    "ds_read_b64_tr_b16 a[2:3], v234 offset:34816\n"
    "v_cvt_pk_bf16_f32 v140, v48, v49\n"
    "v_cvt_pk_bf16_f32 v141, v50, v51\n"
    "v_mfma_f32_16x16x32_bf16 v[68:71], v[210:213], a[100:103], v[68:71]\n"
    "s_add_u32 m0, s69, 136192\n"
    "ds_read_b64_tr_b16 a[4:5], v234 offset:36864\n"
    "v_cvt_pk_bf16_f32 v142, v56, v57\n"
    "v_cvt_pk_bf16_f32 v143, v58, v59\n"
    "v_mfma_f32_16x16x32_bf16 v[76:79], v[208:211], a[100:103], v[76:79]\n"
    "buffer_load_dwordx4 v238, s[60:63], s68 offen lds\n"
    "ds_read_b64_tr_b16 a[6:7], v234 offset:38912\n"
    "v_cvt_pk_bf16_f32 v172, v52, v53\n"
    "v_cvt_pk_bf16_f32 v173, v54, v55\n"
    "v_mfma_f32_16x16x32_bf16 v[80:83], v[210:213], a[104:107], v[80:83]\n"
    "s_waitcnt lgkmcnt(7)\n"
    "ds_read_b64_tr_b16 a[8:9], v234 offset:40960\n"
    "v_cvt_pk_bf16_f32 v174, v60, v61\n"
    "v_cvt_pk_bf16_f32 v175, v62, v63\n"
    "v_mfma_f32_16x16x32_bf16 v[88:91], v[208:211], a[104:107], v[88:91]\n"
    "s_mov_b32 s68, 43136\n"
    "ds_read_b64_tr_b16 a[10:11], v234 offset:43008\n"
    "v_cvt_pk_bf16_f32 v144, v64, v65\n"
    "v_cvt_pk_bf16_f32 v145, v66, v67\n"
    "v_mfma_f32_16x16x32_bf16 v[84:87], v[210:213], a[108:111], v[84:87]\n"
    "s_add_u32 m0, s69, 137216\n"
    "ds_read_b64_tr_b16 a[12:13], v234 offset:45056\n"
    "v_cvt_pk_bf16_f32 v146, v72, v73\n"
    "v_cvt_pk_bf16_f32 v147, v74, v75\n"
    "v_mfma_f32_16x16x32_bf16 v[92:95], v[208:211], a[108:111], v[92:95]\n"
    "buffer_load_dwordx4 v238, s[60:63], s68 offen lds\n"
    "ds_read_b64_tr_b16 a[14:15], v234 offset:47104\n"
    "v_cvt_pk_bf16_f32 v176, v68, v69\n"
    "v_cvt_pk_bf16_f32 v177, v70, v71\n"
    "v_mfma_f32_16x16x32_bf16 v[96:99], v[210:213], a[112:115], v[96:99]\n"
    "s_mov_b32 s68, 50304\n"
    "ds_read_b64_tr_b16 a[16:17], v234 offset:49152\n"
    "v_cvt_pk_bf16_f32 v178, v76, v77\n"
    "v_cvt_pk_bf16_f32 v179, v78, v79\n"
    "v_mfma_f32_16x16x32_bf16 v[104:107], v[208:211], a[112:115], v[104:107]\n"
    "s_waitcnt lgkmcnt(11)\n"
    "s_add_u32 m0, s69, 138240\n"
    "ds_read_b64_tr_b16 a[18:19], v234 offset:51200\n"
    "v_cvt_pk_bf16_f32 v148, v80, v81\n"
    "v_cvt_pk_bf16_f32 v149, v82, v83\n"
    "v_mfma_f32_16x16x32_bf16 v[100:103], v[210:213], a[116:119], v[100:103]\n"
    "buffer_load_dwordx4 v238, s[60:63], s68 offen lds\n"
    "ds_read_b64_tr_b16 a[20:21], v234 offset:53248\n"
    "v_cvt_pk_bf16_f32 v150, v88, v89\n"
    "v_cvt_pk_bf16_f32 v151, v90, v91\n"
    "v_mfma_f32_16x16x32_bf16 v[108:111], v[208:211], a[116:119], v[108:111]\n"
    "s_waitcnt lgkmcnt(12)\n"
    "ds_read_b64_tr_b16 a[22:23], v234 offset:55296\n"
    "v_cvt_pk_bf16_f32 v180, v84, v85\n"
    "v_cvt_pk_bf16_f32 v181, v86, v87\n"
    "v_mfma_f32_16x16x32_bf16 v[112:115], v[210:213], a[120:123], v[112:115]\n"
    "ds_read_b64_tr_b16 a[24:25], v234 offset:57344\n"
    "v_cvt_pk_bf16_f32 v182, v92, v93\n"
    "v_cvt_pk_bf16_f32 v183, v94, v95\n"
    "v_mfma_f32_16x16x32_bf16 v[120:123], v[208:211], a[120:123], v[120:123]\n"
    "s_waitcnt lgkmcnt(13)\n"
    "ds_read_b64_tr_b16 a[26:27], v234 offset:59392\n"
    "v_cvt_pk_bf16_f32 v152, v96, v97\n"
    "v_cvt_pk_bf16_f32 v153, v98, v99\n"
    "v_mfma_f32_16x16x32_bf16 v[116:119], v[210:213], a[124:127], v[116:119]\n"
    "ds_read_b64_tr_b16 a[28:29], v234 offset:61440\n"
    "v_cvt_pk_bf16_f32 v154, v104, v105\n"
    "v_cvt_pk_bf16_f32 v155, v106, v107\n"
    "v_mfma_f32_16x16x32_bf16 v[124:127], v[208:211], a[124:127], v[124:127]\n"
    "ds_read_b64_tr_b16 a[30:31], v234 offset:63488\n"
    "v_cvt_pk_bf16_f32 v184, v100, v101\n"
    "v_cvt_pk_bf16_f32 v185, v102, v103\n"
    "v_cvt_pk_bf16_f32 v186, v108, v109\n"
    "v_cvt_pk_bf16_f32 v187, v110, v111\n"
    "v_cvt_pk_bf16_f32 v156, v112, v113\n"
    "v_cvt_pk_bf16_f32 v157, v114, v115\n"
    "v_cvt_pk_bf16_f32 v158, v120, v121\n"
    "v_cvt_pk_bf16_f32 v159, v122, v123\n"
    "v_cvt_pk_bf16_f32 v188, v116, v117\n"
    "v_cvt_pk_bf16_f32 v189, v118, v119\n"
    "v_cvt_pk_bf16_f32 v190, v124, v125\n"
    "v_cvt_pk_bf16_f32 v191, v126, v127\n"
    "v_mov_b32 v112, v216\n"
    "v_mov_b32 v113, v218\n"
    "s_waitcnt lgkmcnt(14)\n"
    "v_mfma_f32_16x16x32_bf16 v[0:3], a[0:3], v[128:131], 0\n"
    "s_waitcnt lgkmcnt(6)\n"
    "ds_read_b64_tr_b16 a[32:33], v235 offset:32768\n"
    "ds_read_b64_tr_b16 a[34:35], v235 offset:34816\n"
    "v_mfma_f32_16x16x32_bf16 v[0:3], a[4:7], v[132:135], v[0:3]\n"
    "s_barrier\n"
    "ds_read_b64_tr_b16 a[36:37], v235 offset:36864\n"
    "ds_read_b64_tr_b16 a[38:39], v235 offset:38912\n"
    "v_mfma_f32_16x16x32_bf16 v[0:3], a[8:11], v[136:139], v[0:3]\n"
    "s_and_b32 s49, s52, 7\n"
    "s_lshl_b32 s49, s49, 2\n"
    "s_add_u32 s49, s49, s50\n"
    "v_mfma_f32_16x16x32_bf16 v[0:3], a[12:15], v[140:143], v[0:3]\n"
    "s_lshr_b32 s48, s49, 1\n"
    "ds_read_b64_tr_b16 a[40:41], v235 offset:40960\n"
    "ds_read_b64_tr_b16 a[42:43], v235 offset:43008\n"
    "v_mfma_f32_16x16x32_bf16 v[0:3], a[16:19], v[144:147], v[0:3]\n"
    "s_waitcnt lgkmcnt(6)\n"
    "s_add_u32 s48, s48, 7\n"
    "ds_read_b64_tr_b16 a[44:45], v235 offset:45056\n"
    "ds_read_b64_tr_b16 a[46:47], v235 offset:47104\n"
    "v_mfma_f32_16x16x32_bf16 v[0:3], a[20:23], v[148:151], v[0:3]\n"
    "s_mov_b32 s44, 0\n"
    "ds_read_b64_tr_b16 a[48:49], v235 offset:49152\n"
    "ds_read_b64_tr_b16 a[50:51], v235 offset:51200\n"
    "v_mfma_f32_16x16x32_bf16 v[0:3], a[24:27], v[152:155], v[0:3]\n"
    "s_lshr_b32 s48, s43, s48\n"
    "ds_read_b64_tr_b16 a[52:53], v235 offset:53248\n"
    "ds_read_b64_tr_b16 a[54:55], v235 offset:55296\n"
    "v_mfma_f32_16x16x32_bf16 v[0:3], a[28:31], v[156:159], v[0:3]\n"
    "s_and_b32 s48, s48, 1\n"
    "ds_read_b64_tr_b16 a[56:57], v235 offset:57344\n"
    "ds_read_b64_tr_b16 a[58:59], v235 offset:59392\n"
    "v_mfma_f32_16x16x32_bf16 v[4:7], a[0:3], v[160:163], 0\n"
    "s_lshl_b32 s48, s48, 13\n"
    "ds_read_b64_tr_b16 a[60:61], v235 offset:61440\n"
    "ds_read_b64_tr_b16 a[62:63], v235 offset:63488\n"
    "v_mfma_f32_16x16x32_bf16 v[4:7], a[4:7], v[164:167], v[4:7]\n"
    "s_add_u32 s44, s44, s48\n"
    "s_mov_b32 s68, 256\n"
    "ds_read_b64_tr_b16 a[0:1], v234 offset:33280\n"
    "ds_read_b64_tr_b16 a[2:3], v234 offset:35328\n"
    "v_mfma_f32_16x16x32_bf16 v[4:7], a[8:11], v[168:171], v[4:7]\n"
    "s_add_u32 m0, s69, 65536\n"
    "ds_read_b64_tr_b16 a[4:5], v234 offset:37376\n"
    "ds_read_b64_tr_b16 a[6:7], v234 offset:39424\n"
    "v_mfma_f32_16x16x32_bf16 v[4:7], a[12:15], v[172:175], v[4:7]\n"
    "buffer_load_dwordx4 v238, s[60:63], s68 offen lds\n"
    "v_mfma_f32_16x16x32_bf16 v[4:7], a[16:19], v[176:179], v[4:7]\n"
    "s_mov_b32 s68, 7424\n"
    "v_mul_f32 v0, v0, v112\n"
    "v_mul_f32 v1, v1, v112\n"
    "v_mfma_f32_16x16x32_bf16 v[4:7], a[20:23], v[180:183], v[4:7]\n"
    "s_add_u32 m0, s69, 66560\n"
    "v_mul_f32 v2, v2, v112\n"
    "v_mul_f32 v3, v3, v112\n"
    "v_mfma_f32_16x16x32_bf16 v[4:7], a[24:27], v[184:187], v[4:7]\n"
    "buffer_load_dwordx4 v238, s[60:63], s68 offen lds\n"
    "v_mfma_f32_16x16x32_bf16 v[4:7], a[28:31], v[188:191], v[4:7]\n"
    "s_waitcnt lgkmcnt(12)\n"
    "s_mov_b32 s68, 14592\n"
    "v_cvt_pk_bf16_f32 v16, v0, v1\n"
    "v_cvt_pk_bf16_f32 v17, v2, v3\n"
    "v_mfma_f32_16x16x32_bf16 v[8:11], a[32:35], v[128:131], 0\n"
    "s_add_u32 m0, s69, 67584\n"
    "ds_read_b64_tr_b16 a[8:9], v234 offset:41472\n"
    "v_add_u32 v239, s44, v239\n"
    "v_mfma_f32_16x16x32_bf16 v[8:11], a[36:39], v[132:135], v[8:11]\n"
    "buffer_load_dwordx4 v238, s[60:63], s68 offen lds\n"
    "v_mfma_f32_16x16x32_bf16 v[8:11], a[40:43], v[136:139], v[8:11]\n"
    "s_mov_b32 s68, 21760\n"
    "ds_read_b64_tr_b16 a[10:11], v234 offset:43520\n"
    "ds_read_b64_tr_b16 a[12:13], v234 offset:45568\n"
    "v_mfma_f32_16x16x32_bf16 v[8:11], a[44:47], v[140:143], v[8:11]\n"
    "s_waitcnt lgkmcnt(7)\n"
    "s_add_u32 m0, s69, 68608\n"
    "ds_read_b64_tr_b16 a[14:15], v234 offset:47616\n"
    "v_min_u32 v106, 1, v250\n"
    "v_mfma_f32_16x16x32_bf16 v[8:11], a[48:51], v[144:147], v[8:11]\n"
    "buffer_load_dwordx4 v238, s[60:63], s68 offen lds\n"
    "v_mfma_f32_16x16x32_bf16 v[8:11], a[52:55], v[148:151], v[8:11]\n"
    "s_mov_b32 s68, 28928\n"
    "v_mul_f32 v4, v4, v113\n"
    "v_mul_f32 v5, v5, v113\n"
    "v_mfma_f32_16x16x32_bf16 v[8:11], a[56:59], v[152:155], v[8:11]\n"
    "s_add_u32 m0, s69, 69632\n"
    "v_mul_f32 v6, v6, v113\n"
    "v_mul_f32 v7, v7, v113\n"
    "v_mfma_f32_16x16x32_bf16 v[8:11], a[60:63], v[156:159], v[8:11]\n"
    "buffer_load_dwordx4 v238, s[60:63], s68 offen lds\n"
    "v_mfma_f32_16x16x32_bf16 v[12:15], a[32:35], v[160:163], 0\n"
    "v_cvt_pk_bf16_f32 v20, v4, v5\n"
    "v_cvt_pk_bf16_f32 v21, v6, v7\n"
    "v_mfma_f32_16x16x32_bf16 v[12:15], a[36:39], v[164:167], v[12:15]\n"
    "s_nop 0\n"
    "ds_read_b64_tr_b16 a[16:17], v234 offset:49664\n"
    "ds_read_b64_tr_b16 a[18:19], v234 offset:51712\n"
    "v_mfma_f32_16x16x32_bf16 v[12:15], a[40:43], v[168:171], v[12:15]\n"
    "s_mov_b32 s68, 36096\n"
    "ds_read_b64_tr_b16 a[20:21], v234 offset:53760\n"
    "ds_read_b64_tr_b16 a[22:23], v234 offset:55808\n"
    "v_mfma_f32_16x16x32_bf16 v[12:15], a[44:47], v[172:175], v[12:15]\n"
    "s_add_u32 m0, s69, 70656\n"
    "ds_read_b64_tr_b16 a[24:25], v234 offset:57856\n"
    "v_lshlrev_b32 v106, 30, v106\n"
    "v_mfma_f32_16x16x32_bf16 v[12:15], a[48:51], v[176:179], v[12:15]\n"
    "buffer_load_dwordx4 v238, s[60:63], s68 offen lds\n"
    "v_mfma_f32_16x16x32_bf16 v[12:15], a[52:55], v[180:183], v[12:15]\n"
    "ds_read_b64_tr_b16 a[26:27], v234 offset:59904\n"
    "v_mul_f32 v8, v8, v112\n"
    "v_mfma_f32_16x16x32_bf16 v[12:15], a[56:59], v[184:187], v[12:15]\n"
    "s_waitcnt lgkmcnt(8)\n"
    "ds_read_b64_tr_b16 a[28:29], v234 offset:61952\n"
    "ds_read_b64_tr_b16 a[30:31], v234 offset:64000\n"
    "v_mfma_f32_16x16x32_bf16 v[12:15], a[60:63], v[188:191], v[12:15]\n"
    "s_mov_b32 s68, 43264\n"
    "v_mul_f32 v9, v9, v112\n"
    "v_lshrrev_b32 v107, 4, v250\n"
    "v_mfma_f32_16x16x32_bf16 v[0:3], a[0:3], v[128:131], 0\n"
    "s_add_u32 m0, s69, 71680\n"
    "v_mul_f32 v10, v10, v112\n"
    "v_mul_f32 v11, v11, v112\n"
    "v_mfma_f32_16x16x32_bf16 v[0:3], a[4:7], v[132:135], v[0:3]\n"
    "buffer_load_dwordx4 v238, s[60:63], s68 offen lds\n"
    "v_mfma_f32_16x16x32_bf16 v[0:3], a[8:11], v[136:139], v[0:3]\n"
    "s_waitcnt lgkmcnt(8)\n"
    "v_lshlrev_b32 v107, 27, v107\n"
    "v_cvt_pk_bf16_f32 v18, v8, v9\n"
    "v_mfma_f32_16x16x32_bf16 v[0:3], a[12:15], v[140:143], v[0:3]\n"
    "s_waitcnt lgkmcnt(0)\n"
    "v_cvt_pk_bf16_f32 v19, v10, v11\n"
    "v_lshl_add_u32 v107, v250, 2, v107\n"
    "v_mfma_f32_16x16x32_bf16 v[0:3], a[16:19], v[144:147], v[0:3]\n"
    "s_mov_b32 s56, 14680064\n"
    "v_mov_b32 v104, s53\n"
    "v_mul_f32 v12, v12, v113\n"
    "v_mfma_f32_16x16x32_bf16 v[0:3], a[20:23], v[148:151], v[0:3]\n"
    "buffer_store_dwordx4 v[16:19], v239, s[64:67], s56 offen sc1\n"
    "v_mfma_f32_16x16x32_bf16 v[0:3], a[24:27], v[152:155], v[0:3]\n"
    "s_waitcnt vmcnt(8)\n"
    "v_mul_f32 v13, v13, v113\n"
    "v_mov_b32 v108, 0\n"
    "v_mfma_f32_16x16x32_bf16 v[0:3], a[28:31], v[156:159], v[0:3]\n"
    "s_barrier\n"
    "ds_read_b64_tr_b16 a[32:33], v235 offset:33280\n"
    "ds_read_b64_tr_b16 a[34:35], v235 offset:35328\n"
    "v_mfma_f32_16x16x32_bf16 v[4:7], a[0:3], v[160:163], 0\n"
    "s_mov_b32 s68, 50432\n"
    "ds_read_b64_tr_b16 a[36:37], v235 offset:37376\n"
    "v_mul_f32 v14, v14, v113\n"
    "v_mfma_f32_16x16x32_bf16 v[4:7], a[4:7], v[164:167], v[4:7]\n"
    "s_add_u32 m0, s69, 72704\n"
    "ds_read_b64_tr_b16 a[38:39], v235 offset:39424\n"
    "v_mul_f32 v15, v15, v113\n"
    "v_mfma_f32_16x16x32_bf16 v[4:7], a[8:11], v[168:171], v[4:7]\n"
    "buffer_load_dwordx4 v238, s[60:63], s68 offen lds\n"
    "v_lshlrev_b32 v110, 4, v250\n"
    "v_mfma_f32_16x16x32_bf16 v[4:7], a[12:15], v[172:175], v[4:7]\n"
    "s_lshl_b32 s49, s49, 5\n"
    "ds_read_b64_tr_b16 a[40:41], v235 offset:41472\n"
    "ds_read_b64_tr_b16 a[42:43], v235 offset:43520\n"
    "v_cvt_pk_bf16_f32 v22, v12, v13\n"
    "v_mfma_f32_16x16x32_bf16 v[4:7], a[16:19], v[176:179], v[4:7]\n"
    "s_add_u32 s49, s49, s51\n"
    "ds_read_b64_tr_b16 a[44:45], v235 offset:45568\n"
    "ds_read_b64_tr_b16 a[46:47], v235 offset:47616\n"
    "v_cvt_pk_bf16_f32 v23, v14, v15\n"
    "v_mfma_f32_16x16x32_bf16 v[4:7], a[20:23], v[180:183], v[4:7]\n"
    "s_lshl_b32 s49, s49, 2\n"
    "ds_read_b64_tr_b16 a[48:49], v235 offset:49664\n"
    "ds_read_b64_tr_b16 a[50:51], v235 offset:51712\n"
    "v_mfma_f32_16x16x32_bf16 v[4:7], a[24:27], v[184:187], v[4:7]\n"
    "s_add_u32 s49, s49, 262144\n"
    "ds_read_b64_tr_b16 a[52:53], v235 offset:53760\n"
    "ds_read_b64_tr_b16 a[54:55], v235 offset:55808\n"
    "v_mfma_f32_16x16x32_bf16 v[4:7], a[28:31], v[188:191], v[4:7]\n"
    "s_waitcnt lgkmcnt(4)\n"
    "s_bfe_u32 s48, s43, 65536\n"
    "ds_read_b64_tr_b16 a[56:57], v235 offset:57856\n"
    "ds_read_b64_tr_b16 a[58:59], v235 offset:59904\n"
    "v_mul_f32 v0, v0, v112\n"
    "v_mfma_f32_16x16x32_bf16 v[8:11], a[32:35], v[128:131], 0\n"
    "s_lshl_b32 s48, s48, 13\n"
    "ds_read_b64_tr_b16 a[60:61], v235 offset:61952\n"
    "ds_read_b64_tr_b16 a[62:63], v235 offset:64000\n"
    "v_mul_f32 v1, v1, v112\n"
    "v_mfma_f32_16x16x32_bf16 v[8:11], a[36:39], v[132:135], v[8:11]\n"
    "s_add_u32 s49, s49, s48\n"
    "v_mul_f32 v2, v2, v112\n"
    "v_mul_f32 v3, v3, v112\n"
    "v_mfma_f32_16x16x32_bf16 v[8:11], a[40:43], v[136:139], v[8:11]\n"
    "s_bfe_u32 s51, s70, 0x50002\n"
    "v_cvt_pk_bf16_f32 v24, v0, v1\n"
    "v_cvt_pk_bf16_f32 v25, v2, v3\n"
    "v_mfma_f32_16x16x32_bf16 v[8:11], a[44:47], v[140:143], v[8:11]\n"
    "s_waitcnt lgkmcnt(0)\n"
    "s_lshl_b32 s51, s51, 7\n"
    "ds_read_b64_tr_b16 a[0:1], v236 offset:0\n"
    "ds_read_b64_tr_b16 a[2:3], v236 offset:2048\n"
    "v_mfma_f32_16x16x32_bf16 v[8:11], a[48:51], v[144:147], v[8:11]\n"
    "s_add_u32 s51, s51, 262144\n"
    "s_mov_b32 s56, 15204352\n"
    "v_mul_f32 v4, v4, v113\n"
    "v_mul_f32 v5, v5, v113\n"
    "v_mfma_f32_16x16x32_bf16 v[8:11], a[52:55], v[148:151], v[8:11]\n"
    "buffer_store_dwordx4 v[20:23], v239, s[64:67], s56 offen sc1\n"
    "v_mfma_f32_16x16x32_bf16 v[8:11], a[56:59], v[152:155], v[8:11]\n"
    "s_bfe_u32 s48, s42, 65536\n"
    "ds_read_b64_tr_b16 a[4:5], v236 offset:4096\n"
    "v_mul_f32 v6, v6, v113\n"
    "v_mfma_f32_16x16x32_bf16 v[8:11], a[60:63], v[156:159], v[8:11]\n"
    "s_lshl_b32 s48, s48, 13\n"
    "s_mov_b32 s68, 384\n"
    "ds_read_b64_tr_b16 a[6:7], v236 offset:6144\n"
    "v_mul_f32 v7, v7, v113\n"
    "v_mfma_f32_16x16x32_bf16 v[12:15], a[32:35], v[160:163], 0\n"
    "s_add_u32 s51, s51, s48\n"
    "s_add_u32 m0, s69, 0\n"
    "ds_read_b64_tr_b16 a[8:9], v236 offset:8192\n"
    "ds_read_b64_tr_b16 a[10:11], v236 offset:10240\n"
    "v_mfma_f32_16x16x32_bf16 v[12:15], a[36:39], v[164:167], v[12:15]\n"
    "buffer_load_dwordx4 v238, s[60:63], s68 offen lds\n"
    "v_mfma_f32_16x16x32_bf16 v[12:15], a[40:43], v[168:171], v[12:15]\n"
    "s_and_b32 s45, s70, 127\n"
    "ds_read_b64_tr_b16 a[12:13], v236 offset:12288\n"
    "v_cvt_pk_bf16_f32 v28, v4, v5\n"
    "v_mfma_f32_16x16x32_bf16 v[12:15], a[44:47], v[172:175], v[12:15]\n"
    "s_lshl_b32 s45, s45, 4\n"
    "ds_read_b64_tr_b16 a[14:15], v236 offset:14336\n"
    "v_cvt_pk_bf16_f32 v29, v6, v7\n"
    "v_mfma_f32_16x16x32_bf16 v[12:15], a[48:51], v[176:179], v[12:15]\n"
    "s_add_u32 s45, s45, s50\n"
    "s_mov_b32 s68, 7552\n"
    "ds_read_b64_tr_b16 a[16:17], v236 offset:16384\n"
    "ds_read_b64_tr_b16 a[18:19], v236 offset:18432\n"
    "v_mfma_f32_16x16x32_bf16 v[12:15], a[52:55], v[180:183], v[12:15]\n"
    "s_lshl_b32 s45, s45, 14\n"
    "s_add_u32 m0, s69, 1024\n"
    "ds_read_b64_tr_b16 a[20:21], v236 offset:20480\n"
    "ds_read_b64_tr_b16 a[22:23], v236 offset:22528\n"
    "v_mfma_f32_16x16x32_bf16 v[12:15], a[56:59], v[184:187], v[12:15]\n"
    "buffer_load_dwordx4 v238, s[60:63], s68 offen lds\n"
    "v_mfma_f32_16x16x32_bf16 v[12:15], a[60:63], v[188:191], v[12:15]\n"
    "s_waitcnt lgkmcnt(4)\n"
    "s_add_u32 s45, s45, 14680064\n"
    "ds_read_b64_tr_b16 a[24:25], v236 offset:24576\n"
    "ds_read_b64_tr_b16 a[26:27], v236 offset:26624\n"
    "v_mul_f32 v8, v8, v112\n"
    "v_mfma_f32_16x16x32_bf16 v[0:3], a[0:3], v[128:131], 0\n"
    "s_lshr_b32 s48, s45, 21\n"
    "ds_read_b64_tr_b16 a[28:29], v236 offset:28672\n"
    "ds_read_b64_tr_b16 a[30:31], v236 offset:30720\n"
    "v_mul_f32 v9, v9, v112\n"
    "v_mfma_f32_16x16x32_bf16 v[0:3], a[4:7], v[132:135], v[0:3]\n"
    "s_lshr_b32 s48, s42, s48\n"
    "s_mov_b32 s68, 14720\n"
    "v_mul_f32 v10, v10, v112\n"
    "v_mul_f32 v11, v11, v112\n"
    "v_mfma_f32_16x16x32_bf16 v[0:3], a[8:11], v[136:139], v[0:3]\n"
    "s_and_b32 s48, s48, 1\n"
    "s_add_u32 m0, s69, 2048\n"
    "v_cvt_pk_bf16_f32 v26, v8, v9\n"
    "v_cvt_pk_bf16_f32 v27, v10, v11\n"
    "v_mfma_f32_16x16x32_bf16 v[0:3], a[12:15], v[140:143], v[0:3]\n"
    "s_waitcnt lgkmcnt(0)\n"
    "buffer_load_dwordx4 v238, s[60:63], s68 offen lds\n"
    "v_mfma_f32_16x16x32_bf16 v[0:3], a[16:19], v[144:147], v[0:3]\n"
    "s_lshl_b32 s48, s48, 13\n"
    "v_mul_f32 v12, v12, v113\n"
    "v_mul_f32 v13, v13, v113\n"
    "v_mfma_f32_16x16x32_bf16 v[0:3], a[20:23], v[148:151], v[0:3]\n"
    "s_add_u32 s45, s45, s48\n"
    "ds_read_b64_tr_b16 a[32:33], v237 offset:0\n"
    "ds_read_b64_tr_b16 a[34:35], v237 offset:2048\n"
    "v_mfma_f32_16x16x32_bf16 v[0:3], a[24:27], v[152:155], v[0:3]\n"
    "s_lshl_b32 s52, s50, 10\n"
    "s_mov_b32 s68, 21888\n"
    "ds_read_b64_tr_b16 a[36:37], v237 offset:4096\n"
    "v_mul_f32 v14, v14, v113\n"
    "v_mfma_f32_16x16x32_bf16 v[0:3], a[28:31], v[156:159], v[0:3]\n"
    "s_mov_b64 s[92:93], s[8:9]\n"
    "s_add_u32 m0, s69, 3072\n"
    "ds_read_b64_tr_b16 a[38:39], v237 offset:6144\n"
    "v_mul_f32 v15, v15, v113\n"
    "v_mfma_f32_16x16x32_bf16 v[4:7], a[0:3], v[160:163], 0\n"
    "buffer_load_dwordx4 v238, s[60:63], s68 offen lds\n"
    "v_mfma_f32_16x16x32_bf16 v[4:7], a[4:7], v[164:167], v[4:7]\n"
    "s_and_b32 s93, s93, 0xffff\n"
    "ds_read_b64_tr_b16 a[40:41], v237 offset:8192\n"
    "ds_read_b64_tr_b16 a[42:43], v237 offset:10240\n"
    "v_mfma_f32_16x16x32_bf16 v[4:7], a[8:11], v[168:171], v[4:7]\n"
    "s_mov_b32 s94, 1835008\n"
    "ds_read_b64_tr_b16 a[44:45], v237 offset:12288\n"
    "v_cvt_pk_bf16_f32 v30, v12, v13\n"
    "v_mfma_f32_16x16x32_bf16 v[4:7], a[12:15], v[172:175], v[4:7]\n"
    "s_mov_b32 s95, 0x20000\n"
    "s_mov_b32 s68, 29056\n"
    "ds_read_b64_tr_b16 a[46:47], v237 offset:14336\n"
    "v_cvt_pk_bf16_f32 v31, v14, v15\n"
    "v_mfma_f32_16x16x32_bf16 v[4:7], a[16:19], v[176:179], v[4:7]\n"
    "s_mov_b32 s56, 14696448\n"
    "ds_read_b64_tr_b16 a[48:49], v237 offset:16384\n"
    "ds_read_b64_tr_b16 a[50:51], v237 offset:18432\n"
    "v_mfma_f32_16x16x32_bf16 v[4:7], a[20:23], v[180:183], v[4:7]\n"
    "buffer_store_dwordx4 v[24:27], v239, s[64:67], s56 offen sc1\n"
    "s_add_u32 m0, s69, 4096\n"
    "s_nop 0\n"
    "buffer_load_dwordx4 v238, s[60:63], s68 offen lds\n"
    "v_mfma_f32_16x16x32_bf16 v[4:7], a[24:27], v[184:187], v[4:7]\n"
    "s_mov_b32 s68, 36224\n"
    "ds_read_b64_tr_b16 a[52:53], v237 offset:20480\n"
    "ds_read_b64_tr_b16 a[54:55], v237 offset:22528\n"
    "v_mfma_f32_16x16x32_bf16 v[4:7], a[28:31], v[188:191], v[4:7]\n"
    "s_waitcnt lgkmcnt(4)\n"
    "s_add_u32 m0, s69, 5120\n"
    "ds_read_b64_tr_b16 a[56:57], v237 offset:24576\n"
    "ds_read_b64_tr_b16 a[58:59], v237 offset:26624\n"
    "v_mul_f32 v0, v0, v112\n"
    "v_mfma_f32_16x16x32_bf16 v[8:11], a[32:35], v[128:131], 0\n"
    "s_nop 0\n"
    "buffer_load_dwordx4 v238, s[60:63], s68 offen lds\n"
    "ds_read_b64_tr_b16 a[60:61], v237 offset:28672\n"
    "ds_read_b64_tr_b16 a[62:63], v237 offset:30720\n"
    "v_mul_f32 v1, v1, v112\n"
    "v_mfma_f32_16x16x32_bf16 v[8:11], a[36:39], v[132:135], v[8:11]\n"
    "s_mov_b32 s56, 15220736\n"
    "v_mul_f32 v2, v2, v112\n"
    "v_mul_f32 v3, v3, v112\n"
    "v_mfma_f32_16x16x32_bf16 v[8:11], a[40:43], v[136:139], v[8:11]\n"
    "buffer_store_dwordx4 v[28:31], v239, s[64:67], s56 offen sc1\n"
    "v_mfma_f32_16x16x32_bf16 v[8:11], a[44:47], v[140:143], v[8:11]\n"
    "s_waitcnt lgkmcnt(0)\n"
    "s_mov_b32 s68, 43392\n"
    "v_cvt_pk_bf16_f32 v16, v0, v1\n"
    "v_cvt_pk_bf16_f32 v17, v2, v3\n"
    "v_mfma_f32_16x16x32_bf16 v[8:11], a[48:51], v[144:147], v[8:11]\n"
    "s_add_u32 m0, s69, 6144\n"
    "v_mul_f32 v4, v4, v113\n"
    "v_mul_f32 v5, v5, v113\n"
    "v_mfma_f32_16x16x32_bf16 v[8:11], a[52:55], v[148:151], v[8:11]\n"
    "buffer_load_dwordx4 v238, s[60:63], s68 offen lds\n"
    "v_mfma_f32_16x16x32_bf16 v[8:11], a[56:59], v[152:155], v[8:11]\n"
    "s_mov_b32 s68, 50560\n"
    "ds_read_b64_tr_b16 a[0:1], v236 offset:512\n"
    "ds_read_b64_tr_b16 a[2:3], v236 offset:2560\n"
    "v_mfma_f32_16x16x32_bf16 v[8:11], a[60:63], v[156:159], v[8:11]\n"
    "s_add_u32 m0, s69, 7168\n"
    "ds_read_b64_tr_b16 a[4:5], v236 offset:4608\n"
    "v_mul_f32 v6, v6, v113\n"
    "v_mfma_f32_16x16x32_bf16 v[12:15], a[32:35], v[160:163], 0\n"
    "buffer_load_dwordx4 v238, s[60:63], s68 offen lds\n"
    "v_mfma_f32_16x16x32_bf16 v[12:15], a[36:39], v[164:167], v[12:15]\n"
    "ds_read_b64_tr_b16 a[6:7], v236 offset:6656\n"
    "v_mul_f32 v7, v7, v113\n"
    "v_mfma_f32_16x16x32_bf16 v[12:15], a[40:43], v[168:171], v[12:15]\n"
    "s_nop 0\n"
    "ds_read_b64_tr_b16 a[8:9], v236 offset:8704\n"
    "ds_read_b64_tr_b16 a[10:11], v236 offset:10752\n"
    "v_mfma_f32_16x16x32_bf16 v[12:15], a[44:47], v[172:175], v[12:15]\n"
    "ds_read_b64_tr_b16 a[12:13], v236 offset:12800\n"
    "v_cvt_pk_bf16_f32 v20, v4, v5\n"
    "v_mfma_f32_16x16x32_bf16 v[12:15], a[48:51], v[176:179], v[12:15]\n"
    "s_waitcnt lgkmcnt(5)\n"
    "ds_read_b64_tr_b16 a[14:15], v236 offset:14848\n"
    "v_cvt_pk_bf16_f32 v21, v6, v7\n"
    "v_mfma_f32_16x16x32_bf16 v[12:15], a[52:55], v[180:183], v[12:15]\n"
    "s_mov_b32 s68, 512\n"
    "ds_read_b64_tr_b16 a[16:17], v236 offset:16896\n"
    "ds_read_b64_tr_b16 a[18:19], v236 offset:18944\n"
    "v_mfma_f32_16x16x32_bf16 v[12:15], a[56:59], v[184:187], v[12:15]\n"
    "s_add_u32 m0, s69, 32768\n"
    "ds_read_b64_tr_b16 a[20:21], v236 offset:20992\n"
    "ds_read_b64_tr_b16 a[22:23], v236 offset:23040\n"
    "v_mfma_f32_16x16x32_bf16 v[12:15], a[60:63], v[188:191], v[12:15]\n"
    "s_nop 0\n"
    "buffer_load_dwordx4 v238, s[60:63], s68 offen lds\n"
    "ds_read_b64_tr_b16 a[24:25], v236 offset:25088\n"
    "ds_read_b64_tr_b16 a[26:27], v236 offset:27136\n"
    "v_mul_f32 v8, v8, v112\n"
    "v_mfma_f32_16x16x32_bf16 v[0:3], a[0:3], v[128:131], 0\n"
    "s_waitcnt lgkmcnt(10)\n"
    "ds_read_b64_tr_b16 a[28:29], v236 offset:29184\n"
    "ds_read_b64_tr_b16 a[30:31], v236 offset:31232\n"
    "v_mul_f32 v9, v9, v112\n"
    "v_mfma_f32_16x16x32_bf16 v[0:3], a[4:7], v[132:135], v[0:3]\n"
    "s_waitcnt lgkmcnt(4)\n"
    "v_mul_f32 v10, v10, v112\n"
    "v_mul_f32 v11, v11, v112\n"
    "v_mfma_f32_16x16x32_bf16 v[0:3], a[8:11], v[136:139], v[0:3]\n"
    "s_mov_b32 s68, 7680\n"
    "v_cvt_pk_bf16_f32 v18, v8, v9\n"
    "v_cvt_pk_bf16_f32 v19, v10, v11\n"
    "v_mfma_f32_16x16x32_bf16 v[0:3], a[12:15], v[140:143], v[0:3]\n"
    "s_add_u32 m0, s69, 33792\n"
    "ds_read_b64_tr_b16 a[32:33], v237 offset:512\n"
    "v_and_b32 v248, 7, v250\n"
    "v_mfma_f32_16x16x32_bf16 v[0:3], a[16:19], v[144:147], v[0:3]\n"
    "buffer_load_dwordx4 v238, s[60:63], s68 offen lds\n"
    "v_mfma_f32_16x16x32_bf16 v[0:3], a[20:23], v[148:151], v[0:3]\n"
    "s_waitcnt lgkmcnt(1)\n"
    "s_lshl_b32 s54, s70, 3\n"
    "v_mul_f32 v12, v12, v113\n"
    "v_mul_f32 v13, v13, v113\n"
    "v_mfma_f32_16x16x32_bf16 v[0:3], a[24:27], v[152:155], v[0:3]\n"
    "s_waitcnt vmcnt(13)\n"
    "ds_read_b64_tr_b16 a[34:35], v237 offset:2560\n"
    "v_add_u32 v248, s54, v248\n"
    "v_mfma_f32_16x16x32_bf16 v[0:3], a[28:31], v[156:159], v[0:3]\n"
    "s_barrier\n"
    "ds_read_b64_tr_b16 a[36:37], v237 offset:4608\n"
    "v_mul_f32 v14, v14, v113\n"
    "v_mfma_f32_16x16x32_bf16 v[4:7], a[0:3], v[160:163], 0\n"
    "s_mov_b32 s56, 14712832\n"
    "ds_read_b64_tr_b16 a[38:39], v237 offset:6656\n"
    "v_mul_f32 v15, v15, v113\n"
    "v_mfma_f32_16x16x32_bf16 v[4:7], a[4:7], v[164:167], v[4:7]\n"
    "buffer_store_dwordx4 v[16:19], v239, s[64:67], s56 offen sc1\n"
    "v_mfma_f32_16x16x32_bf16 v[4:7], a[8:11], v[168:171], v[4:7]\n"
    "s_mov_b32 s68, 14848\n"
    "ds_read_b64_tr_b16 a[40:41], v237 offset:8704\n"
    "v_mul_u32_u24 v248, 896, v248\n"
    "v_mfma_f32_16x16x32_bf16 v[4:7], a[12:15], v[172:175], v[4:7]\n"
    "s_add_u32 m0, s69, 34816\n"
    "ds_read_b64_tr_b16 a[42:43], v237 offset:10752\n"
    "v_cvt_pk_bf16_f32 v22, v12, v13\n"
    "v_mfma_f32_16x16x32_bf16 v[4:7], a[16:19], v[176:179], v[4:7]\n"
    "s_nop 0\n"
    "buffer_load_dwordx4 v238, s[60:63], s68 offen lds\n"
    "ds_read_b64_tr_b16 a[44:45], v237 offset:12800\n"
    "ds_read_b64_tr_b16 a[46:47], v237 offset:14848\n"
    "v_cvt_pk_bf16_f32 v23, v14, v15\n"
    "v_lshrrev_b32 v249, 3, v250\n"
    "v_mfma_f32_16x16x32_bf16 v[4:7], a[20:23], v[180:183], v[4:7]\n"
    "s_mov_b32 s56, 15237120\n"
    "ds_read_b64_tr_b16 a[48:49], v237 offset:16896\n"
    "ds_read_b64_tr_b16 a[50:51], v237 offset:18944\n"
    "v_mfma_f32_16x16x32_bf16 v[4:7], a[24:27], v[184:187], v[4:7]\n"
    "s_nop 0\n"
    "buffer_store_dwordx4 v[20:23], v239, s[64:67], s56 offen sc1\n"
    "ds_read_b64_tr_b16 a[52:53], v237 offset:20992\n"
    "ds_read_b64_tr_b16 a[54:55], v237 offset:23040\n"
    "v_lshl_add_u32 v248, v249, 3, v248\n"
    "v_mfma_f32_16x16x32_bf16 v[4:7], a[28:31], v[188:191], v[4:7]\n"
    "s_waitcnt lgkmcnt(4)\n"
    "s_mov_b32 s68, 22016\n"
    "ds_read_b64_tr_b16 a[56:57], v237 offset:25088\n"
    "ds_read_b64_tr_b16 a[58:59], v237 offset:27136\n"
    "v_mul_f32 v0, v0, v112\n"
    "v_mfma_f32_16x16x32_bf16 v[8:11], a[32:35], v[128:131], 0\n"
    "s_add_u32 m0, s69, 35840\n"
    "ds_read_b64_tr_b16 a[60:61], v237 offset:29184\n"
    "ds_read_b64_tr_b16 a[62:63], v237 offset:31232\n"
    "v_mul_f32 v1, v1, v112\n"
    "v_lshlrev_b32 v249, 4, v249\n"
    "v_mfma_f32_16x16x32_bf16 v[8:11], a[36:39], v[132:135], v[8:11]\n"
    "buffer_load_dwordx4 v238, s[60:63], s68 offen lds\n"
    "v_mul_f32 v2, v2, v112\n"
    "v_mfma_f32_16x16x32_bf16 v[8:11], a[40:43], v[136:139], v[8:11]\n"
    "s_mul_i32 s46, s96, 7\n"
    "v_mul_f32 v3, v3, v112\n"
    "v_cvt_pk_bf16_f32 v24, v0, v1\n"
    "v_mfma_f32_16x16x32_bf16 v[8:11], a[44:47], v[140:143], v[8:11]\n"
    "s_waitcnt lgkmcnt(0)\n"
    "s_and_b32 s54, s96, 1\n"
    "s_xor_b32 s55, s54, 1\n"
    "v_cvt_pk_bf16_f32 v25, v2, v3\n"
    "v_mfma_f32_16x16x32_bf16 v[8:11], a[48:51], v[144:147], v[8:11]\n"
    "s_mul_i32 s55, s55, 6\n"
    "v_mul_f32 v4, v4, v113\n"
    "v_mul_f32 v5, v5, v113\n"
    "v_mfma_f32_16x16x32_bf16 v[8:11], a[52:55], v[148:151], v[8:11]\n"
    "s_add_u32 s47, s46, s55\n"
    "ds_read_b64_tr_b16 a[0:1], v234 offset:0\n"
    "ds_read_b64_tr_b16 a[2:3], v234 offset:2048\n"
    "v_mfma_f32_16x16x32_bf16 v[8:11], a[56:59], v[152:155], v[8:11]\n"
    "s_add_u32 s46, s46, s54\n"
    "ds_read_b64_tr_b16 a[4:5], v234 offset:4096\n"
    "v_mul_f32 v6, v6, v113\n"
    "v_mfma_f32_16x16x32_bf16 v[8:11], a[60:63], v[156:159], v[8:11]\n"
    "s_mul_i32 s46, s46, 28672\n"
    "ds_read_b64_tr_b16 a[6:7], v234 offset:6144\n"
    "v_mul_f32 v7, v7, v113\n"
    "v_mfma_f32_16x16x32_bf16 v[12:15], a[32:35], v[160:163], 0\n"
    "s_mul_i32 s47, s47, 28672\n"
    "ds_read_b64_tr_b16 a[8:9], v234 offset:8192\n"
    "ds_read_b64_tr_b16 a[10:11], v234 offset:10240\n"
    "v_mfma_f32_16x16x32_bf16 v[12:15], a[36:39], v[164:167], v[12:15]\n"
    "s_mov_b32 s68, 29184\n"
    "ds_read_b64_tr_b16 a[12:13], v234 offset:12288\n"
    "v_cvt_pk_bf16_f32 v28, v4, v5\n"
    "v_mfma_f32_16x16x32_bf16 v[12:15], a[40:43], v[168:171], v[12:15]\n"
    "s_add_u32 m0, s69, 36864\n"
    "ds_read_b64_tr_b16 a[14:15], v234 offset:14336\n"
    "v_cvt_pk_bf16_f32 v29, v6, v7\n"
    "v_mfma_f32_16x16x32_bf16 v[12:15], a[44:47], v[172:175], v[12:15]\n"
    "buffer_load_dwordx4 v238, s[60:63], s68 offen lds\n"
    "v_mfma_f32_16x16x32_bf16 v[12:15], a[48:51], v[176:179], v[12:15]\n"
    "s_mov_b32 s68, 36352\n"
    "ds_read_b64_tr_b16 a[16:17], v234 offset:16384\n"
    "ds_read_b64_tr_b16 a[18:19], v234 offset:18432\n"
    "v_mfma_f32_16x16x32_bf16 v[12:15], a[52:55], v[180:183], v[12:15]\n"
    "s_add_u32 m0, s69, 37888\n"
    "ds_read_b64_tr_b16 a[20:21], v234 offset:20480\n"
    "ds_read_b64_tr_b16 a[22:23], v234 offset:22528\n"
    "v_mfma_f32_16x16x32_bf16 v[12:15], a[56:59], v[184:187], v[12:15]\n"
    "buffer_load_dwordx4 v238, s[60:63], s68 offen lds\n"
    "v_mfma_f32_16x16x32_bf16 v[12:15], a[60:63], v[188:191], v[12:15]\n"
    "s_waitcnt lgkmcnt(4)\n"
    "s_mov_b32 s68, 43520\n"
    "ds_read_b64_tr_b16 a[24:25], v234 offset:24576\n"
    "ds_read_b64_tr_b16 a[26:27], v234 offset:26624\n"
    "v_mul_f32 v8, v8, v112\n"
    "v_mfma_f32_16x16x32_bf16 v[0:3], a[0:3], v[128:131], 0\n"
    "s_add_u32 m0, s69, 38912\n"
    "ds_read_b64_tr_b16 a[28:29], v234 offset:28672\n"
    "ds_read_b64_tr_b16 a[30:31], v234 offset:30720\n"
    "v_mul_f32 v9, v9, v112\n"
    "v_mfma_f32_16x16x32_bf16 v[0:3], a[4:7], v[132:135], v[0:3]\n"
    "buffer_load_dwordx4 v238, s[60:63], s68 offen lds\n"
    "v_mul_f32 v10, v10, v112\n"
    "v_mfma_f32_16x16x32_bf16 v[0:3], a[8:11], v[136:139], v[0:3]\n"
    "s_mov_b32 s68, 50688\n"
    "v_mul_f32 v11, v11, v112\n"
    "v_lshlrev_b32 v251, 4, v250\n"
    "v_mfma_f32_16x16x32_bf16 v[0:3], a[12:15], v[140:143], v[0:3]\n"
    "s_waitcnt lgkmcnt(0)\n"
    "s_add_u32 m0, s69, 39936\n"
    "v_cvt_pk_bf16_f32 v26, v8, v9\n"
    "v_cvt_pk_bf16_f32 v27, v10, v11\n"
    "v_mfma_f32_16x16x32_bf16 v[0:3], a[16:19], v[144:147], v[0:3]\n"
    "buffer_load_dwordx4 v238, s[60:63], s68 offen lds\n"
    "v_add_u32 v251, s52, v251\n"
    "v_mfma_f32_16x16x32_bf16 v[0:3], a[20:23], v[148:151], v[0:3]\n"
    "s_mov_b32 s56, 14729216\n"
    "v_mul_f32 v12, v12, v113\n"
    "v_mul_f32 v13, v13, v113\n"
    "v_mfma_f32_16x16x32_bf16 v[0:3], a[24:27], v[152:155], v[0:3]\n"
    "buffer_store_dwordx4 v[24:27], v239, s[64:67], s56 offen sc1\n"
    "v_mfma_f32_16x16x32_bf16 v[0:3], a[28:31], v[156:159], v[0:3]\n"
    "s_nop 0\n"
    "ds_read_b64_tr_b16 a[32:33], v235 offset:0\n"
    "ds_read_b64_tr_b16 a[34:35], v235 offset:2048\n"
    "v_mfma_f32_16x16x32_bf16 v[4:7], a[0:3], v[160:163], 0\n"
    "ds_read_b64_tr_b16 a[36:37], v235 offset:4096\n"
    "v_mul_f32 v14, v14, v113\n"
    "v_mfma_f32_16x16x32_bf16 v[4:7], a[4:7], v[164:167], v[4:7]\n"
    "ds_read_b64_tr_b16 a[38:39], v235 offset:6144\n"
    "v_mul_f32 v15, v15, v113\n"
    "v_mfma_f32_16x16x32_bf16 v[4:7], a[8:11], v[168:171], v[4:7]\n"
    "s_nop 0\n"
    "ds_read_b64_tr_b16 a[40:41], v235 offset:8192\n"
    "ds_read_b64_tr_b16 a[42:43], v235 offset:10240\n"
    "v_mfma_f32_16x16x32_bf16 v[4:7], a[12:15], v[172:175], v[4:7]\n"
    "ds_read_b64_tr_b16 a[44:45], v235 offset:12288\n"
    "v_cvt_pk_bf16_f32 v30, v12, v13\n"
    "v_mfma_f32_16x16x32_bf16 v[4:7], a[16:19], v[176:179], v[4:7]\n"
    "s_mov_b32 s56, 15253504\n"
    "ds_read_b64_tr_b16 a[46:47], v235 offset:14336\n"
    "v_cvt_pk_bf16_f32 v31, v14, v15\n"
    "v_mfma_f32_16x16x32_bf16 v[4:7], a[20:23], v[180:183], v[4:7]\n"
    "s_nop 0\n"
    "buffer_store_dwordx4 v[28:31], v239, s[64:67], s56 offen sc1\n"
    "ds_read_b64_tr_b16 a[48:49], v235 offset:16384\n"
    "ds_read_b64_tr_b16 a[50:51], v235 offset:18432\n"
    "v_mfma_f32_16x16x32_bf16 v[4:7], a[24:27], v[184:187], v[4:7]\n"
    "s_mov_b32 s68, 640\n"
    "ds_read_b64_tr_b16 a[52:53], v235 offset:20480\n"
    "ds_read_b64_tr_b16 a[54:55], v235 offset:22528\n"
    "v_mfma_f32_16x16x32_bf16 v[4:7], a[28:31], v[188:191], v[4:7]\n"
    "s_waitcnt lgkmcnt(4)\n"
    "s_add_u32 m0, s69, 98304\n"
    "ds_read_b64_tr_b16 a[56:57], v235 offset:24576\n"
    "ds_read_b64_tr_b16 a[58:59], v235 offset:26624\n"
    "v_mul_f32 v0, v0, v112\n"
    "v_mfma_f32_16x16x32_bf16 v[8:11], a[32:35], v[128:131], 0\n"
    "s_nop 0\n"
    "buffer_load_dwordx4 v238, s[60:63], s68 offen lds\n"
    "ds_read_b64_tr_b16 a[60:61], v235 offset:28672\n"
    "ds_read_b64_tr_b16 a[62:63], v235 offset:30720\n"
    "v_mul_f32 v1, v1, v112\n"
    "v_mfma_f32_16x16x32_bf16 v[8:11], a[36:39], v[132:135], v[8:11]\n"
    "s_mov_b32 s68, 7808\n"
    "v_mul_f32 v2, v2, v112\n"
    "v_mul_f32 v3, v3, v112\n"
    "v_mfma_f32_16x16x32_bf16 v[8:11], a[40:43], v[136:139], v[8:11]\n"
    "s_add_u32 m0, s69, 99328\n"
    "v_cvt_pk_bf16_f32 v16, v0, v1\n"
    "v_cvt_pk_bf16_f32 v17, v2, v3\n"
    "v_mfma_f32_16x16x32_bf16 v[8:11], a[44:47], v[140:143], v[8:11]\n"
    "s_waitcnt lgkmcnt(6)\n"
    "buffer_load_dwordx4 v238, s[60:63], s68 offen lds\n"
    "v_mfma_f32_16x16x32_bf16 v[8:11], a[48:51], v[144:147], v[8:11]\n"
    "s_waitcnt lgkmcnt(0)\n"
    "v_mul_f32 v4, v4, v113\n"
    "v_mul_f32 v5, v5, v113\n"
    "v_mfma_f32_16x16x32_bf16 v[8:11], a[52:55], v[148:151], v[8:11]\n"
    "s_mov_b32 s68, 14976\n"
    "ds_read_b64_tr_b16 a[0:1], v234 offset:512\n"
    "ds_read_b64_tr_b16 a[2:3], v234 offset:2560\n"
    "v_mfma_f32_16x16x32_bf16 v[8:11], a[56:59], v[152:155], v[8:11]\n"
    "s_add_u32 m0, s69, 100352\n"
    "ds_read_b64_tr_b16 a[4:5], v234 offset:4608\n"
    "v_mul_f32 v6, v6, v113\n"
    "v_mfma_f32_16x16x32_bf16 v[8:11], a[60:63], v[156:159], v[8:11]\n"
    "buffer_load_dwordx4 v238, s[60:63], s68 offen lds\n"
    "v_mfma_f32_16x16x32_bf16 v[12:15], a[32:35], v[160:163], 0\n"
    "ds_read_b64_tr_b16 a[6:7], v234 offset:6656\n"
    "v_mul_f32 v7, v7, v113\n"
    "v_mfma_f32_16x16x32_bf16 v[12:15], a[36:39], v[164:167], v[12:15]\n"
    "s_nop 0\n"
    "ds_read_b64_tr_b16 a[8:9], v234 offset:8704\n"
    "ds_read_b64_tr_b16 a[10:11], v234 offset:10752\n"
    "v_mfma_f32_16x16x32_bf16 v[12:15], a[40:43], v[168:171], v[12:15]\n"
    "s_mov_b32 s68, 22144\n"
    "ds_read_b64_tr_b16 a[12:13], v234 offset:12800\n"
    "v_cvt_pk_bf16_f32 v20, v4, v5\n"
    "v_mfma_f32_16x16x32_bf16 v[12:15], a[44:47], v[172:175], v[12:15]\n"
    "s_add_u32 m0, s69, 101376\n"
    "ds_read_b64_tr_b16 a[14:15], v234 offset:14848\n"
    "v_cvt_pk_bf16_f32 v21, v6, v7\n"
    "v_mfma_f32_16x16x32_bf16 v[12:15], a[48:51], v[176:179], v[12:15]\n"
    "buffer_load_dwordx4 v238, s[60:63], s68 offen lds\n"
    "v_mfma_f32_16x16x32_bf16 v[12:15], a[52:55], v[180:183], v[12:15]\n"
    "s_nop 0\n"
    "ds_read_b64_tr_b16 a[16:17], v234 offset:16896\n"
    "ds_read_b64_tr_b16 a[18:19], v234 offset:18944\n"
    "v_mfma_f32_16x16x32_bf16 v[12:15], a[56:59], v[184:187], v[12:15]\n"
    "s_waitcnt lgkmcnt(4)\n"
    "ds_read_b64_tr_b16 a[20:21], v234 offset:20992\n"
    "ds_read_b64_tr_b16 a[22:23], v234 offset:23040\n"
    "v_mfma_f32_16x16x32_bf16 v[12:15], a[60:63], v[188:191], v[12:15]\n"
    "s_mov_b32 s68, 29312\n"
    "ds_read_b64_tr_b16 a[24:25], v234 offset:25088\n"
    "ds_read_b64_tr_b16 a[26:27], v234 offset:27136\n"
    "v_mul_f32 v8, v8, v112\n"
    "v_mfma_f32_16x16x32_bf16 v[0:3], a[0:3], v[128:131], 0\n"
    "s_add_u32 m0, s69, 102400\n"
    "ds_read_b64_tr_b16 a[28:29], v234 offset:29184\n"
    "ds_read_b64_tr_b16 a[30:31], v234 offset:31232\n"
    "v_mul_f32 v9, v9, v112\n"
    "v_mfma_f32_16x16x32_bf16 v[0:3], a[4:7], v[132:135], v[0:3]\n"
    "buffer_load_dwordx4 v238, s[60:63], s68 offen lds\n"
    "v_mul_f32 v10, v10, v112\n"
    "v_mfma_f32_16x16x32_bf16 v[0:3], a[8:11], v[136:139], v[0:3]\n"
    "s_waitcnt lgkmcnt(8)\n"
    "v_mul_f32 v11, v11, v112\n"
    "v_cvt_pk_bf16_f32 v18, v8, v9\n"
    "v_mfma_f32_16x16x32_bf16 v[0:3], a[12:15], v[140:143], v[0:3]\n"
    "s_waitcnt lgkmcnt(0)\n"
    "ds_read_b64_tr_b16 a[32:33], v235 offset:512\n"
    "v_cvt_pk_bf16_f32 v19, v10, v11\n"
    "v_mfma_f32_16x16x32_bf16 v[0:3], a[16:19], v[144:147], v[0:3]\n"
    "s_mov_b32 s56, 14745600\n"
    "v_mul_f32 v12, v12, v113\n"
    "v_mul_f32 v13, v13, v113\n"
    "v_mfma_f32_16x16x32_bf16 v[0:3], a[20:23], v[148:151], v[0:3]\n"
    "buffer_store_dwordx4 v[16:19], v239, s[64:67], s56 offen sc1\n"
    "v_mfma_f32_16x16x32_bf16 v[0:3], a[24:27], v[152:155], v[0:3]\n"
    "s_waitcnt vmcnt(18)\n"
    "ds_read_b64_tr_b16 a[34:35], v235 offset:2560\n"
    "v_mul_f32 v14, v14, v113\n"
    "v_mfma_f32_16x16x32_bf16 v[0:3], a[28:31], v[156:159], v[0:3]\n"
    "s_barrier\n"
    "ds_read_b64_tr_b16 a[36:37], v235 offset:4608\n"
    "ds_read_b64_tr_b16 a[38:39], v235 offset:6656\n"
    "v_mfma_f32_16x16x32_bf16 v[4:7], a[0:3], v[160:163], 0\n"
    "s_mov_b32 s68, 36480\n"
    "ds_read_b64_tr_b16 a[40:41], v235 offset:8704\n"
    "v_mul_f32 v15, v15, v113\n"
    "v_mfma_f32_16x16x32_bf16 v[4:7], a[4:7], v[164:167], v[4:7]\n"
    "s_add_u32 m0, s69, 103424\n"
    "ds_read_b64_tr_b16 a[42:43], v235 offset:10752\n"
    "v_cvt_pk_bf16_f32 v22, v12, v13\n"
    "v_mfma_f32_16x16x32_bf16 v[4:7], a[8:11], v[168:171], v[4:7]\n"
    "buffer_load_dwordx4 v238, s[60:63], s68 offen lds\n"
    "v_mfma_f32_16x16x32_bf16 v[4:7], a[12:15], v[172:175], v[4:7]\n"
    "s_mov_b32 s68, 43648\n"
    "ds_read_b64_tr_b16 a[44:45], v235 offset:12800\n"
    "ds_read_b64_tr_b16 a[46:47], v235 offset:14848\n"
    "v_mfma_f32_16x16x32_bf16 v[4:7], a[16:19], v[176:179], v[4:7]\n"
    "s_add_u32 m0, s69, 104448\n"
    "ds_read_b64_tr_b16 a[48:49], v235 offset:16896\n"
    "v_cvt_pk_bf16_f32 v23, v14, v15\n"
    "v_mfma_f32_16x16x32_bf16 v[4:7], a[20:23], v[180:183], v[4:7]\n"
    "buffer_load_dwordx4 v238, s[60:63], s68 offen lds\n"
    "ds_read_b64_tr_b16 a[50:51], v235 offset:18944\n"
    "v_mfma_f32_16x16x32_bf16 v[4:7], a[24:27], v[184:187], v[4:7]\n"
    "s_mov_b32 s56, 15269888\n"
    "ds_read_b64_tr_b16 a[52:53], v235 offset:20992\n"
    "ds_read_b64_tr_b16 a[54:55], v235 offset:23040\n"
    "v_mfma_f32_16x16x32_bf16 v[4:7], a[28:31], v[188:191], v[4:7]\n"
    "s_waitcnt lgkmcnt(4)\n"
    "buffer_store_dwordx4 v[20:23], v239, s[64:67], s56 offen sc1\n"
    "ds_read_b64_tr_b16 a[56:57], v235 offset:25088\n"
    "ds_read_b64_tr_b16 a[58:59], v235 offset:27136\n"
    "v_mul_f32 v0, v0, v112\n"
    "v_mfma_f32_16x16x32_bf16 v[8:11], a[32:35], v[128:131], 0\n"
    "s_mov_b32 s68, 50816\n"
    "ds_read_b64_tr_b16 a[60:61], v235 offset:29184\n"
    "ds_read_b64_tr_b16 a[62:63], v235 offset:31232\n"
    "v_mul_f32 v1, v1, v112\n"
    "v_mfma_f32_16x16x32_bf16 v[8:11], a[36:39], v[132:135], v[8:11]\n"
    "s_add_u32 m0, s69, 105472\n"
    "v_mul_f32 v2, v2, v112\n"
    "v_mul_f32 v3, v3, v112\n"
    "v_mfma_f32_16x16x32_bf16 v[8:11], a[40:43], v[136:139], v[8:11]\n"
    "buffer_load_dwordx4 v238, s[60:63], s68 offen lds\n"
    "v_mfma_f32_16x16x32_bf16 v[8:11], a[44:47], v[140:143], v[8:11]\n"
    "s_waitcnt lgkmcnt(6)\n"
    "v_cvt_pk_bf16_f32 v24, v0, v1\n"
    "v_cvt_pk_bf16_f32 v25, v2, v3\n"
    "v_mfma_f32_16x16x32_bf16 v[8:11], a[48:51], v[144:147], v[8:11]\n"
    "s_waitcnt lgkmcnt(4)\n"
    "v_mul_f32 v4, v4, v113\n"
    "v_mul_f32 v5, v5, v113\n"
    "v_mfma_f32_16x16x32_bf16 v[8:11], a[52:55], v[148:151], v[8:11]\n"
    "s_waitcnt lgkmcnt(2)\n"
    "ds_read_b64_tr_b16 a[0:1], v232 offset:0\n"
    "ds_read_b64_tr_b16 a[2:3], v232 offset:2048\n"
    "v_mfma_f32_16x16x32_bf16 v[8:11], a[56:59], v[152:155], v[8:11]\n"
    "s_waitcnt lgkmcnt(2)\n"
    "ds_read_b64_tr_b16 a[4:5], v232 offset:4096\n"
    "v_mul_f32 v6, v6, v113\n"
    "v_mfma_f32_16x16x32_bf16 v[8:11], a[60:63], v[156:159], v[8:11]\n"
    "ds_read_b64_tr_b16 a[6:7], v232 offset:6144\n"
    "v_mul_f32 v7, v7, v113\n"
    "v_mfma_f32_16x16x32_bf16 v[12:15], a[32:35], v[160:163], 0\n"
    "s_nop 0\n"
    "ds_read_b64_tr_b16 a[8:9], v232 offset:8192\n"
    "ds_read_b64_tr_b16 a[10:11], v232 offset:10240\n"
    "v_mfma_f32_16x16x32_bf16 v[12:15], a[36:39], v[164:167], v[12:15]\n"
    "s_mov_b32 s68, 768\n"
    "ds_read_b64_tr_b16 a[12:13], v232 offset:12288\n"
    "v_cvt_pk_bf16_f32 v28, v4, v5\n"
    "v_mfma_f32_16x16x32_bf16 v[12:15], a[40:43], v[168:171], v[12:15]\n"
    "s_add_u32 m0, s69, 131072\n"
    "ds_read_b64_tr_b16 a[14:15], v232 offset:14336\n"
    "v_cvt_pk_bf16_f32 v29, v6, v7\n"
    "v_mfma_f32_16x16x32_bf16 v[12:15], a[44:47], v[172:175], v[12:15]\n"
    "buffer_load_dwordx4 v238, s[60:63], s68 offen lds\n"
    "v_mfma_f32_16x16x32_bf16 v[12:15], a[48:51], v[176:179], v[12:15]\n"
    "s_nop 0\n"
    "ds_read_b64_tr_b16 a[16:17], v232 offset:16384\n"
    "ds_read_b64_tr_b16 a[18:19], v232 offset:18432\n"
    "v_mfma_f32_16x16x32_bf16 v[12:15], a[52:55], v[180:183], v[12:15]\n"
    "s_waitcnt lgkmcnt(6)\n"
    "ds_read_b64_tr_b16 a[20:21], v232 offset:20480\n"
    "ds_read_b64_tr_b16 a[22:23], v232 offset:22528\n"
    "v_mfma_f32_16x16x32_bf16 v[12:15], a[56:59], v[184:187], v[12:15]\n"
    "s_mov_b32 s68, 7936\n"
    "ds_read_b64_tr_b16 a[24:25], v232 offset:24576\n"
    "ds_read_b64_tr_b16 a[26:27], v232 offset:26624\n"
    "v_mfma_f32_16x16x32_bf16 v[12:15], a[60:63], v[188:191], v[12:15]\n"
    "s_add_u32 m0, s69, 132096\n"
    "ds_read_b64_tr_b16 a[28:29], v232 offset:28672\n"
    "v_mul_f32 v8, v8, v112\n"
    "v_mfma_f32_16x16x32_bf16 v[0:3], a[0:3], v[128:131], 0\n"
    "buffer_load_dwordx4 v238, s[60:63], s68 offen lds\n"
    "ds_read_b64_tr_b16 a[30:31], v232 offset:30720\n"
    "v_mul_f32 v9, v9, v112\n"
    "v_mfma_f32_16x16x32_bf16 v[0:3], a[4:7], v[132:135], v[0:3]\n"
    "s_waitcnt lgkmcnt(10)\n"
    "v_mul_f32 v10, v10, v112\n"
    "v_mul_f32 v11, v11, v112\n"
    "v_mfma_f32_16x16x32_bf16 v[0:3], a[8:11], v[136:139], v[0:3]\n"
    "s_waitcnt lgkmcnt(8)\n"
    "v_cvt_pk_bf16_f32 v26, v8, v9\n"
    "v_cvt_pk_bf16_f32 v27, v10, v11\n"
    "v_mfma_f32_16x16x32_bf16 v[0:3], a[12:15], v[140:143], v[0:3]\n"
    "s_waitcnt lgkmcnt(0)\n"
    "ds_read_b64_tr_b16 a[32:33], v233 offset:0\n"
    "ds_read_b64_tr_b16 a[34:35], v233 offset:2048\n"
    "v_mfma_f32_16x16x32_bf16 v[0:3], a[16:19], v[144:147], v[0:3]\n"
    "s_mov_b32 s56, 14761984\n"
    "v_mul_f32 v12, v12, v113\n"
    "v_mul_f32 v13, v13, v113\n"
    "v_mfma_f32_16x16x32_bf16 v[0:3], a[20:23], v[148:151], v[0:3]\n"
    "buffer_store_dwordx4 v[24:27], v239, s[64:67], s56 offen sc1\n"
    "v_mfma_f32_16x16x32_bf16 v[0:3], a[24:27], v[152:155], v[0:3]\n"
    "s_mov_b32 s68, 15104\n"
    "ds_read_b64_tr_b16 a[36:37], v233 offset:4096\n"
    "v_mul_f32 v14, v14, v113\n"
    "v_mfma_f32_16x16x32_bf16 v[0:3], a[28:31], v[156:159], v[0:3]\n"
    "s_add_u32 m0, s69, 133120\n"
    "ds_read_b64_tr_b16 a[38:39], v233 offset:6144\n"
    "v_mul_f32 v15, v15, v113\n"
    "v_mfma_f32_16x16x32_bf16 v[4:7], a[0:3], v[160:163], 0\n"
    "buffer_load_dwordx4 v238, s[60:63], s68 offen lds\n"
    "v_mfma_f32_16x16x32_bf16 v[4:7], a[4:7], v[164:167], v[4:7]\n"
    "s_mov_b32 s68, 22272\n"
    "ds_read_b64_tr_b16 a[40:41], v233 offset:8192\n"
    "ds_read_b64_tr_b16 a[42:43], v233 offset:10240\n"
    "v_mfma_f32_16x16x32_bf16 v[4:7], a[8:11], v[168:171], v[4:7]\n"
    "s_add_u32 m0, s69, 134144\n"
    "ds_read_b64_tr_b16 a[44:45], v233 offset:12288\n"
    "v_cvt_pk_bf16_f32 v30, v12, v13\n"
    "v_mfma_f32_16x16x32_bf16 v[4:7], a[12:15], v[172:175], v[4:7]\n"
    "buffer_load_dwordx4 v238, s[60:63], s68 offen lds\n"
    "v_mfma_f32_16x16x32_bf16 v[4:7], a[16:19], v[176:179], v[4:7]\n"
    "s_mov_b32 s56, 15286272\n"
    "ds_read_b64_tr_b16 a[46:47], v233 offset:14336\n"
    "v_cvt_pk_bf16_f32 v31, v14, v15\n"
    "v_mfma_f32_16x16x32_bf16 v[4:7], a[20:23], v[180:183], v[4:7]\n"
    "s_nop 0\n"
    "buffer_store_dwordx4 v[28:31], v239, s[64:67], s56 offen sc1\n"
    "ds_read_b64_tr_b16 a[48:49], v233 offset:16384\n"
    "ds_read_b64_tr_b16 a[50:51], v233 offset:18432\n"
    "v_mfma_f32_16x16x32_bf16 v[4:7], a[24:27], v[184:187], v[4:7]\n"
    "s_mov_b32 s68, 29440\n"
    "ds_read_b64_tr_b16 a[52:53], v233 offset:20480\n"
    "ds_read_b64_tr_b16 a[54:55], v233 offset:22528\n"
    "v_mfma_f32_16x16x32_bf16 v[4:7], a[28:31], v[188:191], v[4:7]\n"
    "s_waitcnt lgkmcnt(8)\n"
    "s_add_u32 m0, s69, 135168\n"
    "ds_read_b64_tr_b16 a[56:57], v233 offset:24576\n"
    "ds_read_b64_tr_b16 a[58:59], v233 offset:26624\n"
    "v_mul_f32 v0, v0, v112\n"
    "v_mfma_f32_16x16x32_bf16 v[8:11], a[32:35], v[128:131], 0\n"
    "s_nop 0\n"
    "buffer_load_dwordx4 v238, s[60:63], s68 offen lds\n"
    "ds_read_b64_tr_b16 a[60:61], v233 offset:28672\n"
    "ds_read_b64_tr_b16 a[62:63], v233 offset:30720\n"
    "v_mul_f32 v1, v1, v112\n"
    "v_mfma_f32_16x16x32_bf16 v[8:11], a[36:39], v[132:135], v[8:11]\n"
    "s_waitcnt lgkmcnt(4)\n"
    "v_mul_f32 v2, v2, v112\n"
    "v_mul_f32 v3, v3, v112\n"
    "v_mfma_f32_16x16x32_bf16 v[8:11], a[40:43], v[136:139], v[8:11]\n"
    "s_mov_b32 s68, 36608\n"
    "v_cvt_pk_bf16_f32 v16, v0, v1\n"
    "v_cvt_pk_bf16_f32 v17, v2, v3\n"
    "v_mfma_f32_16x16x32_bf16 v[8:11], a[44:47], v[140:143], v[8:11]\n"
    "s_add_u32 m0, s69, 136192\n"
    "ds_read_b64_tr_b16 a[0:1], v232 offset:512\n"
    "ds_read_b64_tr_b16 a[2:3], v232 offset:2560\n"
    "v_mfma_f32_16x16x32_bf16 v[8:11], a[48:51], v[144:147], v[8:11]\n"
    "buffer_load_dwordx4 v238, s[60:63], s68 offen lds\n"
    "v_mfma_f32_16x16x32_bf16 v[8:11], a[52:55], v[148:151], v[8:11]\n"
    "s_waitcnt lgkmcnt(4)\n"
    "v_mul_f32 v4, v4, v113\n"
    "v_mul_f32 v5, v5, v113\n"
    "v_mfma_f32_16x16x32_bf16 v[8:11], a[56:59], v[152:155], v[8:11]\n"
    "s_waitcnt lgkmcnt(2)\n"
    "ds_read_b64_tr_b16 a[4:5], v232 offset:4608\n"
    "v_mul_f32 v6, v6, v113\n"
    "v_mfma_f32_16x16x32_bf16 v[8:11], a[60:63], v[156:159], v[8:11]\n"
    "s_mov_b32 s68, 43776\n"
    "ds_read_b64_tr_b16 a[6:7], v232 offset:6656\n"
    "v_mul_f32 v7, v7, v113\n"
    "v_mfma_f32_16x16x32_bf16 v[12:15], a[32:35], v[160:163], 0\n"
    "s_add_u32 m0, s69, 137216\n"
    "ds_read_b64_tr_b16 a[8:9], v232 offset:8704\n"
    "ds_read_b64_tr_b16 a[10:11], v232 offset:10752\n"
    "v_mfma_f32_16x16x32_bf16 v[12:15], a[36:39], v[164:167], v[12:15]\n"
    "buffer_load_dwordx4 v238, s[60:63], s68 offen lds\n"
    "v_mfma_f32_16x16x32_bf16 v[12:15], a[40:43], v[168:171], v[12:15]\n"
    "ds_read_b64_tr_b16 a[12:13], v232 offset:12800\n"
    "v_cvt_pk_bf16_f32 v20, v4, v5\n"
    "v_mfma_f32_16x16x32_bf16 v[12:15], a[44:47], v[172:175], v[12:15]\n"
    "ds_read_b64_tr_b16 a[14:15], v232 offset:14848\n"
    "v_cvt_pk_bf16_f32 v21, v6, v7\n"
    "v_mfma_f32_16x16x32_bf16 v[12:15], a[48:51], v[176:179], v[12:15]\n"
    "s_mov_b32 s68, 50944\n"
    "ds_read_b64_tr_b16 a[16:17], v232 offset:16896\n"
    "ds_read_b64_tr_b16 a[18:19], v232 offset:18944\n"
    "v_mfma_f32_16x16x32_bf16 v[12:15], a[52:55], v[180:183], v[12:15]\n"
    "s_add_u32 m0, s69, 138240\n"
    "ds_read_b64_tr_b16 a[20:21], v232 offset:20992\n"
    "ds_read_b64_tr_b16 a[22:23], v232 offset:23040\n"
    "v_mfma_f32_16x16x32_bf16 v[12:15], a[56:59], v[184:187], v[12:15]\n"
    "buffer_load_dwordx4 v238, s[60:63], s68 offen lds\n"
    "v_mfma_f32_16x16x32_bf16 v[12:15], a[60:63], v[188:191], v[12:15]\n"
    "s_waitcnt lgkmcnt(10)\n"
    "ds_read_b64_tr_b16 a[24:25], v232 offset:25088\n"
    "ds_read_b64_tr_b16 a[26:27], v232 offset:27136\n"
    "v_mul_f32 v8, v8, v112\n"
    "v_mfma_f32_16x16x32_bf16 v[0:3], a[0:3], v[128:131], 0\n"
    "s_waitcnt lgkmcnt(10)\n"
    "ds_read_b64_tr_b16 a[28:29], v232 offset:29184\n"
    "ds_read_b64_tr_b16 a[30:31], v232 offset:31232\n"
    "v_mul_f32 v9, v9, v112\n"
    "v_mfma_f32_16x16x32_bf16 v[0:3], a[4:7], v[132:135], v[0:3]\n"
    "s_waitcnt lgkmcnt(10)\n"
    "v_mul_f32 v10, v10, v112\n"
    "v_mul_f32 v11, v11, v112\n"
    "v_mfma_f32_16x16x32_bf16 v[0:3], a[8:11], v[136:139], v[0:3]\n"
    "s_waitcnt lgkmcnt(8)\n"
    "v_cvt_pk_bf16_f32 v18, v8, v9\n"
    "v_cvt_pk_bf16_f32 v19, v10, v11\n"
    "v_mfma_f32_16x16x32_bf16 v[0:3], a[12:15], v[140:143], v[0:3]\n"
    "s_waitcnt lgkmcnt(0)\n"
    "ds_read_b64_tr_b16 a[32:33], v233 offset:512\n"
    "ds_read_b64_tr_b16 a[34:35], v233 offset:2560\n"
    "v_mfma_f32_16x16x32_bf16 v[0:3], a[16:19], v[144:147], v[0:3]\n"
    "s_mov_b32 s56, 14778368\n"
    "v_mul_f32 v12, v12, v113\n"
    "v_mul_f32 v13, v13, v113\n"
    "v_mfma_f32_16x16x32_bf16 v[0:3], a[20:23], v[148:151], v[0:3]\n"
    "buffer_store_dwordx4 v[16:19], v239, s[64:67], s56 offen sc1\n"
    "v_mfma_f32_16x16x32_bf16 v[0:3], a[24:27], v[152:155], v[0:3]\n"
    "s_waitcnt vmcnt(23)\n"
    "ds_read_b64_tr_b16 a[36:37], v233 offset:4608\n"
    "v_mul_f32 v14, v14, v113\n"
    "v_mfma_f32_16x16x32_bf16 v[0:3], a[28:31], v[156:159], v[0:3]\n"
    "s_barrier\n"
    "ds_read_b64_tr_b16 a[38:39], v233 offset:6656\n"
    "v_mul_f32 v15, v15, v113\n"
    "v_mfma_f32_16x16x32_bf16 v[4:7], a[0:3], v[160:163], 0\n"
    "s_nop 0\n"
    "ds_read_b64_tr_b16 a[40:41], v233 offset:8704\n"
    "ds_read_b64_tr_b16 a[42:43], v233 offset:10752\n"
    "v_mfma_f32_16x16x32_bf16 v[4:7], a[4:7], v[164:167], v[4:7]\n"
    "ds_read_b64_tr_b16 a[44:45], v233 offset:12800\n"
    "v_cvt_pk_bf16_f32 v22, v12, v13\n"
    "v_mfma_f32_16x16x32_bf16 v[4:7], a[8:11], v[168:171], v[4:7]\n"
    "s_lshl_b32 s44, s50, 6\n"
    "ds_read_b64_tr_b16 a[46:47], v233 offset:14848\n"
    "v_cvt_pk_bf16_f32 v23, v14, v15\n"
    "v_mfma_f32_16x16x32_bf16 v[4:7], a[12:15], v[172:175], v[4:7]\n"
    "s_add_u32 s44, s44, 0\n"
    "ds_read_b64_tr_b16 a[48:49], v233 offset:16896\n"
    "ds_read_b64_tr_b16 a[50:51], v233 offset:18944\n"
    "v_mfma_f32_16x16x32_bf16 v[4:7], a[16:19], v[176:179], v[4:7]\n"
    "buffer_load_dwordx2 v[64:65], v248, s[92:95], s44 offen\n"
    "v_mfma_f32_16x16x32_bf16 v[4:7], a[20:23], v[180:183], v[4:7]\n"
    "s_mov_b32 s56, 15302656\n"
    "ds_read_b64_tr_b16 a[52:53], v233 offset:20992\n"
    "ds_read_b64_tr_b16 a[54:55], v233 offset:23040\n"
    "v_mfma_f32_16x16x32_bf16 v[4:7], a[24:27], v[184:187], v[4:7]\n"
    "buffer_store_dwordx4 v[20:23], v239, s[64:67], s56 offen sc1\n"
    "v_mfma_f32_16x16x32_bf16 v[4:7], a[28:31], v[188:191], v[4:7]\n"
    "s_waitcnt lgkmcnt(10)\n"
    "ds_read_b64_tr_b16 a[56:57], v233 offset:25088\n"
    "ds_read_b64_tr_b16 a[58:59], v233 offset:27136\n"
    "v_mul_f32 v0, v0, v112\n"
    "v_mfma_f32_16x16x32_bf16 v[8:11], a[32:35], v[128:131], 0\n"
    "s_waitcnt lgkmcnt(10)\n"
    "ds_read_b64_tr_b16 a[60:61], v233 offset:29184\n"
    "ds_read_b64_tr_b16 a[62:63], v233 offset:31232\n"
    "v_mul_f32 v1, v1, v112\n"
    "v_mfma_f32_16x16x32_bf16 v[8:11], a[36:39], v[132:135], v[8:11]\n"
    "s_waitcnt lgkmcnt(10)\n"
    "v_mul_f32 v2, v2, v112\n"
    "v_mul_f32 v3, v3, v112\n"
    "v_mfma_f32_16x16x32_bf16 v[8:11], a[40:43], v[136:139], v[8:11]\n"
    "s_waitcnt lgkmcnt(2)\n"
    "v_cvt_pk_bf16_f32 v24, v0, v1\n"
    "v_cvt_pk_bf16_f32 v25, v2, v3\n"
    "v_mfma_f32_16x16x32_bf16 v[8:11], a[44:47], v[140:143], v[8:11]\n"
    "s_lshl_b32 s44, s50, 6\n"
    "ds_read_b64_tr_b16 a[0:1], v232 offset:32768\n"
    "ds_read_b64_tr_b16 a[2:3], v232 offset:34816\n"
    "v_mfma_f32_16x16x32_bf16 v[8:11], a[48:51], v[144:147], v[8:11]\n"
    "s_add_u32 s44, s44, 256\n"
    "v_mul_f32 v4, v4, v113\n"
    "v_mul_f32 v5, v5, v113\n"
    "v_mfma_f32_16x16x32_bf16 v[8:11], a[52:55], v[148:151], v[8:11]\n"
    "buffer_load_dwordx2 v[66:67], v248, s[92:95], s44 offen\n"
    "v_mfma_f32_16x16x32_bf16 v[8:11], a[56:59], v[152:155], v[8:11]\n"
    "s_waitcnt lgkmcnt(2)\n"
    "ds_read_b64_tr_b16 a[4:5], v232 offset:36864\n"
    "v_mul_f32 v6, v6, v113\n"
    "v_mfma_f32_16x16x32_bf16 v[8:11], a[60:63], v[156:159], v[8:11]\n"
    "ds_read_b64_tr_b16 a[6:7], v232 offset:38912\n"
    "v_mul_f32 v7, v7, v113\n"
    "v_mfma_f32_16x16x32_bf16 v[12:15], a[32:35], v[160:163], 0\n"
    "s_nop 0\n"
    "ds_read_b64_tr_b16 a[8:9], v232 offset:40960\n"
    "ds_read_b64_tr_b16 a[10:11], v232 offset:43008\n"
    "v_mfma_f32_16x16x32_bf16 v[12:15], a[36:39], v[164:167], v[12:15]\n"
    "ds_read_b64_tr_b16 a[12:13], v232 offset:45056\n"
    "v_cvt_pk_bf16_f32 v28, v4, v5\n"
    "v_mfma_f32_16x16x32_bf16 v[12:15], a[40:43], v[168:171], v[12:15]\n"
    "ds_read_b64_tr_b16 a[14:15], v232 offset:47104\n"
    "v_cvt_pk_bf16_f32 v29, v6, v7\n"
    "v_mfma_f32_16x16x32_bf16 v[12:15], a[44:47], v[172:175], v[12:15]\n"
    "s_nop 0\n"
    "ds_read_b64_tr_b16 a[16:17], v232 offset:49152\n"
    "ds_read_b64_tr_b16 a[18:19], v232 offset:51200\n"
    "v_mfma_f32_16x16x32_bf16 v[12:15], a[48:51], v[176:179], v[12:15]\n"
    "s_lshl_b32 s44, s50, 6\n"
    "ds_read_b64_tr_b16 a[20:21], v232 offset:53248\n"
    "ds_read_b64_tr_b16 a[22:23], v232 offset:55296\n"
    "v_mfma_f32_16x16x32_bf16 v[12:15], a[52:55], v[180:183], v[12:15]\n"
    "s_add_u32 s44, s44, 512\n"
    "ds_read_b64_tr_b16 a[24:25], v232 offset:57344\n"
    "ds_read_b64_tr_b16 a[26:27], v232 offset:59392\n"
    "v_mfma_f32_16x16x32_bf16 v[12:15], a[56:59], v[184:187], v[12:15]\n"
    "buffer_load_dwordx2 v[68:69], v248, s[92:95], s44 offen\n"
    "v_mfma_f32_16x16x32_bf16 v[12:15], a[60:63], v[188:191], v[12:15]\n"
    "s_waitcnt lgkmcnt(12)\n"
    "ds_read_b64_tr_b16 a[28:29], v232 offset:61440\n"
    "v_mul_f32 v8, v8, v112\n"
    "v_mfma_f32_16x16x32_bf16 v[0:3], a[0:3], v[128:131], 0\n"
    "s_waitcnt lgkmcnt(11)\n"
    "ds_read_b64_tr_b16 a[30:31], v232 offset:63488\n"
    "v_mul_f32 v9, v9, v112\n"
    "v_mfma_f32_16x16x32_bf16 v[0:3], a[4:7], v[132:135], v[0:3]\n"
    "s_waitcnt lgkmcnt(10)\n"
    "v_mul_f32 v10, v10, v112\n"
    "v_mul_f32 v11, v11, v112\n"
    "v_mfma_f32_16x16x32_bf16 v[0:3], a[8:11], v[136:139], v[0:3]\n"
    "s_waitcnt lgkmcnt(8)\n"
    "v_cvt_pk_bf16_f32 v26, v8, v9\n"
    "v_cvt_pk_bf16_f32 v27, v10, v11\n"
    "v_mfma_f32_16x16x32_bf16 v[0:3], a[12:15], v[140:143], v[0:3]\n"
    "s_waitcnt lgkmcnt(0)\n"
    "ds_read_b64_tr_b16 a[32:33], v233 offset:32768\n"
    "ds_read_b64_tr_b16 a[34:35], v233 offset:34816\n"
    "v_mfma_f32_16x16x32_bf16 v[0:3], a[16:19], v[144:147], v[0:3]\n"
    "s_mul_i32 s91, s98, 56\n"
    "v_mul_f32 v12, v12, v113\n"
    "v_mul_f32 v13, v13, v113\n"
    "v_mfma_f32_16x16x32_bf16 v[0:3], a[20:23], v[148:151], v[0:3]\n"
    "s_mul_i32 s54, s50, 14\n"
    "ds_read_b64_tr_b16 a[36:37], v233 offset:36864\n"
    "v_mul_f32 v14, v14, v113\n"
    "v_mfma_f32_16x16x32_bf16 v[0:3], a[24:27], v[152:155], v[0:3]\n"
    "s_add_u32 s91, s91, s54\n"
    "ds_read_b64_tr_b16 a[38:39], v233 offset:38912\n"
    "v_mul_f32 v15, v15, v113\n"
    "v_mfma_f32_16x16x32_bf16 v[0:3], a[28:31], v[156:159], v[0:3]\n"
    "s_lshl_b32 s91, s91, 12\n"
    "ds_read_b64_tr_b16 a[40:41], v233 offset:40960\n"
    "ds_read_b64_tr_b16 a[42:43], v233 offset:43008\n"
    "v_mfma_f32_16x16x32_bf16 v[4:7], a[0:3], v[160:163], 0\n"
    "s_add_u32 s55, s98, 1\n"
    "ds_read_b64_tr_b16 a[44:45], v233 offset:45056\n"
    "v_cvt_pk_bf16_f32 v30, v12, v13\n"
    "v_mfma_f32_16x16x32_bf16 v[4:7], a[4:7], v[164:167], v[4:7]\n"
    "s_lshr_b32 s55, s55, 3\n"
    "ds_read_b64_tr_b16 a[46:47], v233 offset:47104\n"
    "v_cvt_pk_bf16_f32 v31, v14, v15\n"
    "v_mfma_f32_16x16x32_bf16 v[4:7], a[8:11], v[168:171], v[4:7]\n"
    "s_lshl_b32 s55, s55, 30\n"
    "ds_read_b64_tr_b16 a[48:49], v233 offset:49152\n"
    "ds_read_b64_tr_b16 a[50:51], v233 offset:51200\n"
    "v_mfma_f32_16x16x32_bf16 v[4:7], a[12:15], v[172:175], v[4:7]\n"
    "s_add_u32 s91, s91, s55\n"
    "ds_read_b64_tr_b16 a[52:53], v233 offset:53248\n"
    "ds_read_b64_tr_b16 a[54:55], v233 offset:55296\n"
    "v_mfma_f32_16x16x32_bf16 v[4:7], a[16:19], v[176:179], v[4:7]\n"
    "s_add_u32 s91, s91, 51904512\n"
    "s_mov_b32 s56, 14794752\n"
    "ds_read_b64_tr_b16 a[56:57], v233 offset:57344\n"
    "ds_read_b64_tr_b16 a[58:59], v233 offset:59392\n"
    "v_mfma_f32_16x16x32_bf16 v[4:7], a[20:23], v[180:183], v[4:7]\n"
    "buffer_store_dwordx4 v[24:27], v239, s[64:67], s56 offen sc1\n"
    "v_mfma_f32_16x16x32_bf16 v[4:7], a[24:27], v[184:187], v[4:7]\n"
    "s_and_b32 s54, s98, 1\n"
    "s_lshl_b32 s44, s50, 6\n"
    "s_add_u32 s44, s44, 768\n"
    "s_cmp_lt_u32 s50, 2\n"
    "s_cselect_b32 s44, s44, 2146435072\n"
    "ds_read_b64_tr_b16 a[60:61], v233 offset:61440\n"
    "v_mul_f32 v0, v0, v112\n"
    "v_mfma_f32_16x16x32_bf16 v[4:7], a[28:31], v[188:191], v[4:7]\n"
    "s_waitcnt lgkmcnt(7)\n"
    "buffer_load_dwordx2 v[70:71], v248, s[92:95], s44 offen\n"
    "v_mfma_f32_16x16x32_bf16 v[8:11], a[32:35], v[128:131], 0\n"
    "s_mul_i32 s54, s54, 28672\n"
    "ds_read_b64_tr_b16 a[62:63], v233 offset:63488\n"
    "v_mul_f32 v1, v1, v112\n"
    "v_mfma_f32_16x16x32_bf16 v[8:11], a[36:39], v[132:135], v[8:11]\n"
    "s_add_u32 s69, s91, s54\n"
    "v_mul_f32 v2, v2, v112\n"
    "v_mul_f32 v3, v3, v112\n"
    "v_mfma_f32_16x16x32_bf16 v[8:11], a[40:43], v[136:139], v[8:11]\n"
    "s_sub_u32 s91, s91, s54\n"
    "v_cvt_pk_bf16_f32 v16, v0, v1\n"
    "v_cvt_pk_bf16_f32 v17, v2, v3\n"
    "v_mfma_f32_16x16x32_bf16 v[8:11], a[44:47], v[140:143], v[8:11]\n"
    "s_waitcnt lgkmcnt(4)\n"
    "s_mov_b32 s56, 15319040\n"
    "ds_read_b64_tr_b16 a[0:1], v232 offset:33280\n"
    "ds_read_b64_tr_b16 a[2:3], v232 offset:35328\n"
    "v_mfma_f32_16x16x32_bf16 v[8:11], a[48:51], v[144:147], v[8:11]\n"
    "buffer_store_dwordx4 v[28:31], v239, s[64:67], s56 offen sc1\n"
    "v_mfma_f32_16x16x32_bf16 v[8:11], a[52:55], v[148:151], v[8:11]\n"
    "s_waitcnt lgkmcnt(4)\n"
    "v_mul_f32 v4, v4, v113\n"
    "v_mul_f32 v5, v5, v113\n"
    "v_mfma_f32_16x16x32_bf16 v[8:11], a[56:59], v[152:155], v[8:11]\n"
    "s_waitcnt lgkmcnt(2)\n"
    "ds_read_b64_tr_b16 a[4:5], v232 offset:37376\n"
    "v_mul_f32 v6, v6, v113\n"
    "v_mfma_f32_16x16x32_bf16 v[8:11], a[60:63], v[156:159], v[8:11]\n"
    "s_add_u32 s44, s50, 0\n"
    "s_min_u32 s44, s44, 13\n"
    "s_lshl_b32 s44, s44, 7\n"
    "s_add_u32 s44, s44, 7680\n"
    "ds_read_b64_tr_b16 a[6:7], v232 offset:39424\n"
    "v_mul_f32 v7, v7, v113\n"
    "v_mfma_f32_16x16x32_bf16 v[12:15], a[32:35], v[160:163], 0\n"
    "buffer_load_dwordx4 v[192:195], v249, s[64:67], s44 offen offset:0 sc1\n"
    "v_mfma_f32_16x16x32_bf16 v[12:15], a[36:39], v[164:167], v[12:15]\n"
    "s_nop 0\n"
    "ds_read_b64_tr_b16 a[8:9], v232 offset:41472\n"
    "ds_read_b64_tr_b16 a[10:11], v232 offset:43520\n"
    "v_mfma_f32_16x16x32_bf16 v[12:15], a[40:43], v[168:171], v[12:15]\n"
    "buffer_load_dwordx4 v[196:199], v249, s[64:67], s44 offen offset:1792 sc1\n"
    "v_mfma_f32_16x16x32_bf16 v[12:15], a[44:47], v[172:175], v[12:15]\n"
    "ds_read_b64_tr_b16 a[12:13], v232 offset:45568\n"
    "v_cvt_pk_bf16_f32 v20, v4, v5\n"
    "v_mfma_f32_16x16x32_bf16 v[12:15], a[48:51], v[176:179], v[12:15]\n"
    "ds_read_b64_tr_b16 a[14:15], v232 offset:47616\n"
    "v_cvt_pk_bf16_f32 v21, v6, v7\n"
    "v_mfma_f32_16x16x32_bf16 v[12:15], a[52:55], v[180:183], v[12:15]\n"
    "s_nop 0\n"
    "ds_read_b64_tr_b16 a[16:17], v232 offset:49664\n"
    "ds_read_b64_tr_b16 a[18:19], v232 offset:51712\n"
    "v_mfma_f32_16x16x32_bf16 v[12:15], a[56:59], v[184:187], v[12:15]\n"
    "s_nop 0\n"
    "ds_read_b64_tr_b16 a[20:21], v232 offset:53760\n"
    "ds_read_b64_tr_b16 a[22:23], v232 offset:55808\n"
    "v_mfma_f32_16x16x32_bf16 v[12:15], a[60:63], v[188:191], v[12:15]\n"
    "s_waitcnt lgkmcnt(10)\n"
    "ds_read_b64_tr_b16 a[24:25], v232 offset:57856\n"
    "ds_read_b64_tr_b16 a[26:27], v232 offset:59904\n"
    "v_mul_f32 v8, v8, v112\n"
    "v_mfma_f32_16x16x32_bf16 v[0:3], a[0:3], v[128:131], 0\n"
    "s_waitcnt lgkmcnt(10)\n"
    "ds_read_b64_tr_b16 a[28:29], v232 offset:61952\n"
    "ds_read_b64_tr_b16 a[30:31], v232 offset:64000\n"
    "v_mul_f32 v9, v9, v112\n"
    "v_mfma_f32_16x16x32_bf16 v[0:3], a[4:7], v[132:135], v[0:3]\n"
    "s_waitcnt lgkmcnt(8)\n"
    "v_mul_f32 v10, v10, v112\n"
    "v_mul_f32 v11, v11, v112\n"
    "v_mfma_f32_16x16x32_bf16 v[0:3], a[8:11], v[136:139], v[0:3]\n"
    "buffer_load_dwordx4 v[200:203], v249, s[64:67], s44 offen offset:3584 sc1\n"
    "v_mfma_f32_16x16x32_bf16 v[0:3], a[12:15], v[140:143], v[0:3]\n"
    "s_waitcnt lgkmcnt(0)\n"
    "v_cvt_pk_bf16_f32 v18, v8, v9\n"
    "v_cvt_pk_bf16_f32 v19, v10, v11\n"
    "v_mfma_f32_16x16x32_bf16 v[0:3], a[16:19], v[144:147], v[0:3]\n"
    "s_mov_b32 s56, 14811136\n"
    "v_mul_f32 v12, v12, v113\n"
    "v_mul_f32 v13, v13, v113\n"
    "v_mfma_f32_16x16x32_bf16 v[0:3], a[20:23], v[148:151], v[0:3]\n"
    "buffer_store_dwordx4 v[16:19], v239, s[64:67], s56 offen sc1\n"
    "v_mfma_f32_16x16x32_bf16 v[0:3], a[24:27], v[152:155], v[0:3]\n"
    "s_waitcnt vmcnt(22)\n"
    "ds_read_b64_tr_b16 a[32:33], v233 offset:33280\n"
    "v_lshlrev_b32 v255, 4, v250\n"
    "v_mfma_f32_16x16x32_bf16 v[0:3], a[28:31], v[156:159], v[0:3]\n"
    "s_barrier\n"
    "ds_read_b64_tr_b16 a[34:35], v233 offset:35328\n"
    "v_mul_f32 v14, v14, v113\n"
    "v_mfma_f32_16x16x32_bf16 v[4:7], a[0:3], v[160:163], 0\n"
    "s_nop 0\n"
    "ds_read_b64_tr_b16 a[36:37], v233 offset:37376\n"
    "ds_read_b64_tr_b16 a[38:39], v233 offset:39424\n"
    "v_mfma_f32_16x16x32_bf16 v[4:7], a[4:7], v[164:167], v[4:7]\n"
    "ds_read_b64_tr_b16 a[40:41], v233 offset:41472\n"
    "v_mul_f32 v15, v15, v113\n"
    "v_mfma_f32_16x16x32_bf16 v[4:7], a[8:11], v[168:171], v[4:7]\n"
    "ds_read_b64_tr_b16 a[42:43], v233 offset:43520\n"
    "v_cvt_pk_bf16_f32 v22, v12, v13\n"
    "v_mfma_f32_16x16x32_bf16 v[4:7], a[12:15], v[172:175], v[4:7]\n"
    "s_nop 0\n"
    "ds_read_b64_tr_b16 a[44:45], v233 offset:45568\n"
    "ds_read_b64_tr_b16 a[46:47], v233 offset:47616\n"
    "v_mfma_f32_16x16x32_bf16 v[4:7], a[16:19], v[176:179], v[4:7]\n"
    "ds_read_b64_tr_b16 a[48:49], v233 offset:49664\n"
    "v_cvt_pk_bf16_f32 v23, v14, v15\n"
    "v_mfma_f32_16x16x32_bf16 v[4:7], a[20:23], v[180:183], v[4:7]\n"
    "s_nop 0\n"
    "ds_read_b64_tr_b16 a[50:51], v233 offset:51712\n"
    "ds_read_b64_tr_b16 a[52:53], v233 offset:53760\n"
    "v_mfma_f32_16x16x32_bf16 v[4:7], a[24:27], v[184:187], v[4:7]\n"
    "s_nop 0\n"
    "ds_read_b64_tr_b16 a[54:55], v233 offset:55808\n"
    "ds_read_b64_tr_b16 a[56:57], v233 offset:57856\n"
    "v_mfma_f32_16x16x32_bf16 v[4:7], a[28:31], v[188:191], v[4:7]\n"
    "s_waitcnt lgkmcnt(11)\n"
    "ds_read_b64_tr_b16 a[58:59], v233 offset:59904\n"
    "v_mul_f32 v0, v0, v112\n"
    "v_mfma_f32_16x16x32_bf16 v[8:11], a[32:35], v[128:131], 0\n"
    "s_waitcnt lgkmcnt(10)\n"
    "ds_read_b64_tr_b16 a[60:61], v233 offset:61952\n"
    "ds_read_b64_tr_b16 a[62:63], v233 offset:64000\n"
    "v_mul_f32 v1, v1, v112\n"
    "v_mfma_f32_16x16x32_bf16 v[8:11], a[36:39], v[132:135], v[8:11]\n"
    "s_waitcnt lgkmcnt(4)\n"
    "v_mul_f32 v2, v2, v112\n"
    "v_mul_f32 v3, v3, v112\n"
    "v_mfma_f32_16x16x32_bf16 v[8:11], a[40:43], v[136:139], v[8:11]\n"
    "s_add_u32 s44, s50, 4\n"
    "s_min_u32 s44, s44, 13\n"
    "s_lshl_b32 s44, s44, 7\n"
    "s_add_u32 s44, s44, 7680\n"
    "v_cvt_pk_bf16_f32 v24, v0, v1\n"
    "v_cvt_pk_bf16_f32 v25, v2, v3\n"
    "v_mfma_f32_16x16x32_bf16 v[8:11], a[44:47], v[140:143], v[8:11]\n"
    "buffer_load_dwordx4 v[204:207], v249, s[64:67], s44 offen offset:0 sc1\n"
    "v_mfma_f32_16x16x32_bf16 v[8:11], a[48:51], v[144:147], v[8:11]\n"
    "s_mov_b32 s56, 15335424\n"
    "v_mul_f32 v4, v4, v113\n"
    "v_mul_f32 v5, v5, v113\n"
    "v_mfma_f32_16x16x32_bf16 v[8:11], a[52:55], v[148:151], v[8:11]\n"
    "s_waitcnt lgkmcnt(2)\n"
    "buffer_store_dwordx4 v[20:23], v239, s[64:67], s56 offen sc1\n"
    "v_mfma_f32_16x16x32_bf16 v[8:11], a[56:59], v[152:155], v[8:11]\n"
    "s_waitcnt lgkmcnt(0)\n"
    "ds_read_b64_tr_b16 a[0:1], v234 offset:32768\n"
    "ds_read_b64_tr_b16 a[2:3], v234 offset:34816\n"
    "v_mfma_f32_16x16x32_bf16 v[8:11], a[60:63], v[156:159], v[8:11]\n"
    "buffer_load_dwordx4 v[208:211], v249, s[64:67], s44 offen offset:1792 sc1\n"
    "v_mfma_f32_16x16x32_bf16 v[12:15], a[32:35], v[160:163], 0\n"
    "ds_read_b64_tr_b16 a[4:5], v234 offset:36864\n"
    "v_mul_f32 v6, v6, v113\n"
    "v_mfma_f32_16x16x32_bf16 v[12:15], a[36:39], v[164:167], v[12:15]\n"
    "ds_read_b64_tr_b16 a[6:7], v234 offset:38912\n"
    "v_mul_f32 v7, v7, v113\n"
    "v_mfma_f32_16x16x32_bf16 v[12:15], a[40:43], v[168:171], v[12:15]\n"
    "s_nop 0\n"
    "ds_read_b64_tr_b16 a[8:9], v234 offset:40960\n"
    "ds_read_b64_tr_b16 a[10:11], v234 offset:43008\n"
    "v_mfma_f32_16x16x32_bf16 v[12:15], a[44:47], v[172:175], v[12:15]\n"
    "ds_read_b64_tr_b16 a[12:13], v234 offset:45056\n"
    "v_cvt_pk_bf16_f32 v28, v4, v5\n"
    "v_mfma_f32_16x16x32_bf16 v[12:15], a[48:51], v[176:179], v[12:15]\n"
    "ds_read_b64_tr_b16 a[14:15], v234 offset:47104\n"
    "v_cvt_pk_bf16_f32 v29, v6, v7\n"
    "v_mfma_f32_16x16x32_bf16 v[12:15], a[52:55], v[180:183], v[12:15]\n"
    "s_nop 0\n"
    "buffer_load_dwordx4 v[212:215], v249, s[64:67], s44 offen offset:3584 sc1\n"
    "ds_read_b64_tr_b16 a[16:17], v234 offset:49152\n"
    "ds_read_b64_tr_b16 a[18:19], v234 offset:51200\n"
    "v_mfma_f32_16x16x32_bf16 v[12:15], a[56:59], v[184:187], v[12:15]\n"
    "s_nop 0\n"
    "ds_read_b64_tr_b16 a[20:21], v234 offset:53248\n"
    "ds_read_b64_tr_b16 a[22:23], v234 offset:55296\n"
    "v_mfma_f32_16x16x32_bf16 v[12:15], a[60:63], v[188:191], v[12:15]\n"
    "s_waitcnt lgkmcnt(10)\n"
    "ds_read_b64_tr_b16 a[24:25], v234 offset:57344\n"
    "ds_read_b64_tr_b16 a[26:27], v234 offset:59392\n"
    "v_mul_f32 v8, v8, v112\n"
    "v_mfma_f32_16x16x32_bf16 v[0:3], a[0:3], v[128:131], 0\n"
    "s_waitcnt lgkmcnt(10)\n"
    "ds_read_b64_tr_b16 a[28:29], v234 offset:61440\n"
    "ds_read_b64_tr_b16 a[30:31], v234 offset:63488\n"
    "v_mul_f32 v9, v9, v112\n"
    "v_mfma_f32_16x16x32_bf16 v[0:3], a[4:7], v[132:135], v[0:3]\n"
    "s_waitcnt lgkmcnt(10)\n"
    "v_mul_f32 v10, v10, v112\n"
    "v_mul_f32 v11, v11, v112\n"
    "v_mfma_f32_16x16x32_bf16 v[0:3], a[8:11], v[136:139], v[0:3]\n"
    "s_waitcnt lgkmcnt(8)\n"
    "v_cvt_pk_bf16_f32 v26, v8, v9\n"
    "v_cvt_pk_bf16_f32 v27, v10, v11\n"
    "v_mfma_f32_16x16x32_bf16 v[0:3], a[12:15], v[140:143], v[0:3]\n"
    "s_waitcnt lgkmcnt(2)\n"
    "ds_read_b64_tr_b16 a[32:33], v235 offset:32768\n"
    "ds_read_b64_tr_b16 a[34:35], v235 offset:34816\n"
    "v_mfma_f32_16x16x32_bf16 v[0:3], a[16:19], v[144:147], v[0:3]\n"
    "s_mov_b32 s56, 14827520\n"
    "v_mul_f32 v12, v12, v113\n"
    "v_mul_f32 v13, v13, v113\n"
    "v_mfma_f32_16x16x32_bf16 v[0:3], a[20:23], v[148:151], v[0:3]\n"
    "buffer_store_dwordx4 v[24:27], v239, s[64:67], s56 offen sc1\n"
    "v_mfma_f32_16x16x32_bf16 v[0:3], a[24:27], v[152:155], v[0:3]\n"
    "s_waitcnt lgkmcnt(2)\n"
    "ds_read_b64_tr_b16 a[36:37], v235 offset:36864\n"
    "v_mul_f32 v14, v14, v113\n"
    "v_mfma_f32_16x16x32_bf16 v[0:3], a[28:31], v[156:159], v[0:3]\n"
    "ds_read_b64_tr_b16 a[38:39], v235 offset:38912\n"
    "v_mul_f32 v15, v15, v113\n"
    "v_mfma_f32_16x16x32_bf16 v[4:7], a[0:3], v[160:163], 0\n"
    "s_nop 0\n"
    "ds_read_b64_tr_b16 a[40:41], v235 offset:40960\n"
    "ds_read_b64_tr_b16 a[42:43], v235 offset:43008\n"
    "v_mfma_f32_16x16x32_bf16 v[4:7], a[4:7], v[164:167], v[4:7]\n"
    "ds_read_b64_tr_b16 a[44:45], v235 offset:45056\n"
    "v_cvt_pk_bf16_f32 v30, v12, v13\n"
    "v_mfma_f32_16x16x32_bf16 v[4:7], a[8:11], v[168:171], v[4:7]\n"
    "ds_read_b64_tr_b16 a[46:47], v235 offset:47104\n"
    "v_cvt_pk_bf16_f32 v31, v14, v15\n"
    "v_mfma_f32_16x16x32_bf16 v[4:7], a[12:15], v[172:175], v[4:7]\n"
    "s_nop 0\n"
    "ds_read_b64_tr_b16 a[48:49], v235 offset:49152\n"
    "ds_read_b64_tr_b16 a[50:51], v235 offset:51200\n"
    "v_mfma_f32_16x16x32_bf16 v[4:7], a[16:19], v[176:179], v[4:7]\n"
    "s_waitcnt lgkmcnt(8)\n"
    "ds_read_b64_tr_b16 a[52:53], v235 offset:53248\n"
    "ds_read_b64_tr_b16 a[54:55], v235 offset:55296\n"
    "v_mfma_f32_16x16x32_bf16 v[4:7], a[20:23], v[180:183], v[4:7]\n"
    "s_add_u32 s44, s50, 8\n"
    "s_min_u32 s44, s44, 13\n"
    "s_lshl_b32 s44, s44, 7\n"
    "s_add_u32 s44, s44, 7680\n"
    "ds_read_b64_tr_b16 a[56:57], v235 offset:57344\n"
    "ds_read_b64_tr_b16 a[58:59], v235 offset:59392\n"
    "v_mfma_f32_16x16x32_bf16 v[4:7], a[24:27], v[184:187], v[4:7]\n"
    "buffer_load_dwordx4 v[216:219], v249, s[64:67], s44 offen offset:0 sc1\n"
    "v_mfma_f32_16x16x32_bf16 v[4:7], a[28:31], v[188:191], v[4:7]\n"
    "s_mov_b32 s56, 15351808\n"
    "ds_read_b64_tr_b16 a[60:61], v235 offset:61440\n"
    "v_mul_f32 v0, v0, v112\n"
    "v_mfma_f32_16x16x32_bf16 v[8:11], a[32:35], v[128:131], 0\n"
    "s_waitcnt lgkmcnt(11)\n"
    "buffer_store_dwordx4 v[28:31], v239, s[64:67], s56 offen sc1\n"
    "ds_read_b64_tr_b16 a[62:63], v235 offset:63488\n"
    "v_mul_f32 v1, v1, v112\n"
    "v_mfma_f32_16x16x32_bf16 v[8:11], a[36:39], v[132:135], v[8:11]\n"
    "s_waitcnt lgkmcnt(8)\n"
    "v_mul_f32 v2, v2, v112\n"
    "v_mul_f32 v3, v3, v112\n"
    "v_mfma_f32_16x16x32_bf16 v[8:11], a[40:43], v[136:139], v[8:11]\n"
    "buffer_load_dwordx4 v[220:223], v249, s[64:67], s44 offen offset:1792 sc1\n"
    "v_mfma_f32_16x16x32_bf16 v[8:11], a[44:47], v[140:143], v[8:11]\n"
    "s_waitcnt lgkmcnt(6)\n"
    "v_cvt_pk_bf16_f32 v16, v0, v1\n"
    "v_cvt_pk_bf16_f32 v17, v2, v3\n"
    "v_mfma_f32_16x16x32_bf16 v[8:11], a[48:51], v[144:147], v[8:11]\n"
    "s_waitcnt lgkmcnt(4)\n"
    "v_mul_f32 v4, v4, v113\n"
    "v_mul_f32 v5, v5, v113\n"
    "v_mfma_f32_16x16x32_bf16 v[8:11], a[52:55], v[148:151], v[8:11]\n"
    "s_waitcnt lgkmcnt(2)\n"
    "ds_read_b64_tr_b16 a[0:1], v234 offset:33280\n"
    "ds_read_b64_tr_b16 a[2:3], v234 offset:35328\n"
    "v_mfma_f32_16x16x32_bf16 v[8:11], a[56:59], v[152:155], v[8:11]\n"
    "s_waitcnt lgkmcnt(2)\n"
    "ds_read_b64_tr_b16 a[4:5], v234 offset:37376\n"
    "v_mul_f32 v6, v6, v113\n"
    "v_mfma_f32_16x16x32_bf16 v[8:11], a[60:63], v[156:159], v[8:11]\n"
    "ds_read_b64_tr_b16 a[6:7], v234 offset:39424\n"
    "v_mul_f32 v7, v7, v113\n"
    "v_mfma_f32_16x16x32_bf16 v[12:15], a[32:35], v[160:163], 0\n"
    "buffer_load_dwordx4 v[224:227], v249, s[64:67], s44 offen offset:3584 sc1\n"
    "v_mfma_f32_16x16x32_bf16 v[12:15], a[36:39], v[164:167], v[12:15]\n"
    "s_nop 0\n"
    "ds_read_b64_tr_b16 a[8:9], v234 offset:41472\n"
    "ds_read_b64_tr_b16 a[10:11], v234 offset:43520\n"
    "v_mfma_f32_16x16x32_bf16 v[12:15], a[40:43], v[168:171], v[12:15]\n"
    "ds_read_b64_tr_b16 a[12:13], v234 offset:45568\n"
    "v_cvt_pk_bf16_f32 v20, v4, v5\n"
    "v_mfma_f32_16x16x32_bf16 v[12:15], a[44:47], v[172:175], v[12:15]\n"
    "ds_read_b64_tr_b16 a[14:15], v234 offset:47616\n"
    "v_cvt_pk_bf16_f32 v21, v6, v7\n"
    "v_mfma_f32_16x16x32_bf16 v[12:15], a[48:51], v[176:179], v[12:15]\n"
    "s_nop 0\n"
    "ds_read_b64_tr_b16 a[16:17], v234 offset:49664\n"
    "ds_read_b64_tr_b16 a[18:19], v234 offset:51712\n"
    "v_mfma_f32_16x16x32_bf16 v[12:15], a[52:55], v[180:183], v[12:15]\n"
    "s_waitcnt vmcnt(13)\n"
    "ds_read_b64_tr_b16 a[20:21], v234 offset:53760\n"
    "ds_read_b64_tr_b16 a[22:23], v234 offset:55808\n"
    "v_mfma_f32_16x16x32_bf16 v[12:15], a[56:59], v[184:187], v[12:15]\n"
    "buffer_atomic_swap v104, v106, s[64:67], s49 offen sc1\n"
    "v_mfma_f32_16x16x32_bf16 v[12:15], a[60:63], v[188:191], v[12:15]\n"
    "s_waitcnt lgkmcnt(10)\n"
    "ds_read_b64_tr_b16 a[24:25], v234 offset:57856\n"
    "ds_read_b64_tr_b16 a[26:27], v234 offset:59904\n"
    "v_mul_f32 v8, v8, v112\n"
    "v_mfma_f32_16x16x32_bf16 v[0:3], a[0:3], v[128:131], 0\n"
    "s_waitcnt lgkmcnt(10)\n"
    "ds_read_b64_tr_b16 a[28:29], v234 offset:61952\n"
    "ds_read_b64_tr_b16 a[30:31], v234 offset:64000\n"
    "v_mul_f32 v9, v9, v112\n"
    "v_mfma_f32_16x16x32_bf16 v[0:3], a[4:7], v[132:135], v[0:3]\n"
    "s_waitcnt lgkmcnt(10)\n"
    "v_mul_f32 v10, v10, v112\n"
    "v_mul_f32 v11, v11, v112\n"
    "v_mfma_f32_16x16x32_bf16 v[0:3], a[8:11], v[136:139], v[0:3]\n"
    "s_waitcnt lgkmcnt(8)\n"
    "v_cvt_pk_bf16_f32 v18, v8, v9\n"
    "v_cvt_pk_bf16_f32 v19, v10, v11\n"
    "v_mfma_f32_16x16x32_bf16 v[0:3], a[12:15], v[140:143], v[0:3]\n"
    "s_waitcnt lgkmcnt(6)\n"
    "ds_read_b64_tr_b16 a[32:33], v235 offset:33280\n"
    "ds_read_b64_tr_b16 a[34:35], v235 offset:35328\n"
    "v_mfma_f32_16x16x32_bf16 v[0:3], a[16:19], v[144:147], v[0:3]\n"
    "s_waitcnt lgkmcnt(6)\n"
    "v_mul_f32 v12, v12, v113\n"
    "v_mul_f32 v13, v13, v113\n"
    "v_mfma_f32_16x16x32_bf16 v[0:3], a[20:23], v[148:151], v[0:3]\n"
    "s_waitcnt lgkmcnt(2)\n"
    "ds_read_b64_tr_b16 a[36:37], v235 offset:37376\n"
    "v_mul_f32 v14, v14, v113\n"
    "v_mfma_f32_16x16x32_bf16 v[0:3], a[24:27], v[152:155], v[0:3]\n"
    "s_waitcnt vmcnt(22)\n"
    "ds_read_b64_tr_b16 a[38:39], v235 offset:39424\n"
    "v_mul_f32 v15, v15, v113\n"
    "v_mfma_f32_16x16x32_bf16 v[0:3], a[28:31], v[156:159], v[0:3]\n"
    "s_barrier\n"
    "ds_read_b64_tr_b16 a[40:41], v235 offset:41472\n"
    "ds_read_b64_tr_b16 a[42:43], v235 offset:43520\n"
    "v_mfma_f32_16x16x32_bf16 v[4:7], a[0:3], v[160:163], 0\n"
    "s_add_u32 s44, s50, 12\n"
    "s_min_u32 s44, s44, 13\n"
    "s_lshl_b32 s44, s44, 7\n"
    "s_add_u32 s44, s44, 7680\n"
    "ds_read_b64_tr_b16 a[44:45], v235 offset:45568\n"
    "v_cvt_pk_bf16_f32 v22, v12, v13\n"
    "v_mfma_f32_16x16x32_bf16 v[4:7], a[4:7], v[164:167], v[4:7]\n"
    "buffer_load_dwordx4 v[228:231], v249, s[64:67], s44 offen offset:0 sc1\n"
    "v_mfma_f32_16x16x32_bf16 v[4:7], a[8:11], v[168:171], v[4:7]\n"
    "s_mov_b32 s56, 14843904\n"
    "ds_read_b64_tr_b16 a[46:47], v235 offset:47616\n"
    "v_cvt_pk_bf16_f32 v23, v14, v15\n"
    "v_mfma_f32_16x16x32_bf16 v[4:7], a[12:15], v[172:175], v[4:7]\n"
    "buffer_store_dwordx4 v[16:19], v239, s[64:67], s56 offen sc1\n"
    "v_mfma_f32_16x16x32_bf16 v[4:7], a[16:19], v[176:179], v[4:7]\n"
    "s_waitcnt lgkmcnt(6)\n"
    "ds_read_b64_tr_b16 a[48:49], v235 offset:49664\n"
    "ds_read_b64_tr_b16 a[50:51], v235 offset:51712\n"
    "v_mfma_f32_16x16x32_bf16 v[4:7], a[20:23], v[180:183], v[4:7]\n"
    "buffer_load_dwordx4 v[240:243], v249, s[64:67], s44 offen offset:1792 sc1\n"
    "v_mfma_f32_16x16x32_bf16 v[4:7], a[24:27], v[184:187], v[4:7]\n"
    "s_mov_b32 s56, 15368192\n"
    "ds_read_b64_tr_b16 a[52:53], v235 offset:53760\n"
    "ds_read_b64_tr_b16 a[54:55], v235 offset:55808\n"
    "v_add_u32_e64 v104, s53, 1\n"
    "v_mfma_f32_16x16x32_bf16 v[4:7], a[28:31], v[188:191], v[4:7]\n"
    "s_nop 0\n"
    "buffer_store_dwordx4 v[20:23], v239, s[64:67], s56 offen sc1\n"
    "ds_read_b64_tr_b16 a[56:57], v235 offset:57856\n"
    "ds_read_b64_tr_b16 a[58:59], v235 offset:59904\n"
    "v_mul_f32 v0, v0, v112\n"
    "v_mfma_f32_16x16x32_bf16 v[8:11], a[32:35], v[128:131], 0\n"
    "s_waitcnt lgkmcnt(10)\n"
    "ds_read_b64_tr_b16 a[60:61], v235 offset:61952\n"
    "ds_read_b64_tr_b16 a[62:63], v235 offset:64000\n"
    "v_mul_f32 v1, v1, v112\n"
    "v_mfma_f32_16x16x32_bf16 v[8:11], a[36:39], v[132:135], v[8:11]\n"
    "s_waitcnt lgkmcnt(10)\n"
    "v_mul_f32 v2, v2, v112\n"
    "v_mul_f32 v3, v3, v112\n"
    "v_mfma_f32_16x16x32_bf16 v[8:11], a[40:43], v[136:139], v[8:11]\n"
    "s_waitcnt lgkmcnt(6)\n"
    "v_cvt_pk_bf16_f32 v24, v0, v1\n"
    "v_cvt_pk_bf16_f32 v25, v2, v3\n"
    "v_mfma_f32_16x16x32_bf16 v[8:11], a[44:47], v[140:143], v[8:11]\n"
    "buffer_load_dwordx4 v[244:247], v249, s[64:67], s44 offen offset:3584 sc1\n"
    "v_mfma_f32_16x16x32_bf16 v[8:11], a[48:51], v[144:147], v[8:11]\n"
    "s_waitcnt lgkmcnt(4)\n"
    "v_mul_f32 v4, v4, v113\n"
    "v_mul_f32 v5, v5, v113\n"
    "v_mfma_f32_16x16x32_bf16 v[8:11], a[52:55], v[148:151], v[8:11]\n"
    "s_waitcnt lgkmcnt(2)\n"
    "ds_read_b64_tr_b16 a[0:1], v236 offset:0\n"
    "ds_read_b64_tr_b16 a[2:3], v236 offset:2048\n"
    "v_mfma_f32_16x16x32_bf16 v[8:11], a[56:59], v[152:155], v[8:11]\n"
    "s_waitcnt lgkmcnt(2)\n"
    "ds_read_b64_tr_b16 a[4:5], v236 offset:4096\n"
    "v_mul_f32 v6, v6, v113\n"
    "v_mfma_f32_16x16x32_bf16 v[8:11], a[60:63], v[156:159], v[8:11]\n"
    "ds_read_b64_tr_b16 a[6:7], v236 offset:6144\n"
    "v_mul_f32 v7, v7, v113\n"
    "v_mfma_f32_16x16x32_bf16 v[12:15], a[32:35], v[160:163], 0\n"
    "s_nop 0\n"
    "ds_read_b64_tr_b16 a[8:9], v236 offset:8192\n"
    "ds_read_b64_tr_b16 a[10:11], v236 offset:10240\n"
    "v_mfma_f32_16x16x32_bf16 v[12:15], a[36:39], v[164:167], v[12:15]\n"
    "ds_read_b64_tr_b16 a[12:13], v236 offset:12288\n"
    "v_cvt_pk_bf16_f32 v28, v4, v5\n"
    "v_mfma_f32_16x16x32_bf16 v[12:15], a[40:43], v[168:171], v[12:15]\n"
    "ds_read_b64_tr_b16 a[14:15], v236 offset:14336\n"
    "v_cvt_pk_bf16_f32 v29, v6, v7\n"
    "v_mfma_f32_16x16x32_bf16 v[12:15], a[44:47], v[172:175], v[12:15]\n"
    "s_nop 0\n"
    "ds_read_b64_tr_b16 a[16:17], v236 offset:16384\n"
    "ds_read_b64_tr_b16 a[18:19], v236 offset:18432\n"
    "v_mfma_f32_16x16x32_bf16 v[12:15], a[48:51], v[176:179], v[12:15]\n"
    "s_nop 0\n"
    "ds_read_b64_tr_b16 a[20:21], v236 offset:20480\n"
    "ds_read_b64_tr_b16 a[22:23], v236 offset:22528\n"
    "v_mfma_f32_16x16x32_bf16 v[12:15], a[52:55], v[180:183], v[12:15]\n"
    "s_nop 0\n"
    "ds_read_b64_tr_b16 a[24:25], v236 offset:24576\n"
    "ds_read_b64_tr_b16 a[26:27], v236 offset:26624\n"
    "v_mfma_f32_16x16x32_bf16 v[12:15], a[56:59], v[184:187], v[12:15]\n"
    "ds_read_b64_tr_b16 a[28:29], v236 offset:28672\n"
    "v_mul_f32 v8, v8, v112\n"
    "v_mfma_f32_16x16x32_bf16 v[12:15], a[60:63], v[188:191], v[12:15]\n"
    "s_waitcnt lgkmcnt(13)\n"
    "ds_read_b64_tr_b16 a[30:31], v236 offset:30720\n"
    "v_mul_f32 v9, v9, v112\n"
    "v_mfma_f32_16x16x32_bf16 v[0:3], a[0:3], v[128:131], 0\n"
    "s_waitcnt lgkmcnt(12)\n"
    "v_mul_f32 v10, v10, v112\n"
    "v_mul_f32 v11, v11, v112\n"
    "v_mfma_f32_16x16x32_bf16 v[0:3], a[4:7], v[132:135], v[0:3]\n"
    "s_waitcnt lgkmcnt(10)\n"
    "v_cvt_pk_bf16_f32 v26, v8, v9\n"
    "v_cvt_pk_bf16_f32 v27, v10, v11\n"
    "v_mfma_f32_16x16x32_bf16 v[0:3], a[8:11], v[136:139], v[0:3]\n"
    "s_waitcnt lgkmcnt(8)\n"
    "ds_read_b64_tr_b16 a[32:33], v237 offset:0\n"
    "ds_read_b64_tr_b16 a[34:35], v237 offset:2048\n"
    "v_mfma_f32_16x16x32_bf16 v[0:3], a[12:15], v[140:143], v[0:3]\n"
    "s_waitcnt lgkmcnt(4)\n"
    "ds_read_b64_tr_b16 a[36:37], v237 offset:4096\n"
    "ds_read_b64_tr_b16 a[38:39], v237 offset:6144\n"
    "v_mfma_f32_16x16x32_bf16 v[0:3], a[16:19], v[144:147], v[0:3]\n"
    "s_mov_b32 s56, 14860288\n"
    "v_mul_f32 v12, v12, v113\n"
    "v_mul_f32 v13, v13, v113\n"
    "v_mfma_f32_16x16x32_bf16 v[0:3], a[20:23], v[148:151], v[0:3]\n"
    "buffer_store_dwordx4 v[24:27], v239, s[64:67], s56 offen sc1\n"
    "v_mfma_f32_16x16x32_bf16 v[0:3], a[24:27], v[152:155], v[0:3]\n"
    "s_waitcnt lgkmcnt(4)\n"
    "v_mul_f32 v14, v14, v113\n"
    "v_mul_f32 v15, v15, v113\n"
    "v_mfma_f32_16x16x32_bf16 v[0:3], a[28:31], v[156:159], v[0:3]\n"
    "s_nop 0\n"
    "ds_read_b64_tr_b16 a[40:41], v237 offset:8192\n"
    "ds_read_b64_tr_b16 a[42:43], v237 offset:10240\n"
    "v_mfma_f32_16x16x32_bf16 v[4:7], a[0:3], v[160:163], 0\n"
    "ds_read_b64_tr_b16 a[44:45], v237 offset:12288\n"
    "v_cvt_pk_bf16_f32 v30, v12, v13\n"
    "v_mfma_f32_16x16x32_bf16 v[4:7], a[4:7], v[164:167], v[4:7]\n"
    "ds_read_b64_tr_b16 a[46:47], v237 offset:14336\n"
    "v_cvt_pk_bf16_f32 v31, v14, v15\n"
    "v_mfma_f32_16x16x32_bf16 v[4:7], a[8:11], v[168:171], v[4:7]\n"
    "s_nop 0\n"
    "ds_read_b64_tr_b16 a[48:49], v237 offset:16384\n"
    "ds_read_b64_tr_b16 a[50:51], v237 offset:18432\n"
    "v_mfma_f32_16x16x32_bf16 v[4:7], a[12:15], v[172:175], v[4:7]\n"
    "s_nop 0\n"
    "ds_read_b64_tr_b16 a[52:53], v237 offset:20480\n"
    "ds_read_b64_tr_b16 a[54:55], v237 offset:22528\n"
    "v_mfma_f32_16x16x32_bf16 v[4:7], a[16:19], v[176:179], v[4:7]\n"
    "s_mov_b32 s56, 15384576\n"
    "ds_read_b64_tr_b16 a[56:57], v237 offset:24576\n"
    "ds_read_b64_tr_b16 a[58:59], v237 offset:26624\n"
    "v_mfma_f32_16x16x32_bf16 v[4:7], a[20:23], v[180:183], v[4:7]\n"
    "buffer_store_dwordx4 v[28:31], v239, s[64:67], s56 offen sc1\n"
    "v_mfma_f32_16x16x32_bf16 v[4:7], a[24:27], v[184:187], v[4:7]\n"
    "ds_read_b64_tr_b16 a[60:61], v237 offset:28672\n"
    "v_mul_f32 v0, v0, v112\n"
    "v_mfma_f32_16x16x32_bf16 v[4:7], a[28:31], v[188:191], v[4:7]\n"
    "s_waitcnt lgkmcnt(13)\n"
    "ds_read_b64_tr_b16 a[62:63], v237 offset:30720\n"
    "v_mul_f32 v1, v1, v112\n"
    "v_mfma_f32_16x16x32_bf16 v[8:11], a[32:35], v[128:131], 0\n"
    "s_waitcnt lgkmcnt(12)\n"
    "v_mul_f32 v2, v2, v112\n"
    "v_mul_f32 v3, v3, v112\n"
    "v_mfma_f32_16x16x32_bf16 v[8:11], a[36:39], v[132:135], v[8:11]\n"
    "s_waitcnt lgkmcnt(10)\n"
    "v_cvt_pk_bf16_f32 v16, v0, v1\n"
    "v_cvt_pk_bf16_f32 v17, v2, v3\n"
    "v_mfma_f32_16x16x32_bf16 v[8:11], a[40:43], v[136:139], v[8:11]\n"
    "s_waitcnt lgkmcnt(8)\n"
    "ds_read_b64_tr_b16 a[0:1], v236 offset:512\n"
    "ds_read_b64_tr_b16 a[2:3], v236 offset:2560\n"
    "v_mfma_f32_16x16x32_bf16 v[8:11], a[44:47], v[140:143], v[8:11]\n"
    "s_waitcnt lgkmcnt(8)\n"
    "ds_read_b64_tr_b16 a[4:5], v236 offset:4608\n"
    "ds_read_b64_tr_b16 a[6:7], v236 offset:6656\n"
    "v_mfma_f32_16x16x32_bf16 v[8:11], a[48:51], v[144:147], v[8:11]\n"
    "s_waitcnt lgkmcnt(8)\n"
    "v_mul_f32 v4, v4, v113\n"
    "v_mul_f32 v5, v5, v113\n"
    "v_mfma_f32_16x16x32_bf16 v[8:11], a[52:55], v[148:151], v[8:11]\n"
    "s_waitcnt lgkmcnt(6)\n"
    "v_mul_f32 v6, v6, v113\n"
    "v_mul_f32 v7, v7, v113\n"
    "v_mfma_f32_16x16x32_bf16 v[8:11], a[56:59], v[152:155], v[8:11]\n"
    "s_waitcnt lgkmcnt(4)\n"
    "ds_read_b64_tr_b16 a[8:9], v236 offset:8704\n"
    "ds_read_b64_tr_b16 a[10:11], v236 offset:10752\n"
    "v_mfma_f32_16x16x32_bf16 v[8:11], a[60:63], v[156:159], v[8:11]\n"
    "ds_read_b64_tr_b16 a[12:13], v236 offset:12800\n"
    "v_cvt_pk_bf16_f32 v20, v4, v5\n"
    "v_mfma_f32_16x16x32_bf16 v[12:15], a[32:35], v[160:163], 0\n"
    "ds_read_b64_tr_b16 a[14:15], v236 offset:14848\n"
    "v_cvt_pk_bf16_f32 v21, v6, v7\n"
    "v_mfma_f32_16x16x32_bf16 v[12:15], a[36:39], v[164:167], v[12:15]\n"
    "s_nop 0\n"
    "ds_read_b64_tr_b16 a[16:17], v236 offset:16896\n"
    "ds_read_b64_tr_b16 a[18:19], v236 offset:18944\n"
    "v_mfma_f32_16x16x32_bf16 v[12:15], a[40:43], v[168:171], v[12:15]\n"
    "s_nop 0\n"
    "ds_read_b64_tr_b16 a[20:21], v236 offset:20992\n"
    "ds_read_b64_tr_b16 a[22:23], v236 offset:23040\n"
    "v_mfma_f32_16x16x32_bf16 v[12:15], a[44:47], v[172:175], v[12:15]\n"
    "s_nop 0\n"
    "ds_read_b64_tr_b16 a[24:25], v236 offset:25088\n"
    "ds_read_b64_tr_b16 a[26:27], v236 offset:27136\n"
    "v_mfma_f32_16x16x32_bf16 v[12:15], a[48:51], v[176:179], v[12:15]\n"
    "ds_read_b64_tr_b16 a[28:29], v236 offset:29184\n"
    "v_mul_f32 v8, v8, v112\n"
    "v_mfma_f32_16x16x32_bf16 v[12:15], a[52:55], v[180:183], v[12:15]\n"
    "ds_read_b64_tr_b16 a[30:31], v236 offset:31232\n"
    "v_mul_f32 v9, v9, v112\n"
    "v_mfma_f32_16x16x32_bf16 v[12:15], a[56:59], v[184:187], v[12:15]\n"
    "v_mul_f32 v10, v10, v112\n"
    "v_mul_f32 v11, v11, v112\n"
    "v_mfma_f32_16x16x32_bf16 v[12:15], a[60:63], v[188:191], v[12:15]\n"
    "s_waitcnt lgkmcnt(14)\n"
    "v_cvt_pk_bf16_f32 v18, v8, v9\n"
    "v_cvt_pk_bf16_f32 v19, v10, v11\n"
    "v_mfma_f32_16x16x32_bf16 v[0:3], a[0:3], v[128:131], 0\n"
    "s_waitcnt lgkmcnt(12)\n"
    "ds_read_b64_tr_b16 a[32:33], v237 offset:512\n"
    "ds_read_b64_tr_b16 a[34:35], v237 offset:2560\n"
    "v_mfma_f32_16x16x32_bf16 v[0:3], a[4:7], v[132:135], v[0:3]\n"
    "s_waitcnt lgkmcnt(12)\n"
    "ds_read_b64_tr_b16 a[36:37], v237 offset:4608\n"
    "ds_read_b64_tr_b16 a[38:39], v237 offset:6656\n"
    "v_mfma_f32_16x16x32_bf16 v[0:3], a[8:11], v[136:139], v[0:3]\n"
    "s_waitcnt lgkmcnt(12)\n"
    "ds_read_b64_tr_b16 a[40:41], v237 offset:8704\n"
    "ds_read_b64_tr_b16 a[42:43], v237 offset:10752\n"
    "v_mfma_f32_16x16x32_bf16 v[0:3], a[12:15], v[140:143], v[0:3]\n"
    "s_waitcnt lgkmcnt(6)\n"
    "ds_read_b64_tr_b16 a[44:45], v237 offset:12800\n"
    "ds_read_b64_tr_b16 a[46:47], v237 offset:14848\n"
    "v_mfma_f32_16x16x32_bf16 v[0:3], a[16:19], v[144:147], v[0:3]\n"
    "buffer_atomic_add v108, v107, s[64:67], s51 offen sc0 sc1\n"
    "v_mfma_f32_16x16x32_bf16 v[0:3], a[20:23], v[148:151], v[0:3]\n"
    "s_mov_b32 s56, 14876672\n"
    "v_mul_f32 v12, v12, v113\n"
    "v_mul_f32 v13, v13, v113\n"
    "v_mfma_f32_16x16x32_bf16 v[0:3], a[24:27], v[152:155], v[0:3]\n"
    "buffer_store_dwordx4 v[16:19], v239, s[64:67], s56 offen sc1\n"
    "v_mfma_f32_16x16x32_bf16 v[0:3], a[28:31], v[156:159], v[0:3]\n"
    "v_mul_f32 v14, v14, v113\n"
    "v_mul_f32 v15, v15, v113\n"
    "v_mfma_f32_16x16x32_bf16 v[4:7], a[0:3], v[160:163], 0\n"
    "v_cvt_pk_bf16_f32 v22, v12, v13\n"
    "v_cvt_pk_bf16_f32 v23, v14, v15\n"
    "v_mfma_f32_16x16x32_bf16 v[4:7], a[4:7], v[164:167], v[4:7]\n"
    "s_nop 0\n"
    "ds_read_b64_tr_b16 a[48:49], v237 offset:16896\n"
    "ds_read_b64_tr_b16 a[50:51], v237 offset:18944\n"
    "v_mfma_f32_16x16x32_bf16 v[4:7], a[8:11], v[168:171], v[4:7]\n"
    "s_nop 0\n"
    "ds_read_b64_tr_b16 a[52:53], v237 offset:20992\n"
    "ds_read_b64_tr_b16 a[54:55], v237 offset:23040\n"
    "v_mfma_f32_16x16x32_bf16 v[4:7], a[12:15], v[172:175], v[4:7]\n"
    "s_nop 0\n"
    "ds_read_b64_tr_b16 a[56:57], v237 offset:25088\n"
    "ds_read_b64_tr_b16 a[58:59], v237 offset:27136\n"
    "v_mfma_f32_16x16x32_bf16 v[4:7], a[16:19], v[176:179], v[4:7]\n"
    "s_mov_b32 s56, 15400960\n"
    "ds_read_b64_tr_b16 a[60:61], v237 offset:29184\n"
    "v_mul_f32 v0, v0, v112\n"
    "v_mfma_f32_16x16x32_bf16 v[4:7], a[20:23], v[180:183], v[4:7]\n"
    "buffer_store_dwordx4 v[20:23], v239, s[64:67], s56 offen sc1\n"
    "v_mfma_f32_16x16x32_bf16 v[4:7], a[24:27], v[184:187], v[4:7]\n"
    "ds_read_b64_tr_b16 a[62:63], v237 offset:31232\n"
    "v_mul_f32 v1, v1, v112\n"
    "v_mfma_f32_16x16x32_bf16 v[4:7], a[28:31], v[188:191], v[4:7]\n"
    "s_waitcnt lgkmcnt(14)\n"
    "v_mul_f32 v2, v2, v112\n"
    "v_mul_f32 v3, v3, v112\n"
    "v_mfma_f32_16x16x32_bf16 v[8:11], a[32:35], v[128:131], 0\n"
    "s_waitcnt lgkmcnt(12)\n"
    "v_cvt_pk_bf16_f32 v24, v0, v1\n"
    "v_cvt_pk_bf16_f32 v25, v2, v3\n"
    "v_mfma_f32_16x16x32_bf16 v[8:11], a[36:39], v[132:135], v[8:11]\n"
    "s_waitcnt lgkmcnt(10)\n"
    "v_mfma_f32_16x16x32_bf16 v[8:11], a[40:43], v[136:139], v[8:11]\n"
    "s_waitcnt lgkmcnt(8)\n"
    "v_mfma_f32_16x16x32_bf16 v[8:11], a[44:47], v[140:143], v[8:11]\n"
    "s_waitcnt lgkmcnt(6)\n"
    "v_mfma_f32_16x16x32_bf16 v[8:11], a[48:51], v[144:147], v[8:11]\n"
    "s_waitcnt lgkmcnt(4)\n"
    "v_mul_f32 v4, v4, v113\n"
    "v_mul_f32 v5, v5, v113\n"
    "v_mfma_f32_16x16x32_bf16 v[8:11], a[52:55], v[148:151], v[8:11]\n"
    "s_waitcnt lgkmcnt(2)\n"
    "v_mul_f32 v6, v6, v113\n"
    "v_mul_f32 v7, v7, v113\n"
    "v_mfma_f32_16x16x32_bf16 v[8:11], a[56:59], v[152:155], v[8:11]\n"
    "s_waitcnt lgkmcnt(0)\n"
    "v_cvt_pk_bf16_f32 v28, v4, v5\n"
    "v_cvt_pk_bf16_f32 v29, v6, v7\n"
    "v_mfma_f32_16x16x32_bf16 v[8:11], a[60:63], v[156:159], v[8:11]\n"
    "v_mfma_f32_16x16x32_bf16 v[12:15], a[32:35], v[160:163], 0\n"
    "v_mfma_f32_16x16x32_bf16 v[12:15], a[36:39], v[164:167], v[12:15]\n"
    "v_mfma_f32_16x16x32_bf16 v[12:15], a[40:43], v[168:171], v[12:15]\n"
    "v_mfma_f32_16x16x32_bf16 v[12:15], a[44:47], v[172:175], v[12:15]\n"
    "v_mfma_f32_16x16x32_bf16 v[12:15], a[48:51], v[176:179], v[12:15]\n"
    "s_nop 2\n"
    "v_mul_f32 v8, v8, v112\n"
    "v_mul_f32 v9, v9, v112\n"
    "v_mfma_f32_16x16x32_bf16 v[12:15], a[52:55], v[180:183], v[12:15]\n"
    "v_mul_f32 v10, v10, v112\n"
    "v_mul_f32 v11, v11, v112\n"
    "v_mfma_f32_16x16x32_bf16 v[12:15], a[56:59], v[184:187], v[12:15]\n"
    "v_cvt_pk_bf16_f32 v26, v8, v9\n"
    "v_cvt_pk_bf16_f32 v27, v10, v11\n"
    "v_mfma_f32_16x16x32_bf16 v[12:15], a[60:63], v[188:191], v[12:15]\n"
    "s_mov_b32 s56, 14893056\n"
    "buffer_store_dwordx4 v[24:27], v239, s[64:67], s56 offen sc1\n"
    "s_nop 5\n"
    "v_mul_f32 v12, v12, v113\n"
    "v_mul_f32 v13, v13, v113\n"
    "v_mul_f32 v14, v14, v113\n"
    "v_mul_f32 v15, v15, v113\n"
    "s_nop 1\n"
    "v_cvt_pk_bf16_f32 v30, v12, v13\n"
    "v_cvt_pk_bf16_f32 v31, v14, v15\n"
    "s_mov_b32 s56, 15417344\n"
    "buffer_store_dwordx4 v[28:31], v239, s[64:67], s56 offen sc1\n"
    "s_barrier\n"
    "s_waitcnt vmcnt(4)\n"
    "v_subrev_u32 v108, s53, v108\n"
    "v_cmp_lt_u32 vcc, 2, v108\n"
    "v_mov_b32 v108, 0\n"
    "s_and_b32 vcc_lo, vcc_lo, 65535\n"
    "s_cbranch_scc0 L_fd7_%=\n"
    "s_nop 0\n"
    "s_waitcnt vmcnt(0)\n"
    "buffer_atomic_swap v104, v106, s[64:67], s49 offen sc1\n"
    "L_fw6_%=:\n"
    "s_sub_u32 s99, s99, 1\n"
    "s_cbranch_scc1 L_fd7_%=\n"
    "buffer_atomic_add v108, v107, s[64:67], s51 offen sc0 sc1\n"
    "s_waitcnt vmcnt(0)\n"
    "v_subrev_u32 v108, s53, v108\n"
    "v_cmp_lt_u32 vcc, 2, v108\n"
    "v_mov_b32 v108, 0\n"
    "s_and_b32 vcc_lo, vcc_lo, 65535\n"
    "s_cbranch_scc1 L_fw6_%=\n"
    "L_fd7_%=:\n"
    "s_max_i32 s99, s99, 0\n"
    "s_add_u32 s54, s45, 0\n"
    "buffer_load_dwordx4 v[32:35], v110, s[64:67], s54 offen offset:0 sc1 nt\n"
    "s_add_u32 s68, s46, 48234496\n"
    "s_add_u32 m0, s52, 0\n"
    "s_nop 0\n"
    "buffer_load_dwordx4 v251, s[64:67], s68 offen sc1 lds\n"
    "buffer_load_dwordx4 v[36:39], v110, s[64:67], s54 offen offset:1024 sc1 nt\n"
    "s_add_u32 s68, s46, 48238592\n"
    "s_add_u32 m0, s52, 4096\n"
    "s_nop 0\n"
    "buffer_load_dwordx4 v251, s[64:67], s68 offen sc1 lds\n"
    "buffer_load_dwordx4 v[40:43], v110, s[64:67], s54 offen offset:2048 sc1 nt\n"
    "s_add_u32 s68, s46, 48242688\n"
    "s_add_u32 m0, s52, 8192\n"
    "s_nop 0\n"
    "buffer_load_dwordx4 v251, s[64:67], s68 offen sc1 lds\n"
    "buffer_load_dwordx4 v[44:47], v110, s[64:67], s54 offen offset:3072 sc1 nt\n"
    "s_add_u32 s68, s46, 48246784\n"
    "s_add_u32 m0, s52, 12288\n"
    "s_nop 0\n"
    "buffer_load_dwordx4 v251, s[64:67], s68 offen sc1 lds\n"
    "s_add_u32 s68, s46, 48250880\n"
    "s_add_u32 m0, s52, 16384\n"
    "s_nop 0\n"
    "buffer_load_dwordx4 v251, s[64:67], s68 offen sc1 lds\n"
    "s_add_u32 s55, s54, 4096\n"
    "buffer_load_dwordx4 v[48:51], v110, s[64:67], s55 offen offset:0 sc1 nt\n"
    "s_add_u32 s68, s46, 48254976\n"
    "s_add_u32 m0, s52, 20480\n"
    "s_nop 0\n"
    "buffer_load_dwordx4 v251, s[64:67], s68 offen sc1 lds\n"
    "buffer_load_dwordx4 v[52:55], v110, s[64:67], s55 offen offset:1024 sc1 nt\n"
    "s_waitcnt vmcnt(12)\n"
    "buffer_atomic_swap v104, v106, s[64:67], s49 offen sc1\n"
    "L_gr8_%=:\n"
    "v_xor_b32 v114, s53, v193\n"
    "v_xor_b32 v115, s53, v195\n"
    "v_or_b32 v114, v114, v115\n"
    "v_xor_b32 v115, s53, v197\n"
    "v_or_b32 v114, v114, v115\n"
    "v_xor_b32 v115, s53, v199\n"
    "v_or_b32 v114, v114, v115\n"
    "v_xor_b32 v115, s53, v201\n"
    "v_or_b32 v114, v114, v115\n"
    "v_xor_b32 v115, s53, v203\n"
    "v_or_b32 v114, v114, v115\n"
    "v_xor_b32 v115, s53, v205\n"
    "v_or_b32 v114, v114, v115\n"
    "v_xor_b32 v115, s53, v207\n"
    "v_or_b32 v114, v114, v115\n"
    "v_xor_b32 v115, s53, v209\n"
    "v_or_b32 v114, v114, v115\n"
    "v_xor_b32 v115, s53, v211\n"
    "v_or_b32 v114, v114, v115\n"
    "v_xor_b32 v115, s53, v213\n"
    "v_or_b32 v114, v114, v115\n"
    "v_xor_b32 v115, s53, v215\n"
    "v_or_b32 v114, v114, v115\n"
    "v_xor_b32 v115, s53, v217\n"
    "v_or_b32 v114, v114, v115\n"
    "v_xor_b32 v115, s53, v219\n"
    "v_or_b32 v114, v114, v115\n"
    "v_xor_b32 v115, s53, v221\n"
    "v_or_b32 v114, v114, v115\n"
    "v_xor_b32 v115, s53, v223\n"
    "v_or_b32 v114, v114, v115\n"
    "v_xor_b32 v115, s53, v225\n"
    "v_or_b32 v114, v114, v115\n"
    "v_xor_b32 v115, s53, v227\n"
    "v_or_b32 v114, v114, v115\n"
    "v_xor_b32 v115, s53, v229\n"
    "v_or_b32 v114, v114, v115\n"
    "v_xor_b32 v115, s53, v231\n"
    "v_or_b32 v114, v114, v115\n"
    "v_xor_b32 v115, s53, v241\n"
    "v_or_b32 v114, v114, v115\n"
    "v_xor_b32 v115, s53, v243\n"
    "v_or_b32 v114, v114, v115\n"
    "v_xor_b32 v115, s53, v245\n"
    "v_or_b32 v114, v114, v115\n"
    "v_xor_b32 v115, s53, v247\n"
    "v_or_b32 v114, v114, v115\n"
    "v_cmp_ne_u32 vcc, 0, v114\n"
    "s_nop 1\n"
    "s_cbranch_vccz L_grd9_%=\n"
    "s_sub_u32 s99, s99, 1\n"
    "s_cbranch_scc1 L_grd9_%=\n"
    "s_nop 0\n"
    "s_add_u32 s44, s50, 0\n"
    "s_min_u32 s44, s44, 13\n"
    "s_lshl_b32 s44, s44, 7\n"
    "s_add_u32 s44, s44, 7680\n"
    "buffer_load_dwordx4 v[192:195], v249, s[64:67], s44 offen offset:0 sc1\n"
    "buffer_load_dwordx4 v[196:199], v249, s[64:67], s44 offen offset:1792 sc1\n"
    "buffer_load_dwordx4 v[200:203], v249, s[64:67], s44 offen offset:3584 sc1\n"
    "s_add_u32 s44, s50, 4\n"
    "s_min_u32 s44, s44, 13\n"
    "s_lshl_b32 s44, s44, 7\n"
    "s_add_u32 s44, s44, 7680\n"
    "buffer_load_dwordx4 v[204:207], v249, s[64:67], s44 offen offset:0 sc1\n"
    "buffer_load_dwordx4 v[208:211], v249, s[64:67], s44 offen offset:1792 sc1\n"
    "buffer_load_dwordx4 v[212:215], v249, s[64:67], s44 offen offset:3584 sc1\n"
    "s_add_u32 s44, s50, 8\n"
    "s_min_u32 s44, s44, 13\n"
    "s_lshl_b32 s44, s44, 7\n"
    "s_add_u32 s44, s44, 7680\n"
    "buffer_load_dwordx4 v[216:219], v249, s[64:67], s44 offen offset:0 sc1\n"
    "buffer_load_dwordx4 v[220:223], v249, s[64:67], s44 offen offset:1792 sc1\n"
    "buffer_load_dwordx4 v[224:227], v249, s[64:67], s44 offen offset:3584 sc1\n"
    "s_add_u32 s44, s50, 12\n"
    "s_min_u32 s44, s44, 13\n"
    "s_lshl_b32 s44, s44, 7\n"
    "s_add_u32 s44, s44, 7680\n"
    "buffer_load_dwordx4 v[228:231], v249, s[64:67], s44 offen offset:0 sc1\n"
    "buffer_load_dwordx4 v[240:243], v249, s[64:67], s44 offen offset:1792 sc1\n"
    "buffer_load_dwordx4 v[244:247], v249, s[64:67], s44 offen offset:3584 sc1\n"
    "s_waitcnt vmcnt(0)\n"
    "s_branch L_gr8_%=\n"
    "L_grd9_%=:\n"
    "s_max_i32 s99, s99, 0\n"
    "s_add_u32 s68, s46, 48259072\n"
    "s_add_u32 m0, s52, 24576\n"
    "s_nop 0\n"
    "buffer_load_dwordx4 v251, s[64:67], s68 offen sc1 lds\n"
    "v_and_b32 v193, 0xffff0000, v192\n"
    "v_lshlrev_b32 v192, 16, v192\n"
    "v_and_b32 v195, 0xffff0000, v194\n"
    "v_lshlrev_b32 v194, 16, v194\n"
    "v_and_b32 v197, 0xffff0000, v196\n"
    "v_lshlrev_b32 v196, 16, v196\n"
    "v_and_b32 v199, 0xffff0000, v198\n"
    "v_lshlrev_b32 v198, 16, v198\n"
    "buffer_load_dwordx4 v[56:59], v110, s[64:67], s55 offen offset:2048 sc1 nt\n"
    "v_and_b32 v201, 0xffff0000, v200\n"
    "v_lshlrev_b32 v200, 16, v200\n"
    "v_and_b32 v203, 0xffff0000, v202\n"
    "v_lshlrev_b32 v202, 16, v202\n"
    "v_and_b32 v205, 0xffff0000, v204\n"
    "v_lshlrev_b32 v204, 16, v204\n"
    "v_and_b32 v207, 0xffff0000, v206\n"
    "v_lshlrev_b32 v206, 16, v206\n"
    "s_add_u32 s68, s46, 48263168\n"
    "s_add_u32 m0, s52, 28672\n"
    "s_nop 0\n"
    "buffer_load_dwordx4 v251, s[64:67], s68 offen sc1 lds\n"
    "v_and_b32 v209, 0xffff0000, v208\n"
    "v_lshlrev_b32 v208, 16, v208\n"
    "v_and_b32 v211, 0xffff0000, v210\n"
    "v_lshlrev_b32 v210, 16, v210\n"
    "v_and_b32 v213, 0xffff0000, v212\n"
    "v_lshlrev_b32 v212, 16, v212\n"
    "v_and_b32 v215, 0xffff0000, v214\n"
    "v_lshlrev_b32 v214, 16, v214\n"
    "buffer_load_dwordx4 v[60:63], v110, s[64:67], s55 offen offset:3072 sc1 nt\n"
    "v_and_b32 v217, 0xffff0000, v216\n"
    "v_lshlrev_b32 v216, 16, v216\n"
    "v_and_b32 v219, 0xffff0000, v218\n"
    "v_lshlrev_b32 v218, 16, v218\n"
    "v_and_b32 v221, 0xffff0000, v220\n"
    "v_lshlrev_b32 v220, 16, v220\n"
    "v_and_b32 v223, 0xffff0000, v222\n"
    "v_lshlrev_b32 v222, 16, v222\n"
    "s_add_u32 s68, s46, 48267264\n"
    "s_add_u32 m0, s52, 32768\n"
    "s_nop 0\n"
    "buffer_load_dwordx4 v251, s[64:67], s68 offen sc1 lds\n"
    "v_and_b32 v225, 0xffff0000, v224\n"
    "v_lshlrev_b32 v224, 16, v224\n"
    "v_and_b32 v227, 0xffff0000, v226\n"
    "v_lshlrev_b32 v226, 16, v226\n"
    "v_and_b32 v229, 0xffff0000, v228\n"
    "v_lshlrev_b32 v228, 16, v228\n"
    "v_and_b32 v231, 0xffff0000, v230\n"
    "v_lshlrev_b32 v230, 16, v230\n"
    "s_add_u32 s68, s46, 48271360\n"
    "s_add_u32 m0, s52, 36864\n"
    "s_nop 0\n"
    "buffer_load_dwordx4 v251, s[64:67], s68 offen sc1 lds\n"
    "v_and_b32 v241, 0xffff0000, v240\n"
    "v_lshlrev_b32 v240, 16, v240\n"
    "v_and_b32 v243, 0xffff0000, v242\n"
    "v_lshlrev_b32 v242, 16, v242\n"
    "v_and_b32 v245, 0xffff0000, v244\n"
    "v_lshlrev_b32 v244, 16, v244\n"
    "v_and_b32 v247, 0xffff0000, v246\n"
    "v_lshlrev_b32 v246, 16, v246\n"
    "s_add_u32 s54, s45, 65536\n"
    "buffer_load_dwordx4 v[72:75], v110, s[64:67], s54 offen offset:0 sc1 nt\n"
    "v_pk_add_f32 v[200:201], v[200:201], 1.0 op_sel_hi:[1,0]\n"
    "v_cvt_pk_bf16_f32 v114, v200, v201\n"
    "v_and_b32 v201, 0xffff0000, v114\n"
    "v_lshlrev_b32 v200, 16, v114\n"
    "v_pk_add_f32 v[202:203], v[202:203], 1.0 op_sel_hi:[1,0]\n"
    "v_cvt_pk_bf16_f32 v114, v202, v203\n"
    "v_and_b32 v203, 0xffff0000, v114\n"
    "v_lshlrev_b32 v202, 16, v114\n"
    "s_add_u32 s68, s46, 48275456\n"
    "s_add_u32 m0, s52, 40960\n"
    "s_nop 0\n"
    "buffer_load_dwordx4 v251, s[64:67], s68 offen sc1 lds\n"
    "v_pk_add_f32 v[212:213], v[212:213], 1.0 op_sel_hi:[1,0]\n"
    "v_cvt_pk_bf16_f32 v114, v212, v213\n"
    "v_and_b32 v213, 0xffff0000, v114\n"
    "v_lshlrev_b32 v212, 16, v114\n"
    "v_pk_add_f32 v[214:215], v[214:215], 1.0 op_sel_hi:[1,0]\n"
    "v_cvt_pk_bf16_f32 v114, v214, v215\n"
    "v_and_b32 v215, 0xffff0000, v114\n"
    "v_lshlrev_b32 v214, 16, v114\n"
    "buffer_load_dwordx4 v[76:79], v110, s[64:67], s54 offen offset:1024 sc1 nt\n"
    "v_pk_add_f32 v[224:225], v[224:225], 1.0 op_sel_hi:[1,0]\n"
    "v_cvt_pk_bf16_f32 v114, v224, v225\n"
    "v_and_b32 v225, 0xffff0000, v114\n"
    "v_lshlrev_b32 v224, 16, v114\n"
    "v_pk_add_f32 v[226:227], v[226:227], 1.0 op_sel_hi:[1,0]\n"
    "v_cvt_pk_bf16_f32 v114, v226, v227\n"
    "v_and_b32 v227, 0xffff0000, v114\n"
    "v_lshlrev_b32 v226, 16, v114\n"
    "v_pk_add_f32 v[244:245], v[244:245], 1.0 op_sel_hi:[1,0]\n"
    "v_cvt_pk_bf16_f32 v114, v244, v245\n"
    "v_and_b32 v245, 0xffff0000, v114\n"
    "v_lshlrev_b32 v244, 16, v114\n"
    "v_pk_add_f32 v[246:247], v[246:247], 1.0 op_sel_hi:[1,0]\n"
    "v_cvt_pk_bf16_f32 v114, v246, v247\n"
    "v_and_b32 v247, 0xffff0000, v114\n"
    "v_lshlrev_b32 v246, 16, v114\n"
    "v_mov_b32 v112, 0x3f80\n"
    "v_mov_b32 v113, 0x3f800000\n"
    "v_and_b32 v125, 7, v250\n"
    "v_lshlrev_b32 v125, 5, v125\n"
    "v_add_u32 v125, 159744, v125\n"
    "v_lshrrev_b32 v124, 5, v250\n"
    "v_lshl_add_u32 v124, v124, 4, v125\n"
    "s_lshl_b32 s44, s50, 2\n"
    "v_add_u32 v124, s44, v124\n"
    "v_lshrrev_b32 v114, 5, v250\n"
    "v_lshlrev_b32 v126, 12, v114\n"
    "v_bfe_u32 v115, v250, 3, 2\n"
    "v_lshl_add_u32 v126, v115, 8, v126\n"
    "v_and_b32 v115, 7, v250\n"
    "v_lshl_add_u32 v126, v115, 4, v126\n"
    "s_bfe_u32 s44, s70, 0x60001\n"
    "s_lshl_b32 s44, s44, 15\n"
    "s_and_b32 s57, s70, 1\n"
    "s_lshl_b32 s57, s57, 7\n"
    "s_add_u32 s44, s44, s57\n"
    "s_lshl_b32 s57, s50, 10\n"
    "s_add_u32 s44, s44, s57\n"
    "v_add_u32 v126, s44, v126\n"
    "s_lshr_b32 s57, s50, 1\n"
    "v_and_b32 v114, s57, v114\n"
    "v_lshlrev_b32 v114, 30, v114\n"
    "v_add_u32 v114, 0x4000, v114\n"
    "v_add_u32 v127, v114, v126\n"
    "s_mov_b32 s38, 4194304\n"
    "s_mov_b32 s37, 2097152\n"
    "s_bfe_u32 s57, s42, 65538\n"
    "s_lshl_b32 s57, s57, 13\n"
    "s_add_u32 s38, s38, s57\n"
    "s_bfe_u32 s57, s42, 65537\n"
    "s_lshl_b32 s57, s57, 13\n"
    "s_add_u32 s37, s37, s57\n"
    "v_lshlrev_b32 v235, 4, v250\n"
    "v_add_u32 v236, 57344, v235\n"
    "v_add_u32 v237, 114688, v235\n"
    "buffer_atomic_add v108, v107, s[64:67], s51 offen sc0 sc1\n"
    "s_add_u32 s68, s46, 48279552\n"
    "s_add_u32 m0, s52, 45056\n"
    "s_nop 0\n"
    "buffer_load_dwordx4 v251, s[64:67], s68 offen sc1 lds\n"
    "buffer_load_dwordx4 v[80:83], v110, s[64:67], s54 offen offset:2048 sc1 nt\n"
    "s_add_u32 s68, s46, 48283648\n"
    "s_add_u32 m0, s52, 49152\n"
    "s_nop 0\n"
    "buffer_load_dwordx4 v251, s[64:67], s68 offen sc1 lds\n"
    "buffer_load_dwordx4 v[84:87], v110, s[64:67], s54 offen offset:3072 sc1 nt\n"
    "s_add_u32 s68, s46, 48287744\n"
    "s_add_u32 m0, s52, 53248\n"
    "s_nop 0\n"
    "buffer_load_dwordx4 v251, s[64:67], s68 offen sc1 lds\n"
    "s_add_u32 s68, s46, 48291840\n"
    "s_add_u32 m0, s52, 57344\n"
    "s_nop 0\n"
    "buffer_load_dwordx4 v251, s[64:67], s68 offen sc1 lds\n"
    "s_add_u32 s55, s54, 4096\n"
    "buffer_load_dwordx4 v[88:91], v110, s[64:67], s55 offen offset:0 sc1 nt\n"
    "s_add_u32 s68, s46, 48295936\n"
    "s_add_u32 m0, s52, 61440\n"
    "s_nop 0\n"
    "buffer_load_dwordx4 v251, s[64:67], s68 offen sc1 lds\n"
    "buffer_load_dwordx4 v[92:95], v110, s[64:67], s55 offen offset:1024 sc1 nt\n"
    "s_add_u32 s68, s46, 48300032\n"
    "s_add_u32 m0, s52, 65536\n"
    "s_nop 0\n"
    "buffer_load_dwordx4 v251, s[64:67], s68 offen sc1 lds\n"
    "buffer_load_dwordx4 v[96:99], v110, s[64:67], s55 offen offset:2048 sc1 nt\n"
    "s_add_u32 s68, s46, 48304128\n"
    "s_add_u32 m0, s52, 69632\n"
    "s_nop 0\n"
    "buffer_load_dwordx4 v251, s[64:67], s68 offen sc1 lds\n"
    "buffer_load_dwordx4 v[100:103], v110, s[64:67], s55 offen offset:3072 sc1 nt\n"
    "s_waitcnt vmcnt(22)\n"
    "s_barrier\n"
    "ds_read_b128 a[144:147], v235 offset:0\n"
    "ds_read_b128 a[148:151], v235 offset:1024\n"
    "ds_read_b128 a[152:155], v235 offset:2048\n"
    "ds_read_b128 a[156:159], v235 offset:3072\n"
    "ds_read_b128 a[160:163], v235 offset:4096\n"
    "ds_read_b128 a[164:167], v235 offset:5120\n"
    "ds_read_b128 a[168:171], v235 offset:6144\n"
    "ds_read_b128 a[172:175], v235 offset:7168\n"
    "ds_read_b128 a[176:179], v235 offset:8192\n"
    "ds_read_b128 a[180:183], v235 offset:9216\n"
    "ds_read_b128 a[184:187], v235 offset:10240\n"
    "ds_read_b128 a[188:191], v235 offset:11264\n"
    "ds_read_b128 a[192:195], v235 offset:12288\n"
    "ds_read_b128 a[196:199], v235 offset:13312\n"
    "ds_read_b128 a[200:203], v235 offset:14336\n"
    "ds_read_b128 a[204:207], v235 offset:15360\n"
    "ds_read_b128 a[208:211], v235 offset:16384\n"
    "ds_read_b128 a[212:215], v235 offset:17408\n"
    "ds_read_b128 a[216:219], v235 offset:18432\n"
    "ds_read_b128 a[220:223], v235 offset:19456\n"
    "ds_read_b128 a[224:227], v235 offset:20480\n"
    "ds_read_b128 a[228:231], v235 offset:21504\n"
    "ds_read_b128 a[232:235], v235 offset:22528\n"
    "ds_read_b128 a[236:239], v235 offset:23552\n"
    "ds_read_b128 a[240:243], v235 offset:24576\n"
    "ds_read_b128 a[244:247], v235 offset:25600\n"
    "ds_read_b128 a[248:251], v235 offset:26624\n"
    "ds_read_b128 a[252:255], v235 offset:27648\n"
    "s_waitcnt vmcnt(13)\n"
    "v_subrev_u32 v108, s53, v108\n"
    "v_subrev_u32 v108, 1, v108\n"
    "v_cmp_lt_u32 vcc, 1, v108\n"
    "v_mov_b32 v108, 0\n"
    "s_and_b32 vcc_lo, vcc_lo, 65535\n"
    "s_cbranch_scc0 L_fd11_%=\n"
    "s_nop 0\n"
    "L_fw10_%=:\n"
    "s_sub_u32 s99, s99, 1\n"
    "s_cbranch_scc1 L_fd11_%=\n"
    "buffer_atomic_add v108, v107, s[64:67], s51 offen sc0 sc1\n"
    "s_waitcnt vmcnt(0)\n"
    "v_subrev_u32 v108, s53, v108\n"
    "v_subrev_u32 v108, 1, v108\n"
    "v_cmp_lt_u32 vcc, 1, v108\n"
    "v_mov_b32 v108, 0\n"
    "s_and_b32 vcc_lo, vcc_lo, 65535\n"
    "s_cbranch_scc1 L_fw10_%=\n"
    "L_fd11_%=:\n"
    "s_max_i32 s99, s99, 0\n"
    "s_or_b32 s44, %1, s50\n"
    "s_cbranch_scc1 L_ep12_%=\n"
    "s_nop 0\n"
    "v_mov_b32 v116, s53\n"
    "v_mov_b32 v117, s53\n"
    "s_mov_b64 exec, 1\n"
    "buffer_store_dwordx2 v[116:117], off, s[64:67], s44 sc1\n"
    "s_mov_b64 exec, -1\n"
    "s_nop 0\n"
    "L_ep12_%=:\n"
    "s_add_u32 s68, s46, 48308224\n"
    "s_add_u32 m0, s52, 73728\n"
    "s_nop 0\n"
    "buffer_load_dwordx4 v251, s[64:67], s68 offen sc1 lds\n"
    "s_waitcnt vmcnt(36)\n"
    "v_lshlrev_b32 v0, 16, v32\n"
    "v_and_b32 v1, 0xffff0000, v32\n"
    "v_lshlrev_b32 v2, 16, v33\n"
    "v_and_b32 v3, 0xffff0000, v33\n"
    "s_add_u32 s68, s46, 48312320\n"
    "s_add_u32 m0, s52, 77824\n"
    "s_nop 0\n"
    "buffer_load_dwordx4 v251, s[64:67], s68 offen sc1 lds\n"
    "v_lshlrev_b32 v4, 16, v34\n"
    "v_and_b32 v5, 0xffff0000, v34\n"
    "v_lshlrev_b32 v6, 16, v35\n"
    "v_and_b32 v7, 0xffff0000, v35\n"
    "s_waitcnt vmcnt(35)\n"
    "s_add_u32 s54, s45, 131072\n"
    "buffer_load_dwordx4 v[128:131], v110, s[64:67], s54 offen offset:0 sc1 nt\n"
    "v_dot2c_f32_bf16 v0, v112, v36\n"
    "v_dot2c_f32_bf16 v1, v113, v36\n"
    "v_dot2c_f32_bf16 v2, v112, v37\n"
    "v_dot2c_f32_bf16 v3, v113, v37\n"
    "v_dot2c_f32_bf16 v4, v112, v38\n"
    "s_add_u32 s68, s46, 48316416\n"
    "s_add_u32 m0, s52, 81920\n"
    "s_nop 0\n"
    "buffer_load_dwordx4 v251, s[64:67], s68 offen sc1 lds\n"
    "v_dot2c_f32_bf16 v5, v113, v38\n"
    "v_dot2c_f32_bf16 v6, v112, v39\n"
    "v_dot2c_f32_bf16 v7, v113, v39\n"
    "s_waitcnt vmcnt(35)\n"
    "v_dot2c_f32_bf16 v0, v112, v40\n"
    "buffer_load_dwordx4 v[132:135], v110, s[64:67], s54 offen offset:1024 sc1 nt\n"
    "v_dot2c_f32_bf16 v1, v113, v40\n"
    "v_dot2c_f32_bf16 v2, v112, v41\n"
    "v_dot2c_f32_bf16 v3, v113, v41\n"
    "v_dot2c_f32_bf16 v4, v112, v42\n"
    "v_dot2c_f32_bf16 v5, v113, v42\n"
    "s_add_u32 s68, s46, 48320512\n"
    "s_add_u32 m0, s52, 86016\n"
    "s_nop 0\n"
    "buffer_load_dwordx4 v251, s[64:67], s68 offen sc1 lds\n"
    "v_dot2c_f32_bf16 v6, v112, v43\n"
    "v_dot2c_f32_bf16 v7, v113, v43\n"
    "s_waitcnt vmcnt(35)\n"
    "v_dot2c_f32_bf16 v0, v112, v44\n"
    "v_dot2c_f32_bf16 v1, v113, v44\n"
    "buffer_load_dwordx4 v[136:139], v110, s[64:67], s54 offen offset:2048 sc1 nt\n"
    "v_dot2c_f32_bf16 v2, v112, v45\n"
    "v_dot2c_f32_bf16 v3, v113, v45\n"
    "v_dot2c_f32_bf16 v4, v112, v46\n"
    "v_dot2c_f32_bf16 v5, v113, v46\n"
    "v_dot2c_f32_bf16 v6, v112, v47\n"
    "s_add_u32 s68, s46, 48324608\n"
    "s_add_u32 m0, s52, 90112\n"
    "s_nop 0\n"
    "buffer_load_dwordx4 v251, s[64:67], s68 offen sc1 lds\n"
    "v_dot2c_f32_bf16 v7, v113, v47\n"
    "s_waitcnt vmcnt(34)\n"
    "v_dot2c_f32_bf16 v0, v112, v48\n"
    "v_dot2c_f32_bf16 v1, v113, v48\n"
    "v_dot2c_f32_bf16 v2, v112, v49\n"
    "buffer_load_dwordx4 v[140:143], v110, s[64:67], s54 offen offset:3072 sc1 nt\n"
    "v_dot2c_f32_bf16 v3, v113, v49\n"
    "v_dot2c_f32_bf16 v4, v112, v50\n"
    "v_dot2c_f32_bf16 v5, v113, v50\n"
    "v_dot2c_f32_bf16 v6, v112, v51\n"
    "v_dot2c_f32_bf16 v7, v113, v51\n"
    "s_add_u32 s68, s46, 48328704\n"
    "s_add_u32 m0, s52, 94208\n"
    "s_nop 0\n"
    "buffer_load_dwordx4 v251, s[64:67], s68 offen sc1 lds\n"
    "s_waitcnt vmcnt(34)\n"
    "v_dot2c_f32_bf16 v0, v112, v52\n"
    "v_dot2c_f32_bf16 v1, v113, v52\n"
    "v_dot2c_f32_bf16 v2, v112, v53\n"
    "v_dot2c_f32_bf16 v3, v113, v53\n"
    "s_add_u32 s68, s46, 48332800\n"
    "s_add_u32 m0, s52, 98304\n"
    "s_nop 0\n"
    "buffer_load_dwordx4 v251, s[64:67], s68 offen sc1 lds\n"
    "v_dot2c_f32_bf16 v4, v112, v54\n"
    "v_dot2c_f32_bf16 v5, v113, v54\n"
    "v_dot2c_f32_bf16 v6, v112, v55\n"
    "v_dot2c_f32_bf16 v7, v113, v55\n"
    "s_waitcnt vmcnt(32)\n"
    "s_add_u32 s55, s54, 4096\n"
    "buffer_load_dwordx4 v[144:147], v110, s[64:67], s55 offen offset:0 sc1 nt\n"
    "v_dot2c_f32_bf16 v0, v112, v56\n"
    "v_dot2c_f32_bf16 v1, v113, v56\n"
    "v_dot2c_f32_bf16 v2, v112, v57\n"
    "v_dot2c_f32_bf16 v3, v113, v57\n"
    "v_dot2c_f32_bf16 v4, v112, v58\n"
    "s_add_u32 s68, s46, 48336896\n"
    "s_add_u32 m0, s52, 102400\n"
    "s_nop 0\n"
    "buffer_load_dwordx4 v251, s[64:67], s68 offen sc1 lds\n"
    "v_dot2c_f32_bf16 v5, v113, v58\n"
    "v_dot2c_f32_bf16 v6, v112, v59\n"
    "v_dot2c_f32_bf16 v7, v113, v59\n"
    "s_waitcnt vmcnt(32)\n"
    "v_dot2c_f32_bf16 v0, v112, v60\n"
    "buffer_load_dwordx4 v[148:151], v110, s[64:67], s55 offen offset:1024 sc1 nt\n"
    "v_dot2c_f32_bf16 v1, v113, v60\n"
    "v_dot2c_f32_bf16 v2, v112, v61\n"
    "v_dot2c_f32_bf16 v3, v113, v61\n"
    "v_dot2c_f32_bf16 v4, v112, v62\n"
    "v_dot2c_f32_bf16 v5, v113, v62\n"
    "s_add_u32 s68, s46, 48340992\n"
    "s_add_u32 m0, s52, 106496\n"
    "s_nop 0\n"
    "buffer_load_dwordx4 v251, s[64:67], s68 offen sc1 lds\n"
    "v_dot2c_f32_bf16 v6, v112, v63\n"
    "v_dot2c_f32_bf16 v7, v113, v63\n"
    "v_lshlrev_b32 v114, 16, v64\n"
    "v_and_b32 v115, 0xffff0000, v64\n"
    "v_lshlrev_b32 v116, 16, v65\n"
    "buffer_load_dwordx4 v[152:155], v110, s[64:67], s55 offen offset:2048 sc1 nt\n"
    "v_and_b32 v117, 0xffff0000, v65\n"
    "v_permlane32_swap_b32 v0, v4\n"
    "v_permlane32_swap_b32 v1, v5\n"
    "v_permlane32_swap_b32 v2, v6\n"
    "v_permlane32_swap_b32 v3, v7\n"
    "s_add_u32 s68, s46, 48345088\n"
    "s_add_u32 m0, s52, 110592\n"
    "s_nop 0\n"
    "buffer_load_dwordx4 v251, s[64:67], s68 offen sc1 lds\n"
    "v_pk_add_f32 v[0:1], v[0:1], v[4:5]\n"
    "v_pk_add_f32 v[2:3], v[2:3], v[6:7]\n"
    "v_pk_fma_f32 v[114:115], v[0:1], v[192:193], v[114:115]\n"
    "v_cvt_pk_bf16_f32 v64, v114, v115\n"
    "v_lshlrev_b32 v0, 16, v64\n"
    "buffer_load_dwordx4 v[156:159], v110, s[64:67], s55 offen offset:3072 sc1 nt\n"
    "v_and_b32 v1, 0xffff0000, v64\n"
    "v_pk_mul_f32 v[120:121], v[0:1], v[0:1]\n"
    "v_mov_b64 v[118:119], v[0:1]\n"
    "v_pk_fma_f32 v[116:117], v[2:3], v[194:195], v[116:117]\n"
    "v_cvt_pk_bf16_f32 v65, v116, v117\n"
    "s_add_u32 s68, s47, 48234496\n"
    "s_add_u32 m0, s52, 114688\n"
    "s_nop 0\n"
    "buffer_load_dwordx4 v251, s[64:67], s68 offen sc1 lds\n"
    "v_lshlrev_b32 v2, 16, v65\n"
    "v_and_b32 v3, 0xffff0000, v65\n"
    "v_pk_fma_f32 v[120:121], v[2:3], v[2:3], v[120:121]\n"
    "v_pk_add_f32 v[118:119], v[118:119], v[2:3]\n"
    "s_waitcnt vmcnt(35)\n"
    "s_add_u32 s68, s47, 48238592\n"
    "s_add_u32 m0, s52, 118784\n"
    "s_nop 0\n"
    "buffer_load_dwordx4 v251, s[64:67], s68 offen sc1 lds\n"
    "v_lshlrev_b32 v8, 16, v72\n"
    "v_and_b32 v9, 0xffff0000, v72\n"
    "v_lshlrev_b32 v10, 16, v73\n"
    "v_and_b32 v11, 0xffff0000, v73\n"
    "v_lshlrev_b32 v12, 16, v74\n"
    "s_add_u32 s54, s45, 196608\n"
    "s_cmp_lt_u32 s50, 2\n"
    "s_cselect_b32 s54, s54, 2146435072\n"
    "buffer_load_dwordx4 v[160:163], v110, s[64:67], s54 offen offset:0 sc1 nt\n"
    "v_and_b32 v13, 0xffff0000, v74\n"
    "v_lshlrev_b32 v14, 16, v75\n"
    "v_and_b32 v15, 0xffff0000, v75\n"
    "s_waitcnt vmcnt(35)\n"
    "v_dot2c_f32_bf16 v8, v112, v76\n"
    "s_add_u32 s68, s47, 48242688\n"
    "s_add_u32 m0, s52, 122880\n"
    "s_nop 0\n"
    "buffer_load_dwordx4 v251, s[64:67], s68 offen sc1 lds\n"
    "v_dot2c_f32_bf16 v9, v113, v76\n"
    "v_dot2c_f32_bf16 v10, v112, v77\n"
    "v_dot2c_f32_bf16 v11, v113, v77\n"
    "v_dot2c_f32_bf16 v12, v112, v78\n"
    "v_dot2c_f32_bf16 v13, v113, v78\n"
    "buffer_load_dwordx4 v[164:167], v110, s[64:67], s54 offen offset:1024 sc1 nt\n"
    "v_dot2c_f32_bf16 v14, v112, v79\n"
    "v_dot2c_f32_bf16 v15, v113, v79\n"
    "s_waitcnt vmcnt(34)\n"
    "v_dot2c_f32_bf16 v8, v112, v80\n"
    "v_dot2c_f32_bf16 v9, v113, v80\n"
    "s_add_u32 s68, s47, 48246784\n"
    "s_add_u32 m0, s52, 126976\n"
    "s_nop 0\n"
    "buffer_load_dwordx4 v251, s[64:67], s68 offen sc1 lds\n"
    "v_dot2c_f32_bf16 v10, v112, v81\n"
    "v_dot2c_f32_bf16 v11, v113, v81\n"
    "v_dot2c_f32_bf16 v12, v112, v82\n"
    "v_dot2c_f32_bf16 v13, v113, v82\n"
    "v_dot2c_f32_bf16 v14, v112, v83\n"
    "buffer_load_dwordx4 v[168:171], v110, s[64:67], s54 offen offset:2048 sc1 nt\n"
    "v_dot2c_f32_bf16 v15, v113, v83\n"
    "s_waitcnt vmcnt(34)\n"
    "v_dot2c_f32_bf16 v8, v112, v84\n"
    "v_dot2c_f32_bf16 v9, v113, v84\n"
    "v_dot2c_f32_bf16 v10, v112, v85\n"
    "s_add_u32 s68, s47, 48250880\n"
    "s_add_u32 m0, s52, 131072\n"
    "s_nop 0\n"
    "buffer_load_dwordx4 v251, s[64:67], s68 offen sc1 lds\n"
    "v_dot2c_f32_bf16 v11, v113, v85\n"
    "v_dot2c_f32_bf16 v12, v112, v86\n"
    "v_dot2c_f32_bf16 v13, v113, v86\n"
    "v_dot2c_f32_bf16 v14, v112, v87\n"
    "v_dot2c_f32_bf16 v15, v113, v87\n"
    "buffer_load_dwordx4 v[172:175], v110, s[64:67], s54 offen offset:3072 sc1 nt\n"
    "s_waitcnt vmcnt(33)\n"
    "v_dot2c_f32_bf16 v8, v112, v88\n"
    "v_dot2c_f32_bf16 v9, v113, v88\n"
    "v_dot2c_f32_bf16 v10, v112, v89\n"
    "v_dot2c_f32_bf16 v11, v113, v89\n"
    "s_add_u32 s68, s47, 48254976\n"
    "s_add_u32 m0, s52, 135168\n"
    "s_nop 0\n"
    "buffer_load_dwordx4 v251, s[64:67], s68 offen sc1 lds\n"
    "v_dot2c_f32_bf16 v12, v112, v90\n"
    "v_dot2c_f32_bf16 v13, v113, v90\n"
    "v_dot2c_f32_bf16 v14, v112, v91\n"
    "v_dot2c_f32_bf16 v15, v113, v91\n"
    "s_waitcnt vmcnt(32)\n"
    "s_add_u32 s68, s47, 48259072\n"
    "s_add_u32 m0, s52, 139264\n"
    "s_nop 0\n"
    "buffer_load_dwordx4 v251, s[64:67], s68 offen sc1 lds\n"
    "v_dot2c_f32_bf16 v8, v112, v92\n"
    "v_dot2c_f32_bf16 v9, v113, v92\n"
    "v_dot2c_f32_bf16 v10, v112, v93\n"
    "v_dot2c_f32_bf16 v11, v113, v93\n"
    "v_dot2c_f32_bf16 v12, v112, v94\n"
    "s_add_u32 s55, s54, 4096\n"
    "buffer_load_dwordx4 v[176:179], v110, s[64:67], s55 offen offset:0 sc1 nt\n"
    "v_dot2c_f32_bf16 v13, v113, v94\n"
    "v_dot2c_f32_bf16 v14, v112, v95\n"
    "v_dot2c_f32_bf16 v15, v113, v95\n"
    "s_waitcnt vmcnt(32)\n"
    "v_dot2c_f32_bf16 v8, v112, v96\n"
    "buffer_load_dwordx4 v[180:183], v110, s[64:67], s55 offen offset:1024 sc1 nt\n"
    "v_dot2c_f32_bf16 v9, v113, v96\n"
    "v_dot2c_f32_bf16 v10, v112, v97\n"
    "v_dot2c_f32_bf16 v11, v113, v97\n"
    "v_dot2c_f32_bf16 v12, v112, v98\n"
    "v_dot2c_f32_bf16 v13, v113, v98\n"
    "buffer_load_dwordx4 v[184:187], v110, s[64:67], s55 offen offset:2048 sc1 nt\n"
    "v_dot2c_f32_bf16 v14, v112, v99\n"
    "v_dot2c_f32_bf16 v15, v113, v99\n"
    "s_waitcnt vmcnt(32)\n"
    "v_dot2c_f32_bf16 v8, v112, v100\n"
    "v_dot2c_f32_bf16 v9, v113, v100\n"
    "buffer_load_dwordx4 v[188:191], v110, s[64:67], s55 offen offset:3072 sc1 nt\n"
    "v_dot2c_f32_bf16 v10, v112, v101\n"
    "v_dot2c_f32_bf16 v11, v113, v101\n"
    "v_dot2c_f32_bf16 v12, v112, v102\n"
    "v_dot2c_f32_bf16 v13, v113, v102\n"
    "v_dot2c_f32_bf16 v14, v112, v103\n"
    "v_dot2c_f32_bf16 v15, v113, v103\n"
    "v_lshlrev_b32 v114, 16, v66\n"
    "v_and_b32 v115, 0xffff0000, v66\n"
    "v_lshlrev_b32 v116, 16, v67\n"
    "v_and_b32 v117, 0xffff0000, v67\n"
    "v_permlane32_swap_b32 v8, v12\n"
    "v_permlane32_swap_b32 v9, v13\n"
    "v_permlane32_swap_b32 v10, v14\n"
    "v_permlane32_swap_b32 v11, v15\n"
    "v_pk_add_f32 v[8:9], v[8:9], v[12:13]\n"
    "v_pk_add_f32 v[10:11], v[10:11], v[14:15]\n"
    "v_pk_fma_f32 v[114:115], v[8:9], v[204:205], v[114:115]\n"
    "v_cvt_pk_bf16_f32 v66, v114, v115\n"
    "v_lshlrev_b32 v8, 16, v66\n"
    "v_and_b32 v9, 0xffff0000, v66\n"
    "v_pk_fma_f32 v[120:121], v[8:9], v[8:9], v[120:121]\n"
    "v_pk_add_f32 v[118:119], v[118:119], v[8:9]\n"
    "v_pk_fma_f32 v[116:117], v[10:11], v[206:207], v[116:117]\n"
    "v_cvt_pk_bf16_f32 v67, v116, v117\n"
    "v_lshlrev_b32 v10, 16, v67\n"
    "v_and_b32 v11, 0xffff0000, v67\n"
    "v_pk_fma_f32 v[120:121], v[10:11], v[10:11], v[120:121]\n"
    "v_pk_add_f32 v[118:119], v[118:119], v[10:11]\n"
    "s_nop 0\n"
    "v_permlane32_swap_b32 v64, v66\n"
    "v_permlane32_swap_b32 v65, v67\n"
    "buffer_store_dwordx4 v[64:67], v126, s[64:67], s38 offen sc1\n"
    "s_waitcnt vmcnt(31)\n"
    "v_lshlrev_b32 v16, 16, v128\n"
    "v_and_b32 v17, 0xffff0000, v128\n"
    "v_lshlrev_b32 v18, 16, v129\n"
    "v_and_b32 v19, 0xffff0000, v129\n"
    "v_lshlrev_b32 v20, 16, v130\n"
    "v_and_b32 v21, 0xffff0000, v130\n"
    "v_lshlrev_b32 v22, 16, v131\n"
    "v_and_b32 v23, 0xffff0000, v131\n"
    "s_waitcnt vmcnt(29)\n"
    "v_dot2c_f32_bf16 v16, v112, v132\n"
    "v_dot2c_f32_bf16 v17, v113, v132\n"
    "v_dot2c_f32_bf16 v18, v112, v133\n"
    "v_dot2c_f32_bf16 v19, v113, v133\n"
    "v_dot2c_f32_bf16 v20, v112, v134\n"
    "v_dot2c_f32_bf16 v21, v113, v134\n"
    "v_dot2c_f32_bf16 v22, v112, v135\n"
    "v_dot2c_f32_bf16 v23, v113, v135\n"
    "s_waitcnt vmcnt(27)\n"
    "v_dot2c_f32_bf16 v16, v112, v136\n"
    "v_dot2c_f32_bf16 v17, v113, v136\n"
    "v_dot2c_f32_bf16 v18, v112, v137\n"
    "v_dot2c_f32_bf16 v19, v113, v137\n"
    "v_dot2c_f32_bf16 v20, v112, v138\n"
    "v_dot2c_f32_bf16 v21, v113, v138\n"
    "v_dot2c_f32_bf16 v22, v112, v139\n"
    "v_dot2c_f32_bf16 v23, v113, v139\n"
    "s_waitcnt vmcnt(25)\n"
    "v_dot2c_f32_bf16 v16, v112, v140\n"
    "v_dot2c_f32_bf16 v17, v113, v140\n"
    "v_dot2c_f32_bf16 v18, v112, v141\n"
    "v_dot2c_f32_bf16 v19, v113, v141\n"
    "v_dot2c_f32_bf16 v20, v112, v142\n"
    "v_dot2c_f32_bf16 v21, v113, v142\n"
    "v_dot2c_f32_bf16 v22, v112, v143\n"
    "v_dot2c_f32_bf16 v23, v113, v143\n"
    "s_waitcnt vmcnt(22)\n"
    "v_dot2c_f32_bf16 v16, v112, v144\n"
    "v_dot2c_f32_bf16 v17, v113, v144\n"
    "v_dot2c_f32_bf16 v18, v112, v145\n"
    "v_dot2c_f32_bf16 v19, v113, v145\n"
    "v_dot2c_f32_bf16 v20, v112, v146\n"
    "v_dot2c_f32_bf16 v21, v113, v146\n"
    "v_dot2c_f32_bf16 v22, v112, v147\n"
    "v_dot2c_f32_bf16 v23, v113, v147\n"
    "s_waitcnt vmcnt(20)\n"
    "v_dot2c_f32_bf16 v16, v112, v148\n"
    "v_dot2c_f32_bf16 v17, v113, v148\n"
    "v_dot2c_f32_bf16 v18, v112, v149\n"
    "v_dot2c_f32_bf16 v19, v113, v149\n"
    "v_dot2c_f32_bf16 v20, v112, v150\n"
    "v_dot2c_f32_bf16 v21, v113, v150\n"
    "v_dot2c_f32_bf16 v22, v112, v151\n"
    "v_dot2c_f32_bf16 v23, v113, v151\n"
    "s_waitcnt vmcnt(18)\n"
    "v_dot2c_f32_bf16 v16, v112, v152\n"
    "v_dot2c_f32_bf16 v17, v113, v152\n"
    "v_dot2c_f32_bf16 v18, v112, v153\n"
    "v_dot2c_f32_bf16 v19, v113, v153\n"
    "v_dot2c_f32_bf16 v20, v112, v154\n"
    "v_dot2c_f32_bf16 v21, v113, v154\n"
    "v_dot2c_f32_bf16 v22, v112, v155\n"
    "v_dot2c_f32_bf16 v23, v113, v155\n"
    "s_waitcnt vmcnt(16)\n"
    "v_dot2c_f32_bf16 v16, v112, v156\n"
    "v_dot2c_f32_bf16 v17, v113, v156\n"
    "v_dot2c_f32_bf16 v18, v112, v157\n"
    "v_dot2c_f32_bf16 v19, v113, v157\n"
    "v_dot2c_f32_bf16 v20, v112, v158\n"
    "v_dot2c_f32_bf16 v21, v113, v158\n"
    "v_dot2c_f32_bf16 v22, v112, v159\n"
    "v_dot2c_f32_bf16 v23, v113, v159\n"
    "v_lshlrev_b32 v114, 16, v68\n"
    "v_and_b32 v115, 0xffff0000, v68\n"
    "v_lshlrev_b32 v116, 16, v69\n"
    "v_and_b32 v117, 0xffff0000, v69\n"
    "v_permlane32_swap_b32 v16, v20\n"
    "v_permlane32_swap_b32 v17, v21\n"
    "v_permlane32_swap_b32 v18, v22\n"
    "v_permlane32_swap_b32 v19, v23\n"
    "v_pk_add_f32 v[16:17], v[16:17], v[20:21]\n"
    "v_pk_add_f32 v[18:19], v[18:19], v[22:23]\n"
    "v_pk_fma_f32 v[114:115], v[16:17], v[216:217], v[114:115]\n"
    "v_cvt_pk_bf16_f32 v68, v114, v115\n"
    "v_lshlrev_b32 v16, 16, v68\n"
    "v_and_b32 v17, 0xffff0000, v68\n"
    "v_pk_fma_f32 v[120:121], v[16:17], v[16:17], v[120:121]\n"
    "v_pk_add_f32 v[118:119], v[118:119], v[16:17]\n"
    "v_pk_fma_f32 v[116:117], v[18:19], v[218:219], v[116:117]\n"
    "v_cvt_pk_bf16_f32 v69, v116, v117\n"
    "v_lshlrev_b32 v18, 16, v69\n"
    "v_and_b32 v19, 0xffff0000, v69\n"
    "v_pk_fma_f32 v[120:121], v[18:19], v[18:19], v[120:121]\n"
    "v_pk_add_f32 v[118:119], v[118:119], v[18:19]\n"
    "s_waitcnt vmcnt(13)\n"
    "v_lshlrev_b32 v24, 16, v160\n"
    "v_and_b32 v25, 0xffff0000, v160\n"
    "v_lshlrev_b32 v26, 16, v161\n"
    "v_and_b32 v27, 0xffff0000, v161\n"
    "v_lshlrev_b32 v28, 16, v162\n"
    "v_and_b32 v29, 0xffff0000, v162\n"
    "v_lshlrev_b32 v30, 16, v163\n"
    "v_and_b32 v31, 0xffff0000, v163\n"
    "s_waitcnt vmcnt(11)\n"
    "v_dot2c_f32_bf16 v24, v112, v164\n"
    "v_dot2c_f32_bf16 v25, v113, v164\n"
    "v_dot2c_f32_bf16 v26, v112, v165\n"
    "v_dot2c_f32_bf16 v27, v113, v165\n"
    "v_dot2c_f32_bf16 v28, v112, v166\n"
    "v_dot2c_f32_bf16 v29, v113, v166\n"
    "v_dot2c_f32_bf16 v30, v112, v167\n"
    "v_dot2c_f32_bf16 v31, v113, v167\n"
    "s_waitcnt vmcnt(9)\n"
    "v_dot2c_f32_bf16 v24, v112, v168\n"
    "v_dot2c_f32_bf16 v25, v113, v168\n"
    "v_dot2c_f32_bf16 v26, v112, v169\n"
    "v_dot2c_f32_bf16 v27, v113, v169\n"
    "v_dot2c_f32_bf16 v28, v112, v170\n"
    "v_dot2c_f32_bf16 v29, v113, v170\n"
    "v_dot2c_f32_bf16 v30, v112, v171\n"
    "v_dot2c_f32_bf16 v31, v113, v171\n"
    "s_waitcnt vmcnt(7)\n"
    "v_dot2c_f32_bf16 v24, v112, v172\n"
    "v_dot2c_f32_bf16 v25, v113, v172\n"
    "v_dot2c_f32_bf16 v26, v112, v173\n"
    "v_dot2c_f32_bf16 v27, v113, v173\n"
    "v_dot2c_f32_bf16 v28, v112, v174\n"
    "v_dot2c_f32_bf16 v29, v113, v174\n"
    "v_dot2c_f32_bf16 v30, v112, v175\n"
    "v_dot2c_f32_bf16 v31, v113, v175\n"
    "s_waitcnt vmcnt(4)\n"
    "v_dot2c_f32_bf16 v24, v112, v176\n"
    "v_dot2c_f32_bf16 v25, v113, v176\n"
    "v_dot2c_f32_bf16 v26, v112, v177\n"
    "v_dot2c_f32_bf16 v27, v113, v177\n"
    "v_dot2c_f32_bf16 v28, v112, v178\n"
    "v_dot2c_f32_bf16 v29, v113, v178\n"
    "v_dot2c_f32_bf16 v30, v112, v179\n"
    "v_dot2c_f32_bf16 v31, v113, v179\n"
    "s_waitcnt vmcnt(3)\n"
    "v_dot2c_f32_bf16 v24, v112, v180\n"
    "v_dot2c_f32_bf16 v25, v113, v180\n"
    "v_dot2c_f32_bf16 v26, v112, v181\n"
    "v_dot2c_f32_bf16 v27, v113, v181\n"
    "v_dot2c_f32_bf16 v28, v112, v182\n"
    "v_dot2c_f32_bf16 v29, v113, v182\n"
    "v_dot2c_f32_bf16 v30, v112, v183\n"
    "v_dot2c_f32_bf16 v31, v113, v183\n"
    "s_waitcnt vmcnt(2)\n"
    "v_dot2c_f32_bf16 v24, v112, v184\n"
    "v_dot2c_f32_bf16 v25, v113, v184\n"
    "v_dot2c_f32_bf16 v26, v112, v185\n"
    "v_dot2c_f32_bf16 v27, v113, v185\n"
    "v_dot2c_f32_bf16 v28, v112, v186\n"
    "v_dot2c_f32_bf16 v29, v113, v186\n"
    "v_dot2c_f32_bf16 v30, v112, v187\n"
    "v_dot2c_f32_bf16 v31, v113, v187\n"
    "s_waitcnt vmcnt(1)\n"
    "v_dot2c_f32_bf16 v24, v112, v188\n"
    "v_dot2c_f32_bf16 v25, v113, v188\n"
    "v_dot2c_f32_bf16 v26, v112, v189\n"
    "v_dot2c_f32_bf16 v27, v113, v189\n"
    "v_dot2c_f32_bf16 v28, v112, v190\n"
    "v_dot2c_f32_bf16 v29, v113, v190\n"
    "v_dot2c_f32_bf16 v30, v112, v191\n"
    "v_dot2c_f32_bf16 v31, v113, v191\n"
    "v_lshlrev_b32 v114, 16, v70\n"
    "v_and_b32 v115, 0xffff0000, v70\n"
    "v_lshlrev_b32 v116, 16, v71\n"
    "v_and_b32 v117, 0xffff0000, v71\n"
    "v_permlane32_swap_b32 v24, v28\n"
    "v_permlane32_swap_b32 v25, v29\n"
    "v_permlane32_swap_b32 v26, v30\n"
    "v_permlane32_swap_b32 v27, v31\n"
    "v_pk_add_f32 v[24:25], v[24:25], v[28:29]\n"
    "v_pk_add_f32 v[26:27], v[26:27], v[30:31]\n"
    "v_pk_fma_f32 v[114:115], v[24:25], v[228:229], v[114:115]\n"
    "v_cvt_pk_bf16_f32 v70, v114, v115\n"
    "v_lshlrev_b32 v24, 16, v70\n"
    "v_and_b32 v25, 0xffff0000, v70\n"
    "v_pk_fma_f32 v[120:121], v[24:25], v[24:25], v[120:121]\n"
    "v_pk_add_f32 v[118:119], v[118:119], v[24:25]\n"
    "v_pk_fma_f32 v[116:117], v[26:27], v[230:231], v[116:117]\n"
    "v_cvt_pk_bf16_f32 v71, v116, v117\n"
    "v_lshlrev_b32 v26, 16, v71\n"
    "v_and_b32 v27, 0xffff0000, v71\n"
    "v_pk_fma_f32 v[120:121], v[26:27], v[26:27], v[120:121]\n"
    "v_pk_add_f32 v[118:119], v[118:119], v[26:27]\n"
    "v_add_f32 v122, v118, v119\n"
    "v_add_f32 v123, v120, v121\n"
    "s_nop 1\n"
    "v_permlane32_swap_b32 v122, v123\n"
    "v_add_f32 v122, v122, v123\n"
    "v_mov_b32 v123, v122\n"
    "s_nop 1\n"
    "v_permlane16_swap_b32 v122, v123\n"
    "v_add_f32 v122, v122, v123\n"
    "s_nop 1\n"
    "v_add_f32_dpp v122, v122, v122 row_ror:8 row_mask:0xf bank_mask:0xf\n"
    "ds_write_b32 v124, v122\n"
    "s_nop 0\n"
    "v_permlane32_swap_b32 v68, v70\n"
    "v_permlane32_swap_b32 v69, v71\n"
    "buffer_store_dwordx4 v[68:71], v127, s[64:67], s38 offen sc1\n"
    "s_waitcnt lgkmcnt(0)\n"
    "s_barrier\n"
    "ds_read_b128 v[114:117], v125\n"
    "ds_read_b128 v[118:121], v125 offset:16\n"
    "s_waitcnt lgkmcnt(1)\n"
    "v_pk_add_f32 v[114:115], v[114:115], v[116:117]\n"
    "v_add_f32 v114, v114, v115\n"
    "v_mul_f32 v114, 0x3b124925, v114\n"
    "s_waitcnt lgkmcnt(0)\n"
    "v_pk_add_f32 v[118:119], v[118:119], v[120:121]\n"
    "v_add_f32 v118, v118, v119\n"
    "v_mul_f32 v118, 0x3b124925, v118\n"
    "v_fma_f32 v118, -v114, v114, v118\n"
    "v_max_f32 v118, 0, v118\n"
    "v_add_f32 v118, 0x3727c5ac, v118\n"
    "v_rsq_f32 v118, v118\n"
    "s_nop 0\n"
    "v_mul_f32 v114, v114, v118\n"
    "v_pk_fma_f32 v[0:1], v[0:1], v[118:119], v[114:115] op_sel_hi:[1,0,0] neg_lo:[0,0,1] neg_hi:[0,0,1]\n"
    "v_pk_fma_f32 v[0:1], v[0:1], v[200:201], v[196:197]\n"
    "v_cvt_pk_bf16_f32 v32, v0, v1\n"
    "v_pk_fma_f32 v[2:3], v[2:3], v[118:119], v[114:115] op_sel_hi:[1,0,0] neg_lo:[0,0,1] neg_hi:[0,0,1]\n"
    "v_pk_fma_f32 v[2:3], v[2:3], v[202:203], v[198:199]\n"
    "v_cvt_pk_bf16_f32 v33, v2, v3\n"
    "v_pk_fma_f32 v[8:9], v[8:9], v[118:119], v[114:115] op_sel_hi:[1,0,0] neg_lo:[0,0,1] neg_hi:[0,0,1]\n"
    "v_pk_fma_f32 v[8:9], v[8:9], v[212:213], v[208:209]\n"
    "v_cvt_pk_bf16_f32 v34, v8, v9\n"
    "v_pk_fma_f32 v[10:11], v[10:11], v[118:119], v[114:115] op_sel_hi:[1,0,0] neg_lo:[0,0,1] neg_hi:[0,0,1]\n"
    "v_pk_fma_f32 v[10:11], v[10:11], v[214:215], v[210:211]\n"
    "v_cvt_pk_bf16_f32 v35, v10, v11\n"
    "s_nop 1\n"
    "v_permlane32_swap_b32 v32, v34\n"
    "v_permlane32_swap_b32 v33, v35\n"
    "buffer_store_dwordx4 v[32:35], v126, s[64:67], s37 offen sc1\n"
    "v_pk_fma_f32 v[16:17], v[16:17], v[118:119], v[114:115] op_sel_hi:[1,0,0] neg_lo:[0,0,1] neg_hi:[0,0,1]\n"
    "v_pk_fma_f32 v[16:17], v[16:17], v[224:225], v[220:221]\n"
    "v_cvt_pk_bf16_f32 v36, v16, v17\n"
    "v_pk_fma_f32 v[18:19], v[18:19], v[118:119], v[114:115] op_sel_hi:[1,0,0] neg_lo:[0,0,1] neg_hi:[0,0,1]\n"
    "v_pk_fma_f32 v[18:19], v[18:19], v[226:227], v[222:223]\n"
    "v_cvt_pk_bf16_f32 v37, v18, v19\n"
    "v_pk_fma_f32 v[24:25], v[24:25], v[118:119], v[114:115] op_sel_hi:[1,0,0] neg_lo:[0,0,1] neg_hi:[0,0,1]\n"
    "v_pk_fma_f32 v[24:25], v[24:25], v[244:245], v[240:241]\n"
    "v_cvt_pk_bf16_f32 v38, v24, v25\n"
    "v_pk_fma_f32 v[26:27], v[26:27], v[118:119], v[114:115] op_sel_hi:[1,0,0] neg_lo:[0,0,1] neg_hi:[0,0,1]\n"
    "v_pk_fma_f32 v[26:27], v[26:27], v[246:247], v[242:243]\n"
    "v_cvt_pk_bf16_f32 v39, v26, v27\n"
    "s_nop 1\n"
    "v_permlane32_swap_b32 v36, v38\n"
    "v_permlane32_swap_b32 v37, v39\n"
    "buffer_store_dwordx4 v[36:39], v127, s[64:67], s37 offen sc1\n"
    "s_add_u32 s68, s46, 48349184\n"
    "s_add_u32 m0, s52, 0\n"
    "s_nop 0\n"
    "buffer_load_dwordx4 v251, s[64:67], s68 offen sc1 lds\n"
    "s_add_u32 s68, s46, 48353280\n"
    "s_add_u32 m0, s52, 4096\n"
    "s_nop 0\n"
    "buffer_load_dwordx4 v251, s[64:67], s68 offen sc1 lds\n"
    "s_add_u32 s68, s46, 48357376\n"
    "s_add_u32 m0, s52, 8192\n"
    "s_nop 0\n"
    "buffer_load_dwordx4 v251, s[64:67], s68 offen sc1 lds\n"
    "s_add_u32 s68, s46, 48361472\n"
    "s_add_u32 m0, s52, 12288\n"
    "s_nop 0\n"
    "buffer_load_dwordx4 v251, s[64:67], s68 offen sc1 lds\n"
    "s_bfe_u32 s56, s70, 0x50002\n"
    "s_lshl_b32 s56, s56, 3\n"
    "s_and_b32 s57, s70, 3\n"
    "s_add_u32 s56, s56, s57\n"
    "s_lshl_b32 s56, s56, 2\n"
    "s_add_u32 s56, s56, s50\n"
    "s_lshl_b32 s56, s56, 2\n"
    "s_add_u32 s56, s56, 524288\n"
    "s_and_b32 s57, s71, 7\n"
    "s_lshl_b32 s57, s57, 2\n"
    "s_add_u32 s57, s57, s50\n"
    "s_lshl_b32 s57, s57, 7\n"
    "s_add_u32 s57, s57, 524288\n"
    "s_bfe_u32 s54, s42, 65536\n"
    "s_lshl_b32 s54, s54, 13\n"
    "s_add_u32 s56, s56, s54\n"
    "s_add_u32 s57, s57, s54\n"
    "v_min_u32 v170, 1, v250\n"
    "v_lshlrev_b32 v170, 30, v170\n"
    "v_lshrrev_b32 v171, 4, v250\n"
    "v_lshlrev_b32 v171, 27, v171\n"
    "v_lshl_add_u32 v171, v250, 2, v171\n"
    "v_mov_b32 v168, s53\n"
    "v_mov_b32 v172, 0\n"
    "s_and_b32 s54, s71, 7\n"
    "s_lshl_b32 s54, s54, 2\n"
    "s_add_u32 s54, s54, s50\n"
    "s_lshl_b32 s54, s54, 4\n"
    "s_lshr_b32 s55, s96, 1\n"
    "s_add_u32 s54, s54, s55\n"
    "s_lshl_b32 s54, s54, 14\n"
    "s_lshr_b32 s55, s54, 21\n"
    "s_add_u32 s55, s55, 3\n"
    "s_lshr_b32 s55, s42, s55\n"
    "s_and_b32 s55, s55, 1\n"
    "s_lshl_b32 s55, s55, 13\n"
    "s_add_u32 s54, s54, s55\n"
    "s_and_b32 s55, s96, 1\n"
    "s_lshl_b32 s44, s55, 12\n"
    "s_add_u32 s44, s44, s54\n"
    "v_add_u32 v253, s44, v235\n"
    "s_lshl_b32 s44, s55, 9\n"
    "s_add_u32 s44, s44, s54\n"
    "s_add_u32 s44, s44, 3072\n"
    "v_lshlrev_b32 v114, 3, v250\n"
    "v_add_u32 v254, s44, v114\n"
    "s_and_b32 s44, s71, 7\n"
    "s_lshl_b32 s44, s44, 2\n"
    "s_add_u32 s44, s44, s50\n"
    "s_lshl_b32 s44, s44, 16\n"
    "s_add_u32 s44, s44, s37\n"
    "s_and_b32 s54, s96, 1\n"
    "s_xor_b32 s55, s54, 1\n"
    "s_lshl_b32 s49, s54, 17\n"
    "s_add_u32 s49, s49, 6291456\n"
    "s_lshl_b32 s51, s55, 17\n"
    "s_add_u32 s51, s51, 6291456\n"
    "s_lshl_b32 s54, s54, 15\n"
    "s_lshl_b32 s55, s55, 15\n"
    "s_add_u32 s45, s44, s55\n"
    "s_add_u32 s44, s44, s54\n"
    "s_waitcnt vmcnt(4)\n"
    "buffer_atomic_swap v168, v170, s[64:67], s56 offen sc1\n"
    "s_add_u32 s68, s46, 48365568\n"
    "s_add_u32 m0, s52, 16384\n"
    "s_nop 0\n"
    "buffer_load_dwordx4 v251, s[64:67], s68 offen sc1 lds\n"
    "s_add_u32 s68, s46, 48369664\n"
    "s_add_u32 m0, s52, 20480\n"
    "s_nop 0\n"
    "buffer_load_dwordx4 v251, s[64:67], s68 offen sc1 lds\n"
    "s_add_u32 s68, s46, 48373760\n"
    "s_add_u32 m0, s52, 24576\n"
    "s_nop 0\n"
    "buffer_load_dwordx4 v251, s[64:67], s68 offen sc1 lds\n"
    "buffer_atomic_add v172, v171, s[64:67], s57 offen sc0 sc1\n"
    "s_waitcnt vmcnt(0)\n"
    "v_subrev_u32 v172, s53, v172\n"
    "v_cmp_lt_u32 vcc, 2, v172\n"
    "v_mov_b32 v172, 0\n"
    "s_and_b32 vcc_lo, vcc_lo, 65535\n"
    "s_cbranch_scc0 L_fd14_%=\n"
    "s_nop 0\n"
    "L_fw13_%=:\n"
    "s_sub_u32 s99, s99, 1\n"
    "s_cbranch_scc1 L_fd14_%=\n"
    "buffer_atomic_add v172, v171, s[64:67], s57 offen sc0 sc1\n"
    "s_waitcnt vmcnt(0)\n"
    "v_subrev_u32 v172, s53, v172\n"
    "v_cmp_lt_u32 vcc, 2, v172\n"
    "v_mov_b32 v172, 0\n"
    "s_and_b32 vcc_lo, vcc_lo, 65535\n"
    "s_cbranch_scc1 L_fw13_%=\n"
    "L_fd14_%=:\n"
    "s_max_i32 s99, s99, 0\n"
    "buffer_load_dwordx4 v[0:3], v235, s[64:67], s44 offen offset:0 sc1\n"
    "buffer_load_dwordx4 v[4:7], v235, s[64:67], s44 offen offset:1024 sc1\n"
    "buffer_load_dwordx4 v[8:11], v235, s[64:67], s44 offen offset:2048 sc1\n"
    "buffer_load_dwordx4 v[12:15], v235, s[64:67], s44 offen offset:3072 sc1\n"
    "s_add_u32 s44, s44, 4096\n"
    "buffer_load_dwordx4 v[16:19], v235, s[64:67], s44 offen offset:0 sc1\n"
    "buffer_load_dwordx4 v[20:23], v235, s[64:67], s44 offen offset:1024 sc1\n"
    "buffer_load_dwordx4 v[24:27], v235, s[64:67], s44 offen offset:2048 sc1\n"
    "buffer_load_dwordx4 v[28:31], v235, s[64:67], s44 offen offset:3072 sc1\n"
    "s_add_u32 s44, s44, 12288\n"
    "buffer_load_dwordx4 v[32:35], v235, s[64:67], s44 offen offset:0 sc1\n"
    "buffer_load_dwordx4 v[36:39], v235, s[64:67], s44 offen offset:1024 sc1\n"
    "buffer_load_dwordx4 v[40:43], v235, s[64:67], s44 offen offset:2048 sc1\n"
    "buffer_load_dwordx4 v[44:47], v235, s[64:67], s44 offen offset:3072 sc1\n"
    "s_add_u32 s44, s44, 4096\n"
    "buffer_load_dwordx4 v[48:51], v235, s[64:67], s44 offen offset:0 sc1\n"
    "buffer_load_dwordx4 v[52:55], v235, s[64:67], s44 offen offset:1024 sc1\n"
    "buffer_load_dwordx4 v[56:59], v235, s[64:67], s45 offen offset:0 sc1\n"
    "buffer_load_dwordx4 v[60:63], v235, s[64:67], s45 offen offset:1024 sc1\n"
    "s_mov_b32 s58, 0xbdd2d3e8\n"
    "v_mov_b32 v160, 0xc0135761\n"
    "s_and_b32 s36, s97, 15\n"
    "s_lshl_b32 s36, s36, 4\n"
    "s_add_u32 s36, s36, s50\n"
    "s_lshl_b32 s36, s36, 15\n"
    "s_add_u32 s36, s36, 6291456\n"
    "s_lshr_b32 s54, s36, 21\n"
    "s_lshr_b32 s54, s42, s54\n"
    "s_and_b32 s54, s54, 1\n"
    "s_lshl_b32 s54, s54, 13\n"
    "s_add_u32 s36, s36, s54\n"
    "s_and_b32 s54, s98, 1\n"
    "s_lshl_b32 s54, s54, 14\n"
    "s_add_u32 s36, s36, s54\n"
    "s_xor_b32 s37, s36, 0x4000\n"
    "v_lshlrev_b32 v244, 4, v250\n"
    "v_lshlrev_b32 v245, 3, v250\n"
    "s_add_u32 s54, s50, 0\n"
    "s_and_b32 s54, s54, 3\n"
    "s_lshl_b32 s55, s54, 17\n"
    "v_add_u32 v224, s55, v244\n"
    "v_add_u32 v228, s55, v245\n"
    "s_mul_i32 s55, s54, 12288\n"
    "v_add_u32 v238, s55, v244\n"
    "s_add_u32 s54, s50, 1\n"
    "s_and_b32 s54, s54, 3\n"
    "s_lshl_b32 s55, s54, 17\n"
    "v_add_u32 v225, s55, v244\n"
    "v_add_u32 v229, s55, v245\n"
    "s_mul_i32 s55, s54, 12288\n"
    "s_add_u32 s55, s55, 0\n"
    "v_add_u32 v232, s55, v244\n"
    "s_add_u32 s54, s50, 2\n"
    "s_and_b32 s54, s54, 3\n"
    "s_lshl_b32 s55, s54, 17\n"
    "v_add_u32 v226, s55, v244\n"
    "v_add_u32 v230, s55, v245\n"
    "s_mul_i32 s55, s54, 12288\n"
    "s_add_u32 s55, s55, 4096\n"
    "v_add_u32 v233, s55, v244\n"
    "s_add_u32 s54, s50, 3\n"
    "s_and_b32 s54, s54, 3\n"
    "s_lshl_b32 s55, s54, 17\n"
    "v_add_u32 v227, s55, v244\n"
    "v_add_u32 v231, s55, v245\n"
    "s_mul_i32 s55, s54, 12288\n"
    "s_add_u32 s55, s55, 8192\n"
    "v_add_u32 v234, s55, v244\n"
    "v_and_b32 v240, 15, v250\n"
    "v_mul_u32_u24 v240, 144, v240\n"
    "v_lshrrev_b32 v242, 4, v250\n"
    "v_lshl_add_u32 v240, v242, 3, v240\n"
    "s_mul_i32 s54, s50, 2304\n"
    "s_add_u32 s54, s54, 0x20000\n"
    "v_add_u32 v240, s54, v240\n"
    "v_lshrrev_b32 v242, 3, v250\n"
    "v_mul_u32_u24 v241, 144, v242\n"
    "v_and_b32 v245, 7, v250\n"
    "v_lshlrev_b32 v245, 4, v245\n"
    "v_add3_u32 v241, v241, v245, s54\n"
    "s_lshl_b32 s54, s97, 6\n"
    "s_lshl_b32 s55, s50, 4\n"
    "s_add_u32 s54, s54, s55\n"
    "v_add_u32 v242, s54, v242\n"
    "v_mul_u32_u24 v242, 896, v242\n"
    "s_lshl_b32 s54, s98, 7\n"
    "v_add3_u32 v242, v242, v245, s54\n"
    "v_add_u32 v243, 7168, v242\n"
    "s_and_b32 s54, s97, 15\n"
    "s_lshl_b32 s54, s54, 2\n"
    "s_add_u32 s54, s54, s50\n"
    "s_lshl_b32 s54, s54, 1\n"
    "s_lshr_b32 s55, s98, 2\n"
    "s_add_u32 s54, s54, s55\n"
    "s_lshl_b32 s54, s54, 3\n"
    "s_and_b32 s55, s98, 3\n"
    "s_add_u32 s54, s54, s55\n"
    "s_lshl_b32 s54, s54, 11\n"
    "s_add_u32 s38, s38, s54\n"
    "s_and_b32 s39, s71, 7\n"
    "s_lshl_b32 s39, s39, 1\n"
    "s_lshr_b32 s54, s50, 1\n"
    "s_add_u32 s39, s39, s54\n"
    "s_lshl_b32 s39, s39, 2\n"
    "s_lshr_b32 s54, s96, 2\n"
    "s_add_u32 s39, s39, s54\n"
    "s_lshl_b32 s39, s39, 5\n"
    "s_and_b32 s54, s50, 1\n"
    "s_lshl_b32 s54, s54, 2\n"
    "s_add_u32 s39, s39, s54\n"
    "s_and_b32 s54, s96, 3\n"
    "s_add_u32 s39, s39, s54\n"
    "s_lshl_b32 s39, s39, 2\n"
    "s_add_u32 s39, s39, 786432\n"
    "s_and_b32 s40, s97, 15\n"
    "s_lshl_b32 s40, s40, 2\n"
    "s_add_u32 s40, s40, s50\n"
    "s_lshl_b32 s40, s40, 7\n"
    "s_add_u32 s40, s40, 786432\n"
    "s_bfe_u32 s54, s42, 65536\n"
    "s_lshl_b32 s54, s54, 13\n"
    "s_add_u32 s39, s39, s54\n"
    "s_add_u32 s40, s40, s54\n"
    "v_min_u32 v194, 1, v250\n"
    "v_lshlrev_b32 v194, 30, v194\n"
    "v_lshrrev_b32 v195, 3, v250\n"
    "v_lshlrev_b32 v195, 27, v195\n"
    "v_lshl_add_u32 v195, v250, 2, v195\n"
    "v_mov_b32 v192, s53\n"
    "v_mov_b32 v196, 0\n"
    "s_lshl_b32 s41, s50, 12\n"
    "s_add_u32 s54, s41, 57344\n"
    "v_add_u32 v246, s54, v244\n"
    "v_add_u32 v247, 86016, v246\n"
    "v_lshrrev_b32 v249, 4, v250\n"
    "v_lshlrev_b32 v249, 4, v249\n"
    "s_waitcnt vmcnt(15)\n"
    "v_mfma_f32_16x16x32_bf16 v[112:115], a[144:147], v[0:3], 0\n"
    "s_waitcnt vmcnt(13)\n"
    "v_mfma_f32_16x16x32_bf16 v[112:115], a[148:151], v[4:7], v[112:115]\n"
    "buffer_load_dwordx4 v[64:67], v235, s[64:67], s45 offen offset:2048 sc1\n"
    "v_mfma_f32_16x16x32_bf16 v[112:115], a[152:155], v[8:11], v[112:115]\n"
    "s_waitcnt vmcnt(12)\n"
    "v_mfma_f32_16x16x32_bf16 v[112:115], a[156:159], v[12:15], v[112:115]\n"
    "buffer_load_dwordx4 v[68:71], v235, s[64:67], s45 offen offset:3072 sc1\n"
    "v_mfma_f32_16x16x32_bf16 v[112:115], a[160:163], v[16:19], v[112:115]\n"
    "s_waitcnt vmcnt(11)\n"
    "s_add_u32 s45, s45, 4096\n"
    "v_mfma_f32_16x16x32_bf16 v[112:115], a[164:167], v[20:23], v[112:115]\n"
    "buffer_load_dwordx4 v[72:75], v235, s[64:67], s45 offen offset:0 sc1\n"
    "v_mfma_f32_16x16x32_bf16 v[112:115], a[168:171], v[24:27], v[112:115]\n"
    "s_waitcnt vmcnt(10)\n"
    "v_mfma_f32_16x16x32_bf16 v[112:115], a[172:175], v[28:31], v[112:115]\n"
    "buffer_load_dwordx4 v[76:79], v235, s[64:67], s45 offen offset:1024 sc1\n"
    "v_mfma_f32_16x16x32_bf16 v[112:115], a[176:179], v[32:35], v[112:115]\n"
    "s_waitcnt vmcnt(9)\n"
    "v_mfma_f32_16x16x32_bf16 v[112:115], a[180:183], v[36:39], v[112:115]\n"
    "buffer_load_dwordx4 v[80:83], v235, s[64:67], s45 offen offset:2048 sc1\n"
    "v_mfma_f32_16x16x32_bf16 v[112:115], a[184:187], v[40:43], v[112:115]\n"
    "s_waitcnt vmcnt(8)\n"
    "v_mfma_f32_16x16x32_bf16 v[112:115], a[188:191], v[44:47], v[112:115]\n"
    "buffer_load_dwordx4 v[84:87], v235, s[64:67], s45 offen offset:3072 sc1\n"
    "v_mfma_f32_16x16x32_bf16 v[112:115], a[192:195], v[48:51], v[112:115]\n"
    "s_waitcnt vmcnt(8)\n"
    "s_add_u32 s45, s45, 12288\n"
    "v_mfma_f32_16x16x32_bf16 v[112:115], a[196:199], v[52:55], v[112:115]\n"
    "buffer_load_dwordx4 v[88:91], v235, s[64:67], s45 offen offset:0 sc1\n"
    "v_mfma_f32_16x16x32_bf16 v[120:123], a[200:203], v[0:3], 0\n"
    "v_mfma_f32_16x16x32_bf16 v[120:123], a[204:207], v[4:7], v[120:123]\n"
    "buffer_load_dwordx4 v[92:95], v235, s[64:67], s45 offen offset:1024 sc1\n"
    "v_mfma_f32_16x16x32_bf16 v[120:123], a[208:211], v[8:11], v[120:123]\n"
    "v_mfma_f32_16x16x32_bf16 v[120:123], a[212:215], v[12:15], v[120:123]\n"
    "buffer_load_dwordx4 v[96:99], v235, s[64:67], s45 offen offset:2048 sc1\n"
    "v_mfma_f32_16x16x32_bf16 v[120:123], a[216:219], v[16:19], v[120:123]\n"
    "v_mul_f32 v144, v112, v112\n"
    "v_mul_f32 v145, v113, v113\n"
    "v_mfma_f32_16x16x32_bf16 v[120:123], a[220:223], v[20:23], v[120:123]\n"
    "buffer_load_dwordx4 v[100:103], v235, s[64:67], s45 offen offset:3072 sc1\n"
    "v_mfma_f32_16x16x32_bf16 v[120:123], a[224:227], v[24:27], v[120:123]\n"
    "s_add_u32 s45, s45, 4096\n"
    "v_mul_f32 v146, v114, v114\n"
    "v_mul_f32 v147, v115, v115\n"
    "v_mfma_f32_16x16x32_bf16 v[120:123], a[228:231], v[28:31], v[120:123]\n"
    "buffer_load_dwordx4 v[104:107], v235, s[64:67], s45 offen offset:0 sc1\n"
    "v_mfma_f32_16x16x32_bf16 v[120:123], a[232:235], v[32:35], v[120:123]\n"
    "v_fma_f32 v144, v144, s58, v160\n"
    "v_fma_f32 v145, v145, s58, v160\n"
    "v_mfma_f32_16x16x32_bf16 v[120:123], a[236:239], v[36:39], v[120:123]\n"
    "buffer_load_dwordx4 v[108:111], v235, s[64:67], s45 offen offset:1024 sc1\n"
    "v_mfma_f32_16x16x32_bf16 v[120:123], a[240:243], v[40:43], v[120:123]\n"
    "s_add_u32 s59, s69, 0\n"
    "v_fma_f32 v146, v146, s58, v160\n"
    "v_fma_f32 v147, v147, s58, v160\n"
    "v_mfma_f32_16x16x32_bf16 v[120:123], a[244:247], v[44:47], v[120:123]\n"
    "buffer_load_dwordx4 a[0:3], v255, s[64:67], s59 offen offset:0 sc1\n"
    "v_mfma_f32_16x16x32_bf16 v[120:123], a[248:251], v[48:51], v[120:123]\n"
    "s_waitcnt vmcnt(14)\n"
    "v_mul_f32 v144, v144, v112\n"
    "v_mul_f32 v145, v145, v113\n"
    "v_mfma_f32_16x16x32_bf16 v[120:123], a[252:255], v[52:55], v[120:123]\n"
    "buffer_load_dwordx4 a[4:7], v255, s[64:67], s59 offen offset:1024 sc1\n"
    "v_mfma_f32_16x16x32_bf16 v[116:119], a[144:147], v[56:59], 0\n"
    "s_waitcnt vmcnt(13)\n"
    "ds_read_b128 a[144:147], v235 offset:28672\n"
    "v_mul_f32 v146, v146, v114\n"
    "v_mfma_f32_16x16x32_bf16 v[116:119], a[148:151], v[60:63], v[116:119]\n"
    "buffer_load_dwordx4 a[8:11], v255, s[64:67], s59 offen offset:2048 sc1\n"
    "v_mfma_f32_16x16x32_bf16 v[116:119], a[152:155], v[64:67], v[116:119]\n"
    "s_waitcnt vmcnt(12)\n"
    "ds_read_b128 a[148:151], v235 offset:29696\n"
    "ds_read_b128 a[152:155], v235 offset:30720\n"
    "v_mfma_f32_16x16x32_bf16 v[116:119], a[156:159], v[68:71], v[116:119]\n"
    "buffer_load_dwordx4 a[12:15], v255, s[64:67], s59 offen offset:3072 sc1\n"
    "v_mfma_f32_16x16x32_bf16 v[116:119], a[160:163], v[72:75], v[116:119]\n"
    "s_waitcnt vmcnt(11)\n"
    "s_add_u32 s59, s69, 4096\n"
    "ds_read_b128 a[156:159], v235 offset:31744\n"
    "v_mul_f32 v147, v147, v115\n"
    "v_mfma_f32_16x16x32_bf16 v[116:119], a[164:167], v[76:79], v[116:119]\n"
    "buffer_load_dwordx4 a[16:19], v255, s[64:67], s59 offen offset:0 sc1\n"
    "v_mfma_f32_16x16x32_bf16 v[116:119], a[168:171], v[80:83], v[116:119]\n"
    "s_waitcnt vmcnt(10)\n"
    "v_exp_f32 v144, v144\n"
    "v_mfma_f32_16x16x32_bf16 v[116:119], a[172:175], v[84:87], v[116:119]\n"
    "buffer_load_dwordx4 a[20:23], v255, s[64:67], s59 offen offset:1024 sc1\n"
    "v_mfma_f32_16x16x32_bf16 v[116:119], a[176:179], v[88:91], v[116:119]\n"
    "s_waitcnt vmcnt(9)\n"
    "ds_read_b128 a[160:163], v235 offset:32768\n"
    "ds_read_b128 a[164:167], v235 offset:33792\n"
    "v_mfma_f32_16x16x32_bf16 v[116:119], a[180:183], v[92:95], v[116:119]\n"
    "buffer_load_dwordx4 a[24:27], v255, s[64:67], s59 offen offset:2048 sc1\n"
    "v_mfma_f32_16x16x32_bf16 v[116:119], a[184:187], v[96:99], v[116:119]\n"
    "s_waitcnt vmcnt(8)\n"
    "v_exp_f32 v145, v145\n"
    "v_mfma_f32_16x16x32_bf16 v[116:119], a[188:191], v[100:103], v[116:119]\n"
    "buffer_load_dwordx4 a[28:31], v255, s[64:67], s59 offen offset:3072 sc1\n"
    "v_mfma_f32_16x16x32_bf16 v[116:119], a[192:195], v[104:107], v[116:119]\n"
    "s_waitcnt vmcnt(8)\n"
    "s_add_u32 s59, s69, 16384\n"
    "v_exp_f32 v146, v146\n"
    "v_mfma_f32_16x16x32_bf16 v[116:119], a[196:199], v[108:111], v[116:119]\n"
    "buffer_load_dwordx4 a[32:35], v255, s[64:67], s59 offen offset:0 sc1\n"
    "v_mfma_f32_16x16x32_bf16 v[124:127], a[200:203], v[56:59], 0\n"
    "v_exp_f32 v147, v147\n"
    "v_mfma_f32_16x16x32_bf16 v[124:127], a[204:207], v[60:63], v[124:127]\n"
    "buffer_load_dwordx4 a[36:39], v255, s[64:67], s59 offen offset:1024 sc1\n"
    "v_mfma_f32_16x16x32_bf16 v[124:127], a[208:211], v[64:67], v[124:127]\n"
    "ds_read_b128 a[168:171], v235 offset:34816\n"
    "v_add_f32 v144, 1.0, v144\n"
    "v_mfma_f32_16x16x32_bf16 v[124:127], a[212:215], v[68:71], v[124:127]\n"
    "buffer_load_dwordx4 a[40:43], v255, s[64:67], s59 offen offset:2048 sc1\n"
    "v_mfma_f32_16x16x32_bf16 v[124:127], a[216:219], v[72:75], v[124:127]\n"
    "ds_read_b128 a[172:175], v235 offset:35840\n"
    "v_add_f32 v145, 1.0, v145\n"
    "v_mfma_f32_16x16x32_bf16 v[124:127], a[220:223], v[76:79], v[124:127]\n"
    "buffer_load_dwordx4 a[44:47], v255, s[64:67], s59 offen offset:3072 sc1\n"
    "v_add_f32 v146, 1.0, v146\n"
    "v_mfma_f32_16x16x32_bf16 v[124:127], a[224:227], v[80:83], v[124:127]\n"
    "ds_read_b128 a[176:179], v235 offset:36864\n"
    "v_add_f32 v147, 1.0, v147\n"
    "v_mfma_f32_16x16x32_bf16 v[124:127], a[228:231], v[84:87], v[124:127]\n"
    "ds_read_b128 a[180:183], v235 offset:37888\n"
    "v_mul_f32 v152, v116, v116\n"
    "v_mfma_f32_16x16x32_bf16 v[124:127], a[232:235], v[88:91], v[124:127]\n"
    "v_rcp_f32 v144, v144\n"
    "v_mfma_f32_16x16x32_bf16 v[124:127], a[236:239], v[92:95], v[124:127]\n"
    "ds_read_b128 a[184:187], v235 offset:38912\n"
    "v_mul_f32 v153, v117, v117\n"
    "v_mfma_f32_16x16x32_bf16 v[124:127], a[240:243], v[96:99], v[124:127]\n"
    "v_rcp_f32 v145, v145\n"
    "v_mul_f32 v154, v118, v118\n"
    "v_mfma_f32_16x16x32_bf16 v[124:127], a[244:247], v[100:103], v[124:127]\n"
    "v_rcp_f32 v146, v146\n"
    "v_mul_f32 v155, v119, v119\n"
    "v_mfma_f32_16x16x32_bf16 v[124:127], a[248:251], v[104:107], v[124:127]\n"
    "ds_read_b128 a[188:191], v235 offset:39936\n"
    "v_rcp_f32 v147, v147\n"
    "v_fma_f32 v152, v152, s58, v160\n"
    "v_mfma_f32_16x16x32_bf16 v[124:127], a[252:255], v[108:111], v[124:127]\n"
    "s_waitcnt lgkmcnt(11)\n"
    "ds_read_b128 a[192:195], v235 offset:40960\n"
    "v_fma_f32 v153, v153, s58, v160\n"
    "v_mfma_f32_16x16x32_bf16 v[128:131], a[144:147], v[0:3], 0\n"
    "s_waitcnt lgkmcnt(11)\n"
    "v_mul_f32 v144, v144, v112\n"
    "v_fma_f32 v154, v154, s58, v160\n"
    "v_mfma_f32_16x16x32_bf16 v[128:131], a[148:151], v[4:7], v[128:131]\n"
    "s_waitcnt lgkmcnt(10)\n"
    "v_mul_f32 v145, v145, v113\n"
    "v_fma_f32 v155, v155, s58, v160\n"
    "v_mfma_f32_16x16x32_bf16 v[128:131], a[152:155], v[8:11], v[128:131]\n"
    "s_waitcnt lgkmcnt(9)\n"
    "ds_read_b128 a[196:199], v235 offset:41984\n"
    "v_mul_f32 v152, v152, v116\n"
    "v_mfma_f32_16x16x32_bf16 v[128:131], a[156:159], v[12:15], v[128:131]\n"
    "s_waitcnt lgkmcnt(9)\n"
    "v_mul_f32 v146, v146, v114\n"
    "v_mul_f32 v153, v153, v117\n"
    "v_mfma_f32_16x16x32_bf16 v[128:131], a[160:163], v[16:19], v[128:131]\n"
    "s_waitcnt lgkmcnt(8)\n"
    "ds_read_b128 a[200:203], v235 offset:43008\n"
    "v_mul_f32 v147, v147, v115\n"
    "v_mul_f32 v154, v154, v118\n"
    "v_mfma_f32_16x16x32_bf16 v[128:131], a[164:167], v[20:23], v[128:131]\n"
    "s_waitcnt lgkmcnt(8)\n"
    "ds_read_b128 a[204:207], v235 offset:44032\n"
    "v_mul_f32 v155, v155, v119\n"
    "v_mfma_f32_16x16x32_bf16 v[128:131], a[168:171], v[24:27], v[128:131]\n"
    "s_waitcnt lgkmcnt(8)\n"
    "v_mul_f32 v144, v144, v120\n"
    "v_exp_f32 v152, v152\n"
    "v_mfma_f32_16x16x32_bf16 v[128:131], a[172:175], v[28:31], v[128:131]\n"
    "s_waitcnt lgkmcnt(7)\n"
    "v_mul_f32 v145, v145, v121\n"
    "v_exp_f32 v153, v153\n"
    "v_mfma_f32_16x16x32_bf16 v[128:131], a[176:179], v[32:35], v[128:131]\n"
    "s_waitcnt lgkmcnt(6)\n"
    "ds_read_b128 a[208:211], v235 offset:45056\n"
    "v_mul_f32 v146, v146, v122\n"
    "v_exp_f32 v154, v154\n"
    "v_mfma_f32_16x16x32_bf16 v[128:131], a[180:183], v[36:39], v[128:131]\n"
    "s_waitcnt lgkmcnt(6)\n"
    "v_mul_f32 v147, v147, v123\n"
    "v_exp_f32 v155, v155\n"
    "v_mfma_f32_16x16x32_bf16 v[128:131], a[184:187], v[40:43], v[128:131]\n"
    "s_waitcnt lgkmcnt(5)\n"
    "ds_read_b128 a[212:215], v235 offset:46080\n"
    "v_cvt_pk_bf16_f32 v176, v144, v145\n"
    "v_add_f32 v152, 1.0, v152\n"
    "v_mfma_f32_16x16x32_bf16 v[128:131], a[188:191], v[44:47], v[128:131]\n"
    "s_waitcnt lgkmcnt(5)\n"
    "v_cvt_pk_bf16_f32 v177, v146, v147\n"
    "v_add_f32 v153, 1.0, v153\n"
    "v_mfma_f32_16x16x32_bf16 v[128:131], a[192:195], v[48:51], v[128:131]\n"
    "s_waitcnt lgkmcnt(4)\n"
    "ds_read_b128 a[216:219], v235 offset:47104\n"
    "v_add_f32 v154, 1.0, v154\n"
    "v_mfma_f32_16x16x32_bf16 v[128:131], a[196:199], v[52:55], v[128:131]\n"
    "s_add_u32 s59, s69, 20480\n"
    "ds_read_b128 a[220:223], v235 offset:48128\n"
    "v_add_f32 v155, 1.0, v155\n"
    "v_mfma_f32_16x16x32_bf16 v[132:135], a[144:147], v[56:59], 0\n"
    "buffer_load_dwordx4 a[48:51], v255, s[64:67], s59 offen offset:0 sc1\n"
    "v_rcp_f32 v152, v152\n"
    "v_mfma_f32_16x16x32_bf16 v[132:135], a[148:151], v[60:63], v[132:135]\n"
    "v_rcp_f32 v153, v153\n"
    "v_mfma_f32_16x16x32_bf16 v[132:135], a[152:155], v[64:67], v[132:135]\n"
    "ds_read_b128 a[224:227], v235 offset:49152\n"
    "v_rcp_f32 v154, v154\n"
    "v_mfma_f32_16x16x32_bf16 v[132:135], a[156:159], v[68:71], v[132:135]\n"
    "v_rcp_f32 v155, v155\n"
    "v_mfma_f32_16x16x32_bf16 v[132:135], a[160:163], v[72:75], v[132:135]\n"
    "ds_read_b128 a[228:231], v235 offset:50176\n"
    "v_mul_f32 v152, v152, v116\n"
    "v_mfma_f32_16x16x32_bf16 v[132:135], a[164:167], v[76:79], v[132:135]\n"
    "buffer_load_dwordx4 a[52:55], v255, s[64:67], s59 offen offset:1024 sc1\n"
    "v_mul_f32 v153, v153, v117\n"
    "v_mfma_f32_16x16x32_bf16 v[132:135], a[168:171], v[80:83], v[132:135]\n"
    "ds_read_b128 a[232:235], v235 offset:51200\n"
    "v_mul_f32 v154, v154, v118\n"
    "v_mfma_f32_16x16x32_bf16 v[132:135], a[172:175], v[84:87], v[132:135]\n"
    "ds_read_b128 a[236:239], v235 offset:52224\n"
    "v_mul_f32 v155, v155, v119\n"
    "v_mfma_f32_16x16x32_bf16 v[132:135], a[176:179], v[88:91], v[132:135]\n"
    "v_mul_f32 v144, v128, v128\n"
    "v_mul_f32 v152, v152, v124\n"
    "v_mfma_f32_16x16x32_bf16 v[132:135], a[180:183], v[92:95], v[132:135]\n"
    "v_mul_f32 v145, v129, v129\n"
    "v_mul_f32 v153, v153, v125\n"
    "v_mfma_f32_16x16x32_bf16 v[132:135], a[184:187], v[96:99], v[132:135]\n"
    "ds_read_b128 a[240:243], v235 offset:53248\n"
    "v_mul_f32 v146, v130, v130\n"
    "v_mul_f32 v154, v154, v126\n"
    "v_mfma_f32_16x16x32_bf16 v[132:135], a[188:191], v[100:103], v[132:135]\n"
    "buffer_load_dwordx4 a[56:59], v255, s[64:67], s59 offen offset:2048 sc1\n"
    "v_mul_f32 v155, v155, v127\n"
    "v_mfma_f32_16x16x32_bf16 v[132:135], a[192:195], v[104:107], v[132:135]\n"
    "ds_read_b128 a[244:247], v235 offset:54272\n"
    "v_mul_f32 v147, v131, v131\n"
    "v_cvt_pk_bf16_f32 v180, v152, v153\n"
    "v_mfma_f32_16x16x32_bf16 v[132:135], a[196:199], v[108:111], v[132:135]\n"
    "s_waitcnt lgkmcnt(11)\n"
    "v_fma_f32 v144, v144, s58, v160\n"
    "v_cvt_pk_bf16_f32 v181, v154, v155\n"
    "v_mfma_f32_16x16x32_bf16 v[136:139], a[200:203], v[0:3], 0\n"
    "s_waitcnt lgkmcnt(10)\n"
    "ds_read_b128 a[248:251], v235 offset:55296\n"
    "v_fma_f32 v145, v145, s58, v160\n"
    "v_mfma_f32_16x16x32_bf16 v[136:139], a[204:207], v[4:7], v[136:139]\n"
    "s_waitcnt lgkmcnt(10)\n"
    "ds_read_b128 a[252:255], v235 offset:56320\n"
    "v_fma_f32 v146, v146, s58, v160\n"
    "v_mfma_f32_16x16x32_bf16 v[136:139], a[208:211], v[8:11], v[136:139]\n"
    "s_waitcnt lgkmcnt(9)\n"
    "ds_read_b128 a[144:147], v236 offset:0\n"
    "v_fma_f32 v147, v147, s58, v160\n"
    "v_mfma_f32_16x16x32_bf16 v[136:139], a[212:215], v[12:15], v[136:139]\n"
    "buffer_load_dwordx4 a[60:63], v255, s[64:67], s59 offen offset:3072 sc1\n"
    "v_mfma_f32_16x16x32_bf16 v[136:139], a[216:219], v[16:19], v[136:139]\n"
    "s_waitcnt lgkmcnt(9)\n"
    "ds_read_b128 a[148:151], v236 offset:1024\n"
    "v_mul_f32 v144, v144, v128\n"
    "v_mfma_f32_16x16x32_bf16 v[136:139], a[220:223], v[20:23], v[136:139]\n"
    "s_waitcnt lgkmcnt(9)\n"
    "ds_read_b128 a[152:155], v236 offset:2048\n"
    "v_mul_f32 v145, v145, v129\n"
    "v_mfma_f32_16x16x32_bf16 v[136:139], a[224:227], v[24:27], v[136:139]\n"
    "s_waitcnt lgkmcnt(9)\n"
    "v_mul_f32 v146, v146, v130\n"
    "v_mul_f32 v152, v132, v132\n"
    "v_mfma_f32_16x16x32_bf16 v[136:139], a[228:231], v[28:31], v[136:139]\n"
    "s_waitcnt lgkmcnt(6)\n"
    "v_mul_f32 v147, v147, v131\n"
    "v_mul_f32 v153, v133, v133\n"
    "v_mfma_f32_16x16x32_bf16 v[136:139], a[232:235], v[32:35], v[136:139]\n"
    "s_add_u32 s59, s69, 12288\n"
    "ds_read_b128 a[156:159], v236 offset:3072\n"
    "v_mul_f32 v154, v134, v134\n"
    "v_mfma_f32_16x16x32_bf16 v[136:139], a[236:239], v[36:39], v[136:139]\n"
    "buffer_load_dwordx4 a[64:67], v255, s[64:67], s59 offen offset:0 sc1\n"
    "v_mfma_f32_16x16x32_bf16 v[136:139], a[240:243], v[40:43], v[136:139]\n"
    "s_waitcnt lgkmcnt(6)\n"
    "v_exp_f32 v144, v144\n"
    "v_mfma_f32_16x16x32_bf16 v[136:139], a[244:247], v[44:47], v[136:139]\n"
    "s_waitcnt lgkmcnt(5)\n"
    "ds_read_b128 a[160:163], v236 offset:4096\n"
    "v_mul_f32 v155, v135, v135\n"
    "v_mfma_f32_16x16x32_bf16 v[136:139], a[248:251], v[48:51], v[136:139]\n"
    "s_waitcnt lgkmcnt(5)\n"
    "v_exp_f32 v145, v145\n"
    "v_mfma_f32_16x16x32_bf16 v[136:139], a[252:255], v[52:55], v[136:139]\n"
    "ds_read_b128 a[164:167], v236 offset:5120\n"
    "v_fma_f32 v152, v152, s58, v160\n"
    "v_mfma_f32_16x16x32_bf16 v[140:143], a[200:203], v[56:59], 0\n"
    "v_exp_f32 v146, v146\n"
    "v_mfma_f32_16x16x32_bf16 v[140:143], a[204:207], v[60:63], v[140:143]\n"
    "buffer_load_dwordx4 a[68:71], v255, s[64:67], s59 offen offset:1024 sc1\n"
    "v_exp_f32 v147, v147\n"
    "v_fma_f32 v153, v153, s58, v160\n"
    "v_mfma_f32_16x16x32_bf16 v[140:143], a[208:211], v[64:67], v[140:143]\n"
    "ds_read_b128 a[168:171], v236 offset:6144\n"
    "v_add_f32 v144, 1.0, v144\n"
    "v_fma_f32 v154, v154, s58, v160\n"
    "v_mfma_f32_16x16x32_bf16 v[140:143], a[212:215], v[68:71], v[140:143]\n"
    "ds_read_b128 a[172:175], v236 offset:7168\n"
    "v_add_f32 v145, 1.0, v145\n"
    "v_mfma_f32_16x16x32_bf16 v[140:143], a[216:219], v[72:75], v[140:143]\n"
    "v_fma_f32 v155, v155, s58, v160\n"
    "v_add_f32 v146, 1.0, v146\n"
    "v_mfma_f32_16x16x32_bf16 v[140:143], a[220:223], v[76:79], v[140:143]\n"
    "ds_read_b128 a[176:179], v236 offset:8192\n"
    "v_mul_f32 v152, v152, v132\n"
    "v_mfma_f32_16x16x32_bf16 v[140:143], a[224:227], v[80:83], v[140:143]\n"
    "v_mul_f32 v153, v153, v133\n"
    "v_add_f32 v147, 1.0, v147\n"
    "v_mfma_f32_16x16x32_bf16 v[140:143], a[228:231], v[84:87], v[140:143]\n"
    "buffer_load_dwordx4 a[72:75], v255, s[64:67], s59 offen offset:2048 sc1\n"
    "v_mfma_f32_16x16x32_bf16 v[140:143], a[232:235], v[88:91], v[140:143]\n"
    "ds_read_b128 a[180:183], v236 offset:9216\n"
    "v_rcp_f32 v144, v144\n"
    "v_mul_f32 v154, v154, v134\n"
    "v_mfma_f32_16x16x32_bf16 v[140:143], a[236:239], v[92:95], v[140:143]\n"
    "ds_read_b128 a[184:187], v236 offset:10240\n"
    "v_mul_f32 v155, v155, v135\n"
    "v_mfma_f32_16x16x32_bf16 v[140:143], a[240:243], v[96:99], v[140:143]\n"
    "v_rcp_f32 v145, v145\n"
    "v_mfma_f32_16x16x32_bf16 v[140:143], a[244:247], v[100:103], v[140:143]\n"
    "v_rcp_f32 v146, v146\n"
    "v_exp_f32 v152, v152\n"
    "v_mfma_f32_16x16x32_bf16 v[140:143], a[248:251], v[104:107], v[140:143]\n"
    "s_waitcnt lgkmcnt(10)\n"
    "ds_read_b128 a[188:191], v236 offset:11264\n"
    "v_rcp_f32 v147, v147\n"
    "v_exp_f32 v153, v153\n"
    "v_mfma_f32_16x16x32_bf16 v[140:143], a[252:255], v[108:111], v[140:143]\n"
    "buffer_load_dwordx4 a[76:79], v255, s[64:67], s59 offen offset:3072 sc1\n"
    "v_mfma_f32_16x16x32_bf16 v[112:115], a[144:147], v[0:3], 0\n"
    "s_waitcnt lgkmcnt(10)\n"
    "ds_read_b128 a[192:195], v236 offset:12288\n"
    "v_mul_f32 v144, v144, v128\n"
    "v_exp_f32 v154, v154\n"
    "v_mfma_f32_16x16x32_bf16 v[112:115], a[148:151], v[4:7], v[112:115]\n"
    "s_waitcnt lgkmcnt(10)\n"
    "v_mul_f32 v145, v145, v129\n"
    "v_exp_f32 v155, v155\n"
    "v_mfma_f32_16x16x32_bf16 v[112:115], a[152:155], v[8:11], v[112:115]\n"
    "s_waitcnt lgkmcnt(9)\n"
    "ds_read_b128 a[196:199], v236 offset:13312\n"
    "v_add_f32 v152, 1.0, v152\n"
    "v_mfma_f32_16x16x32_bf16 v[112:115], a[156:159], v[12:15], v[112:115]\n"
    "s_waitcnt lgkmcnt(7)\n"
    "ds_read_b128 a[200:203], v236 offset:14336\n"
    "v_mul_f32 v146, v146, v130\n"
    "v_mfma_f32_16x16x32_bf16 v[112:115], a[160:163], v[16:19], v[112:115]\n"
    "s_add_u32 s59, s91, 28672\n"
    "v_mul_f32 v147, v147, v131\n"
    "v_add_f32 v153, 1.0, v153\n"
    "v_mfma_f32_16x16x32_bf16 v[112:115], a[164:167], v[20:23], v[112:115]\n"
    "buffer_load_dwordx4 a[80:83], v255, s[64:67], s59 offen offset:0 sc1\n"
    "v_add_f32 v154, 1.0, v154\n"
    "v_mfma_f32_16x16x32_bf16 v[112:115], a[168:171], v[24:27], v[112:115]\n"
    "s_waitcnt lgkmcnt(7)\n"
    "ds_read_b128 a[204:207], v236 offset:15360\n"
    "v_mul_f32 v144, v144, v136\n"
    "v_mfma_f32_16x16x32_bf16 v[112:115], a[172:175], v[28:31], v[112:115]\n"
    "s_waitcnt lgkmcnt(7)\n"
    "v_mul_f32 v145, v145, v137\n"
    "v_add_f32 v155, 1.0, v155\n"
    "v_mfma_f32_16x16x32_bf16 v[112:115], a[176:179], v[32:35], v[112:115]\n"
    "s_waitcnt lgkmcnt(6)\n"
    "ds_read_b128 a[208:211], v236 offset:16384\n"
    "v_mul_f32 v146, v146, v138\n"
    "v_rcp_f32 v152, v152\n"
    "v_mfma_f32_16x16x32_bf16 v[112:115], a[180:183], v[36:39], v[112:115]\n"
    "s_waitcnt lgkmcnt(6)\n"
    "ds_read_b128 a[212:215], v236 offset:17408\n"
    "v_mul_f32 v147, v147, v139\n"
    "v_mfma_f32_16x16x32_bf16 v[112:115], a[184:187], v[40:43], v[112:115]\n"
    "s_waitcnt lgkmcnt(5)\n"
    "v_cvt_pk_bf16_f32 v178, v144, v145\n"
    "v_rcp_f32 v153, v153\n"
    "v_mfma_f32_16x16x32_bf16 v[112:115], a[188:191], v[44:47], v[112:115]\n"
    "buffer_load_dwordx4 a[84:87], v255, s[64:67], s59 offen offset:1024 sc1\n"
    "v_cvt_pk_bf16_f32 v179, v146, v147\n"
    "v_rcp_f32 v154, v154\n"
    "v_mfma_f32_16x16x32_bf16 v[112:115], a[192:195], v[48:51], v[112:115]\n"
    "s_waitcnt lgkmcnt(4)\n"
    "ds_read_b128 a[216:219], v236 offset:18432\n"
    "v_rcp_f32 v155, v155\n"
    "v_mfma_f32_16x16x32_bf16 v[112:115], a[196:199], v[52:55], v[112:115]\n"
    "buffer_store_dwordx4 v[176:179], v253, s[64:67], s49 offen offset:0 sc1\n"
    "v_mfma_f32_16x16x32_bf16 v[116:119], a[144:147], v[56:59], 0\n"
    "ds_read_b128 a[220:223], v236 offset:19456\n"
    "v_mul_f32 v152, v152, v132\n"
    "v_mfma_f32_16x16x32_bf16 v[116:119], a[148:151], v[60:63], v[116:119]\n"
    "ds_read_b128 a[224:227], v236 offset:20480\n"
    "v_mul_f32 v153, v153, v133\n"
    "v_mfma_f32_16x16x32_bf16 v[116:119], a[152:155], v[64:67], v[116:119]\n"
    "ds_read_b128 a[228:231], v236 offset:21504\n"
    "v_mul_f32 v154, v154, v134\n"
    "v_mfma_f32_16x16x32_bf16 v[116:119], a[156:159], v[68:71], v[116:119]\n"
    "buffer_load_dwordx4 a[88:91], v255, s[64:67], s59 offen offset:2048 sc1\n"
    "v_mfma_f32_16x16x32_bf16 v[116:119], a[160:163], v[72:75], v[116:119]\n"
    "ds_read_b128 a[232:235], v236 offset:22528\n"
    "v_mul_f32 v155, v155, v135\n"
    "v_mfma_f32_16x16x32_bf16 v[116:119], a[164:167], v[76:79], v[116:119]\n"
    "v_mul_f32 v152, v152, v140\n"
    "v_mul_f32 v153, v153, v141\n"
    "v_mfma_f32_16x16x32_bf16 v[116:119], a[168:171], v[80:83], v[116:119]\n"
    "ds_read_b128 a[236:239], v236 offset:23552\n"
    "v_mul_f32 v144, v112, v112\n"
    "v_mfma_f32_16x16x32_bf16 v[116:119], a[172:175], v[84:87], v[116:119]\n"
    "v_mul_f32 v154, v154, v142\n"
    "v_mul_f32 v145, v113, v113\n"
    "v_mfma_f32_16x16x32_bf16 v[116:119], a[176:179], v[88:91], v[116:119]\n"
    "ds_read_b128 a[240:243], v236 offset:24576\n"
    "v_mul_f32 v155, v155, v143\n"
    "v_mfma_f32_16x16x32_bf16 v[116:119], a[180:183], v[92:95], v[116:119]\n"
    "buffer_load_dwordx4 a[92:95], v255, s[64:67], s59 offen offset:3072 sc1\n"
    "v_mfma_f32_16x16x32_bf16 v[116:119], a[184:187], v[96:99], v[116:119]\n"
    "v_mul_f32 v146, v114, v114\n"
    "v_cvt_pk_bf16_f32 v182, v152, v153\n"
    "v_mfma_f32_16x16x32_bf16 v[116:119], a[188:191], v[100:103], v[116:119]\n"
    "ds_read_b128 a[244:247], v236 offset:25600\n"
    "v_cvt_pk_bf16_f32 v183, v154, v155\n"
    "v_mfma_f32_16x16x32_bf16 v[116:119], a[192:195], v[104:107], v[116:119]\n"
    "s_waitcnt lgkmcnt(9)\n"
    "v_mul_f32 v147, v115, v115\n"
    "v_fma_f32 v144, v144, s58, v160\n"
    "v_mfma_f32_16x16x32_bf16 v[116:119], a[196:199], v[108:111], v[116:119]\n"
    "buffer_store_dwordx4 v[180:183], v253, s[64:67], s51 offen offset:0 sc1\n"
    "v_mfma_f32_16x16x32_bf16 v[120:123], a[200:203], v[0:3], 0\n"
    "s_add_u32 s59, s91, 32768\n"
    "ds_read_b128 a[248:251], v236 offset:26624\n"
    "v_fma_f32 v145, v145, s58, v160\n"
    "v_mfma_f32_16x16x32_bf16 v[120:123], a[204:207], v[4:7], v[120:123]\n"
    "buffer_load_dwordx4 a[96:99], v255, s[64:67], s59 offen offset:0 sc1\n"
    "v_mfma_f32_16x16x32_bf16 v[120:123], a[208:211], v[8:11], v[120:123]\n"
    "s_waitcnt lgkmcnt(6)\n"
    "s_barrier\n"
    "ds_read_b128 a[252:255], v236 offset:27648\n"
    "v_fma_f32 v146, v146, s58, v160\n"
    "v_mfma_f32_16x16x32_bf16 v[120:123], a[212:215], v[12:15], v[120:123]\n"
    "s_add_u32 s68, s46, 48377856\n"
    "ds_read_b128 a[144:147], v236 offset:28672\n"
    "v_fma_f32 v147, v147, s58, v160\n"
    "v_mfma_f32_16x16x32_bf16 v[120:123], a[216:219], v[16:19], v[120:123]\n"
    "s_add_u32 m0, s52, 28672\n"
    "ds_read_b128 a[148:151], v236 offset:29696\n"
    "v_mul_f32 v144, v144, v112\n"
    "v_mfma_f32_16x16x32_bf16 v[120:123], a[220:223], v[20:23], v[120:123]\n"
    "buffer_load_dwordx4 v251, s[64:67], s68 offen sc1 lds\n"
    "v_mfma_f32_16x16x32_bf16 v[120:123], a[224:227], v[24:27], v[120:123]\n"
    "s_waitcnt lgkmcnt(5)\n"
    "ds_read_b128 a[152:155], v236 offset:30720\n"
    "v_mul_f32 v145, v145, v113\n"
    "v_mfma_f32_16x16x32_bf16 v[120:123], a[228:231], v[28:31], v[120:123]\n"
    "buffer_load_dwordx4 a[100:103], v255, s[64:67], s59 offen offset:1024 sc1\n"
    "v_mfma_f32_16x16x32_bf16 v[120:123], a[232:235], v[32:35], v[120:123]\n"
    "s_add_u32 s68, s46, 48381952\n"
    "v_mul_f32 v146, v146, v114\n"
    "v_mul_f32 v152, v116, v116\n"
    "v_mfma_f32_16x16x32_bf16 v[120:123], a[236:239], v[36:39], v[120:123]\n"
    "s_add_u32 m0, s52, 32768\n"
    "v_mul_f32 v147, v147, v115\n"
    "v_mul_f32 v153, v117, v117\n"
    "v_mfma_f32_16x16x32_bf16 v[120:123], a[240:243], v[40:43], v[120:123]\n"
    "s_waitcnt lgkmcnt(5)\n"
    "buffer_load_dwordx4 v251, s[64:67], s68 offen sc1 lds\n"
    "ds_read_b128 a[156:159], v236 offset:31744\n"
    "v_mul_f32 v154, v118, v118\n"
    "v_mfma_f32_16x16x32_bf16 v[120:123], a[244:247], v[44:47], v[120:123]\n"
    "s_waitcnt lgkmcnt(5)\n"
    "v_exp_f32 v144, v144\n"
    "v_mfma_f32_16x16x32_bf16 v[120:123], a[248:251], v[48:51], v[120:123]\n"
    "s_waitcnt lgkmcnt(4)\n"
    "ds_read_b128 a[160:163], v236 offset:32768\n"
    "v_exp_f32 v145, v145\n"
    "v_mul_f32 v155, v119, v119\n"
    "v_mfma_f32_16x16x32_bf16 v[120:123], a[252:255], v[52:55], v[120:123]\n"
    "buffer_load_dwordx4 a[104:107], v255, s[64:67], s59 offen offset:2048 sc1\n"
    "v_fma_f32 v152, v152, s58, v160\n"
    "v_mfma_f32_16x16x32_bf16 v[124:127], a[200:203], v[56:59], 0\n"
    "s_add_u32 s68, s46, 48386048\n"
    "ds_read_b128 a[164:167], v236 offset:33792\n"
    "v_exp_f32 v146, v146\n"
    "v_mfma_f32_16x16x32_bf16 v[124:127], a[204:207], v[60:63], v[124:127]\n"
    "s_add_u32 m0, s52, 36864\n"
    "v_exp_f32 v147, v147\n"
    "v_fma_f32 v153, v153, s58, v160\n"
    "v_mfma_f32_16x16x32_bf16 v[124:127], a[208:211], v[64:67], v[124:127]\n"
    "buffer_load_dwordx4 v251, s[64:67], s68 offen sc1 lds\n"
    "ds_read_b128 a[168:171], v236 offset:34816\n"
    "v_add_f32 v144, 1.0, v144\n"
    "v_fma_f32 v154, v154, s58, v160\n"
    "v_mfma_f32_16x16x32_bf16 v[124:127], a[212:215], v[68:71], v[124:127]\n"
    "ds_read_b128 a[172:175], v236 offset:35840\n"
    "v_add_f32 v145, 1.0, v145\n"
    "v_mfma_f32_16x16x32_bf16 v[124:127], a[216:219], v[72:75], v[124:127]\n"
    "buffer_load_dwordx4 a[108:111], v255, s[64:67], s59 offen offset:3072 sc1\n"
    "v_fma_f32 v155, v155, s58, v160\n"
    "v_mfma_f32_16x16x32_bf16 v[124:127], a[220:223], v[76:79], v[124:127]\n"
    "s_add_u32 s68, s46, 48390144\n"
    "v_add_f32 v146, 1.0, v146\n"
    "v_mul_f32 v152, v152, v116\n"
    "v_mfma_f32_16x16x32_bf16 v[124:127], a[224:227], v[80:83], v[124:127]\n"
    "s_add_u32 m0, s52, 40960\n"
    "ds_read_b128 a[176:179], v236 offset:36864\n"
    "v_mul_f32 v153, v153, v117\n"
    "v_mfma_f32_16x16x32_bf16 v[124:127], a[228:231], v[84:87], v[124:127]\n"
    "buffer_load_dwordx4 v251, s[64:67], s68 offen sc1 lds\n"
    "v_add_f32 v147, 1.0, v147\n"
    "v_mfma_f32_16x16x32_bf16 v[124:127], a[232:235], v[88:91], v[124:127]\n"
    "s_add_u32 s68, s46, 48394240\n"
    "ds_read_b128 a[180:183], v236 offset:37888\n"
    "v_rcp_f32 v144, v144\n"
    "v_mul_f32 v154, v154, v118\n"
    "v_mfma_f32_16x16x32_bf16 v[124:127], a[236:239], v[92:95], v[124:127]\n"
    "s_add_u32 m0, s52, 45056\n"
    "ds_read_b128 a[184:187], v236 offset:38912\n"
    "v_mul_f32 v155, v155, v119\n"
    "v_mfma_f32_16x16x32_bf16 v[124:127], a[240:243], v[96:99], v[124:127]\n"
    "buffer_load_dwordx4 v251, s[64:67], s68 offen sc1 lds\n"
    "v_rcp_f32 v145, v145\n"
    "v_mfma_f32_16x16x32_bf16 v[124:127], a[244:247], v[100:103], v[124:127]\n"
    "s_add_u32 s59, s91, 45056\n"
    "v_rcp_f32 v146, v146\n"
    "v_exp_f32 v152, v152\n"
    "v_mfma_f32_16x16x32_bf16 v[124:127], a[248:251], v[104:107], v[124:127]\n"
    "buffer_load_dwordx4 a[112:115], v255, s[64:67], s59 offen offset:0 sc1\n"
    "ds_read_b128 a[188:191], v236 offset:39936\n"
    "v_rcp_f32 v147, v147\n"
    "v_exp_f32 v153, v153\n"
    "v_mfma_f32_16x16x32_bf16 v[124:127], a[252:255], v[108:111], v[124:127]\n"
    "s_waitcnt lgkmcnt(9)\n"
    "s_add_u32 s68, s46, 48398336\n"
    "ds_read_b128 a[192:195], v236 offset:40960\n"
    "v_mul_f32 v144, v144, v112\n"
    "v_mfma_f32_16x16x32_bf16 v[128:131], a[144:147], v[0:3], 0\n"
    "s_add_u32 m0, s52, 49152\n"
    "v_exp_f32 v154, v154\n"
    "v_mfma_f32_16x16x32_bf16 v[128:131], a[148:151], v[4:7], v[128:131]\n"
    "buffer_load_dwordx4 v251, s[64:67], s68 offen sc1 lds\n"
    "v_mul_f32 v145, v145, v113\n"
    "v_exp_f32 v155, v155\n"
    "v_mfma_f32_16x16x32_bf16 v[128:131], a[152:155], v[8:11], v[128:131]\n"
    "s_waitcnt lgkmcnt(6)\n"
    "ds_read_b128 a[196:199], v236 offset:41984\n"
    "v_add_f32 v152, 1.0, v152\n"
    "v_mfma_f32_16x16x32_bf16 v[128:131], a[156:159], v[12:15], v[128:131]\n"
    "buffer_load_dwordx4 a[116:119], v255, s[64:67], s59 offen offset:1024 sc1\n"
    "v_mul_f32 v146, v146, v114\n"
    "v_mfma_f32_16x16x32_bf16 v[128:131], a[160:163], v[16:19], v[128:131]\n"
    "s_add_u32 s68, s46, 48402432\n"
    "ds_read_b128 a[200:203], v236 offset:43008\n"
    "v_mul_f32 v147, v147, v115\n"
    "v_add_f32 v153, 1.0, v153\n"
    "v_mfma_f32_16x16x32_bf16 v[128:131], a[164:167], v[20:23], v[128:131]\n"
    "s_add_u32 m0, s52, 53248\n"
    "ds_read_b128 a[204:207], v236 offset:44032\n"
    "v_add_f32 v154, 1.0, v154\n"
    "v_mfma_f32_16x16x32_bf16 v[128:131], a[168:171], v[24:27], v[128:131]\n"
    "s_waitcnt lgkmcnt(8)\n"
    "buffer_load_dwordx4 v251, s[64:67], s68 offen sc1 lds\n"
    "v_mul_f32 v144, v144, v120\n"
    "v_mfma_f32_16x16x32_bf16 v[128:131], a[172:175], v[28:31], v[128:131]\n"
    "s_waitcnt lgkmcnt(6)\n"
    "v_mul_f32 v145, v145, v121\n"
    "v_add_f32 v155, 1.0, v155\n"
    "v_mfma_f32_16x16x32_bf16 v[128:131], a[176:179], v[32:35], v[128:131]\n"
    "buffer_load_dwordx4 a[120:123], v255, s[64:67], s59 offen offset:2048 sc1\n"
    "ds_read_b128 a[208:211], v236 offset:45056\n"
    "v_mul_f32 v146, v146, v122\n"
    "v_rcp_f32 v152, v152\n"
    "v_mfma_f32_16x16x32_bf16 v[128:131], a[180:183], v[36:39], v[128:131]\n"
    "s_waitcnt lgkmcnt(6)\n"
    "ds_read_b128 a[212:215], v236 offset:46080\n"
    "v_mul_f32 v147, v147, v123\n"
    "v_mfma_f32_16x16x32_bf16 v[128:131], a[184:187], v[40:43], v[128:131]\n"
    "s_waitcnt lgkmcnt(6)\n"
    "v_cvt_pk_bf16_f32 v184, v144, v145\n"
    "v_rcp_f32 v153, v153\n"
    "v_mfma_f32_16x16x32_bf16 v[128:131], a[188:191], v[44:47], v[128:131]\n"
    "s_waitcnt lgkmcnt(5)\n"
    "v_cvt_pk_bf16_f32 v185, v146, v147\n"
    "v_rcp_f32 v154, v154\n"
    "v_mfma_f32_16x16x32_bf16 v[128:131], a[192:195], v[48:51], v[128:131]\n"
    "s_waitcnt lgkmcnt(4)\n"
    "ds_read_b128 a[216:219], v236 offset:47104\n"
    "v_rcp_f32 v155, v155\n"
    "v_mfma_f32_16x16x32_bf16 v[128:131], a[196:199], v[52:55], v[128:131]\n"
    "ds_read_b128 a[220:223], v236 offset:48128\n"
    "v_mul_f32 v152, v152, v116\n"
    "v_mfma_f32_16x16x32_bf16 v[132:135], a[144:147], v[56:59], 0\n"
    "buffer_load_dwordx4 a[124:127], v255, s[64:67], s59 offen offset:3072 sc1\n"
    "v_mfma_f32_16x16x32_bf16 v[132:135], a[148:151], v[60:63], v[132:135]\n"
    "ds_read_b128 a[224:227], v236 offset:49152\n"
    "v_mul_f32 v153, v153, v117\n"
    "v_mfma_f32_16x16x32_bf16 v[132:135], a[152:155], v[64:67], v[132:135]\n"
    "ds_read_b128 a[228:231], v236 offset:50176\n"
    "v_mul_f32 v154, v154, v118\n"
    "v_mfma_f32_16x16x32_bf16 v[132:135], a[156:159], v[68:71], v[132:135]\n"
    "ds_read_b128 a[232:235], v236 offset:51200\n"
    "v_mul_f32 v155, v155, v119\n"
    "v_mfma_f32_16x16x32_bf16 v[132:135], a[160:163], v[72:75], v[132:135]\n"
    "v_mul_f32 v152, v152, v124\n"
    "v_mul_f32 v153, v153, v125\n"
    "v_mfma_f32_16x16x32_bf16 v[132:135], a[164:167], v[76:79], v[132:135]\n"
    "s_add_u32 s59, s91, 49152\n"
    "ds_read_b128 a[236:239], v236 offset:52224\n"
    "v_mul_f32 v144, v128, v128\n"
    "v_mfma_f32_16x16x32_bf16 v[132:135], a[168:171], v[80:83], v[132:135]\n"
    "buffer_load_dwordx4 a[128:131], v255, s[64:67], s59 offen offset:0 sc1\n"
    "v_mfma_f32_16x16x32_bf16 v[132:135], a[172:175], v[84:87], v[132:135]\n"
    "v_mul_f32 v154, v154, v126\n"
    "v_mul_f32 v145, v129, v129\n"
    "v_mfma_f32_16x16x32_bf16 v[132:135], a[176:179], v[88:91], v[132:135]\n"
    "ds_read_b128 a[240:243], v236 offset:53248\n"
    "v_mul_f32 v155, v155, v127\n"
    "v_mfma_f32_16x16x32_bf16 v[132:135], a[180:183], v[92:95], v[132:135]\n"
    "v_mul_f32 v146, v130, v130\n"
    "v_cvt_pk_bf16_f32 v188, v152, v153\n"
    "v_mfma_f32_16x16x32_bf16 v[132:135], a[184:187], v[96:99], v[132:135]\n"
    "ds_read_b128 a[244:247], v236 offset:54272\n"
    "v_cvt_pk_bf16_f32 v189, v154, v155\n"
    "v_mfma_f32_16x16x32_bf16 v[132:135], a[188:191], v[100:103], v[132:135]\n"
    "v_mul_f32 v147, v131, v131\n"
    "v_fma_f32 v144, v144, s58, v160\n"
    "v_mfma_f32_16x16x32_bf16 v[132:135], a[192:195], v[104:107], v[132:135]\n"
    "buffer_load_dwordx4 a[132:135], v255, s[64:67], s59 offen offset:1024 sc1\n"
    "v_mfma_f32_16x16x32_bf16 v[132:135], a[196:199], v[108:111], v[132:135]\n"
    "s_waitcnt lgkmcnt(11)\n"
    "ds_read_b128 a[248:251], v236 offset:55296\n"
    "v_fma_f32 v145, v145, s58, v160\n"
    "v_mfma_f32_16x16x32_bf16 v[136:139], a[200:203], v[0:3], 0\n"
    "s_waitcnt lgkmcnt(11)\n"
    "ds_read_b128 a[252:255], v236 offset:56320\n"
    "v_fma_f32 v146, v146, s58, v160\n"
    "v_mfma_f32_16x16x32_bf16 v[136:139], a[204:207], v[4:7], v[136:139]\n"
    "s_waitcnt lgkmcnt(11)\n"
    "ds_read_b128 a[144:147], v237 offset:0\n"
    "v_fma_f32 v147, v147, s58, v160\n"
    "v_mfma_f32_16x16x32_bf16 v[136:139], a[208:211], v[8:11], v[136:139]\n"
    "s_waitcnt lgkmcnt(11)\n"
    "ds_read_b128 a[148:151], v237 offset:1024\n"
    "v_mul_f32 v144, v144, v128\n"
    "v_mfma_f32_16x16x32_bf16 v[136:139], a[212:215], v[12:15], v[136:139]\n"
    "s_waitcnt lgkmcnt(10)\n"
    "ds_read_b128 a[152:155], v237 offset:2048\n"
    "v_mul_f32 v145, v145, v129\n"
    "v_mfma_f32_16x16x32_bf16 v[136:139], a[216:219], v[16:19], v[136:139]\n"
    "buffer_load_dwordx4 a[136:139], v255, s[64:67], s59 offen offset:2048 sc1\n"
    "v_mfma_f32_16x16x32_bf16 v[136:139], a[220:223], v[20:23], v[136:139]\n"
    "s_waitcnt lgkmcnt(10)\n"
    "v_mul_f32 v146, v146, v130\n"
    "v_mul_f32 v152, v132, v132\n"
    "v_mfma_f32_16x16x32_bf16 v[136:139], a[224:227], v[24:27], v[136:139]\n"
    "s_waitcnt lgkmcnt(9)\n"
    "v_mul_f32 v147, v147, v131\n"
    "v_mul_f32 v153, v133, v133\n"
    "v_mfma_f32_16x16x32_bf16 v[136:139], a[228:231], v[28:31], v[136:139]\n"
    "s_waitcnt lgkmcnt(8)\n"
    "ds_read_b128 a[156:159], v237 offset:3072\n"
    "v_mul_f32 v154, v134, v134\n"
    "v_mfma_f32_16x16x32_bf16 v[136:139], a[232:235], v[32:35], v[136:139]\n"
    "s_waitcnt lgkmcnt(8)\n"
    "v_exp_f32 v144, v144\n"
    "v_mfma_f32_16x16x32_bf16 v[136:139], a[236:239], v[36:39], v[136:139]\n"
    "s_waitcnt lgkmcnt(6)\n"
    "ds_read_b128 a[160:163], v237 offset:4096\n"
    "v_mul_f32 v155, v135, v135\n"
    "v_mfma_f32_16x16x32_bf16 v[136:139], a[240:243], v[40:43], v[136:139]\n"
    "buffer_load_dwordx4 a[140:143], v255, s[64:67], s59 offen offset:3072 sc1\n"
    "v_mfma_f32_16x16x32_bf16 v[136:139], a[244:247], v[44:47], v[136:139]\n"
    "s_waitcnt lgkmcnt(6)\n"
    "v_exp_f32 v145, v145\n"
    "v_mfma_f32_16x16x32_bf16 v[136:139], a[248:251], v[48:51], v[136:139]\n"
    "s_waitcnt lgkmcnt(5)\n"
    "ds_read_b128 a[164:167], v237 offset:5120\n"
    "v_fma_f32 v152, v152, s58, v160\n"
    "v_mfma_f32_16x16x32_bf16 v[136:139], a[252:255], v[52:55], v[136:139]\n"
    "v_exp_f32 v146, v146\n"
    "v_mfma_f32_16x16x32_bf16 v[140:143], a[200:203], v[56:59], 0\n"
    "s_lshl_b32 s48, s98, 8\n"
    "v_exp_f32 v147, v147\n"
    "v_mfma_f32_16x16x32_bf16 v[140:143], a[204:207], v[60:63], v[140:143]\n"
    "s_add_u32 s48, s48, 13056\n"
    "ds_read_b128 a[168:171], v237 offset:6144\n"
    "v_fma_f32 v153, v153, s58, v160\n"
    "v_mfma_f32_16x16x32_bf16 v[140:143], a[208:211], v[64:67], v[140:143]\n"
    "buffer_load_dwordx4 v[200:203], v249, s[64:67], s48 offen offset:0 sc1\n"
    "v_add_f32 v144, 1.0, v144\n"
    "v_fma_f32 v154, v154, s58, v160\n"
    "v_mfma_f32_16x16x32_bf16 v[140:143], a[212:215], v[68:71], v[140:143]\n"
    "ds_read_b128 a[172:175], v237 offset:7168\n"
    "v_add_f32 v145, 1.0, v145\n"
    "v_mfma_f32_16x16x32_bf16 v[140:143], a[216:219], v[72:75], v[140:143]\n"
    "v_fma_f32 v155, v155, s58, v160\n"
    "v_add_f32 v146, 1.0, v146\n"
    "v_mfma_f32_16x16x32_bf16 v[140:143], a[220:223], v[76:79], v[140:143]\n"
    "ds_read_b128 a[176:179], v237 offset:8192\n"
    "v_mul_f32 v152, v152, v132\n"
    "v_mfma_f32_16x16x32_bf16 v[140:143], a[224:227], v[80:83], v[140:143]\n"
    "v_mul_f32 v153, v153, v133\n"
    "v_add_f32 v147, 1.0, v147\n"
    "v_mfma_f32_16x16x32_bf16 v[140:143], a[228:231], v[84:87], v[140:143]\n"
    "ds_read_b128 a[180:183], v237 offset:9216\n"
    "v_mul_f32 v154, v154, v134\n"
    "v_mfma_f32_16x16x32_bf16 v[140:143], a[232:235], v[88:91], v[140:143]\n"
    "buffer_load_dwordx4 v[204:207], v249, s[64:67], s48 offen offset:64 sc1\n"
    "v_rcp_f32 v144, v144\n"
    "v_mfma_f32_16x16x32_bf16 v[140:143], a[236:239], v[92:95], v[140:143]\n"
    "ds_read_b128 a[184:187], v237 offset:10240\n"
    "v_mul_f32 v155, v155, v135\n"
    "v_mfma_f32_16x16x32_bf16 v[140:143], a[240:243], v[96:99], v[140:143]\n"
    "v_rcp_f32 v145, v145\n"
    "v_mfma_f32_16x16x32_bf16 v[140:143], a[244:247], v[100:103], v[140:143]\n"
    "v_rcp_f32 v146, v146\n"
    "v_exp_f32 v152, v152\n"
    "v_mfma_f32_16x16x32_bf16 v[140:143], a[248:251], v[104:107], v[140:143]\n"
    "ds_read_b128 a[188:191], v237 offset:11264\n"
    "v_rcp_f32 v147, v147\n"
    "v_exp_f32 v153, v153\n"
    "v_mfma_f32_16x16x32_bf16 v[140:143], a[252:255], v[108:111], v[140:143]\n"
    "s_waitcnt lgkmcnt(10)\n"
    "ds_read_b128 a[192:195], v237 offset:12288\n"
    "v_mul_f32 v144, v144, v128\n"
    "v_mfma_f32_16x16x32_bf16 v[112:115], a[144:147], v[0:3], 0\n"
    "buffer_load_dwordx4 v[208:211], v249, s[64:67], s48 offen offset:128 sc1\n"
    "v_exp_f32 v154, v154\n"
    "v_mfma_f32_16x16x32_bf16 v[112:115], a[148:151], v[4:7], v[112:115]\n"
    "s_waitcnt lgkmcnt(10)\n"
    "v_mul_f32 v145, v145, v129\n"
    "v_exp_f32 v155, v155\n"
    "v_mfma_f32_16x16x32_bf16 v[112:115], a[152:155], v[8:11], v[112:115]\n"
    "s_waitcnt lgkmcnt(9)\n"
    "ds_read_b128 a[196:199], v237 offset:13312\n"
    "v_add_f32 v152, 1.0, v152\n"
    "v_mfma_f32_16x16x32_bf16 v[112:115], a[156:159], v[12:15], v[112:115]\n"
    "s_waitcnt lgkmcnt(9)\n"
    "ds_read_b128 a[200:203], v237 offset:14336\n"
    "v_mul_f32 v146, v146, v130\n"
    "v_mfma_f32_16x16x32_bf16 v[112:115], a[160:163], v[16:19], v[112:115]\n"
    "s_waitcnt lgkmcnt(9)\n"
    "v_mul_f32 v147, v147, v131\n"
    "v_add_f32 v153, 1.0, v153\n"
    "v_mfma_f32_16x16x32_bf16 v[112:115], a[164:167], v[20:23], v[112:115]\n"
    "s_waitcnt lgkmcnt(7)\n"
    "ds_read_b128 a[204:207], v237 offset:15360\n"
    "v_add_f32 v154, 1.0, v154\n"
    "v_mfma_f32_16x16x32_bf16 v[112:115], a[168:171], v[24:27], v[112:115]\n"
    "buffer_load_dwordx4 v[212:215], v249, s[64:67], s48 offen offset:192 sc1\n"
    "v_mul_f32 v144, v144, v136\n"
    "v_mfma_f32_16x16x32_bf16 v[112:115], a[172:175], v[28:31], v[112:115]\n"
    "s_waitcnt lgkmcnt(7)\n"
    "v_mul_f32 v145, v145, v137\n"
    "v_add_f32 v155, 1.0, v155\n"
    "v_mfma_f32_16x16x32_bf16 v[112:115], a[176:179], v[32:35], v[112:115]\n"
    "s_waitcnt lgkmcnt(6)\n"
    "ds_read_b128 a[208:211], v237 offset:16384\n"
    "v_mul_f32 v146, v146, v138\n"
    "v_rcp_f32 v152, v152\n"
    "v_mfma_f32_16x16x32_bf16 v[112:115], a[180:183], v[36:39], v[112:115]\n"
    "s_waitcnt lgkmcnt(6)\n"
    "ds_read_b128 a[212:215], v237 offset:17408\n"
    "v_mul_f32 v147, v147, v139\n"
    "v_mfma_f32_16x16x32_bf16 v[112:115], a[184:187], v[40:43], v[112:115]\n"
    "s_waitcnt lgkmcnt(6)\n"
    "v_cvt_pk_bf16_f32 v186, v144, v145\n"
    "v_rcp_f32 v153, v153\n"
    "v_mfma_f32_16x16x32_bf16 v[112:115], a[188:191], v[44:47], v[112:115]\n"
    "s_waitcnt lgkmcnt(5)\n"
    "v_cvt_pk_bf16_f32 v187, v146, v147\n"
    "v_rcp_f32 v154, v154\n"
    "v_mfma_f32_16x16x32_bf16 v[112:115], a[192:195], v[48:51], v[112:115]\n"
    "s_waitcnt lgkmcnt(4)\n"
    "ds_read_b128 a[216:219], v237 offset:18432\n"
    "v_rcp_f32 v155, v155\n"
    "v_mfma_f32_16x16x32_bf16 v[112:115], a[196:199], v[52:55], v[112:115]\n"
    "buffer_store_dwordx4 v[184:187], v253, s[64:67], s49 offen offset:1024 sc1\n"
    "v_mfma_f32_16x16x32_bf16 v[116:119], a[144:147], v[56:59], 0\n"
    "s_add_u32 s59, s91, 40960\n"
    "ds_read_b128 a[220:223], v237 offset:19456\n"
    "v_mul_f32 v152, v152, v132\n"
    "v_mfma_f32_16x16x32_bf16 v[116:119], a[148:151], v[60:63], v[116:119]\n"
    "s_add_u32 m0, s41, 143360\n"
    "ds_read_b128 a[224:227], v237 offset:20480\n"
    "v_mul_f32 v153, v153, v133\n"
    "v_mfma_f32_16x16x32_bf16 v[116:119], a[152:155], v[64:67], v[116:119]\n"
    "buffer_load_dwordx4 v255, s[64:67], s59 offen offset:0 sc1 lds\n"
    "v_mfma_f32_16x16x32_bf16 v[116:119], a[156:159], v[68:71], v[116:119]\n"
    "s_add_u32 m0, s41, 143360\n"
    "ds_read_b128 a[228:231], v237 offset:21504\n"
    "v_mul_f32 v154, v154, v134\n"
    "v_mfma_f32_16x16x32_bf16 v[116:119], a[160:163], v[72:75], v[116:119]\n"
    "buffer_load_dwordx4 v255, s[64:67], s59 offen offset:1024 sc1 lds\n"
    "v_mul_f32 v155, v155, v135\n"
    "v_mfma_f32_16x16x32_bf16 v[116:119], a[164:167], v[76:79], v[116:119]\n"
    "ds_read_b128 a[232:235], v237 offset:22528\n"
    "v_mul_f32 v152, v152, v140\n"
    "v_mfma_f32_16x16x32_bf16 v[116:119], a[168:171], v[80:83], v[116:119]\n"
    "ds_read_b128 a[236:239], v237 offset:23552\n"
    "v_mul_f32 v153, v153, v141\n"
    "v_mfma_f32_16x16x32_bf16 v[116:119], a[172:175], v[84:87], v[116:119]\n"
    "s_add_u32 m0, s41, 143360\n"
    "v_mul_f32 v144, v112, v112\n"
    "v_mul_f32 v154, v154, v142\n"
    "v_mfma_f32_16x16x32_bf16 v[116:119], a[176:179], v[88:91], v[116:119]\n"
    "buffer_load_dwordx4 v255, s[64:67], s59 offen offset:2048 sc1 lds\n"
    "v_mfma_f32_16x16x32_bf16 v[116:119], a[180:183], v[92:95], v[116:119]\n"
    "v_mul_f32 v145, v113, v113\n"
    "v_mul_f32 v155, v155, v143\n"
    "v_mfma_f32_16x16x32_bf16 v[116:119], a[184:187], v[96:99], v[116:119]\n"
    "ds_read_b128 a[240:243], v237 offset:24576\n"
    "v_mul_f32 v146, v114, v114\n"
    "v_cvt_pk_bf16_f32 v190, v152, v153\n"
    "v_mfma_f32_16x16x32_bf16 v[116:119], a[188:191], v[100:103], v[116:119]\n"
    "s_add_u32 m0, s41, 143360\n"
    "ds_read_b128 a[244:247], v237 offset:25600\n"
    "v_cvt_pk_bf16_f32 v191, v154, v155\n"
    "v_mfma_f32_16x16x32_bf16 v[116:119], a[192:195], v[104:107], v[116:119]\n"
    "buffer_load_dwordx4 v255, s[64:67], s59 offen offset:3072 sc1 lds\n"
    "v_mul_f32 v147, v115, v115\n"
    "v_mfma_f32_16x16x32_bf16 v[116:119], a[196:199], v[108:111], v[116:119]\n"
    "s_waitcnt lgkmcnt(8)\n"
    "ds_read_b128 a[248:251], v237 offset:26624\n"
    "v_fma_f32 v144, v144, s58, v160\n"
    "v_mfma_f32_16x16x32_bf16 v[120:123], a[200:203], v[0:3], 0\n"
    "buffer_store_dwordx4 v[188:191], v253, s[64:67], s51 offen offset:1024 sc1\n"
    "v_mfma_f32_16x16x32_bf16 v[120:123], a[204:207], v[4:7], v[120:123]\n"
    "s_waitcnt vmcnt(24)\n"
    "ds_read_b128 a[252:255], v237 offset:27648\n"
    "v_fma_f32 v145, v145, s58, v160\n"
    "v_mfma_f32_16x16x32_bf16 v[120:123], a[208:211], v[8:11], v[120:123]\n"
    "s_barrier\n"
    "ds_read_b128 a[144:147], v235 offset:0\n"
    "v_fma_f32 v146, v146, s58, v160\n"
    "v_mfma_f32_16x16x32_bf16 v[120:123], a[212:215], v[12:15], v[120:123]\n"
    "s_waitcnt lgkmcnt(8)\n"
    "s_add_u32 s59, s69, 8192\n"
    "v_fma_f32 v147, v147, s58, v160\n"
    "v_mul_f32 v144, v144, v112\n"
    "v_mfma_f32_16x16x32_bf16 v[120:123], a[216:219], v[16:19], v[120:123]\n"
    "s_add_u32 m0, s41, 57344\n"
    "ds_read_b128 a[148:151], v235 offset:1024\n"
    "v_mul_f32 v145, v145, v113\n"
    "v_mfma_f32_16x16x32_bf16 v[120:123], a[220:223], v[20:23], v[120:123]\n"
    "buffer_load_dwordx4 v255, s[64:67], s59 offen offset:0 sc1 lds\n"
    "v_mfma_f32_16x16x32_bf16 v[120:123], a[224:227], v[24:27], v[120:123]\n"
    "s_waitcnt lgkmcnt(8)\n"
    "ds_read_b128 a[152:155], v235 offset:2048\n"
    "v_mul_f32 v146, v146, v114\n"
    "v_mfma_f32_16x16x32_bf16 v[120:123], a[228:231], v[28:31], v[120:123]\n"
    "s_waitcnt lgkmcnt(6)\n"
    "v_mul_f32 v152, v116, v116\n"
    "v_mul_f32 v147, v147, v115\n"
    "v_mfma_f32_16x16x32_bf16 v[120:123], a[232:235], v[32:35], v[120:123]\n"
    "s_add_u32 m0, s41, 57344\n"
    "ds_read_b128 a[156:159], v235 offset:3072\n"
    "v_mul_f32 v153, v117, v117\n"
    "v_mfma_f32_16x16x32_bf16 v[120:123], a[236:239], v[36:39], v[120:123]\n"
    "buffer_load_dwordx4 v255, s[64:67], s59 offen offset:1024 sc1 lds\n"
    "v_mfma_f32_16x16x32_bf16 v[120:123], a[240:243], v[40:43], v[120:123]\n"
    "s_waitcnt lgkmcnt(6)\n"
    "ds_read_b128 a[160:163], v235 offset:4096\n"
    "v_mul_f32 v154, v118, v118\n"
    "v_mfma_f32_16x16x32_bf16 v[120:123], a[244:247], v[44:47], v[120:123]\n"
    "s_waitcnt lgkmcnt(5)\n"
    "v_exp_f32 v144, v144\n"
    "v_mfma_f32_16x16x32_bf16 v[120:123], a[248:251], v[48:51], v[120:123]\n"
    "s_add_u32 m0, s41, 57344\n"
    "v_exp_f32 v145, v145\n"
    "v_mul_f32 v155, v119, v119\n"
    "v_mfma_f32_16x16x32_bf16 v[120:123], a[252:255], v[52:55], v[120:123]\n"
    "buffer_load_dwordx4 v255, s[64:67], s59 offen offset:2048 sc1 lds\n"
    "v_fma_f32 v152, v152, s58, v160\n"
    "v_mfma_f32_16x16x32_bf16 v[124:127], a[200:203], v[56:59], 0\n"
    "ds_read_b128 a[164:167], v235 offset:5120\n"
    "v_exp_f32 v146, v146\n"
    "v_mfma_f32_16x16x32_bf16 v[124:127], a[204:207], v[60:63], v[124:127]\n"
    "v_exp_f32 v147, v147\n"
    "v_fma_f32 v153, v153, s58, v160\n"
    "v_mfma_f32_16x16x32_bf16 v[124:127], a[208:211], v[64:67], v[124:127]\n"
    "s_add_u32 m0, s41, 57344\n"
    "ds_read_b128 a[168:171], v235 offset:6144\n"
    "v_add_f32 v144, 1.0, v144\n"
    "v_fma_f32 v154, v154, s58, v160\n"
    "v_mfma_f32_16x16x32_bf16 v[124:127], a[212:215], v[68:71], v[124:127]\n"
    "buffer_load_dwordx4 v255, s[64:67], s59 offen offset:3072 sc1 lds\n"
    "v_mfma_f32_16x16x32_bf16 v[124:127], a[216:219], v[72:75], v[124:127]\n"
    "ds_read_b128 a[172:175], v235 offset:7168\n"
    "v_add_f32 v145, 1.0, v145\n"
    "v_fma_f32 v155, v155, s58, v160\n"
    "v_mfma_f32_16x16x32_bf16 v[124:127], a[220:223], v[76:79], v[124:127]\n"
    "v_add_f32 v146, 1.0, v146\n"
    "v_mul_f32 v152, v152, v116\n"
    "v_mfma_f32_16x16x32_bf16 v[124:127], a[224:227], v[80:83], v[124:127]\n"
    "ds_read_b128 a[176:179], v235 offset:8192\n"
    "v_mul_f32 v153, v153, v117\n"
    "v_mfma_f32_16x16x32_bf16 v[124:127], a[228:231], v[84:87], v[124:127]\n"
    "s_add_u32 s59, s69, 24576\n"
    "ds_read_b128 a[180:183], v235 offset:9216\n"
    "v_add_f32 v147, 1.0, v147\n"
    "v_mfma_f32_16x16x32_bf16 v[124:127], a[232:235], v[88:91], v[124:127]\n"
    "s_add_u32 m0, s41, 73728\n"
    "v_rcp_f32 v144, v144\n"
    "v_mul_f32 v154, v154, v118\n"
    "v_mfma_f32_16x16x32_bf16 v[124:127], a[236:239], v[92:95], v[124:127]\n"
    "buffer_load_dwordx4 v255, s[64:67], s59 offen offset:0 sc1 lds\n"
    "v_mul_f32 v155, v155, v119\n"
    "v_mfma_f32_16x16x32_bf16 v[124:127], a[240:243], v[96:99], v[124:127]\n"
    "ds_read_b128 a[184:187], v235 offset:10240\n"
    "v_rcp_f32 v145, v145\n"
    "v_mfma_f32_16x16x32_bf16 v[124:127], a[244:247], v[100:103], v[124:127]\n"
    "s_waitcnt lgkmcnt(10)\n"
    "v_rcp_f32 v146, v146\n"
    "v_exp_f32 v152, v152\n"
    "v_mfma_f32_16x16x32_bf16 v[124:127], a[248:251], v[104:107], v[124:127]\n"
    "s_add_u32 m0, s41, 73728\n"
    "ds_read_b128 a[188:191], v235 offset:11264\n"
    "v_rcp_f32 v147, v147\n"
    "v_exp_f32 v153, v153\n"
    "v_mfma_f32_16x16x32_bf16 v[124:127], a[252:255], v[108:111], v[124:127]\n"
    "buffer_load_dwordx4 v255, s[64:67], s59 offen offset:1024 sc1 lds\n"
    "v_mfma_f32_16x16x32_bf16 v[128:131], a[144:147], v[0:3], 0\n"
    "s_waitcnt lgkmcnt(10)\n"
    "ds_read_b128 a[192:195], v235 offset:12288\n"
    "v_mul_f32 v144, v144, v112\n"
    "v_exp_f32 v154, v154\n"
    "v_mfma_f32_16x16x32_bf16 v[128:131], a[148:151], v[4:7], v[128:131]\n"
    "s_waitcnt lgkmcnt(8)\n"
    "v_mul_f32 v145, v145, v113\n"
    "v_exp_f32 v155, v155\n"
    "v_mfma_f32_16x16x32_bf16 v[128:131], a[152:155], v[8:11], v[128:131]\n"
    "s_add_u32 m0, s41, 73728\n"
    "ds_read_b128 a[196:199], v235 offset:13312\n"
    "v_add_f32 v152, 1.0, v152\n"
    "v_mfma_f32_16x16x32_bf16 v[128:131], a[156:159], v[12:15], v[128:131]\n"
    "buffer_load_dwordx4 v255, s[64:67], s59 offen offset:2048 sc1 lds\n"
    "v_mul_f32 v146, v146, v114\n"
    "v_mfma_f32_16x16x32_bf16 v[128:131], a[160:163], v[16:19], v[128:131]\n"
    "s_waitcnt lgkmcnt(8)\n"
    "ds_read_b128 a[200:203], v235 offset:14336\n"
    "v_mul_f32 v147, v147, v115\n"
    "v_add_f32 v153, 1.0, v153\n"
    "v_mfma_f32_16x16x32_bf16 v[128:131], a[164:167], v[20:23], v[128:131]\n"
    "s_waitcnt lgkmcnt(6)\n"
    "ds_read_b128 a[204:207], v235 offset:15360\n"
    "v_add_f32 v154, 1.0, v154\n"
    "v_mfma_f32_16x16x32_bf16 v[128:131], a[168:171], v[24:27], v[128:131]\n"
    "s_add_u32 m0, s41, 73728\n"
    "v_mul_f32 v144, v144, v120\n"
    "v_mul_f32 v145, v145, v121\n"
    "v_mfma_f32_16x16x32_bf16 v[128:131], a[172:175], v[28:31], v[128:131]\n"
    "buffer_load_dwordx4 v255, s[64:67], s59 offen offset:3072 sc1 lds\n"
    "v_add_f32 v155, 1.0, v155\n"
    "v_mfma_f32_16x16x32_bf16 v[128:131], a[176:179], v[32:35], v[128:131]\n"
    "s_waitcnt lgkmcnt(6)\n"
    "ds_read_b128 a[208:211], v235 offset:16384\n"
    "v_mul_f32 v146, v146, v122\n"
    "v_rcp_f32 v152, v152\n"
    "v_mfma_f32_16x16x32_bf16 v[128:131], a[180:183], v[36:39], v[128:131]\n"
    "s_waitcnt lgkmcnt(6)\n"
    "ds_read_b128 a[212:215], v235 offset:17408\n"
    "v_mul_f32 v147, v147, v123\n"
    "v_mfma_f32_16x16x32_bf16 v[128:131], a[184:187], v[40:43], v[128:131]\n"
    "s_waitcnt lgkmcnt(6)\n"
    "v_cvt_pk_bf16_f32 v176, v144, v145\n"
    "v_rcp_f32 v153, v153\n"
    "v_mfma_f32_16x16x32_bf16 v[128:131], a[188:191], v[44:47], v[128:131]\n"
    "s_waitcnt lgkmcnt(5)\n"
    "v_cvt_pk_bf16_f32 v177, v146, v147\n"
    "v_rcp_f32 v154, v154\n"
    "v_mfma_f32_16x16x32_bf16 v[128:131], a[192:195], v[48:51], v[128:131]\n"
    "s_waitcnt lgkmcnt(4)\n"
    "ds_read_b128 a[216:219], v235 offset:18432\n"
    "v_rcp_f32 v155, v155\n"
    "v_mfma_f32_16x16x32_bf16 v[128:131], a[196:199], v[52:55], v[128:131]\n"
    "buffer_store_dwordx2 v[176:177], v254, s[64:67], s49 offen sc1\n"
    "v_mfma_f32_16x16x32_bf16 v[132:135], a[144:147], v[56:59], 0\n"
    "s_add_u32 s59, s91, 36864\n"
    "ds_read_b128 a[220:223], v235 offset:19456\n"
    "v_mul_f32 v152, v152, v116\n"
    "v_mfma_f32_16x16x32_bf16 v[132:135], a[148:151], v[60:63], v[132:135]\n"
    "s_add_u32 m0, s41, 90112\n"
    "ds_read_b128 a[224:227], v235 offset:20480\n"
    "v_mul_f32 v153, v153, v117\n"
    "v_mfma_f32_16x16x32_bf16 v[132:135], a[152:155], v[64:67], v[132:135]\n"
    "buffer_load_dwordx4 v255, s[64:67], s59 offen offset:0 sc1 lds\n"
    "v_mfma_f32_16x16x32_bf16 v[132:135], a[156:159], v[68:71], v[132:135]\n"
    "s_add_u32 m0, s41, 90112\n"
    "ds_read_b128 a[228:231], v235 offset:21504\n"
    "v_mul_f32 v154, v154, v118\n"
    "v_mfma_f32_16x16x32_bf16 v[132:135], a[160:163], v[72:75], v[132:135]\n"
    "buffer_load_dwordx4 v255, s[64:67], s59 offen offset:1024 sc1 lds\n"
    "v_mul_f32 v155, v155, v119\n"
    "v_mfma_f32_16x16x32_bf16 v[132:135], a[164:167], v[76:79], v[132:135]\n"
    "ds_read_b128 a[232:235], v235 offset:22528\n"
    "v_mul_f32 v152, v152, v124\n"
    "v_mfma_f32_16x16x32_bf16 v[132:135], a[168:171], v[80:83], v[132:135]\n"
    "s_add_u32 m0, s41, 90112\n"
    "ds_read_b128 a[236:239], v235 offset:23552\n"
    "v_mul_f32 v153, v153, v125\n"
    "v_mfma_f32_16x16x32_bf16 v[132:135], a[172:175], v[84:87], v[132:135]\n"
    "buffer_load_dwordx4 v255, s[64:67], s59 offen offset:2048 sc1 lds\n"
    "v_mfma_f32_16x16x32_bf16 v[132:135], a[176:179], v[88:91], v[132:135]\n"
    "v_mul_f32 v144, v128, v128\n"
    "v_mul_f32 v154, v154, v126\n"
    "v_mfma_f32_16x16x32_bf16 v[132:135], a[180:183], v[92:95], v[132:135]\n"
    "v_mul_f32 v145, v129, v129\n"
    "v_mul_f32 v155, v155, v127\n"
    "v_mfma_f32_16x16x32_bf16 v[132:135], a[184:187], v[96:99], v[132:135]\n"
    "s_add_u32 m0, s41, 90112\n"
    "ds_read_b128 a[240:243], v235 offset:24576\n"
    "v_mul_f32 v146, v130, v130\n"
    "v_cvt_pk_bf16_f32 v180, v152, v153\n"
    "v_mfma_f32_16x16x32_bf16 v[132:135], a[188:191], v[100:103], v[132:135]\n"
    "buffer_load_dwordx4 v255, s[64:67], s59 offen offset:3072 sc1 lds\n"
    "v_cvt_pk_bf16_f32 v181, v154, v155\n"
    "v_mfma_f32_16x16x32_bf16 v[132:135], a[192:195], v[104:107], v[132:135]\n"
    "s_waitcnt lgkmcnt(10)\n"
    "ds_read_b128 a[244:247], v235 offset:25600\n"
    "v_mul_f32 v147, v131, v131\n"
    "v_mfma_f32_16x16x32_bf16 v[132:135], a[196:199], v[108:111], v[132:135]\n"
    "buffer_store_dwordx2 v[180:181], v254, s[64:67], s51 offen sc1\n"
    "v_fma_f32 v144, v144, s58, v160\n"
    "v_mfma_f32_16x16x32_bf16 v[136:139], a[200:203], v[0:3], 0\n"
    "s_waitcnt lgkmcnt(7)\n"
    "ds_read_b128 a[248:251], v235 offset:26624\n"
    "v_fma_f32 v145, v145, s58, v160\n"
    "v_mfma_f32_16x16x32_bf16 v[136:139], a[204:207], v[4:7], v[136:139]\n"
    "s_waitcnt vmcnt(30)\n"
    "ds_read_b128 a[252:255], v235 offset:27648\n"
    "v_fma_f32 v146, v146, s58, v160\n"
    "v_mfma_f32_16x16x32_bf16 v[136:139], a[208:211], v[8:11], v[136:139]\n"
    "s_barrier\n"
    "ds_read_b128 a[144:147], v235 offset:28672\n"
    "v_fma_f32 v147, v147, s58, v160\n"
    "v_mfma_f32_16x16x32_bf16 v[136:139], a[212:215], v[12:15], v[136:139]\n"
    "s_add_u32 s59, s91, 53248\n"
    "ds_read_b128 a[148:151], v235 offset:29696\n"
    "v_mul_f32 v144, v144, v128\n"
    "v_mfma_f32_16x16x32_bf16 v[136:139], a[216:219], v[16:19], v[136:139]\n"
    "s_waitcnt lgkmcnt(9)\n"
    "s_add_u32 m0, s41, 106496\n"
    "ds_read_b128 a[152:155], v235 offset:30720\n"
    "v_mul_f32 v145, v145, v129\n"
    "v_mfma_f32_16x16x32_bf16 v[136:139], a[220:223], v[20:23], v[136:139]\n"
    "buffer_load_dwordx4 v255, s[64:67], s59 offen offset:0 sc1 lds\n"
    "v_mfma_f32_16x16x32_bf16 v[136:139], a[224:227], v[24:27], v[136:139]\n"
    "s_waitcnt lgkmcnt(9)\n"
    "v_mul_f32 v146, v146, v130\n"
    "v_mul_f32 v152, v132, v132\n"
    "v_mfma_f32_16x16x32_bf16 v[136:139], a[228:231], v[28:31], v[136:139]\n"
    "s_waitcnt lgkmcnt(6)\n"
    "v_mul_f32 v147, v147, v131\n"
    "v_mul_f32 v153, v133, v133\n"
    "v_mfma_f32_16x16x32_bf16 v[136:139], a[232:235], v[32:35], v[136:139]\n"
    "s_add_u32 m0, s41, 106496\n"
    "ds_read_b128 a[156:159], v235 offset:31744\n"
    "v_mul_f32 v154, v134, v134\n"
    "v_mfma_f32_16x16x32_bf16 v[136:139], a[236:239], v[36:39], v[136:139]\n"
    "buffer_load_dwordx4 v255, s[64:67], s59 offen offset:1024 sc1 lds\n"
    "v_mfma_f32_16x16x32_bf16 v[136:139], a[240:243], v[40:43], v[136:139]\n"
    "s_waitcnt lgkmcnt(6)\n"
    "v_exp_f32 v144, v144\n"
    "v_mfma_f32_16x16x32_bf16 v[136:139], a[244:247], v[44:47], v[136:139]\n"
    "s_waitcnt lgkmcnt(4)\n"
    "ds_read_b128 a[160:163], v235 offset:32768\n"
    "v_mul_f32 v155, v135, v135\n"
    "v_mfma_f32_16x16x32_bf16 v[136:139], a[248:251], v[48:51], v[136:139]\n"
    "s_add_u32 m0, s41, 106496\n"
    "v_exp_f32 v145, v145\n"
    "v_mfma_f32_16x16x32_bf16 v[136:139], a[252:255], v[52:55], v[136:139]\n"
    "buffer_load_dwordx4 v255, s[64:67], s59 offen offset:2048 sc1 lds\n"
    "v_fma_f32 v152, v152, s58, v160\n"
    "v_mfma_f32_16x16x32_bf16 v[140:143], a[200:203], v[56:59], 0\n"
    "ds_read_b128 a[164:167], v235 offset:33792\n"
    "v_exp_f32 v146, v146\n"
    "v_mfma_f32_16x16x32_bf16 v[140:143], a[204:207], v[60:63], v[140:143]\n"
    "v_exp_f32 v147, v147\n"
    "v_fma_f32 v153, v153, s58, v160\n"
    "v_mfma_f32_16x16x32_bf16 v[140:143], a[208:211], v[64:67], v[140:143]\n"
    "s_add_u32 m0, s41, 106496\n"
    "ds_read_b128 a[168:171], v235 offset:34816\n"
    "v_add_f32 v144, 1.0, v144\n"
    "v_fma_f32 v154, v154, s58, v160\n"
    "v_mfma_f32_16x16x32_bf16 v[140:143], a[212:215], v[68:71], v[140:143]\n"
    "buffer_load_dwordx4 v255, s[64:67], s59 offen offset:3072 sc1 lds\n"
    "v_mfma_f32_16x16x32_bf16 v[140:143], a[216:219], v[72:75], v[140:143]\n"
    "ds_read_b128 a[172:175], v235 offset:35840\n"
    "v_add_f32 v145, 1.0, v145\n"
    "v_fma_f32 v155, v155, s58, v160\n"
    "v_mfma_f32_16x16x32_bf16 v[140:143], a[220:223], v[76:79], v[140:143]\n"
    "v_add_f32 v146, 1.0, v146\n"
    "v_mul_f32 v152, v152, v132\n"
    "v_mfma_f32_16x16x32_bf16 v[140:143], a[224:227], v[80:83], v[140:143]\n"
    "ds_read_b128 a[176:179], v235 offset:36864\n"
    "v_mul_f32 v153, v153, v133\n"
    "v_mfma_f32_16x16x32_bf16 v[140:143], a[228:231], v[84:87], v[140:143]\n"
    "ds_read_b128 a[180:183], v235 offset:37888\n"
    "v_add_f32 v147, 1.0, v147\n"
    "v_mfma_f32_16x16x32_bf16 v[140:143], a[232:235], v[88:91], v[140:143]\n"
    "v_rcp_f32 v144, v144\n"
    "v_mul_f32 v154, v154, v134\n"
    "v_mfma_f32_16x16x32_bf16 v[140:143], a[236:239], v[92:95], v[140:143]\n"
    "ds_read_b128 a[184:187], v235 offset:38912\n"
    "v_mul_f32 v155, v155, v135\n"
    "v_mfma_f32_16x16x32_bf16 v[140:143], a[240:243], v[96:99], v[140:143]\n"
    "v_rcp_f32 v145, v145\n"
    "v_mfma_f32_16x16x32_bf16 v[140:143], a[244:247], v[100:103], v[140:143]\n"
    "v_rcp_f32 v146, v146\n"
    "v_exp_f32 v152, v152\n"
    "v_mfma_f32_16x16x32_bf16 v[140:143], a[248:251], v[104:107], v[140:143]\n"
    "ds_read_b128 a[188:191], v235 offset:39936\n"
    "v_rcp_f32 v147, v147\n"
    "v_exp_f32 v153, v153\n"
    "v_mfma_f32_16x16x32_bf16 v[140:143], a[252:255], v[108:111], v[140:143]\n"
    "s_waitcnt lgkmcnt(11)\n"
    "ds_read_b128 a[192:195], v235 offset:40960\n"
    "v_mul_f32 v144, v144, v128\n"
    "v_mfma_f32_16x16x32_bf16 v[112:115], a[144:147], v[0:3], 0\n"
    "s_waitcnt lgkmcnt(11)\n"
    "v_exp_f32 v154, v154\n"
    "v_mfma_f32_16x16x32_bf16 v[112:115], a[148:151], v[4:7], v[112:115]\n"
    "s_waitcnt lgkmcnt(10)\n"
    "v_mul_f32 v145, v145, v129\n"
    "v_exp_f32 v155, v155\n"
    "v_mfma_f32_16x16x32_bf16 v[112:115], a[152:155], v[8:11], v[112:115]\n"
    "s_waitcnt lgkmcnt(9)\n"
    "ds_read_b128 a[196:199], v235 offset:41984\n"
    "v_add_f32 v152, 1.0, v152\n"
    "v_mfma_f32_16x16x32_bf16 v[112:115], a[156:159], v[12:15], v[112:115]\n"
    "s_waitcnt lgkmcnt(7)\n"
    "ds_read_b128 a[200:203], v235 offset:43008\n"
    "v_mul_f32 v146, v146, v130\n"
    "v_mfma_f32_16x16x32_bf16 v[112:115], a[160:163], v[16:19], v[112:115]\n"
    "s_waitcnt vmcnt(4)\n"
    "v_mul_f32 v147, v147, v131\n"
    "v_add_f32 v153, 1.0, v153\n"
    "v_mfma_f32_16x16x32_bf16 v[112:115], a[164:167], v[20:23], v[112:115]\n"
    "buffer_atomic_swap v192, v194, s[64:67], s39 offen sc1\n"
    "v_add_f32 v154, 1.0, v154\n"
    "v_mfma_f32_16x16x32_bf16 v[112:115], a[168:171], v[24:27], v[112:115]\n"
    "s_waitcnt lgkmcnt(7)\n"
    "ds_read_b128 a[204:207], v235 offset:44032\n"
    "v_mul_f32 v144, v144, v136\n"
    "v_mfma_f32_16x16x32_bf16 v[112:115], a[172:175], v[28:31], v[112:115]\n"
    "s_waitcnt lgkmcnt(7)\n"
    "v_mul_f32 v145, v145, v137\n"
    "v_add_f32 v155, 1.0, v155\n"
    "v_mfma_f32_16x16x32_bf16 v[112:115], a[176:179], v[32:35], v[112:115]\n"
    "s_waitcnt lgkmcnt(6)\n"
    "ds_read_b128 a[208:211], v235 offset:45056\n"
    "v_mul_f32 v146, v146, v138\n"
    "v_rcp_f32 v152, v152\n"
    "v_mfma_f32_16x16x32_bf16 v[112:115], a[180:183], v[36:39], v[112:115]\n"
    "s_waitcnt lgkmcnt(6)\n"
    "ds_read_b128 a[212:215], v235 offset:46080\n"
    "v_mul_f32 v147, v147, v139\n"
    "v_mfma_f32_16x16x32_bf16 v[112:115], a[184:187], v[40:43], v[112:115]\n"
    "s_waitcnt lgkmcnt(6)\n"
    "v_cvt_pk_bf16_f32 v184, v144, v145\n"
    "v_rcp_f32 v153, v153\n"
    "v_mfma_f32_16x16x32_bf16 v[112:115], a[188:191], v[44:47], v[112:115]\n"
    "s_waitcnt lgkmcnt(5)\n"
    "v_cvt_pk_bf16_f32 v185, v146, v147\n"
    "v_rcp_f32 v154, v154\n"
    "v_mfma_f32_16x16x32_bf16 v[112:115], a[192:195], v[48:51], v[112:115]\n"
    "s_waitcnt lgkmcnt(4)\n"
    "ds_read_b128 a[216:219], v235 offset:47104\n"
    "v_rcp_f32 v155, v155\n"
    "v_mfma_f32_16x16x32_bf16 v[112:115], a[196:199], v[52:55], v[112:115]\n"
    "ds_read_b128 a[220:223], v235 offset:48128\n"
    "v_mul_f32 v152, v152, v132\n"
    "v_mfma_f32_16x16x32_bf16 v[116:119], a[144:147], v[56:59], 0\n"
    "ds_read_b128 a[224:227], v235 offset:49152\n"
    "v_mul_f32 v153, v153, v133\n"
    "v_mfma_f32_16x16x32_bf16 v[116:119], a[148:151], v[60:63], v[116:119]\n"
    "ds_read_b128 a[228:231], v235 offset:50176\n"
    "v_mul_f32 v154, v154, v134\n"
    "v_mfma_f32_16x16x32_bf16 v[116:119], a[152:155], v[64:67], v[116:119]\n"
    "ds_read_b128 a[232:235], v235 offset:51200\n"
    "v_mul_f32 v155, v155, v135\n"
    "v_mfma_f32_16x16x32_bf16 v[116:119], a[156:159], v[68:71], v[116:119]\n"
    "v_mul_f32 v152, v152, v140\n"
    "v_mul_f32 v153, v153, v141\n"
    "v_mfma_f32_16x16x32_bf16 v[116:119], a[160:163], v[72:75], v[116:119]\n"
    "ds_read_b128 a[236:239], v235 offset:52224\n"
    "v_mul_f32 v144, v112, v112\n"
    "v_mfma_f32_16x16x32_bf16 v[116:119], a[164:167], v[76:79], v[116:119]\n"
    "v_mul_f32 v154, v154, v142\n"
    "v_mul_f32 v145, v113, v113\n"
    "v_mfma_f32_16x16x32_bf16 v[116:119], a[168:171], v[80:83], v[116:119]\n"
    "ds_read_b128 a[240:243], v235 offset:53248\n"
    "v_mul_f32 v155, v155, v143\n"
    "v_mfma_f32_16x16x32_bf16 v[116:119], a[172:175], v[84:87], v[116:119]\n"
    "v_mul_f32 v146, v114, v114\n"
    "v_cvt_pk_bf16_f32 v188, v152, v153\n"
    "v_mfma_f32_16x16x32_bf16 v[116:119], a[176:179], v[88:91], v[116:119]\n"
    "v_add_u32_e64 v192, s53, 1\n"
    "v_cvt_pk_bf16_f32 v189, v154, v155\n"
    "v_mfma_f32_16x16x32_bf16 v[116:119], a[180:183], v[92:95], v[116:119]\n"
    "ds_read_b128 a[244:247], v235 offset:54272\n"
    "v_mul_f32 v147, v115, v115\n"
    "v_mfma_f32_16x16x32_bf16 v[116:119], a[184:187], v[96:99], v[116:119]\n"
    "ds_read_b128 a[248:251], v235 offset:55296\n"
    "v_fma_f32 v144, v144, s58, v160\n"
    "v_mfma_f32_16x16x32_bf16 v[116:119], a[188:191], v[100:103], v[116:119]\n"
    "ds_read_b128 a[252:255], v235 offset:56320\n"
    "v_fma_f32 v145, v145, s58, v160\n"
    "v_mfma_f32_16x16x32_bf16 v[116:119], a[192:195], v[104:107], v[116:119]\n"
    "v_fma_f32 v146, v146, s58, v160\n"
    "v_fma_f32 v147, v147, s58, v160\n"
    "v_mfma_f32_16x16x32_bf16 v[116:119], a[196:199], v[108:111], v[116:119]\n"
    "s_waitcnt lgkmcnt(13)\n"
    "v_mul_f32 v144, v144, v112\n"
    "v_mul_f32 v145, v145, v113\n"
    "v_mfma_f32_16x16x32_bf16 v[120:123], a[200:203], v[0:3], 0\n"
    "s_waitcnt lgkmcnt(12)\n"
    "v_mul_f32 v146, v146, v114\n"
    "v_mul_f32 v147, v147, v115\n"
    "v_mfma_f32_16x16x32_bf16 v[120:123], a[204:207], v[4:7], v[120:123]\n"
    "s_waitcnt lgkmcnt(11)\n"
    "v_exp_f32 v144, v144\n"
    "v_mfma_f32_16x16x32_bf16 v[120:123], a[208:211], v[8:11], v[120:123]\n"
    "s_waitcnt lgkmcnt(10)\n"
    "v_exp_f32 v145, v145\n"
    "v_mfma_f32_16x16x32_bf16 v[120:123], a[212:215], v[12:15], v[120:123]\n"
    "s_waitcnt lgkmcnt(9)\n"
    "v_exp_f32 v146, v146\n"
    "v_mfma_f32_16x16x32_bf16 v[120:123], a[216:219], v[16:19], v[120:123]\n"
    "s_waitcnt lgkmcnt(8)\n"
    "v_mul_f32 v152, v116, v116\n"
    "v_mul_f32 v153, v117, v117\n"
    "v_mfma_f32_16x16x32_bf16 v[120:123], a[220:223], v[20:23], v[120:123]\n"
    "s_waitcnt lgkmcnt(7)\n"
    "v_mul_f32 v154, v118, v118\n"
    "v_mul_f32 v155, v119, v119\n"
    "v_mfma_f32_16x16x32_bf16 v[120:123], a[224:227], v[24:27], v[120:123]\n"
    "s_waitcnt lgkmcnt(6)\n"
    "v_fma_f32 v152, v152, s58, v160\n"
    "v_fma_f32 v153, v153, s58, v160\n"
    "v_mfma_f32_16x16x32_bf16 v[120:123], a[228:231], v[28:31], v[120:123]\n"
    "s_waitcnt lgkmcnt(5)\n"
    "v_exp_f32 v147, v147\n"
    "v_mfma_f32_16x16x32_bf16 v[120:123], a[232:235], v[32:35], v[120:123]\n"
    "s_waitcnt lgkmcnt(4)\n"
    "v_add_f32 v144, 1.0, v144\n"
    "v_fma_f32 v154, v154, s58, v160\n"
    "v_mfma_f32_16x16x32_bf16 v[120:123], a[236:239], v[36:39], v[120:123]\n"
    "s_waitcnt lgkmcnt(3)\n"
    "v_add_f32 v145, 1.0, v145\n"
    "v_fma_f32 v155, v155, s58, v160\n"
    "v_mfma_f32_16x16x32_bf16 v[120:123], a[240:243], v[40:43], v[120:123]\n"
    "s_waitcnt lgkmcnt(2)\n"
    "v_add_f32 v146, 1.0, v146\n"
    "v_mul_f32 v152, v152, v116\n"
    "v_mfma_f32_16x16x32_bf16 v[120:123], a[244:247], v[44:47], v[120:123]\n"
    "s_waitcnt lgkmcnt(0)\n"
    "v_mul_f32 v153, v153, v117\n"
    "v_add_f32 v147, 1.0, v147\n"
    "v_mfma_f32_16x16x32_bf16 v[120:123], a[248:251], v[48:51], v[120:123]\n"
    "buffer_atomic_add v196, v195, s[64:67], s40 offen sc0 sc1\n"
    "v_mfma_f32_16x16x32_bf16 v[120:123], a[252:255], v[52:55], v[120:123]\n"
    "v_rcp_f32 v144, v144\n"
    "v_mfma_f32_16x16x32_bf16 v[124:127], a[200:203], v[56:59], 0\n"
    "v_mul_f32 v154, v154, v118\n"
    "v_mul_f32 v155, v155, v119\n"
    "v_mfma_f32_16x16x32_bf16 v[124:127], a[204:207], v[60:63], v[124:127]\n"
    "v_rcp_f32 v145, v145\n"
    "v_mfma_f32_16x16x32_bf16 v[124:127], a[208:211], v[64:67], v[124:127]\n"
    "v_rcp_f32 v146, v146\n"
    "v_mfma_f32_16x16x32_bf16 v[124:127], a[212:215], v[68:71], v[124:127]\n"
    "v_exp_f32 v152, v152\n"
    "v_mfma_f32_16x16x32_bf16 v[124:127], a[216:219], v[72:75], v[124:127]\n"
    "v_rcp_f32 v147, v147\n"
    "v_mfma_f32_16x16x32_bf16 v[124:127], a[220:223], v[76:79], v[124:127]\n"
    "v_exp_f32 v153, v153\n"
    "v_mfma_f32_16x16x32_bf16 v[124:127], a[224:227], v[80:83], v[124:127]\n"
    "v_mul_f32 v144, v144, v112\n"
    "v_mul_f32 v145, v145, v113\n"
    "v_mfma_f32_16x16x32_bf16 v[124:127], a[228:231], v[84:87], v[124:127]\n"
    "v_mul_f32 v146, v146, v114\n"
    "v_mul_f32 v147, v147, v115\n"
    "v_mfma_f32_16x16x32_bf16 v[124:127], a[232:235], v[88:91], v[124:127]\n"
    "v_mul_f32 v144, v144, v120\n"
    "v_mul_f32 v145, v145, v121\n"
    "v_mfma_f32_16x16x32_bf16 v[124:127], a[236:239], v[92:95], v[124:127]\n"
    "v_mul_f32 v146, v146, v122\n"
    "v_mul_f32 v147, v147, v123\n"
    "v_mfma_f32_16x16x32_bf16 v[124:127], a[240:243], v[96:99], v[124:127]\n"
    "v_cvt_pk_bf16_f32 v186, v144, v145\n"
    "v_cvt_pk_bf16_f32 v187, v146, v147\n"
    "v_mfma_f32_16x16x32_bf16 v[124:127], a[244:247], v[100:103], v[124:127]\n"
    "v_exp_f32 v154, v154\n"
    "v_mfma_f32_16x16x32_bf16 v[124:127], a[248:251], v[104:107], v[124:127]\n"
    "v_exp_f32 v155, v155\n"
    "v_mfma_f32_16x16x32_bf16 v[124:127], a[252:255], v[108:111], v[124:127]\n"
    "v_add_f32 v152, 1.0, v152\n"
    "v_add_f32 v153, 1.0, v153\n"
    "v_add_f32 v154, 1.0, v154\n"
    "v_add_f32 v155, 1.0, v155\n"
    "v_rcp_f32 v152, v152\n"
    "v_rcp_f32 v153, v153\n"
    "v_rcp_f32 v154, v154\n"
    "v_rcp_f32 v155, v155\n"
    "v_mul_f32 v152, v152, v116\n"
    "v_mul_f32 v153, v153, v117\n"
    "v_mul_f32 v154, v154, v118\n"
    "v_mul_f32 v155, v155, v119\n"
    "buffer_store_dwordx4 v[184:187], v253, s[64:67], s49 offen offset:2048 sc1\n"
    "v_mul_f32 v152, v152, v124\n"
    "v_mul_f32 v153, v153, v125\n"
    "v_mul_f32 v154, v154, v126\n"
    "v_mul_f32 v155, v155, v127\n"
    "v_cvt_pk_bf16_f32 v190, v152, v153\n"
    "v_cvt_pk_bf16_f32 v191, v154, v155\n"
    "buffer_store_dwordx4 v[188:191], v253, s[64:67], s51 offen offset:2048 sc1\n"
    "s_barrier\n"
    "s_cmp_eq_u32 s98, 7\n"
    "s_cbranch_scc1 L_idle_%=\n"
    "s_waitcnt vmcnt(2)\n"
    "v_subrev_u32 v196, s53, v196\n"
    "v_cmp_lt_u32 vcc, 2, v196\n"
    "v_mov_b32 v196, 0\n"
    "s_and_b32 vcc_lo, vcc_lo, 255\n"
    "s_cbranch_scc0 L_fd16_%=\n"
    "s_nop 0\n"
    "s_waitcnt vmcnt(0)\n"
    "buffer_atomic_swap v192, v194, s[64:67], s39 offen sc1\n"
    "L_fw15_%=:\n"
    "s_sub_u32 s99, s99, 1\n"
    "s_cbranch_scc1 L_fd16_%=\n"
    "buffer_atomic_add v196, v195, s[64:67], s40 offen sc0 sc1\n"
    "s_waitcnt vmcnt(0)\n"
    "v_subrev_u32 v196, s53, v196\n"
    "v_cmp_lt_u32 vcc, 2, v196\n"
    "v_mov_b32 v196, 0\n"
    "s_and_b32 vcc_lo, vcc_lo, 255\n"
    "s_cbranch_scc1 L_fw15_%=\n"
    "L_fd16_%=:\n"
    "s_max_i32 s99, s99, 0\n"
    "s_add_u32 s54, s36, 0\n"
    "buffer_load_dwordx4 v[0:3], v224, s[64:67], s54 offen sc1\n"
    "buffer_load_dwordx4 v[4:7], v225, s[64:67], s54 offen sc1\n"
    "buffer_load_dwordx4 v[8:11], v226, s[64:67], s54 offen sc1\n"
    "buffer_load_dwordx4 v[12:15], v227, s[64:67], s54 offen sc1\n"
    "s_add_u32 s54, s36, 1024\n"
    "buffer_load_dwordx4 v[16:19], v224, s[64:67], s54 offen sc1\n"
    "buffer_load_dwordx4 v[20:23], v225, s[64:67], s54 offen sc1\n"
    "buffer_load_dwordx4 v[24:27], v226, s[64:67], s54 offen sc1\n"
    "buffer_load_dwordx4 v[28:31], v227, s[64:67], s54 offen sc1\n"
    "s_waitcnt vmcnt(8)\n"
    "buffer_atomic_swap v192, v194, s[64:67], s39 offen sc1\n"
    "s_add_u32 s54, s36, 4096\n"
    "buffer_load_dwordx4 v[32:35], v224, s[64:67], s54 offen sc1\n"
    "buffer_load_dwordx4 v[36:39], v225, s[64:67], s54 offen sc1\n"
    "buffer_load_dwordx4 v[40:43], v226, s[64:67], s54 offen sc1\n"
    "buffer_load_dwordx4 v[44:47], v227, s[64:67], s54 offen sc1\n"
    "s_add_u32 s54, s36, 5120\n"
    "buffer_load_dwordx4 v[48:51], v224, s[64:67], s54 offen sc1\n"
    "buffer_load_dwordx4 v[52:55], v225, s[64:67], s54 offen sc1\n"
    "buffer_load_dwordx4 v[56:59], v226, s[64:67], s54 offen sc1\n"
    "buffer_load_dwordx4 v[60:63], v227, s[64:67], s54 offen sc1\n"
    "ds_read_b128 a[144:147], v247 offset:0\n"
    "ds_read_b128 a[148:151], v247 offset:1024\n"
    "ds_read_b128 a[152:155], v247 offset:2048\n"
    "ds_read_b128 a[156:159], v247 offset:3072\n"
    "ds_read_b128 a[160:163], v246 offset:0\n"
    "ds_read_b128 a[164:167], v246 offset:1024\n"
    "ds_read_b128 a[168:171], v246 offset:2048\n"
    "ds_read_b128 a[172:175], v246 offset:3072\n"
    "ds_read_b128 a[176:179], v246 offset:16384\n"
    "ds_read_b128 a[180:183], v246 offset:17408\n"
    "ds_read_b128 a[184:187], v246 offset:18432\n"
    "ds_read_b128 a[188:191], v246 offset:19456\n"
    "ds_read_b128 a[192:195], v246 offset:32768\n"
    "ds_read_b128 a[196:199], v246 offset:33792\n"
    "ds_read_b128 a[200:203], v246 offset:34816\n"
    "ds_read_b128 a[204:207], v246 offset:35840\n"
    "ds_read_b128 a[208:211], v246 offset:49152\n"
    "ds_read_b128 a[212:215], v246 offset:50176\n"
    "ds_read_b128 a[216:219], v246 offset:51200\n"
    "ds_read_b128 a[220:223], v246 offset:52224\n"
    "L_gr17_%=:\n"
    "v_xor_b32 v198, s53, v201\n"
    "v_xor_b32 v199, s53, v203\n"
    "v_or_b32 v198, v198, v199\n"
    "v_xor_b32 v199, s53, v205\n"
    "v_or_b32 v198, v198, v199\n"
    "v_xor_b32 v199, s53, v207\n"
    "v_or_b32 v198, v198, v199\n"
    "v_xor_b32 v199, s53, v209\n"
    "v_or_b32 v198, v198, v199\n"
    "v_xor_b32 v199, s53, v211\n"
    "v_or_b32 v198, v198, v199\n"
    "v_xor_b32 v199, s53, v213\n"
    "v_or_b32 v198, v198, v199\n"
    "v_xor_b32 v199, s53, v215\n"
    "v_or_b32 v198, v198, v199\n"
    "v_cmp_ne_u32 vcc, 0, v198\n"
    "s_nop 1\n"
    "s_cbranch_vccz L_grd18_%=\n"
    "s_sub_u32 s99, s99, 1\n"
    "s_cbranch_scc1 L_grd18_%=\n"
    "s_nop 0\n"
    "s_lshl_b32 s48, s98, 8\n"
    "s_add_u32 s48, s48, 13056\n"
    "buffer_load_dwordx4 v[200:203], v249, s[64:67], s48 offen offset:0 sc1\n"
    "buffer_load_dwordx4 v[204:207], v249, s[64:67], s48 offen offset:64 sc1\n"
    "buffer_load_dwordx4 v[208:211], v249, s[64:67], s48 offen offset:128 sc1\n"
    "buffer_load_dwordx4 v[212:215], v249, s[64:67], s48 offen offset:192 sc1\n"
    "s_waitcnt vmcnt(0)\n"
    "s_branch L_gr17_%=\n"
    "L_grd18_%=:\n"
    "s_max_i32 s99, s99, 0\n"
    "v_and_b32 v201, 0xffff0000, v200\n"
    "v_lshlrev_b32 v200, 16, v200\n"
    "v_and_b32 v203, 0xffff0000, v202\n"
    "v_lshlrev_b32 v202, 16, v202\n"
    "v_and_b32 v205, 0xffff0000, v204\n"
    "v_lshlrev_b32 v204, 16, v204\n"
    "v_and_b32 v207, 0xffff0000, v206\n"
    "v_lshlrev_b32 v206, 16, v206\n"
    "v_and_b32 v209, 0xffff0000, v208\n"
    "v_lshlrev_b32 v208, 16, v208\n"
    "v_and_b32 v211, 0xffff0000, v210\n"
    "v_lshlrev_b32 v210, 16, v210\n"
    "v_and_b32 v213, 0xffff0000, v212\n"
    "v_lshlrev_b32 v212, 16, v212\n"
    "v_and_b32 v215, 0xffff0000, v214\n"
    "v_lshlrev_b32 v214, 16, v214\n"
    "s_waitcnt vmcnt(16)\n"
    "v_mfma_f32_16x16x32_bf16 v[128:131], a[0:3], v[0:3], 0\n"
    "s_add_u32 s54, s36, 3072\n"
    "v_mfma_f32_16x16x32_bf16 v[132:135], a[4:7], v[0:3], 0\n"
    "buffer_load_dwordx2 v[64:65], v228, s[64:67], s54 offen sc1\n"
    "v_mfma_f32_16x16x32_bf16 v[136:139], a[8:11], v[0:3], 0\n"
    "v_mfma_f32_16x16x32_bf16 v[140:143], a[12:15], v[0:3], 0\n"
    "s_waitcnt vmcnt(16)\n"
    "v_mfma_f32_16x16x32_bf16 v[144:147], a[0:3], v[4:7], 0\n"
    "buffer_load_dwordx2 v[66:67], v228, s[64:67], s54 offen offset:512 sc1\n"
    "v_mfma_f32_16x16x32_bf16 v[148:151], a[4:7], v[4:7], 0\n"
    "v_mfma_f32_16x16x32_bf16 v[152:155], a[8:11], v[4:7], 0\n"
    "v_mfma_f32_16x16x32_bf16 v[156:159], a[12:15], v[4:7], 0\n"
    "s_waitcnt vmcnt(16)\n"
    "v_mfma_f32_16x16x32_bf16 v[160:163], a[0:3], v[8:11], 0\n"
    "buffer_load_dwordx2 v[68:69], v229, s[64:67], s54 offen sc1\n"
    "v_mfma_f32_16x16x32_bf16 v[164:167], a[4:7], v[8:11], 0\n"
    "v_mfma_f32_16x16x32_bf16 v[168:171], a[8:11], v[8:11], 0\n"
    "buffer_load_dwordx2 v[70:71], v229, s[64:67], s54 offen offset:512 sc1\n"
    "v_mfma_f32_16x16x32_bf16 v[172:175], a[12:15], v[8:11], 0\n"
    "s_waitcnt vmcnt(17)\n"
    "v_mfma_f32_16x16x32_bf16 v[176:179], a[0:3], v[12:15], 0\n"
    "v_mfma_f32_16x16x32_bf16 v[180:183], a[4:7], v[12:15], 0\n"
    "buffer_load_dwordx2 v[72:73], v230, s[64:67], s54 offen sc1\n"
    "v_mfma_f32_16x16x32_bf16 v[184:187], a[8:11], v[12:15], 0\n"
    "v_mfma_f32_16x16x32_bf16 v[188:191], a[12:15], v[12:15], 0\n"
    "s_waitcnt vmcnt(17)\n"
    "v_mfma_f32_16x16x32_bf16 v[128:131], a[16:19], v[16:19], v[128:131]\n"
    "v_mfma_f32_16x16x32_bf16 v[132:135], a[20:23], v[16:19], v[132:135]\n"
    "buffer_load_dwordx2 v[74:75], v230, s[64:67], s54 offen offset:512 sc1\n"
    "v_mfma_f32_16x16x32_bf16 v[136:139], a[24:27], v[16:19], v[136:139]\n"
    "v_mfma_f32_16x16x32_bf16 v[140:143], a[28:31], v[16:19], v[140:143]\n"
    "s_waitcnt vmcnt(17)\n"
    "v_mfma_f32_16x16x32_bf16 v[144:147], a[16:19], v[20:23], v[144:147]\n"
    "buffer_load_dwordx2 v[76:77], v231, s[64:67], s54 offen sc1\n"
    "v_mfma_f32_16x16x32_bf16 v[148:151], a[20:23], v[20:23], v[148:151]\n"
    "v_mfma_f32_16x16x32_bf16 v[152:155], a[24:27], v[20:23], v[152:155]\n"
    "v_mfma_f32_16x16x32_bf16 v[156:159], a[28:31], v[20:23], v[156:159]\n"
    "s_waitcnt vmcnt(17)\n"
    "v_mfma_f32_16x16x32_bf16 v[160:163], a[16:19], v[24:27], v[160:163]\n"
    "buffer_load_dwordx2 v[78:79], v231, s[64:67], s54 offen offset:512 sc1\n"
    "v_mfma_f32_16x16x32_bf16 v[164:167], a[20:23], v[24:27], v[164:167]\n"
    "s_add_u32 s54, s37, 0\n"
    "v_mfma_f32_16x16x32_bf16 v[168:171], a[24:27], v[24:27], v[168:171]\n"
    "buffer_load_dwordx4 v[80:83], v224, s[64:67], s54 offen sc1\n"
    "v_mfma_f32_16x16x32_bf16 v[172:175], a[28:31], v[24:27], v[172:175]\n"
    "s_waitcnt vmcnt(18)\n"
    "v_mfma_f32_16x16x32_bf16 v[176:179], a[16:19], v[28:31], v[176:179]\n"
    "v_mfma_f32_16x16x32_bf16 v[180:183], a[20:23], v[28:31], v[180:183]\n"
    "buffer_load_dwordx4 v[84:87], v225, s[64:67], s54 offen sc1\n"
    "v_mfma_f32_16x16x32_bf16 v[184:187], a[24:27], v[28:31], v[184:187]\n"
    "v_mfma_f32_16x16x32_bf16 v[188:191], a[28:31], v[28:31], v[188:191]\n"
    "s_waitcnt vmcnt(17)\n"
    "v_mfma_f32_16x16x32_bf16 v[128:131], a[32:35], v[32:35], v[128:131]\n"
    "v_mfma_f32_16x16x32_bf16 v[132:135], a[36:39], v[32:35], v[132:135]\n"
    "buffer_load_dwordx4 v[88:91], v226, s[64:67], s54 offen sc1\n"
    "v_mfma_f32_16x16x32_bf16 v[136:139], a[40:43], v[32:35], v[136:139]\n"
    "v_mfma_f32_16x16x32_bf16 v[140:143], a[44:47], v[32:35], v[140:143]\n"
    "s_waitcnt vmcnt(17)\n"
    "v_mfma_f32_16x16x32_bf16 v[144:147], a[32:35], v[36:39], v[144:147]\n"
    "buffer_load_dwordx4 v[92:95], v227, s[64:67], s54 offen sc1\n"
    "v_mfma_f32_16x16x32_bf16 v[148:151], a[36:39], v[36:39], v[148:151]\n"
    "v_mfma_f32_16x16x32_bf16 v[152:155], a[40:43], v[36:39], v[152:155]\n"
    "v_mfma_f32_16x16x32_bf16 v[156:159], a[44:47], v[36:39], v[156:159]\n"
    "s_waitcnt vmcnt(17)\n"
    "s_add_u32 s54, s37, 1024\n"
    "v_mfma_f32_16x16x32_bf16 v[160:163], a[32:35], v[40:43], v[160:163]\n"
    "buffer_load_dwordx4 v[96:99], v224, s[64:67], s54 offen sc1\n"
    "v_mfma_f32_16x16x32_bf16 v[164:167], a[36:39], v[40:43], v[164:167]\n"
    "v_mfma_f32_16x16x32_bf16 v[168:171], a[40:43], v[40:43], v[168:171]\n"
    "buffer_load_dwordx4 v[100:103], v225, s[64:67], s54 offen sc1\n"
    "v_mfma_f32_16x16x32_bf16 v[172:175], a[44:47], v[40:43], v[172:175]\n"
    "s_waitcnt vmcnt(18)\n"
    "v_mfma_f32_16x16x32_bf16 v[176:179], a[32:35], v[44:47], v[176:179]\n"
    "v_mfma_f32_16x16x32_bf16 v[180:183], a[36:39], v[44:47], v[180:183]\n"
    "buffer_atomic_add v196, v195, s[64:67], s40 offen sc0 sc1\n"
    "v_mfma_f32_16x16x32_bf16 v[184:187], a[40:43], v[44:47], v[184:187]\n"
    "v_mfma_f32_16x16x32_bf16 v[188:191], a[44:47], v[44:47], v[188:191]\n"
    "s_waitcnt vmcnt(18)\n"
    "v_mfma_f32_16x16x32_bf16 v[128:131], a[48:51], v[48:51], v[128:131]\n"
    "buffer_load_dwordx4 v[104:107], v226, s[64:67], s54 offen sc1\n"
    "v_mfma_f32_16x16x32_bf16 v[132:135], a[52:55], v[48:51], v[132:135]\n"
    "v_mfma_f32_16x16x32_bf16 v[136:139], a[56:59], v[48:51], v[136:139]\n"
    "v_mfma_f32_16x16x32_bf16 v[140:143], a[60:63], v[48:51], v[140:143]\n"
    "s_waitcnt vmcnt(18)\n"
    "v_mfma_f32_16x16x32_bf16 v[144:147], a[48:51], v[52:55], v[144:147]\n"
    "buffer_load_dwordx4 v[108:111], v227, s[64:67], s54 offen sc1\n"
    "v_mfma_f32_16x16x32_bf16 v[148:151], a[52:55], v[52:55], v[148:151]\n"
    "s_add_u32 s54, s37, 4096\n"
    "v_mfma_f32_16x16x32_bf16 v[152:155], a[56:59], v[52:55], v[152:155]\n"
    "buffer_load_dwordx4 v[112:115], v224, s[64:67], s54 offen sc1\n"
    "v_mfma_f32_16x16x32_bf16 v[156:159], a[60:63], v[52:55], v[156:159]\n"
    "s_waitcnt vmcnt(19)\n"
    "v_mfma_f32_16x16x32_bf16 v[160:163], a[48:51], v[56:59], v[160:163]\n"
    "v_mfma_f32_16x16x32_bf16 v[164:167], a[52:55], v[56:59], v[164:167]\n"
    "buffer_load_dwordx4 v[116:119], v225, s[64:67], s54 offen sc1\n"
    "v_mfma_f32_16x16x32_bf16 v[168:171], a[56:59], v[56:59], v[168:171]\n"
    "v_mfma_f32_16x16x32_bf16 v[172:175], a[60:63], v[56:59], v[172:175]\n"
    "s_waitcnt vmcnt(19)\n"
    "v_mfma_f32_16x16x32_bf16 v[176:179], a[48:51], v[60:63], v[176:179]\n"
    "buffer_load_dwordx4 v[120:123], v226, s[64:67], s54 offen sc1\n"
    "v_mfma_f32_16x16x32_bf16 v[180:183], a[52:55], v[60:63], v[180:183]\n"
    "v_mfma_f32_16x16x32_bf16 v[184:187], a[56:59], v[60:63], v[184:187]\n"
    "v_mfma_f32_16x16x32_bf16 v[188:191], a[60:63], v[60:63], v[188:191]\n"
    "s_waitcnt vmcnt(18)\n"
    "v_mfma_f32_16x16x32_bf16 v[128:131], a[64:67], v[64:67], v[128:131]\n"
    "buffer_load_dwordx4 v[124:127], v227, s[64:67], s54 offen sc1\n"
    "v_mfma_f32_16x16x32_bf16 v[132:135], a[68:71], v[64:67], v[132:135]\n"
    "v_mfma_f32_16x16x32_bf16 v[136:139], a[72:75], v[64:67], v[136:139]\n"
    "v_mfma_f32_16x16x32_bf16 v[140:143], a[76:79], v[64:67], v[140:143]\n"
    "s_waitcnt vmcnt(17)\n"
    "s_add_u32 s54, s37, 5120\n"
    "v_mfma_f32_16x16x32_bf16 v[144:147], a[64:67], v[68:71], v[144:147]\n"
    "buffer_load_dwordx4 v[0:3], v224, s[64:67], s54 offen sc1\n"
    "v_mfma_f32_16x16x32_bf16 v[148:151], a[68:71], v[68:71], v[148:151]\n"
    "v_mfma_f32_16x16x32_bf16 v[152:155], a[72:75], v[68:71], v[152:155]\n"
    "buffer_load_dwordx4 v[4:7], v225, s[64:67], s54 offen sc1\n"
    "v_mfma_f32_16x16x32_bf16 v[156:159], a[76:79], v[68:71], v[156:159]\n"
    "s_waitcnt vmcnt(17)\n"
    "v_mfma_f32_16x16x32_bf16 v[160:163], a[64:67], v[72:75], v[160:163]\n"
    "v_mfma_f32_16x16x32_bf16 v[164:167], a[68:71], v[72:75], v[164:167]\n"
    "buffer_load_dwordx4 v[8:11], v226, s[64:67], s54 offen sc1\n"
    "v_mfma_f32_16x16x32_bf16 v[168:171], a[72:75], v[72:75], v[168:171]\n"
    "v_mfma_f32_16x16x32_bf16 v[172:175], a[76:79], v[72:75], v[172:175]\n"
    "s_waitcnt vmcnt(16)\n"
    "v_mfma_f32_16x16x32_bf16 v[176:179], a[64:67], v[76:79], v[176:179]\n"
    "buffer_load_dwordx4 v[12:15], v227, s[64:67], s54 offen sc1\n"
    "v_mfma_f32_16x16x32_bf16 v[180:183], a[68:71], v[76:79], v[180:183]\n"
    "v_mfma_f32_16x16x32_bf16 v[184:187], a[72:75], v[76:79], v[184:187]\n"
    "v_mfma_f32_16x16x32_bf16 v[188:191], a[76:79], v[76:79], v[188:191]\n"
    "s_waitcnt vmcnt(16)\n"
    "s_add_u32 s54, s37, 3072\n"
    "v_mfma_f32_16x16x32_bf16 v[128:131], a[80:83], v[80:83], v[128:131]\n"
    "buffer_load_dwordx2 v[16:17], v228, s[64:67], s54 offen sc1\n"
    "v_mfma_f32_16x16x32_bf16 v[132:135], a[84:87], v[80:83], v[132:135]\n"
    "v_mfma_f32_16x16x32_bf16 v[136:139], a[88:91], v[80:83], v[136:139]\n"
    "v_mfma_f32_16x16x32_bf16 v[140:143], a[92:95], v[80:83], v[140:143]\n"
    "s_waitcnt vmcnt(16)\n"
    "v_mfma_f32_16x16x32_bf16 v[144:147], a[80:83], v[84:87], v[144:147]\n"
    "buffer_load_dwordx2 v[18:19], v228, s[64:67], s54 offen offset:512 sc1\n"
    "v_mfma_f32_16x16x32_bf16 v[148:151], a[84:87], v[84:87], v[148:151]\n"
    "v_mfma_f32_16x16x32_bf16 v[152:155], a[88:91], v[84:87], v[152:155]\n"
    "buffer_load_dwordx2 v[20:21], v229, s[64:67], s54 offen sc1\n"
    "v_mfma_f32_16x16x32_bf16 v[156:159], a[92:95], v[84:87], v[156:159]\n"
    "s_waitcnt vmcnt(17)\n"
    "v_mfma_f32_16x16x32_bf16 v[160:163], a[80:83], v[88:91], v[160:163]\n"
    "v_mfma_f32_16x16x32_bf16 v[164:167], a[84:87], v[88:91], v[164:167]\n"
    "buffer_load_dwordx2 v[22:23], v229, s[64:67], s54 offen offset:512 sc1\n"
    "v_mfma_f32_16x16x32_bf16 v[168:171], a[88:91], v[88:91], v[168:171]\n"
    "v_mfma_f32_16x16x32_bf16 v[172:175], a[92:95], v[88:91], v[172:175]\n"
    "s_waitcnt vmcnt(17)\n"
    "v_mfma_f32_16x16x32_bf16 v[176:179], a[80:83], v[92:95], v[176:179]\n"
    "buffer_load_dwordx2 v[24:25], v230, s[64:67], s54 offen sc1\n"
    "v_mfma_f32_16x16x32_bf16 v[180:183], a[84:87], v[92:95], v[180:183]\n"
    "v_mfma_f32_16x16x32_bf16 v[184:187], a[88:91], v[92:95], v[184:187]\n"
    "s_waitcnt vmcnt(15)\n"
    "v_subrev_u32 v196, s53, v196\n"
    "v_subrev_u32 v196, 1, v196\n"
    "v_cmp_lt_u32 vcc, 1, v196\n"
    "v_mov_b32 v196, 0\n"
    "s_and_b32 vcc_lo, vcc_lo, 255\n"
    "s_cbranch_scc0 L_fd20_%=\n"
    "s_nop 0\n"
    "L_fw19_%=:\n"
    "s_sub_u32 s99, s99, 1\n"
    "s_cbranch_scc1 L_fd20_%=\n"
    "buffer_atomic_add v196, v195, s[64:67], s40 offen sc0 sc1\n"
    "s_waitcnt vmcnt(0)\n"
    "v_subrev_u32 v196, s53, v196\n"
    "v_subrev_u32 v196, 1, v196\n"
    "v_cmp_lt_u32 vcc, 1, v196\n"
    "v_mov_b32 v196, 0\n"
    "s_and_b32 vcc_lo, vcc_lo, 255\n"
    "s_cbranch_scc1 L_fw19_%=\n"
    "L_fd20_%=:\n"
    "s_max_i32 s99, s99, 0\n"
    "v_mfma_f32_16x16x32_bf16 v[188:191], a[92:95], v[92:95], v[188:191]\n"
    "s_waitcnt vmcnt(17)\n"
    "v_mfma_f32_16x16x32_bf16 v[128:131], a[96:99], v[96:99], v[128:131]\n"
    "buffer_load_dwordx2 v[26:27], v230, s[64:67], s54 offen offset:512 sc1\n"
    "v_mfma_f32_16x16x32_bf16 v[132:135], a[100:103], v[96:99], v[132:135]\n"
    "v_mfma_f32_16x16x32_bf16 v[136:139], a[104:107], v[96:99], v[136:139]\n"
    "v_mfma_f32_16x16x32_bf16 v[140:143], a[108:111], v[96:99], v[140:143]\n"
    "s_waitcnt vmcnt(17)\n"
    "v_mfma_f32_16x16x32_bf16 v[144:147], a[96:99], v[100:103], v[144:147]\n"
    "buffer_load_dwordx2 v[28:29], v231, s[64:67], s54 offen sc1\n"
    "v_mfma_f32_16x16x32_bf16 v[148:151], a[100:103], v[100:103], v[148:151]\n"
    "v_mfma_f32_16x16x32_bf16 v[152:155], a[104:107], v[100:103], v[152:155]\n"
    "buffer_load_dwordx2 v[30:31], v231, s[64:67], s54 offen offset:512 sc1\n"
    "v_mfma_f32_16x16x32_bf16 v[156:159], a[108:111], v[100:103], v[156:159]\n"
    "s_waitcnt vmcnt(17)\n"
    "v_mfma_f32_16x16x32_bf16 v[160:163], a[96:99], v[104:107], v[160:163]\n"
    "s_add_u32 s54, s36, 2048\n"
    "v_mfma_f32_16x16x32_bf16 v[164:167], a[100:103], v[104:107], v[164:167]\n"
    "buffer_load_dwordx4 v[32:35], v224, s[64:67], s54 offen sc1\n"
    "v_mfma_f32_16x16x32_bf16 v[168:171], a[104:107], v[104:107], v[168:171]\n"
    "v_mfma_f32_16x16x32_bf16 v[172:175], a[108:111], v[104:107], v[172:175]\n"
    "s_waitcnt vmcnt(17)\n"
    "v_mfma_f32_16x16x32_bf16 v[176:179], a[96:99], v[108:111], v[176:179]\n"
    "buffer_load_dwordx4 v[36:39], v225, s[64:67], s54 offen sc1\n"
    "v_mfma_f32_16x16x32_bf16 v[180:183], a[100:103], v[108:111], v[180:183]\n"
    "v_mfma_f32_16x16x32_bf16 v[184:187], a[104:107], v[108:111], v[184:187]\n"
    "v_mfma_f32_16x16x32_bf16 v[188:191], a[108:111], v[108:111], v[188:191]\n"
    "s_waitcnt vmcnt(17)\n"
    "v_mfma_f32_16x16x32_bf16 v[128:131], a[112:115], v[112:115], v[128:131]\n"
    "buffer_load_dwordx4 v[40:43], v226, s[64:67], s54 offen sc1\n"
    "v_mfma_f32_16x16x32_bf16 v[132:135], a[116:119], v[112:115], v[132:135]\n"
    "v_mfma_f32_16x16x32_bf16 v[136:139], a[120:123], v[112:115], v[136:139]\n"
    "v_mfma_f32_16x16x32_bf16 v[140:143], a[124:127], v[112:115], v[140:143]\n"
    "s_waitcnt vmcnt(17)\n"
    "v_mfma_f32_16x16x32_bf16 v[144:147], a[112:115], v[116:119], v[144:147]\n"
    "buffer_load_dwordx4 v[44:47], v227, s[64:67], s54 offen sc1\n"
    "v_mfma_f32_16x16x32_bf16 v[148:151], a[116:119], v[116:119], v[148:151]\n"
    "v_mfma_f32_16x16x32_bf16 v[152:155], a[120:123], v[116:119], v[152:155]\n"
    "buffer_load_dwordx4 v[216:219], v244, s[64:67], s38 offen offset:0 sc1\n"
    "v_mfma_f32_16x16x32_bf16 v[156:159], a[124:127], v[116:119], v[156:159]\n"
    "s_waitcnt vmcnt(18)\n"
    "v_mfma_f32_16x16x32_bf16 v[160:163], a[112:115], v[120:123], v[160:163]\n"
    "v_mfma_f32_16x16x32_bf16 v[164:167], a[116:119], v[120:123], v[164:167]\n"
    "buffer_load_dwordx4 v[220:223], v244, s[64:67], s38 offen offset:1024 sc1\n"
    "v_mfma_f32_16x16x32_bf16 v[168:171], a[120:123], v[120:123], v[168:171]\n"
    "v_mfma_f32_16x16x32_bf16 v[172:175], a[124:127], v[120:123], v[172:175]\n"
    "s_waitcnt vmcnt(18)\n"
    "s_add_u32 s54, s36, 6144\n"
    "v_mfma_f32_16x16x32_bf16 v[176:179], a[112:115], v[124:127], v[176:179]\n"
    "buffer_load_dwordx4 v[52:55], v225, s[64:67], s54 offen sc1\n"
    "v_mfma_f32_16x16x32_bf16 v[180:183], a[116:119], v[124:127], v[180:183]\n"
    "v_mfma_f32_16x16x32_bf16 v[184:187], a[120:123], v[124:127], v[184:187]\n"
    "v_mfma_f32_16x16x32_bf16 v[188:191], a[124:127], v[124:127], v[188:191]\n"
    "s_waitcnt vmcnt(18)\n"
    "s_add_u32 s54, s37, 2048\n"
    "v_mfma_f32_16x16x32_bf16 v[128:131], a[128:131], v[0:3], v[128:131]\n"
    "buffer_load_dwordx4 v[68:71], v225, s[64:67], s54 offen sc1\n"
    "v_mfma_f32_16x16x32_bf16 v[132:135], a[132:135], v[0:3], v[132:135]\n"
    "v_mfma_f32_16x16x32_bf16 v[136:139], a[136:139], v[0:3], v[136:139]\n"
    "v_mfma_f32_16x16x32_bf16 v[140:143], a[140:143], v[0:3], v[140:143]\n"
    "s_waitcnt vmcnt(18)\n"
    "s_add_u32 s54, s37, 6144\n"
    "v_mfma_f32_16x16x32_bf16 v[144:147], a[128:131], v[4:7], v[144:147]\n"
    "buffer_load_dwordx4 v[84:87], v225, s[64:67], s54 offen sc1\n"
    "v_mfma_f32_16x16x32_bf16 v[148:151], a[132:135], v[4:7], v[148:151]\n"
    "s_add_u32 s54, s36, 6144\n"
    "v_mfma_f32_16x16x32_bf16 v[152:155], a[136:139], v[4:7], v[152:155]\n"
    "buffer_load_dwordx4 v[56:59], v226, s[64:67], s54 offen sc1\n"
    "v_mfma_f32_16x16x32_bf16 v[156:159], a[140:143], v[4:7], v[156:159]\n"
    "s_waitcnt vmcnt(19)\n"
    "v_mfma_f32_16x16x32_bf16 v[160:163], a[128:131], v[8:11], v[160:163]\n"
    "s_add_u32 s54, s37, 2048\n"
    "v_mfma_f32_16x16x32_bf16 v[164:167], a[132:135], v[8:11], v[164:167]\n"
    "buffer_load_dwordx4 v[72:75], v226, s[64:67], s54 offen sc1\n"
    "v_mfma_f32_16x16x32_bf16 v[168:171], a[136:139], v[8:11], v[168:171]\n"
    "v_mfma_f32_16x16x32_bf16 v[172:175], a[140:143], v[8:11], v[172:175]\n"
    "s_waitcnt vmcnt(19)\n"
    "s_add_u32 s54, s37, 6144\n"
    "v_mfma_f32_16x16x32_bf16 v[176:179], a[128:131], v[12:15], v[176:179]\n"
    "buffer_load_dwordx4 v[88:91], v226, s[64:67], s54 offen sc1\n"
    "v_mfma_f32_16x16x32_bf16 v[180:183], a[132:135], v[12:15], v[180:183]\n"
    "v_mfma_f32_16x16x32_bf16 v[184:187], a[136:139], v[12:15], v[184:187]\n"
    "v_mfma_f32_16x16x32_bf16 v[188:191], a[140:143], v[12:15], v[188:191]\n"
    "s_waitcnt lgkmcnt(14)\n"
    "s_waitcnt vmcnt(18)\n"
    "s_add_u32 s54, s36, 6144\n"
    "v_mfma_f32_16x16x32_bf16 v[128:131], a[144:147], v[16:19], v[128:131]\n"
    "buffer_load_dwordx4 v[60:63], v227, s[64:67], s54 offen sc1\n"
    "v_mfma_f32_16x16x32_bf16 v[132:135], a[148:151], v[16:19], v[132:135]\n"
    "s_waitcnt lgkmcnt(14)\n"
    "v_mfma_f32_16x16x32_bf16 v[136:139], a[152:155], v[16:19], v[136:139]\n"
    "s_waitcnt lgkmcnt(14)\n"
    "v_mfma_f32_16x16x32_bf16 v[140:143], a[156:159], v[16:19], v[140:143]\n"
    "s_waitcnt vmcnt(17)\n"
    "s_add_u32 s54, s37, 2048\n"
    "v_mfma_f32_16x16x32_bf16 v[144:147], a[144:147], v[20:23], v[144:147]\n"
    "buffer_load_dwordx4 v[76:79], v227, s[64:67], s54 offen sc1\n"
    "v_mfma_f32_16x16x32_bf16 v[148:151], a[148:151], v[20:23], v[148:151]\n"
    "s_add_u32 s54, s37, 6144\n"
    "v_mfma_f32_16x16x32_bf16 v[152:155], a[152:155], v[20:23], v[152:155]\n"
    "buffer_load_dwordx4 v[92:95], v227, s[64:67], s54 offen sc1\n"
    "v_mfma_f32_16x16x32_bf16 v[156:159], a[156:159], v[20:23], v[156:159]\n"
    "s_waitcnt vmcnt(17)\n"
    "v_mfma_f32_16x16x32_bf16 v[160:163], a[144:147], v[24:27], v[160:163]\n"
    "s_add_u32 s54, s36, 6144\n"
    "v_mfma_f32_16x16x32_bf16 v[164:167], a[148:151], v[24:27], v[164:167]\n"
    "buffer_load_dwordx4 v[48:51], v224, s[64:67], s54 offen sc1\n"
    "v_mfma_f32_16x16x32_bf16 v[168:171], a[152:155], v[24:27], v[168:171]\n"
    "v_mfma_f32_16x16x32_bf16 v[172:175], a[156:159], v[24:27], v[172:175]\n"
    "s_waitcnt vmcnt(16)\n"
    "s_add_u32 s54, s37, 2048\n"
    "v_mfma_f32_16x16x32_bf16 v[176:179], a[144:147], v[28:31], v[176:179]\n"
    "buffer_load_dwordx4 v[64:67], v224, s[64:67], s54 offen sc1\n"
    "v_mfma_f32_16x16x32_bf16 v[180:183], a[148:151], v[28:31], v[180:183]\n"
    "v_mfma_f32_16x16x32_bf16 v[184:187], a[152:155], v[28:31], v[184:187]\n"
    "v_mfma_f32_16x16x32_bf16 v[188:191], a[156:159], v[28:31], v[188:191]\n"
    "s_waitcnt lgkmcnt(14)\n"
    "s_waitcnt vmcnt(16)\n"
    "s_add_u32 s54, s37, 6144\n"
    "v_mfma_f32_16x16x32_bf16 v[128:131], a[160:163], v[32:35], v[128:131]\n"
    "buffer_load_dwordx4 v[80:83], v224, s[64:67], s54 offen sc1\n"
    "v_mfma_f32_16x16x32_bf16 v[132:135], a[164:167], v[32:35], v[132:135]\n"
    "s_waitcnt lgkmcnt(13)\n"
    "v_mfma_f32_16x16x32_bf16 v[136:139], a[168:171], v[32:35], v[136:139]\n"
    "s_waitcnt lgkmcnt(12)\n"
    "v_mfma_f32_16x16x32_bf16 v[140:143], a[172:175], v[32:35], v[140:143]\n"
    "s_waitcnt vmcnt(16)\n"
    "v_mfma_f32_16x16x32_bf16 v[144:147], a[160:163], v[36:39], v[144:147]\n"
    "v_mfma_f32_16x16x32_bf16 v[148:151], a[164:167], v[36:39], v[148:151]\n"
    "v_mfma_f32_16x16x32_bf16 v[152:155], a[168:171], v[36:39], v[152:155]\n"
    "v_mfma_f32_16x16x32_bf16 v[156:159], a[172:175], v[36:39], v[156:159]\n"
    "s_waitcnt vmcnt(15)\n"
    "v_mfma_f32_16x16x32_bf16 v[160:163], a[160:163], v[40:43], v[160:163]\n"
    "v_mfma_f32_16x16x32_bf16 v[164:167], a[164:167], v[40:43], v[164:167]\n"
    "v_mfma_f32_16x16x32_bf16 v[168:171], a[168:171], v[40:43], v[168:171]\n"
    "v_mfma_f32_16x16x32_bf16 v[172:175], a[172:175], v[40:43], v[172:175]\n"
    "s_waitcnt vmcnt(14)\n"
    "v_mfma_f32_16x16x32_bf16 v[176:179], a[160:163], v[44:47], v[176:179]\n"
    "v_mfma_f32_16x16x32_bf16 v[180:183], a[164:167], v[44:47], v[180:183]\n"
    "v_mfma_f32_16x16x32_bf16 v[184:187], a[168:171], v[44:47], v[184:187]\n"
    "s_waitcnt vmcnt(12)\n"
    "v_mfma_f32_16x16x32_bf16 v[188:191], a[172:175], v[44:47], v[188:191]\n"
    "s_waitcnt lgkmcnt(11)\n"
    "s_waitcnt vmcnt(11)\n"
    "v_mfma_f32_16x16x32_bf16 v[144:147], a[176:179], v[52:55], v[144:147]\n"
    "s_waitcnt lgkmcnt(10)\n"
    "v_lshlrev_b32 v112, 16, v216\n"
    "v_and_b32 v113, 0xffff0000, v216\n"
    "v_mfma_f32_16x16x32_bf16 v[148:151], a[180:183], v[52:55], v[148:151]\n"
    "s_waitcnt lgkmcnt(9)\n"
    "v_lshlrev_b32 v114, 16, v217\n"
    "v_and_b32 v115, 0xffff0000, v217\n"
    "v_mfma_f32_16x16x32_bf16 v[152:155], a[184:187], v[52:55], v[152:155]\n"
    "s_waitcnt lgkmcnt(7)\n"
    "v_lshlrev_b32 v116, 16, v218\n"
    "v_and_b32 v117, 0xffff0000, v218\n"
    "v_mfma_f32_16x16x32_bf16 v[156:159], a[188:191], v[52:55], v[156:159]\n"
    "s_waitcnt vmcnt(10)\n"
    "v_lshlrev_b32 v118, 16, v219\n"
    "v_and_b32 v119, 0xffff0000, v219\n"
    "v_mfma_f32_16x16x32_bf16 v[144:147], a[192:195], v[68:71], v[144:147]\n"
    "s_waitcnt lgkmcnt(6)\n"
    "v_lshlrev_b32 v120, 16, v220\n"
    "v_and_b32 v121, 0xffff0000, v220\n"
    "v_mfma_f32_16x16x32_bf16 v[148:151], a[196:199], v[68:71], v[148:151]\n"
    "s_waitcnt lgkmcnt(5)\n"
    "v_lshlrev_b32 v122, 16, v221\n"
    "v_and_b32 v123, 0xffff0000, v221\n"
    "v_mfma_f32_16x16x32_bf16 v[152:155], a[200:203], v[68:71], v[152:155]\n"
    "s_waitcnt lgkmcnt(3)\n"
    "v_lshlrev_b32 v124, 16, v222\n"
    "v_and_b32 v125, 0xffff0000, v222\n"
    "v_mfma_f32_16x16x32_bf16 v[156:159], a[204:207], v[68:71], v[156:159]\n"
    "s_waitcnt vmcnt(9)\n"
    "v_lshlrev_b32 v126, 16, v223\n"
    "v_and_b32 v127, 0xffff0000, v223\n"
    "v_mfma_f32_16x16x32_bf16 v[144:147], a[208:211], v[84:87], v[144:147]\n"
    "s_waitcnt lgkmcnt(2)\n"
    "v_mfma_f32_16x16x32_bf16 v[148:151], a[212:215], v[84:87], v[148:151]\n"
    "s_waitcnt lgkmcnt(1)\n"
    "v_mfma_f32_16x16x32_bf16 v[152:155], a[216:219], v[84:87], v[152:155]\n"
    "s_waitcnt lgkmcnt(0)\n"
    "v_mfma_f32_16x16x32_bf16 v[156:159], a[220:223], v[84:87], v[156:159]\n"
    "s_waitcnt vmcnt(8)\n"
    "v_mfma_f32_16x16x32_bf16 v[160:163], a[176:179], v[56:59], v[160:163]\n"
    "v_mfma_f32_16x16x32_bf16 v[164:167], a[180:183], v[56:59], v[164:167]\n"
    "v_mfma_f32_16x16x32_bf16 v[168:171], a[184:187], v[56:59], v[168:171]\n"
    "v_mfma_f32_16x16x32_bf16 v[172:175], a[188:191], v[56:59], v[172:175]\n"
    "s_waitcnt vmcnt(7)\n"
    "ds_write_b128 v232, v[144:147] offset:0\n"
    "v_mfma_f32_16x16x32_bf16 v[160:163], a[192:195], v[72:75], v[160:163]\n"
    "v_mfma_f32_16x16x32_bf16 v[164:167], a[196:199], v[72:75], v[164:167]\n"
    "v_mfma_f32_16x16x32_bf16 v[168:171], a[200:203], v[72:75], v[168:171]\n"
    "ds_write_b128 v232, v[148:151] offset:1024\n"
    "v_mfma_f32_16x16x32_bf16 v[172:175], a[204:207], v[72:75], v[172:175]\n"
    "s_waitcnt vmcnt(6)\n"
    "v_mfma_f32_16x16x32_bf16 v[160:163], a[208:211], v[88:91], v[160:163]\n"
    "v_mfma_f32_16x16x32_bf16 v[164:167], a[212:215], v[88:91], v[164:167]\n"
    "ds_write_b128 v232, v[152:155] offset:2048\n"
    "v_mfma_f32_16x16x32_bf16 v[168:171], a[216:219], v[88:91], v[168:171]\n"
    "v_mfma_f32_16x16x32_bf16 v[172:175], a[220:223], v[88:91], v[172:175]\n"
    "s_waitcnt vmcnt(5)\n"
    "v_mfma_f32_16x16x32_bf16 v[176:179], a[176:179], v[60:63], v[176:179]\n"
    "ds_write_b128 v232, v[156:159] offset:3072\n"
    "v_mfma_f32_16x16x32_bf16 v[180:183], a[180:183], v[60:63], v[180:183]\n"
    "v_mfma_f32_16x16x32_bf16 v[184:187], a[184:187], v[60:63], v[184:187]\n"
    "v_mfma_f32_16x16x32_bf16 v[188:191], a[188:191], v[60:63], v[188:191]\n"
    "s_waitcnt vmcnt(4)\n"
    "ds_write_b128 v233, v[160:163] offset:0\n"
    "v_mfma_f32_16x16x32_bf16 v[176:179], a[192:195], v[76:79], v[176:179]\n"
    "v_mfma_f32_16x16x32_bf16 v[180:183], a[196:199], v[76:79], v[180:183]\n"
    "v_mfma_f32_16x16x32_bf16 v[184:187], a[200:203], v[76:79], v[184:187]\n"
    "ds_write_b128 v233, v[164:167] offset:1024\n"
    "v_mfma_f32_16x16x32_bf16 v[188:191], a[204:207], v[76:79], v[188:191]\n"
    "s_waitcnt vmcnt(3)\n"
    "v_mfma_f32_16x16x32_bf16 v[176:179], a[208:211], v[92:95], v[176:179]\n"
    "v_mfma_f32_16x16x32_bf16 v[180:183], a[212:215], v[92:95], v[180:183]\n"
    "ds_write_b128 v233, v[168:171] offset:2048\n"
    "v_mfma_f32_16x16x32_bf16 v[184:187], a[216:219], v[92:95], v[184:187]\n"
    "v_mfma_f32_16x16x32_bf16 v[188:191], a[220:223], v[92:95], v[188:191]\n"
    "s_waitcnt vmcnt(2)\n"
    "v_mfma_f32_16x16x32_bf16 v[128:131], a[176:179], v[48:51], v[128:131]\n"
    "ds_write_b128 v233, v[172:175] offset:3072\n"
    "v_mfma_f32_16x16x32_bf16 v[132:135], a[180:183], v[48:51], v[132:135]\n"
    "v_mfma_f32_16x16x32_bf16 v[136:139], a[184:187], v[48:51], v[136:139]\n"
    "v_mfma_f32_16x16x32_bf16 v[140:143], a[188:191], v[48:51], v[140:143]\n"
    "s_waitcnt vmcnt(1)\n"
    "ds_write_b128 v234, v[176:179] offset:0\n"
    "v_mfma_f32_16x16x32_bf16 v[128:131], a[192:195], v[64:67], v[128:131]\n"
    "v_mfma_f32_16x16x32_bf16 v[132:135], a[196:199], v[64:67], v[132:135]\n"
    "v_mfma_f32_16x16x32_bf16 v[136:139], a[200:203], v[64:67], v[136:139]\n"
    "ds_write_b128 v234, v[180:183] offset:1024\n"
    "v_mfma_f32_16x16x32_bf16 v[140:143], a[204:207], v[64:67], v[140:143]\n"
    "s_waitcnt vmcnt(0)\n"
    "v_mfma_f32_16x16x32_bf16 v[128:131], a[208:211], v[80:83], v[128:131]\n"
    "v_mfma_f32_16x16x32_bf16 v[132:135], a[212:215], v[80:83], v[132:135]\n"
    "ds_write_b128 v234, v[184:187] offset:2048\n"
    "v_mfma_f32_16x16x32_bf16 v[136:139], a[216:219], v[80:83], v[136:139]\n"
    "v_mfma_f32_16x16x32_bf16 v[140:143], a[220:223], v[80:83], v[140:143]\n"
    "ds_write_b128 v234, v[188:191] offset:3072\n"
    "s_waitcnt lgkmcnt(0)\n"
    "s_barrier\n"
    "ds_read_b128 v[0:3], v238 offset:0\n"
    "ds_read_b128 v[4:7], v238 offset:4096\n"
    "ds_read_b128 v[8:11], v238 offset:8192\n"
    "ds_read_b128 v[16:19], v238 offset:1024\n"
    "ds_read_b128 v[20:23], v238 offset:5120\n"
    "ds_read_b128 v[24:27], v238 offset:9216\n"
    "ds_read_b128 v[32:35], v238 offset:2048\n"
    "ds_read_b128 v[36:39], v238 offset:6144\n"
    "ds_read_b128 v[40:43], v238 offset:10240\n"
    "ds_read_b128 v[48:51], v238 offset:3072\n"
    "ds_read_b128 v[52:55], v238 offset:7168\n"
    "ds_read_b128 v[56:59], v238 offset:11264\n"
    "s_waitcnt lgkmcnt(11)\n"
    "v_pk_add_f32 v[0:1], v[0:1], v[128:129]\n"
    "s_waitcnt lgkmcnt(9)\n"
    "v_pk_add_f32 v[4:5], v[4:5], v[8:9]\n"
    "v_pk_add_f32 v[0:1], v[0:1], v[4:5]\n"
    "v_pk_fma_f32 v[0:1], v[0:1], v[200:201], v[112:113]\n"
    "v_cvt_pk_bf16_f32 v64, v0, v1\n"
    "v_pk_add_f32 v[2:3], v[2:3], v[130:131]\n"
    "v_pk_add_f32 v[6:7], v[6:7], v[10:11]\n"
    "v_pk_add_f32 v[2:3], v[2:3], v[6:7]\n"
    "v_pk_fma_f32 v[2:3], v[2:3], v[202:203], v[114:115]\n"
    "v_cvt_pk_bf16_f32 v65, v2, v3\n"
    "ds_write_b64 v240, v[64:65] offset:0\n"
    "s_waitcnt lgkmcnt(9)\n"
    "v_pk_add_f32 v[16:17], v[16:17], v[132:133]\n"
    "s_waitcnt lgkmcnt(7)\n"
    "v_pk_add_f32 v[20:21], v[20:21], v[24:25]\n"
    "v_pk_add_f32 v[16:17], v[16:17], v[20:21]\n"
    "v_pk_fma_f32 v[16:17], v[16:17], v[204:205], v[116:117]\n"
    "v_cvt_pk_bf16_f32 v66, v16, v17\n"
    "v_pk_add_f32 v[18:19], v[18:19], v[134:135]\n"
    "v_pk_add_f32 v[22:23], v[22:23], v[26:27]\n"
    "v_pk_add_f32 v[18:19], v[18:19], v[22:23]\n"
    "v_pk_fma_f32 v[18:19], v[18:19], v[206:207], v[118:119]\n"
    "v_cvt_pk_bf16_f32 v67, v18, v19\n"
    "ds_write_b64 v240, v[66:67] offset:32\n"
    "s_waitcnt lgkmcnt(7)\n"
    "v_pk_add_f32 v[32:33], v[32:33], v[136:137]\n"
    "s_waitcnt lgkmcnt(5)\n"
    "v_pk_add_f32 v[36:37], v[36:37], v[40:41]\n"
    "v_pk_add_f32 v[32:33], v[32:33], v[36:37]\n"
    "v_pk_fma_f32 v[32:33], v[32:33], v[208:209], v[120:121]\n"
    "v_cvt_pk_bf16_f32 v68, v32, v33\n"
    "v_pk_add_f32 v[34:35], v[34:35], v[138:139]\n"
    "v_pk_add_f32 v[38:39], v[38:39], v[42:43]\n"
    "v_pk_add_f32 v[34:35], v[34:35], v[38:39]\n"
    "v_pk_fma_f32 v[34:35], v[34:35], v[210:211], v[122:123]\n"
    "v_cvt_pk_bf16_f32 v69, v34, v35\n"
    "ds_write_b64 v240, v[68:69] offset:64\n"
    "s_waitcnt lgkmcnt(5)\n"
    "v_pk_add_f32 v[48:49], v[48:49], v[140:141]\n"
    "s_waitcnt lgkmcnt(3)\n"
    "v_pk_add_f32 v[52:53], v[52:53], v[56:57]\n"
    "v_pk_add_f32 v[48:49], v[48:49], v[52:53]\n"
    "v_pk_fma_f32 v[48:49], v[48:49], v[212:213], v[124:125]\n"
    "v_cvt_pk_bf16_f32 v70, v48, v49\n"
    "v_pk_add_f32 v[50:51], v[50:51], v[142:143]\n"
    "v_pk_add_f32 v[54:55], v[54:55], v[58:59]\n"
    "v_pk_add_f32 v[50:51], v[50:51], v[54:55]\n"
    "v_pk_fma_f32 v[50:51], v[50:51], v[214:215], v[126:127]\n"
    "v_cvt_pk_bf16_f32 v71, v50, v51\n"
    "ds_write_b64 v240, v[70:71] offset:96\n"
    "ds_read_b128 v[72:75], v241\n"
    "ds_read_b128 v[76:79], v241 offset:1152\n"
    "s_waitcnt lgkmcnt(1)\n"
    "global_store_dwordx4 v242, v[72:75], s[30:31]\n"
    "s_waitcnt lgkmcnt(0)\n"
    "global_store_dwordx4 v243, v[76:79], s[30:31]\n"
    "s_branch L_fin_%=\n"
    "L_idle_%=:\n"
    "s_waitcnt vmcnt(0)\n"
    "buffer_atomic_swap v192, v194, s[64:67], s39 offen sc1\n"
    "L_fin_%=:\n"
    "s_waitcnt vmcnt(0)\n"
    "s_branch L_end_%=\n"
    "L_cmb_%=_redo:\n"
    "s_cmp_lg_u32 s91, 0\n"
    "s_cbranch_scc1 L_cmb_%=_back\n"
    "s_mov_b32 s91, 1\n"
    "v_mov_b32 v248, s64\n"
    "v_mov_b32 v249, s65\n"
    "v_mov_b32 v228, 0xc2cc0000\n"
    "v_mov_b32 v229, 0xc2cc0000\n"
    "L_again_%=:\n"
    "v_sub_f32 v228, v228, v160\n"
    "v_sub_f32 v229, v229, v164\n"
    "v_sub_f32 v230, v230, v168\n"
    "v_sub_f32 v231, v231, v172\n"
    "s_waitcnt vmcnt(0) lgkmcnt(0)\n"
    "s_barrier\n"
    "s_mov_b32 s44, 0\n"
    "s_add_u32 m0, s47, 49152\n"
    "s_nop 0\n"
    "buffer_load_dwordx4 v254, s[36:39], s44 offen offset:0 nt lds\n"
    "s_add_u32 m0, s47, 51072\n"
    "s_nop 0\n"
    "buffer_load_dwordx4 v254, s[36:39], s44 offen offset:128 nt lds\n"
    "s_add_u32 m0, s47, 52992\n"
    "s_nop 0\n"
    "buffer_load_dwordx4 v254, s[36:39], s44 offen offset:256 nt lds\n"
    "s_add_u32 m0, s47, 54912\n"
    "s_nop 0\n"
    "buffer_load_dwordx4 v254, s[36:39], s44 offen offset:384 nt lds\n"
    "s_mov_b32 s44, 8192\n"
    "s_add_u32 m0, s47, 0\n"
    "s_nop 0\n"
    "buffer_load_dwordx4 v254, s[36:39], s44 offen offset:0 nt lds\n"
    "s_add_u32 m0, s47, 1920\n"
    "s_nop 0\n"
    "buffer_load_dwordx4 v254, s[36:39], s44 offen offset:128 nt lds\n"
    "s_add_u32 m0, s47, 3840\n"
    "s_nop 0\n"
    "buffer_load_dwordx4 v254, s[36:39], s44 offen offset:256 nt lds\n"
    "s_add_u32 m0, s47, 5760\n"
    "s_nop 0\n"
    "buffer_load_dwordx4 v254, s[36:39], s44 offen offset:384 nt lds\n"
    "s_mov_b32 s44, 32768\n"
    "s_add_u32 m0, s47, 16384\n"
    "s_nop 0\n"
    "buffer_load_dwordx4 v254, s[36:39], s44 offen offset:0 nt lds\n"
    "s_add_u32 m0, s47, 18304\n"
    "s_nop 0\n"
    "buffer_load_dwordx4 v254, s[36:39], s44 offen offset:128 nt lds\n"
    "s_add_u32 m0, s47, 20224\n"
    "s_nop 0\n"
    "buffer_load_dwordx4 v254, s[36:39], s44 offen offset:256 nt lds\n"
    "s_add_u32 m0, s47, 22144\n"
    "s_nop 0\n"
    "buffer_load_dwordx4 v254, s[36:39], s44 offen offset:384 nt lds\n"
    "s_mov_b32 s45, 0\n"
    "s_mov_b32 s46, 4096\n"
    "s_add_u32 m0, s48, 32768\n"
    "s_nop 0\n"
    "buffer_load_dwordx4 v255, s[40:43], s45 offen offset:0 nt lds\n"
    "s_add_u32 m0, s48, 33664\n"
    "s_nop 0\n"
    "buffer_load_dwordx4 v255, s[40:43], s45 offen offset:128 nt lds\n"
    "s_add_u32 m0, s48, 34816\n"
    "s_nop 0\n"
    "buffer_load_dwordx4 v255, s[40:43], s46 offen offset:0 nt lds\n"
    "s_add_u32 m0, s48, 35712\n"
    "s_nop 0\n"
    "buffer_load_dwordx4 v255, s[40:43], s46 offen offset:128 nt lds\n"
    "s_mov_b32 s45, 256\n"
    "s_mov_b32 s46, 4352\n"
    "s_add_u32 m0, s48, 49152\n"
    "s_nop 0\n"
    "buffer_load_dwordx4 v255, s[40:43], s45 offen offset:0 nt lds\n"
    "s_add_u32 m0, s48, 50048\n"
    "s_nop 0\n"
    "buffer_load_dwordx4 v255, s[40:43], s45 offen offset:128 nt lds\n"
    "s_add_u32 m0, s48, 51200\n"
    "s_nop 0\n"
    "buffer_load_dwordx4 v255, s[40:43], s46 offen offset:0 nt lds\n"
    "s_add_u32 m0, s48, 52096\n"
    "s_nop 0\n"
    "buffer_load_dwordx4 v255, s[40:43], s46 offen offset:128 nt lds\n"
    "s_waitcnt vmcnt(0)\n"
    "s_barrier\n"
    "ds_read_b128 v[128:131], v251 offset:49152\n"
    "ds_read_b128 v[132:135], v251 offset:49664\n"
    "ds_read_b128 v[136:139], v251 offset:51200\n"
    "ds_read_b128 v[140:143], v251 offset:51712\n"
    "ds_read_b128 v[160:163], v251 offset:53248\n"
    "ds_read_b128 v[164:167], v251 offset:53760\n"
    "ds_read_b128 v[168:171], v251 offset:55296\n"
    "ds_read_b128 v[172:175], v251 offset:55808\n"
    "s_waitcnt lgkmcnt(7)\n"
    "v_mfma_f32_16x16x32_bf16 v[144:147], v[128:131], a[0:3], 0\n"
    "s_waitcnt lgkmcnt(6)\n"
    "v_mfma_f32_16x16x32_bf16 v[144:147], v[132:135], a[4:7], v[144:147]\n"
    "s_waitcnt lgkmcnt(5)\n"
    "v_mfma_f32_16x16x32_bf16 v[144:147], v[136:139], a[8:11], v[144:147]\n"
    "s_waitcnt lgkmcnt(4)\n"
    "v_mfma_f32_16x16x32_bf16 v[144:147], v[140:143], a[12:15], v[144:147]\n"
    "s_waitcnt lgkmcnt(3)\n"
    "v_mfma_f32_16x16x32_bf16 v[144:147], v[160:163], a[16:19], v[144:147]\n"
    "s_waitcnt lgkmcnt(2)\n"
    "v_mfma_f32_16x16x32_bf16 v[144:147], v[164:167], a[20:23], v[144:147]\n"
    "s_waitcnt lgkmcnt(1)\n"
    "v_mfma_f32_16x16x32_bf16 v[144:147], v[168:171], a[24:27], v[144:147]\n"
    "s_waitcnt lgkmcnt(0)\n"
    "v_mfma_f32_16x16x32_bf16 v[144:147], v[172:175], a[28:31], v[144:147]\n"
    "v_mfma_f32_16x16x32_bf16 v[148:151], v[128:131], a[32:35], 0\n"
    "v_mfma_f32_16x16x32_bf16 v[148:151], v[132:135], a[36:39], v[148:151]\n"
    "v_mfma_f32_16x16x32_bf16 v[148:151], v[136:139], a[40:43], v[148:151]\n"
    "v_mfma_f32_16x16x32_bf16 v[148:151], v[140:143], a[44:47], v[148:151]\n"
    "v_mfma_f32_16x16x32_bf16 v[148:151], v[160:163], a[48:51], v[148:151]\n"
    "v_mfma_f32_16x16x32_bf16 v[148:151], v[164:167], a[52:55], v[148:151]\n"
    "s_nop 1\n"
    "v_max3_f32 v228, v228, v144, v145\n"
    "v_max3_f32 v228, v228, v146, v147\n"
    "v_mfma_f32_16x16x32_bf16 v[148:151], v[168:171], a[56:59], v[148:151]\n"
    "v_mfma_f32_16x16x32_bf16 v[148:151], v[172:175], a[60:63], v[148:151]\n"
    "v_mfma_f32_16x16x32_bf16 v[152:155], v[128:131], a[64:67], 0\n"
    "v_mfma_f32_16x16x32_bf16 v[152:155], v[132:135], a[68:71], v[152:155]\n"
    "v_mfma_f32_16x16x32_bf16 v[152:155], v[136:139], a[72:75], v[152:155]\n"
    "v_mfma_f32_16x16x32_bf16 v[152:155], v[140:143], a[76:79], v[152:155]\n"
    "v_mfma_f32_16x16x32_bf16 v[152:155], v[160:163], a[80:83], v[152:155]\n"
    "v_mfma_f32_16x16x32_bf16 v[152:155], v[164:167], a[84:87], v[152:155]\n"
    "s_nop 1\n"
    "v_max3_f32 v229, v229, v148, v149\n"
    "v_max3_f32 v229, v229, v150, v151\n"
    "v_mfma_f32_16x16x32_bf16 v[152:155], v[168:171], a[88:91], v[152:155]\n"
    "ds_read_b128 v[176:179], v251 offset:0\n"
    "v_mfma_f32_16x16x32_bf16 v[152:155], v[172:175], a[92:95], v[152:155]\n"
    "ds_read_b128 v[180:183], v251 offset:512\n"
    "v_mfma_f32_16x16x32_bf16 v[156:159], v[128:131], a[96:99], 0\n"
    "ds_read_b128 v[184:187], v251 offset:2048\n"
    "v_mfma_f32_16x16x32_bf16 v[156:159], v[132:135], a[100:103], v[156:159]\n"
    "ds_read_b128 v[188:191], v251 offset:2560\n"
    "v_mfma_f32_16x16x32_bf16 v[156:159], v[136:139], a[104:107], v[156:159]\n"
    "ds_read_b128 v[192:195], v251 offset:4096\n"
    "v_mfma_f32_16x16x32_bf16 v[156:159], v[140:143], a[108:111], v[156:159]\n"
    "ds_read_b128 v[196:199], v251 offset:4608\n"
    "v_mfma_f32_16x16x32_bf16 v[156:159], v[160:163], a[112:115], v[156:159]\n"
    "ds_read_b128 v[200:203], v251 offset:6144\n"
    "v_mfma_f32_16x16x32_bf16 v[156:159], v[164:167], a[116:119], v[156:159]\n"
    "ds_read_b128 v[204:207], v251 offset:6656\n"
    "v_max3_f32 v230, v230, v152, v153\n"
    "v_mfma_f32_16x16x32_bf16 v[156:159], v[168:171], a[120:123], v[156:159]\n"
    "v_max3_f32 v230, v230, v154, v155\n"
    "v_mfma_f32_16x16x32_bf16 v[156:159], v[172:175], a[124:127], v[156:159]\n"
    "s_waitcnt lgkmcnt(7)\n"
    "v_mfma_f32_16x16x32_bf16 v[208:211], v[176:179], a[0:3], 0\n"
    "s_waitcnt lgkmcnt(6)\n"
    "v_mfma_f32_16x16x32_bf16 v[208:211], v[180:183], a[4:7], v[208:211]\n"
    "s_waitcnt lgkmcnt(5)\n"
    "s_barrier\n"
    "v_mfma_f32_16x16x32_bf16 v[208:211], v[184:187], a[8:11], v[208:211]\n"
    "s_waitcnt lgkmcnt(3)\n"
    "s_mov_b32 s44, 40960\n"
    "s_add_u32 m0, s47, 32768\n"
    "v_mfma_f32_16x16x32_bf16 v[208:211], v[188:191], a[12:15], v[208:211]\n"
    "buffer_load_dwordx4 v254, s[36:39], s44 offen offset:0 nt lds\n"
    "v_mfma_f32_16x16x32_bf16 v[208:211], v[192:195], a[16:19], v[208:211]\n"
    "s_waitcnt lgkmcnt(0)\n"
    "s_add_u32 m0, s47, 34688\n"
    "v_mfma_f32_16x16x32_bf16 v[208:211], v[196:199], a[20:23], v[208:211]\n"
    "buffer_load_dwordx4 v254, s[36:39], s44 offen offset:128 nt lds\n"
    "v_mfma_f32_16x16x32_bf16 v[208:211], v[200:203], a[24:27], v[208:211]\n"
    "s_add_u32 m0, s47, 36608\n"
    "v_max3_f32 v231, v231, v156, v157\n"
    "v_max3_f32 v231, v231, v158, v159\n"
    "v_mfma_f32_16x16x32_bf16 v[208:211], v[204:207], a[28:31], v[208:211]\n"
    "buffer_load_dwordx4 v254, s[36:39], s44 offen offset:256 nt lds\n"
    "v_mfma_f32_16x16x32_bf16 v[212:215], v[176:179], a[32:35], 0\n"
    "s_add_u32 m0, s47, 38528\n"
    "v_mfma_f32_16x16x32_bf16 v[212:215], v[180:183], a[36:39], v[212:215]\n"
    "buffer_load_dwordx4 v254, s[36:39], s44 offen offset:384 nt lds\n"
    "v_mfma_f32_16x16x32_bf16 v[212:215], v[184:187], a[40:43], v[212:215]\n"
    "s_mov_b32 s44, 65536\n"
    "s_add_u32 m0, s47, 49152\n"
    "v_mfma_f32_16x16x32_bf16 v[212:215], v[188:191], a[44:47], v[212:215]\n"
    "buffer_load_dwordx4 v254, s[36:39], s44 offen offset:0 nt lds\n"
    "v_mfma_f32_16x16x32_bf16 v[212:215], v[192:195], a[48:51], v[212:215]\n"
    "s_add_u32 m0, s47, 51072\n"
    "v_mfma_f32_16x16x32_bf16 v[212:215], v[196:199], a[52:55], v[212:215]\n"
    "buffer_load_dwordx4 v254, s[36:39], s44 offen offset:128 nt lds\n"
    "v_mfma_f32_16x16x32_bf16 v[212:215], v[200:203], a[56:59], v[212:215]\n"
    "s_add_u32 m0, s47, 52992\n"
    "v_max3_f32 v228, v228, v208, v209\n"
    "v_max3_f32 v228, v228, v210, v211\n"
    "v_mfma_f32_16x16x32_bf16 v[212:215], v[204:207], a[60:63], v[212:215]\n"
    "buffer_load_dwordx4 v254, s[36:39], s44 offen offset:256 nt lds\n"
    "v_mfma_f32_16x16x32_bf16 v[216:219], v[176:179], a[64:67], 0\n"
    "s_add_u32 m0, s47, 54912\n"
    "v_mfma_f32_16x16x32_bf16 v[216:219], v[180:183], a[68:71], v[216:219]\n"
    "buffer_load_dwordx4 v254, s[36:39], s44 offen offset:384 nt lds\n"
    "v_mfma_f32_16x16x32_bf16 v[216:219], v[184:187], a[72:75], v[216:219]\n"
    "v_mfma_f32_16x16x32_bf16 v[216:219], v[188:191], a[76:79], v[216:219]\n"
    "v_mfma_f32_16x16x32_bf16 v[216:219], v[192:195], a[80:83], v[216:219]\n"
    "v_mfma_f32_16x16x32_bf16 v[216:219], v[196:199], a[84:87], v[216:219]\n"
    "v_max3_f32 v229, v229, v212, v213\n"
    "v_max3_f32 v229, v229, v214, v215\n"
    "v_mfma_f32_16x16x32_bf16 v[216:219], v[200:203], a[88:91], v[216:219]\n"
    "v_mfma_f32_16x16x32_bf16 v[216:219], v[204:207], a[92:95], v[216:219]\n"
    "v_mfma_f32_16x16x32_bf16 v[220:223], v[176:179], a[96:99], 0\n"
    "ds_read_b128 v[176:179], v251 offset:16384\n"
    "v_mfma_f32_16x16x32_bf16 v[220:223], v[180:183], a[100:103], v[220:223]\n"
    "ds_read_b128 v[180:183], v251 offset:16896\n"
    "v_mfma_f32_16x16x32_bf16 v[220:223], v[184:187], a[104:107], v[220:223]\n"
    "ds_read_b128 v[184:187], v251 offset:18432\n"
    "v_mfma_f32_16x16x32_bf16 v[220:223], v[188:191], a[108:111], v[220:223]\n"
    "ds_read_b128 v[188:191], v251 offset:18944\n"
    "v_mfma_f32_16x16x32_bf16 v[220:223], v[192:195], a[112:115], v[220:223]\n"
    "ds_read_b128 v[192:195], v251 offset:20480\n"
    "v_mfma_f32_16x16x32_bf16 v[220:223], v[196:199], a[116:119], v[220:223]\n"
    "ds_read_b128 v[196:199], v251 offset:20992\n"
    "v_max3_f32 v230, v230, v216, v217\n"
    "v_mfma_f32_16x16x32_bf16 v[220:223], v[200:203], a[120:123], v[220:223]\n"
    "ds_read_b128 v[200:203], v251 offset:22528\n"
    "v_max3_f32 v230, v230, v218, v219\n"
    "v_mfma_f32_16x16x32_bf16 v[220:223], v[204:207], a[124:127], v[220:223]\n"
    "ds_read_b128 v[204:207], v251 offset:23040\n"
    "s_mov_b32 s45, 32768\n"
    "s_add_u32 m0, s48, 0\n"
    "s_nop 0\n"
    "buffer_load_dwordx4 v255, s[40:43], s45 offen offset:0 nt lds\n"
    "s_add_u32 m0, s48, 896\n"
    "s_nop 0\n"
    "buffer_load_dwordx4 v255, s[40:43], s45 offen offset:128 nt lds\n"
    "v_max3_f32 v231, v231, v220, v221\n"
    "v_max3_f32 v231, v231, v222, v223\n"
    "s_nop 1\n"
    "v_permlane32_swap_b32 v228, v230\n"
    "v_permlane32_swap_b32 v229, v231\n"
    "v_max_f32 v228, v228, v230\n"
    "v_max_f32 v229, v229, v231\n"
    "s_nop 1\n"
    "v_permlane16_swap_b32 v228, v229\n"
    "v_max_f32 v228, v228, v229\n"
    "v_lshrrev_b32 v128, 4, v250\n"
    "v_lshl_add_u32 v128, v128, 2, v246\n"
    "ds_write_b32 v128, v228\n"
    "v_xor_b32 v129, 0x100, v246\n"
    "s_mov_b32 s46, 36864\n"
    "s_add_u32 m0, s48, 2048\n"
    "s_nop 0\n"
    "buffer_load_dwordx4 v255, s[40:43], s46 offen offset:0 nt lds\n"
    "s_waitcnt vmcnt(15) lgkmcnt(0)\n"
    "s_barrier\n"
    "ds_read_b128 v[228:231], v246\n"
    "ds_read_b128 v[132:135], v129\n"
    "s_add_u32 m0, s48, 2944\n"
    "s_nop 0\n"
    "buffer_load_dwordx4 v255, s[40:43], s46 offen offset:128 nt lds\n"
    "s_waitcnt lgkmcnt(0)\n"
    "v_max_f32 v160, v228, v134\n"
    "v_sub_f32 v161, 0xc2cc0000, v160\n"
    "v_sub_f32 v160, 0xc2cc0000, v160\n"
    "v_mov_b64 v[162:163], v[160:161]\n"
    "v_max_f32 v164, v229, v135\n"
    "v_sub_f32 v165, 0xc2cc0000, v164\n"
    "v_sub_f32 v164, 0xc2cc0000, v164\n"
    "v_mov_b64 v[166:167], v[164:165]\n"
    "v_max_f32 v168, v230, v132\n"
    "v_sub_f32 v169, 0xc2cc0000, v168\n"
    "v_sub_f32 v168, 0xc2cc0000, v168\n"
    "v_mov_b64 v[170:171], v[168:169]\n"
    "v_max_f32 v172, v231, v133\n"
    "v_sub_f32 v173, 0xc2cc0000, v172\n"
    "v_sub_f32 v172, 0xc2cc0000, v172\n"
    "v_mov_b64 v[174:175], v[172:173]\n"
    "v_mov_b32 v228, 0xc2cc0000\n"
    "v_mov_b32 v229, 0xc2cc0000\n"
    "v_mov_b32 v230, 0xc2cc0000\n"
    "v_mov_b32 v231, 0xc2cc0000\n"
    "v_pk_add_f32 v[144:145], v[144:145], v[160:161]\n"
    "v_pk_add_f32 v[146:147], v[146:147], v[160:161]\n"
    "v_pk_add_f32 v[208:209], v[208:209], v[160:161]\n"
    "v_pk_add_f32 v[210:211], v[210:211], v[160:161]\n"
    "v_exp_f32 v144, v144\n"
    "v_exp_f32 v145, v145\n"
    "v_exp_f32 v146, v146\n"
    "v_exp_f32 v147, v147\n"
    "v_exp_f32 v208, v208\n"
    "v_exp_f32 v209, v209\n"
    "v_exp_f32 v210, v210\n"
    "v_exp_f32 v211, v211\n"
    "v_cvt_pk_bf16_f32 v128, v144, v145\n"
    "v_cvt_pk_bf16_f32 v129, v146, v147\n"
    "v_cvt_pk_bf16_f32 v130, v208, v209\n"
    "v_cvt_pk_bf16_f32 v131, v210, v211\n"
    "v_pk_add_f32 v[144:145], v[144:145], v[146:147]\n"
    "v_pk_add_f32 v[144:145], v[144:145], v[208:209]\n"
    "v_pk_add_f32 v[144:145], v[144:145], v[210:211]\n"
    "v_add_f32 v224, v144, v145\n"
    "v_pk_add_f32 v[148:149], v[148:149], v[164:165]\n"
    "v_pk_add_f32 v[150:151], v[150:151], v[164:165]\n"
    "v_pk_add_f32 v[212:213], v[212:213], v[164:165]\n"
    "v_pk_add_f32 v[214:215], v[214:215], v[164:165]\n"
    "v_exp_f32 v148, v148\n"
    "v_exp_f32 v149, v149\n"
    "v_exp_f32 v150, v150\n"
    "v_exp_f32 v151, v151\n"
    "v_exp_f32 v212, v212\n"
    "v_exp_f32 v213, v213\n"
    "v_exp_f32 v214, v214\n"
    "v_exp_f32 v215, v215\n"
    "v_cvt_pk_bf16_f32 v132, v148, v149\n"
    "v_cvt_pk_bf16_f32 v133, v150, v151\n"
    "v_cvt_pk_bf16_f32 v134, v212, v213\n"
    "v_cvt_pk_bf16_f32 v135, v214, v215\n"
    "v_pk_add_f32 v[148:149], v[148:149], v[150:151]\n"
    "v_pk_add_f32 v[148:149], v[148:149], v[212:213]\n"
    "v_pk_add_f32 v[148:149], v[148:149], v[214:215]\n"
    "v_add_f32 v225, v148, v149\n"
    "v_pk_add_f32 v[152:153], v[152:153], v[168:169]\n"
    "v_pk_add_f32 v[154:155], v[154:155], v[168:169]\n"
    "v_pk_add_f32 v[216:217], v[216:217], v[168:169]\n"
    "v_pk_add_f32 v[218:219], v[218:219], v[168:169]\n"
    "v_exp_f32 v152, v152\n"
    "v_exp_f32 v153, v153\n"
    "v_exp_f32 v154, v154\n"
    "v_exp_f32 v155, v155\n"
    "v_exp_f32 v216, v216\n"
    "v_exp_f32 v217, v217\n"
    "v_exp_f32 v218, v218\n"
    "v_exp_f32 v219, v219\n"
    "v_cvt_pk_bf16_f32 v136, v152, v153\n"
    "v_cvt_pk_bf16_f32 v137, v154, v155\n"
    "v_cvt_pk_bf16_f32 v138, v216, v217\n"
    "v_cvt_pk_bf16_f32 v139, v218, v219\n"
    "v_pk_add_f32 v[152:153], v[152:153], v[154:155]\n"
    "v_pk_add_f32 v[152:153], v[152:153], v[216:217]\n"
    "v_pk_add_f32 v[152:153], v[152:153], v[218:219]\n"
    "v_add_f32 v226, v152, v153\n"
    "ds_read_b64_tr_b16 v[208:209], v252 offset:32768\n"
    "ds_read_b64_tr_b16 v[210:211], v252 offset:36864\n"
    "ds_read_b64_tr_b16 v[212:213], v253 offset:32768\n"
    "ds_read_b64_tr_b16 v[214:215], v253 offset:36864\n"
    "v_pk_add_f32 v[156:157], v[156:157], v[172:173]\n"
    "v_pk_add_f32 v[158:159], v[158:159], v[172:173]\n"
    "v_pk_add_f32 v[220:221], v[220:221], v[172:173]\n"
    "v_pk_add_f32 v[222:223], v[222:223], v[172:173]\n"
    "v_exp_f32 v156, v156\n"
    "v_exp_f32 v157, v157\n"
    "v_exp_f32 v158, v158\n"
    "v_exp_f32 v159, v159\n"
    "v_exp_f32 v220, v220\n"
    "v_exp_f32 v221, v221\n"
    "v_cvt_pk_bf16_f32 v140, v156, v157\n"
    "v_cvt_pk_bf16_f32 v141, v158, v159\n"
    "v_pk_add_f32 v[156:157], v[156:157], v[158:159]\n"
    "v_add_f32 v227, v156, v157\n"
    "s_cmp_eq_u32 s91, 2\n"
    "s_cbranch_scc1 L_body1_%=\n"
    "s_mov_b32 s44, 73728\n"
    "s_mov_b32 s45, 33024\n"
    "s_mov_b32 s46, 37120\n"
    "s_mov_b32 s49, 16\n"
    "s_waitcnt vmcnt(0)\n"
    "s_barrier\n"
    "s_branch L_modd_%=\n"
    "L_meven_%=:\n"
    "v_mfma_f32_16x16x32_bf16 v[216:219], v[176:179], a[0:3], v[160:163]\n"
    "v_exp_f32 v222, v222\n"
    "v_mfma_f32_16x16x32_bf16 v[216:219], v[180:183], a[4:7], v[216:219]\n"
    "s_add_u32 m0, s47, 32768\n"
    "v_exp_f32 v223, v223\n"
    "v_mfma_f32_16x16x32_bf16 v[216:219], v[184:187], a[8:11], v[216:219]\n"
    "buffer_load_dwordx4 v254, s[36:39], s44 offen offset:0 nt lds\n"
    "v_mfma_f32_16x16x32_bf16 v[216:219], v[188:191], a[12:15], v[216:219]\n"
    "s_waitcnt lgkmcnt(4)\n"
    "v_add_f32 v227, v227, v220\n"
    "v_add_f32 v227, v227, v221\n"
    "v_mfma_f32_16x16x32_bf16 v[216:219], v[192:195], a[16:19], v[216:219]\n"
    "v_cvt_pk_bf16_f32 v158, v220, v221\n"
    "v_add_f32 v227, v227, v222\n"
    "v_mfma_f32_16x16x32_bf16 v[216:219], v[196:199], a[20:23], v[216:219]\n"
    "s_add_u32 m0, s47, 34688\n"
    "v_add_f32 v227, v227, v223\n"
    "v_cvt_pk_bf16_f32 v159, v222, v223\n"
    "v_mfma_f32_16x16x32_bf16 v[216:219], v[200:203], a[24:27], v[216:219]\n"
    "buffer_load_dwordx4 v254, s[36:39], s44 offen offset:128 nt lds\n"
    "v_mfma_f32_16x16x32_bf16 v[216:219], v[204:207], a[28:31], v[216:219]\n"
    "s_waitcnt lgkmcnt(0)\n"
    "v_mfma_f32_16x16x32_bf16 a[136:139], v[212:215], v[144:147], a[136:139]\n"
    "v_mfma_f32_16x16x32_bf16 a[128:131], v[208:211], v[144:147], a[128:131]\n"
    "s_add_u32 m0, s47, 36608\n"
    "v_mfma_f32_16x16x32_bf16 a[132:135], v[208:211], v[148:151], a[132:135]\n"
    "buffer_load_dwordx4 v254, s[36:39], s44 offen offset:256 nt lds\n"
    "v_mfma_f32_16x16x32_bf16 a[140:143], v[212:215], v[148:151], a[140:143]\n"
    "s_nop 1\n"
    "v_max3_f32 v228, v228, v216, v217\n"
    "v_max3_f32 v228, v228, v218, v219\n"
    "v_mfma_f32_16x16x32_bf16 v[8:11], v[212:215], v[152:155], v[8:11]\n"
    "v_exp_f32 v216, v216\n"
    "v_mfma_f32_16x16x32_bf16 v[0:3], v[208:211], v[152:155], v[0:3]\n"
    "v_exp_f32 v217, v217\n"
    "v_mfma_f32_16x16x32_bf16 v[4:7], v[208:211], v[156:159], v[4:7]\n"
    "s_nop 0\n"
    "ds_read_b64_tr_b16 v[208:209], v252 offset:512\n"
    "ds_read_b64_tr_b16 v[210:211], v252 offset:4608\n"
    "v_mfma_f32_16x16x32_bf16 v[12:15], v[212:215], v[156:159], v[12:15]\n"
    "s_nop 0\n"
    "ds_read_b64_tr_b16 v[212:213], v253 offset:512\n"
    "ds_read_b64_tr_b16 v[214:215], v253 offset:4608\n"
    "v_mfma_f32_16x16x32_bf16 v[220:223], v[176:179], a[32:35], v[164:167]\n"
    "v_exp_f32 v218, v218\n"
    "v_mfma_f32_16x16x32_bf16 v[220:223], v[180:183], a[36:39], v[220:223]\n"
    "s_add_u32 m0, s47, 38528\n"
    "v_exp_f32 v219, v219\n"
    "v_mfma_f32_16x16x32_bf16 v[220:223], v[184:187], a[40:43], v[220:223]\n"
    "buffer_load_dwordx4 v254, s[36:39], s44 offen offset:384 nt lds\n"
    "v_mfma_f32_16x16x32_bf16 v[220:223], v[188:191], a[44:47], v[220:223]\n"
    "v_add_f32 v224, v224, v216\n"
    "v_add_f32 v224, v224, v217\n"
    "v_mfma_f32_16x16x32_bf16 v[220:223], v[192:195], a[48:51], v[220:223]\n"
    "v_cvt_pk_bf16_f32 v128, v216, v217\n"
    "v_add_f32 v224, v224, v218\n"
    "v_mfma_f32_16x16x32_bf16 v[220:223], v[196:199], a[52:55], v[220:223]\n"
    "v_add_f32 v224, v224, v219\n"
    "v_cvt_pk_bf16_f32 v129, v218, v219\n"
    "v_mfma_f32_16x16x32_bf16 v[220:223], v[200:203], a[56:59], v[220:223]\n"
    "global_load_dword v247, v[248:249], off sc1\n"
    "v_mfma_f32_16x16x32_bf16 v[220:223], v[204:207], a[60:63], v[220:223]\n"
    "s_waitcnt lgkmcnt(0)\n"
    "v_mfma_f32_16x16x32_bf16 a[152:155], v[212:215], v[144:147], a[152:155]\n"
    "v_lshl_add_u64 v[248:249], v[248:249], 0, v[244:245]\n"
    "v_mfma_f32_16x16x32_bf16 a[144:147], v[208:211], v[144:147], a[144:147]\n"
    "s_add_u32 m0, s48, 49152\n"
    "v_mfma_f32_16x16x32_bf16 a[148:151], v[208:211], v[148:151], a[148:151]\n"
    "buffer_load_dwordx4 v255, s[40:43], s45 offen offset:0 nt lds\n"
    "v_mfma_f32_16x16x32_bf16 a[156:159], v[212:215], v[148:151], a[156:159]\n"
    "s_nop 1\n"
    "v_max3_f32 v229, v229, v220, v221\n"
    "v_max3_f32 v229, v229, v222, v223\n"
    "v_mfma_f32_16x16x32_bf16 v[24:27], v[212:215], v[152:155], v[24:27]\n"
    "v_exp_f32 v220, v220\n"
    "v_mfma_f32_16x16x32_bf16 v[16:19], v[208:211], v[152:155], v[16:19]\n"
    "v_exp_f32 v221, v221\n"
    "v_mfma_f32_16x16x32_bf16 v[20:23], v[208:211], v[156:159], v[20:23]\n"
    "s_nop 0\n"
    "ds_read_b64_tr_b16 v[208:209], v252 offset:1024\n"
    "ds_read_b64_tr_b16 v[210:211], v252 offset:5120\n"
    "v_mfma_f32_16x16x32_bf16 v[28:31], v[212:215], v[156:159], v[28:31]\n"
    "s_nop 0\n"
    "ds_read_b64_tr_b16 v[212:213], v253 offset:1024\n"
    "ds_read_b64_tr_b16 v[214:215], v253 offset:5120\n"
    "v_mfma_f32_16x16x32_bf16 v[216:219], v[176:179], a[64:67], v[168:171]\n"
    "v_exp_f32 v222, v222\n"
    "v_mfma_f32_16x16x32_bf16 v[216:219], v[180:183], a[68:71], v[216:219]\n"
    "s_add_u32 m0, s48, 50048\n"
    "v_exp_f32 v223, v223\n"
    "v_mfma_f32_16x16x32_bf16 v[216:219], v[184:187], a[72:75], v[216:219]\n"
    "buffer_load_dwordx4 v255, s[40:43], s45 offen offset:128 nt lds\n"
    "v_mfma_f32_16x16x32_bf16 v[216:219], v[188:191], a[76:79], v[216:219]\n"
    "v_add_f32 v225, v225, v220\n"
    "v_add_f32 v225, v225, v221\n"
    "v_mfma_f32_16x16x32_bf16 v[216:219], v[192:195], a[80:83], v[216:219]\n"
    "v_cvt_pk_bf16_f32 v132, v220, v221\n"
    "v_add_f32 v225, v225, v222\n"
    "v_mfma_f32_16x16x32_bf16 v[216:219], v[196:199], a[84:87], v[216:219]\n"
    "s_add_u32 m0, s48, 51200\n"
    "v_add_f32 v225, v225, v223\n"
    "v_cvt_pk_bf16_f32 v133, v222, v223\n"
    "v_mfma_f32_16x16x32_bf16 v[216:219], v[200:203], a[88:91], v[216:219]\n"
    "buffer_load_dwordx4 v255, s[40:43], s46 offen offset:0 nt lds\n"
    "v_mfma_f32_16x16x32_bf16 v[216:219], v[204:207], a[92:95], v[216:219]\n"
    "s_waitcnt lgkmcnt(0)\n"
    "v_mfma_f32_16x16x32_bf16 a[168:171], v[212:215], v[144:147], a[168:171]\n"
    "v_mfma_f32_16x16x32_bf16 a[160:163], v[208:211], v[144:147], a[160:163]\n"
    "s_add_u32 m0, s48, 52096\n"
    "v_mfma_f32_16x16x32_bf16 a[164:167], v[208:211], v[148:151], a[164:167]\n"
    "buffer_load_dwordx4 v255, s[40:43], s46 offen offset:128 nt lds\n"
    "v_mfma_f32_16x16x32_bf16 a[172:175], v[212:215], v[148:151], a[172:175]\n"
    "s_nop 1\n"
    "v_max3_f32 v230, v230, v216, v217\n"
    "v_max3_f32 v230, v230, v218, v219\n"
    "v_mfma_f32_16x16x32_bf16 v[40:43], v[212:215], v[152:155], v[40:43]\n"
    "v_exp_f32 v216, v216\n"
    "v_mfma_f32_16x16x32_bf16 v[32:35], v[208:211], v[152:155], v[32:35]\n"
    "v_exp_f32 v217, v217\n"
    "v_mfma_f32_16x16x32_bf16 v[36:39], v[208:211], v[156:159], v[36:39]\n"
    "s_nop 0\n"
    "ds_read_b64_tr_b16 v[208:209], v252 offset:1536\n"
    "ds_read_b64_tr_b16 v[210:211], v252 offset:5632\n"
    "v_mfma_f32_16x16x32_bf16 v[44:47], v[212:215], v[156:159], v[44:47]\n"
    "s_nop 0\n"
    "ds_read_b64_tr_b16 v[212:213], v253 offset:1536\n"
    "ds_read_b64_tr_b16 v[214:215], v253 offset:5632\n"
    "v_mfma_f32_16x16x32_bf16 v[220:223], v[176:179], a[96:99], v[172:175]\n"
    "v_exp_f32 v218, v218\n"
    "v_mfma_f32_16x16x32_bf16 v[220:223], v[180:183], a[100:103], v[220:223]\n"
    "v_exp_f32 v219, v219\n"
    "v_mfma_f32_16x16x32_bf16 v[220:223], v[184:187], a[104:107], v[220:223]\n"
    "s_add_u32 s44, s44, 24576\n"
    "ds_read_b128 v[176:179], v251 offset:0\n"
    "v_add_f32 v226, v226, v216\n"
    "v_mfma_f32_16x16x32_bf16 v[220:223], v[188:191], a[108:111], v[220:223]\n"
    "s_add_u32 s45, s45, 32512\n"
    "ds_read_b128 v[180:183], v251 offset:512\n"
    "v_add_f32 v226, v226, v217\n"
    "v_mfma_f32_16x16x32_bf16 v[220:223], v[192:195], a[112:115], v[220:223]\n"
    "s_add_u32 s46, s46, 32512\n"
    "ds_read_b128 v[184:187], v251 offset:2048\n"
    "v_cvt_pk_bf16_f32 v136, v216, v217\n"
    "v_mfma_f32_16x16x32_bf16 v[220:223], v[196:199], a[116:119], v[220:223]\n"
    "ds_read_b128 v[188:191], v251 offset:2560\n"
    "v_add_f32 v226, v226, v218\n"
    "v_mfma_f32_16x16x32_bf16 v[220:223], v[200:203], a[120:123], v[220:223]\n"
    "ds_read_b128 v[192:195], v251 offset:4096\n"
    "v_add_f32 v226, v226, v219\n"
    "v_mfma_f32_16x16x32_bf16 v[220:223], v[204:207], a[124:127], v[220:223]\n"
    "s_waitcnt lgkmcnt(5)\n"
    "ds_read_b128 v[196:199], v251 offset:4608\n"
    "v_cvt_pk_bf16_f32 v137, v218, v219\n"
    "v_mfma_f32_16x16x32_bf16 a[184:187], v[212:215], v[144:147], a[184:187]\n"
    "ds_read_b128 v[200:203], v251 offset:6144\n"
    "v_mfma_f32_16x16x32_bf16 a[176:179], v[208:211], v[144:147], a[176:179]\n"
    "ds_read_b128 v[204:207], v251 offset:6656\n"
    "v_mfma_f32_16x16x32_bf16 a[180:183], v[208:211], v[148:151], a[180:183]\n"
    "s_waitcnt vmcnt(13) lgkmcnt(8)\n"
    "s_barrier\n"
    "v_mfma_f32_16x16x32_bf16 a[188:191], v[212:215], v[148:151], a[188:191]\n"
    "s_nop 1\n"
    "v_max3_f32 v231, v231, v220, v221\n"
    "v_max3_f32 v231, v231, v222, v223\n"
    "v_mfma_f32_16x16x32_bf16 v[56:59], v[212:215], v[152:155], v[56:59]\n"
    "v_exp_f32 v220, v220\n"
    "v_mfma_f32_16x16x32_bf16 v[48:51], v[208:211], v[152:155], v[48:51]\n"
    "v_exp_f32 v221, v221\n"
    "v_mfma_f32_16x16x32_bf16 v[52:55], v[208:211], v[156:159], v[52:55]\n"
    "s_nop 0\n"
    "ds_read_b64_tr_b16 v[208:209], v252 offset:16384\n"
    "ds_read_b64_tr_b16 v[210:211], v252 offset:20480\n"
    "v_mfma_f32_16x16x32_bf16 v[60:63], v[212:215], v[156:159], v[60:63]\n"
    "s_waitcnt lgkmcnt(6)\n"
    "ds_read_b64_tr_b16 v[212:213], v253 offset:16384\n"
    "ds_read_b64_tr_b16 v[214:215], v253 offset:20480\n"
    "v_mfma_f32_16x16x32_bf16 v[216:219], v[176:179], a[0:3], v[160:163]\n"
    "v_exp_f32 v222, v222\n"
    "v_mfma_f32_16x16x32_bf16 v[216:219], v[180:183], a[4:7], v[216:219]\n"
    "s_add_u32 m0, s47, 49152\n"
    "v_exp_f32 v223, v223\n"
    "v_mfma_f32_16x16x32_bf16 v[216:219], v[184:187], a[8:11], v[216:219]\n"
    "buffer_load_dwordx4 v254, s[36:39], s44 offen offset:0 nt lds\n"
    "v_mfma_f32_16x16x32_bf16 v[216:219], v[188:191], a[12:15], v[216:219]\n"
    "s_waitcnt lgkmcnt(4)\n"
    "v_add_f32 v227, v227, v220\n"
    "v_add_f32 v227, v227, v221\n"
    "v_mfma_f32_16x16x32_bf16 v[216:219], v[192:195], a[16:19], v[216:219]\n"
    "v_cvt_pk_bf16_f32 v140, v220, v221\n"
    "v_add_f32 v227, v227, v222\n"
    "v_mfma_f32_16x16x32_bf16 v[216:219], v[196:199], a[20:23], v[216:219]\n"
    "s_add_u32 m0, s47, 51072\n"
    "v_add_f32 v227, v227, v223\n"
    "v_cvt_pk_bf16_f32 v141, v222, v223\n"
    "v_mfma_f32_16x16x32_bf16 v[216:219], v[200:203], a[24:27], v[216:219]\n"
    "buffer_load_dwordx4 v254, s[36:39], s44 offen offset:128 nt lds\n"
    "v_mfma_f32_16x16x32_bf16 v[216:219], v[204:207], a[28:31], v[216:219]\n"
    "s_waitcnt lgkmcnt(0)\n"
    "v_mfma_f32_16x16x32_bf16 a[200:203], v[212:215], v[144:147], a[200:203]\n"
    "v_mfma_f32_16x16x32_bf16 a[192:195], v[208:211], v[144:147], a[192:195]\n"
    "s_add_u32 m0, s47, 52992\n"
    "v_mfma_f32_16x16x32_bf16 a[196:199], v[208:211], v[148:151], a[196:199]\n"
    "buffer_load_dwordx4 v254, s[36:39], s44 offen offset:256 nt lds\n"
    "v_mfma_f32_16x16x32_bf16 a[204:207], v[212:215], v[148:151], a[204:207]\n"
    "s_nop 1\n"
    "v_max3_f32 v228, v228, v216, v217\n"
    "v_max3_f32 v228, v228, v218, v219\n"
    "v_mfma_f32_16x16x32_bf16 v[72:75], v[212:215], v[152:155], v[72:75]\n"
    "v_exp_f32 v216, v216\n"
    "v_mfma_f32_16x16x32_bf16 v[64:67], v[208:211], v[152:155], v[64:67]\n"
    "v_exp_f32 v217, v217\n"
    "v_mfma_f32_16x16x32_bf16 v[68:71], v[208:211], v[156:159], v[68:71]\n"
    "s_nop 0\n"
    "ds_read_b64_tr_b16 v[208:209], v252 offset:16896\n"
    "ds_read_b64_tr_b16 v[210:211], v252 offset:20992\n"
    "v_mfma_f32_16x16x32_bf16 v[76:79], v[212:215], v[156:159], v[76:79]\n"
    "s_nop 0\n"
    "ds_read_b64_tr_b16 v[212:213], v253 offset:16896\n"
    "ds_read_b64_tr_b16 v[214:215], v253 offset:20992\n"
    "v_mfma_f32_16x16x32_bf16 v[220:223], v[176:179], a[32:35], v[164:167]\n"
    "v_exp_f32 v218, v218\n"
    "v_mfma_f32_16x16x32_bf16 v[220:223], v[180:183], a[36:39], v[220:223]\n"
    "s_add_u32 m0, s47, 54912\n"
    "v_exp_f32 v219, v219\n"
    "v_mfma_f32_16x16x32_bf16 v[220:223], v[184:187], a[40:43], v[220:223]\n"
    "buffer_load_dwordx4 v254, s[36:39], s44 offen offset:384 nt lds\n"
    "v_mfma_f32_16x16x32_bf16 v[220:223], v[188:191], a[44:47], v[220:223]\n"
    "v_add_f32 v224, v224, v216\n"
    "v_add_f32 v224, v224, v217\n"
    "v_mfma_f32_16x16x32_bf16 v[220:223], v[192:195], a[48:51], v[220:223]\n"
    "v_cvt_pk_bf16_f32 v130, v216, v217\n"
    "v_add_f32 v224, v224, v218\n"
    "v_mfma_f32_16x16x32_bf16 v[220:223], v[196:199], a[52:55], v[220:223]\n"
    "v_add_f32 v224, v224, v219\n"
    "v_cvt_pk_bf16_f32 v131, v218, v219\n"
    "v_mfma_f32_16x16x32_bf16 v[220:223], v[200:203], a[56:59], v[220:223]\n"
    "v_mfma_f32_16x16x32_bf16 v[220:223], v[204:207], a[60:63], v[220:223]\n"
    "s_waitcnt lgkmcnt(0)\n"
    "v_mfma_f32_16x16x32_bf16 a[216:219], v[212:215], v[144:147], a[216:219]\n"
    "v_mfma_f32_16x16x32_bf16 a[208:211], v[208:211], v[144:147], a[208:211]\n"
    "s_add_u32 m0, s48, 0\n"
    "v_mfma_f32_16x16x32_bf16 a[212:215], v[208:211], v[148:151], a[212:215]\n"
    "buffer_load_dwordx4 v255, s[40:43], s45 offen offset:0 nt lds\n"
    "v_mfma_f32_16x16x32_bf16 a[220:223], v[212:215], v[148:151], a[220:223]\n"
    "s_nop 1\n"
    "v_max3_f32 v229, v229, v220, v221\n"
    "v_max3_f32 v229, v229, v222, v223\n"
    "v_mfma_f32_16x16x32_bf16 v[88:91], v[212:215], v[152:155], v[88:91]\n"
    "v_exp_f32 v220, v220\n"
    "v_mfma_f32_16x16x32_bf16 v[80:83], v[208:211], v[152:155], v[80:83]\n"
    "v_exp_f32 v221, v221\n"
    "v_mfma_f32_16x16x32_bf16 v[84:87], v[208:211], v[156:159], v[84:87]\n"
    "s_nop 0\n"
    "ds_read_b64_tr_b16 v[208:209], v252 offset:17408\n"
    "ds_read_b64_tr_b16 v[210:211], v252 offset:21504\n"
    "v_mfma_f32_16x16x32_bf16 v[92:95], v[212:215], v[156:159], v[92:95]\n"
    "s_nop 0\n"
    "ds_read_b64_tr_b16 v[212:213], v253 offset:17408\n"
    "ds_read_b64_tr_b16 v[214:215], v253 offset:21504\n"
    "v_mfma_f32_16x16x32_bf16 v[216:219], v[176:179], a[64:67], v[168:171]\n"
    "v_exp_f32 v222, v222\n"
    "v_mfma_f32_16x16x32_bf16 v[216:219], v[180:183], a[68:71], v[216:219]\n"
    "s_add_u32 m0, s48, 896\n"
    "v_exp_f32 v223, v223\n"
    "v_mfma_f32_16x16x32_bf16 v[216:219], v[184:187], a[72:75], v[216:219]\n"
    "buffer_load_dwordx4 v255, s[40:43], s45 offen offset:128 nt lds\n"
    "v_mfma_f32_16x16x32_bf16 v[216:219], v[188:191], a[76:79], v[216:219]\n"
    "v_add_f32 v225, v225, v220\n"
    "v_add_f32 v225, v225, v221\n"
    "v_mfma_f32_16x16x32_bf16 v[216:219], v[192:195], a[80:83], v[216:219]\n"
    "v_cvt_pk_bf16_f32 v134, v220, v221\n"
    "v_add_f32 v225, v225, v222\n"
    "v_mfma_f32_16x16x32_bf16 v[216:219], v[196:199], a[84:87], v[216:219]\n"
    "s_add_u32 m0, s48, 2048\n"
    "v_add_f32 v225, v225, v223\n"
    "v_cvt_pk_bf16_f32 v135, v222, v223\n"
    "v_mfma_f32_16x16x32_bf16 v[216:219], v[200:203], a[88:91], v[216:219]\n"
    "buffer_load_dwordx4 v255, s[40:43], s46 offen offset:0 nt lds\n"
    "v_mfma_f32_16x16x32_bf16 v[216:219], v[204:207], a[92:95], v[216:219]\n"
    "s_waitcnt lgkmcnt(0)\n"
    "v_mfma_f32_16x16x32_bf16 a[232:235], v[212:215], v[144:147], a[232:235]\n"
    "v_mfma_f32_16x16x32_bf16 a[224:227], v[208:211], v[144:147], a[224:227]\n"
    "s_add_u32 m0, s48, 2944\n"
    "v_mfma_f32_16x16x32_bf16 a[228:231], v[208:211], v[148:151], a[228:231]\n"
    "buffer_load_dwordx4 v255, s[40:43], s46 offen offset:128 nt lds\n"
    "v_mfma_f32_16x16x32_bf16 a[236:239], v[212:215], v[148:151], a[236:239]\n"
    "s_nop 1\n"
    "v_max3_f32 v230, v230, v216, v217\n"
    "v_max3_f32 v230, v230, v218, v219\n"
    "v_mfma_f32_16x16x32_bf16 v[104:107], v[212:215], v[152:155], v[104:107]\n"
    "v_exp_f32 v216, v216\n"
    "v_mfma_f32_16x16x32_bf16 v[96:99], v[208:211], v[152:155], v[96:99]\n"
    "v_exp_f32 v217, v217\n"
    "v_mfma_f32_16x16x32_bf16 v[100:103], v[208:211], v[156:159], v[100:103]\n"
    "s_nop 0\n"
    "ds_read_b64_tr_b16 v[208:209], v252 offset:17920\n"
    "ds_read_b64_tr_b16 v[210:211], v252 offset:22016\n"
    "v_mfma_f32_16x16x32_bf16 v[108:111], v[212:215], v[156:159], v[108:111]\n"
    "s_nop 0\n"
    "ds_read_b64_tr_b16 v[212:213], v253 offset:17920\n"
    "ds_read_b64_tr_b16 v[214:215], v253 offset:22016\n"
    "v_mfma_f32_16x16x32_bf16 v[220:223], v[176:179], a[96:99], v[172:175]\n"
    "v_exp_f32 v218, v218\n"
    "v_mfma_f32_16x16x32_bf16 v[220:223], v[180:183], a[100:103], v[220:223]\n"
    "v_exp_f32 v219, v219\n"
    "v_mfma_f32_16x16x32_bf16 v[220:223], v[184:187], a[104:107], v[220:223]\n"
    "s_add_u32 s44, s44, 8192\n"
    "ds_read_b128 v[176:179], v251 offset:16384\n"
    "v_add_f32 v226, v226, v216\n"
    "v_mfma_f32_16x16x32_bf16 v[220:223], v[188:191], a[108:111], v[220:223]\n"
    "s_add_u32 s45, s45, 256\n"
    "ds_read_b128 v[180:183], v251 offset:16896\n"
    "v_add_f32 v226, v226, v217\n"
    "v_mfma_f32_16x16x32_bf16 v[220:223], v[192:195], a[112:115], v[220:223]\n"
    "s_add_u32 s46, s46, 256\n"
    "ds_read_b128 v[184:187], v251 offset:18432\n"
    "v_cvt_pk_bf16_f32 v138, v216, v217\n"
    "v_mfma_f32_16x16x32_bf16 v[220:223], v[196:199], a[116:119], v[220:223]\n"
    "ds_read_b128 v[188:191], v251 offset:18944\n"
    "v_add_f32 v226, v226, v218\n"
    "v_mfma_f32_16x16x32_bf16 v[220:223], v[200:203], a[120:123], v[220:223]\n"
    "ds_read_b128 v[192:195], v251 offset:20480\n"
    "v_add_f32 v226, v226, v219\n"
    "v_mfma_f32_16x16x32_bf16 v[220:223], v[204:207], a[124:127], v[220:223]\n"
    "s_waitcnt lgkmcnt(5)\n"
    "ds_read_b128 v[196:199], v251 offset:20992\n"
    "v_cvt_pk_bf16_f32 v139, v218, v219\n"
    "v_mfma_f32_16x16x32_bf16 a[248:251], v[212:215], v[144:147], a[248:251]\n"
    "ds_read_b128 v[200:203], v251 offset:22528\n"
    "v_mfma_f32_16x16x32_bf16 a[240:243], v[208:211], v[144:147], a[240:243]\n"
    "ds_read_b128 v[204:207], v251 offset:23040\n"
    "v_mfma_f32_16x16x32_bf16 a[244:247], v[208:211], v[148:151], a[244:247]\n"
    "s_waitcnt vmcnt(13) lgkmcnt(8)\n"
    "s_barrier\n"
    "v_mfma_f32_16x16x32_bf16 a[252:255], v[212:215], v[148:151], a[252:255]\n"
    "s_nop 1\n"
    "v_max3_f32 v231, v231, v220, v221\n"
    "v_max3_f32 v231, v231, v222, v223\n"
    "v_mfma_f32_16x16x32_bf16 v[120:123], v[212:215], v[152:155], v[120:123]\n"
    "v_exp_f32 v220, v220\n"
    "v_mfma_f32_16x16x32_bf16 v[112:115], v[208:211], v[152:155], v[112:115]\n"
    "v_exp_f32 v221, v221\n"
    "v_mfma_f32_16x16x32_bf16 v[116:119], v[208:211], v[156:159], v[116:119]\n"
    "s_nop 0\n"
    "ds_read_b64_tr_b16 v[208:209], v252 offset:32768\n"
    "ds_read_b64_tr_b16 v[210:211], v252 offset:36864\n"
    "v_mfma_f32_16x16x32_bf16 v[124:127], v[212:215], v[156:159], v[124:127]\n"
    "s_waitcnt lgkmcnt(6)\n"
    "ds_read_b64_tr_b16 v[212:213], v253 offset:32768\n"
    "ds_read_b64_tr_b16 v[214:215], v253 offset:36864\n"
    "L_modd_%=:\n"
    "v_mfma_f32_16x16x32_bf16 v[216:219], v[176:179], a[0:3], v[160:163]\n"
    "v_exp_f32 v222, v222\n"
    "v_mfma_f32_16x16x32_bf16 v[216:219], v[180:183], a[4:7], v[216:219]\n"
    "s_add_u32 m0, s47, 0\n"
    "v_exp_f32 v223, v223\n"
    "v_mfma_f32_16x16x32_bf16 v[216:219], v[184:187], a[8:11], v[216:219]\n"
    "buffer_load_dwordx4 v254, s[36:39], s44 offen offset:0 nt lds\n"
    "v_mfma_f32_16x16x32_bf16 v[216:219], v[188:191], a[12:15], v[216:219]\n"
    "s_waitcnt lgkmcnt(4)\n"
    "v_add_f32 v227, v227, v220\n"
    "v_add_f32 v227, v227, v221\n"
    "v_mfma_f32_16x16x32_bf16 v[216:219], v[192:195], a[16:19], v[216:219]\n"
    "v_cvt_pk_bf16_f32 v142, v220, v221\n"
    "v_add_f32 v227, v227, v222\n"
    "v_mfma_f32_16x16x32_bf16 v[216:219], v[196:199], a[20:23], v[216:219]\n"
    "s_add_u32 m0, s47, 1920\n"
    "v_add_f32 v227, v227, v223\n"
    "v_cvt_pk_bf16_f32 v143, v222, v223\n"
    "v_mfma_f32_16x16x32_bf16 v[216:219], v[200:203], a[24:27], v[216:219]\n"
    "buffer_load_dwordx4 v254, s[36:39], s44 offen offset:128 nt lds\n"
    "v_mfma_f32_16x16x32_bf16 v[216:219], v[204:207], a[28:31], v[216:219]\n"
    "s_waitcnt lgkmcnt(0)\n"
    "v_mfma_f32_16x16x32_bf16 a[136:139], v[212:215], v[128:131], a[136:139]\n"
    "v_mfma_f32_16x16x32_bf16 a[128:131], v[208:211], v[128:131], a[128:131]\n"
    "s_add_u32 m0, s47, 3840\n"
    "v_mfma_f32_16x16x32_bf16 a[132:135], v[208:211], v[132:135], a[132:135]\n"
    "buffer_load_dwordx4 v254, s[36:39], s44 offen offset:256 nt lds\n"
    "v_mfma_f32_16x16x32_bf16 a[140:143], v[212:215], v[132:135], a[140:143]\n"
    "s_nop 1\n"
    "v_max3_f32 v228, v228, v216, v217\n"
    "v_max3_f32 v228, v228, v218, v219\n"
    "v_mfma_f32_16x16x32_bf16 v[8:11], v[212:215], v[136:139], v[8:11]\n"
    "v_exp_f32 v216, v216\n"
    "v_mfma_f32_16x16x32_bf16 v[0:3], v[208:211], v[136:139], v[0:3]\n"
    "v_exp_f32 v217, v217\n"
    "v_mfma_f32_16x16x32_bf16 v[4:7], v[208:211], v[140:143], v[4:7]\n"
    "s_nop 0\n"
    "ds_read_b64_tr_b16 v[208:209], v252 offset:33280\n"
    "ds_read_b64_tr_b16 v[210:211], v252 offset:37376\n"
    "v_mfma_f32_16x16x32_bf16 v[12:15], v[212:215], v[140:143], v[12:15]\n"
    "s_nop 0\n"
    "ds_read_b64_tr_b16 v[212:213], v253 offset:33280\n"
    "ds_read_b64_tr_b16 v[214:215], v253 offset:37376\n"
    "v_mfma_f32_16x16x32_bf16 v[220:223], v[176:179], a[32:35], v[164:167]\n"
    "v_exp_f32 v218, v218\n"
    "v_mfma_f32_16x16x32_bf16 v[220:223], v[180:183], a[36:39], v[220:223]\n"
    "s_add_u32 m0, s47, 5760\n"
    "v_exp_f32 v219, v219\n"
    "v_mfma_f32_16x16x32_bf16 v[220:223], v[184:187], a[40:43], v[220:223]\n"
    "buffer_load_dwordx4 v254, s[36:39], s44 offen offset:384 nt lds\n"
    "v_mfma_f32_16x16x32_bf16 v[220:223], v[188:191], a[44:47], v[220:223]\n"
    "v_add_f32 v224, v224, v216\n"
    "v_add_f32 v224, v224, v217\n"
    "v_mfma_f32_16x16x32_bf16 v[220:223], v[192:195], a[48:51], v[220:223]\n"
    "v_cvt_pk_bf16_f32 v144, v216, v217\n"
    "v_add_f32 v224, v224, v218\n"
    "v_mfma_f32_16x16x32_bf16 v[220:223], v[196:199], a[52:55], v[220:223]\n"
    "v_add_f32 v224, v224, v219\n"
    "v_cvt_pk_bf16_f32 v145, v218, v219\n"
    "v_mfma_f32_16x16x32_bf16 v[220:223], v[200:203], a[56:59], v[220:223]\n"
    "global_load_dword v247, v[248:249], off sc1\n"
    "v_mfma_f32_16x16x32_bf16 v[220:223], v[204:207], a[60:63], v[220:223]\n"
    "s_waitcnt lgkmcnt(0)\n"
    "v_mfma_f32_16x16x32_bf16 a[152:155], v[212:215], v[128:131], a[152:155]\n"
    "v_lshl_add_u64 v[248:249], v[248:249], 0, v[244:245]\n"
    "v_mfma_f32_16x16x32_bf16 a[144:147], v[208:211], v[128:131], a[144:147]\n"
    "s_add_u32 m0, s48, 16384\n"
    "v_mfma_f32_16x16x32_bf16 a[148:151], v[208:211], v[132:135], a[148:151]\n"
    "buffer_load_dwordx4 v255, s[40:43], s45 offen offset:0 nt lds\n"
    "v_mfma_f32_16x16x32_bf16 a[156:159], v[212:215], v[132:135], a[156:159]\n"
    "s_nop 1\n"
    "v_max3_f32 v229, v229, v220, v221\n"
    "v_max3_f32 v229, v229, v222, v223\n"
    "v_mfma_f32_16x16x32_bf16 v[24:27], v[212:215], v[136:139], v[24:27]\n"
    "v_exp_f32 v220, v220\n"
    "v_mfma_f32_16x16x32_bf16 v[16:19], v[208:211], v[136:139], v[16:19]\n"
    "v_exp_f32 v221, v221\n"
    "v_mfma_f32_16x16x32_bf16 v[20:23], v[208:211], v[140:143], v[20:23]\n"
    "s_nop 0\n"
    "ds_read_b64_tr_b16 v[208:209], v252 offset:33792\n"
    "ds_read_b64_tr_b16 v[210:211], v252 offset:37888\n"
    "v_mfma_f32_16x16x32_bf16 v[28:31], v[212:215], v[140:143], v[28:31]\n"
    "s_nop 0\n"
    "ds_read_b64_tr_b16 v[212:213], v253 offset:33792\n"
    "ds_read_b64_tr_b16 v[214:215], v253 offset:37888\n"
    "v_mfma_f32_16x16x32_bf16 v[216:219], v[176:179], a[64:67], v[168:171]\n"
    "v_exp_f32 v222, v222\n"
    "v_mfma_f32_16x16x32_bf16 v[216:219], v[180:183], a[68:71], v[216:219]\n"
    "s_add_u32 m0, s48, 17280\n"
    "v_exp_f32 v223, v223\n"
    "v_mfma_f32_16x16x32_bf16 v[216:219], v[184:187], a[72:75], v[216:219]\n"
    "buffer_load_dwordx4 v255, s[40:43], s45 offen offset:128 nt lds\n"
    "v_mfma_f32_16x16x32_bf16 v[216:219], v[188:191], a[76:79], v[216:219]\n"
    "v_add_f32 v225, v225, v220\n"
    "v_add_f32 v225, v225, v221\n"
    "v_mfma_f32_16x16x32_bf16 v[216:219], v[192:195], a[80:83], v[216:219]\n"
    "v_cvt_pk_bf16_f32 v148, v220, v221\n"
    "v_add_f32 v225, v225, v222\n"
    "v_mfma_f32_16x16x32_bf16 v[216:219], v[196:199], a[84:87], v[216:219]\n"
    "s_add_u32 m0, s48, 18432\n"
    "v_add_f32 v225, v225, v223\n"
    "v_cvt_pk_bf16_f32 v149, v222, v223\n"
    "v_mfma_f32_16x16x32_bf16 v[216:219], v[200:203], a[88:91], v[216:219]\n"
    "buffer_load_dwordx4 v255, s[40:43], s46 offen offset:0 nt lds\n"
    "v_mfma_f32_16x16x32_bf16 v[216:219], v[204:207], a[92:95], v[216:219]\n"
    "s_waitcnt lgkmcnt(0)\n"
    "v_mfma_f32_16x16x32_bf16 a[168:171], v[212:215], v[128:131], a[168:171]\n"
    "v_mfma_f32_16x16x32_bf16 a[160:163], v[208:211], v[128:131], a[160:163]\n"
    "s_add_u32 m0, s48, 19328\n"
    "v_mfma_f32_16x16x32_bf16 a[164:167], v[208:211], v[132:135], a[164:167]\n"
    "buffer_load_dwordx4 v255, s[40:43], s46 offen offset:128 nt lds\n"
    "v_mfma_f32_16x16x32_bf16 a[172:175], v[212:215], v[132:135], a[172:175]\n"
    "s_nop 1\n"
    "v_max3_f32 v230, v230, v216, v217\n"
    "v_max3_f32 v230, v230, v218, v219\n"
    "v_mfma_f32_16x16x32_bf16 v[40:43], v[212:215], v[136:139], v[40:43]\n"
    "v_exp_f32 v216, v216\n"
    "v_mfma_f32_16x16x32_bf16 v[32:35], v[208:211], v[136:139], v[32:35]\n"
    "v_exp_f32 v217, v217\n"
    "v_mfma_f32_16x16x32_bf16 v[36:39], v[208:211], v[140:143], v[36:39]\n"
    "s_nop 0\n"
    "ds_read_b64_tr_b16 v[208:209], v252 offset:34304\n"
    "ds_read_b64_tr_b16 v[210:211], v252 offset:38400\n"
    "v_mfma_f32_16x16x32_bf16 v[44:47], v[212:215], v[140:143], v[44:47]\n"
    "s_nop 0\n"
    "ds_read_b64_tr_b16 v[212:213], v253 offset:34304\n"
    "ds_read_b64_tr_b16 v[214:215], v253 offset:38400\n"
    "v_mfma_f32_16x16x32_bf16 v[220:223], v[176:179], a[96:99], v[172:175]\n"
    "v_exp_f32 v218, v218\n"
    "v_mfma_f32_16x16x32_bf16 v[220:223], v[180:183], a[100:103], v[220:223]\n"
    "v_exp_f32 v219, v219\n"
    "v_mfma_f32_16x16x32_bf16 v[220:223], v[184:187], a[104:107], v[220:223]\n"
    "s_add_u32 s44, s44, 24576\n"
    "ds_read_b128 v[176:179], v251 offset:32768\n"
    "v_add_f32 v226, v226, v216\n"
    "v_mfma_f32_16x16x32_bf16 v[220:223], v[188:191], a[108:111], v[220:223]\n"
    "s_add_u32 s45, s45, 32512\n"
    "ds_read_b128 v[180:183], v251 offset:33280\n"
    "v_add_f32 v226, v226, v217\n"
    "v_mfma_f32_16x16x32_bf16 v[220:223], v[192:195], a[112:115], v[220:223]\n"
    "s_add_u32 s46, s46, 32512\n"
    "ds_read_b128 v[184:187], v251 offset:34816\n"
    "v_cvt_pk_bf16_f32 v152, v216, v217\n"
    "v_mfma_f32_16x16x32_bf16 v[220:223], v[196:199], a[116:119], v[220:223]\n"
    "ds_read_b128 v[188:191], v251 offset:35328\n"
    "v_add_f32 v226, v226, v218\n"
    "v_mfma_f32_16x16x32_bf16 v[220:223], v[200:203], a[120:123], v[220:223]\n"
    "ds_read_b128 v[192:195], v251 offset:36864\n"
    "v_add_f32 v226, v226, v219\n"
    "v_mfma_f32_16x16x32_bf16 v[220:223], v[204:207], a[124:127], v[220:223]\n"
    "s_waitcnt lgkmcnt(5)\n"
    "ds_read_b128 v[196:199], v251 offset:37376\n"
    "v_cvt_pk_bf16_f32 v153, v218, v219\n"
    "v_mfma_f32_16x16x32_bf16 a[184:187], v[212:215], v[128:131], a[184:187]\n"
    "ds_read_b128 v[200:203], v251 offset:38912\n"
    "v_mfma_f32_16x16x32_bf16 a[176:179], v[208:211], v[128:131], a[176:179]\n"
    "ds_read_b128 v[204:207], v251 offset:39424\n"
    "v_mfma_f32_16x16x32_bf16 a[180:183], v[208:211], v[132:135], a[180:183]\n"
    "s_waitcnt vmcnt(13) lgkmcnt(8)\n"
    "s_barrier\n"
    "v_mfma_f32_16x16x32_bf16 a[188:191], v[212:215], v[132:135], a[188:191]\n"
    "s_nop 1\n"
    "v_max3_f32 v231, v231, v220, v221\n"
    "v_max3_f32 v231, v231, v222, v223\n"
    "v_mfma_f32_16x16x32_bf16 v[56:59], v[212:215], v[136:139], v[56:59]\n"
    "v_exp_f32 v220, v220\n"
    "v_mfma_f32_16x16x32_bf16 v[48:51], v[208:211], v[136:139], v[48:51]\n"
    "v_exp_f32 v221, v221\n"
    "v_mfma_f32_16x16x32_bf16 v[52:55], v[208:211], v[140:143], v[52:55]\n"
    "s_nop 0\n"
    "ds_read_b64_tr_b16 v[208:209], v252 offset:49152\n"
    "ds_read_b64_tr_b16 v[210:211], v252 offset:53248\n"
    "v_mfma_f32_16x16x32_bf16 v[60:63], v[212:215], v[140:143], v[60:63]\n"
    "s_waitcnt lgkmcnt(6)\n"
    "ds_read_b64_tr_b16 v[212:213], v253 offset:49152\n"
    "ds_read_b64_tr_b16 v[214:215], v253 offset:53248\n"
    "v_mfma_f32_16x16x32_bf16 v[216:219], v[176:179], a[0:3], v[160:163]\n"
    "v_exp_f32 v222, v222\n"
    "v_mfma_f32_16x16x32_bf16 v[216:219], v[180:183], a[4:7], v[216:219]\n"
    "s_add_u32 m0, s47, 16384\n"
    "v_exp_f32 v223, v223\n"
    "v_mfma_f32_16x16x32_bf16 v[216:219], v[184:187], a[8:11], v[216:219]\n"
    "buffer_load_dwordx4 v254, s[36:39], s44 offen offset:0 nt lds\n"
    "v_mfma_f32_16x16x32_bf16 v[216:219], v[188:191], a[12:15], v[216:219]\n"
    "s_waitcnt lgkmcnt(4)\n"
    "v_add_f32 v227, v227, v220\n"
    "v_add_f32 v227, v227, v221\n"
    "v_mfma_f32_16x16x32_bf16 v[216:219], v[192:195], a[16:19], v[216:219]\n"
    "v_cvt_pk_bf16_f32 v156, v220, v221\n"
    "v_add_f32 v227, v227, v222\n"
    "v_mfma_f32_16x16x32_bf16 v[216:219], v[196:199], a[20:23], v[216:219]\n"
    "s_add_u32 m0, s47, 18304\n"
    "v_add_f32 v227, v227, v223\n"
    "v_cvt_pk_bf16_f32 v157, v222, v223\n"
    "v_mfma_f32_16x16x32_bf16 v[216:219], v[200:203], a[24:27], v[216:219]\n"
    "buffer_load_dwordx4 v254, s[36:39], s44 offen offset:128 nt lds\n"
    "v_mfma_f32_16x16x32_bf16 v[216:219], v[204:207], a[28:31], v[216:219]\n"
    "s_waitcnt lgkmcnt(0)\n"
    "v_mfma_f32_16x16x32_bf16 a[200:203], v[212:215], v[128:131], a[200:203]\n"
    "v_mfma_f32_16x16x32_bf16 a[192:195], v[208:211], v[128:131], a[192:195]\n"
    "s_add_u32 m0, s47, 20224\n"
    "v_mfma_f32_16x16x32_bf16 a[196:199], v[208:211], v[132:135], a[196:199]\n"
    "buffer_load_dwordx4 v254, s[36:39], s44 offen offset:256 nt lds\n"
    "v_mfma_f32_16x16x32_bf16 a[204:207], v[212:215], v[132:135], a[204:207]\n"
    "s_nop 1\n"
    "v_max3_f32 v228, v228, v216, v217\n"
    "v_max3_f32 v228, v228, v218, v219\n"
    "v_mfma_f32_16x16x32_bf16 v[72:75], v[212:215], v[136:139], v[72:75]\n"
    "v_exp_f32 v216, v216\n"
    "v_mfma_f32_16x16x32_bf16 v[64:67], v[208:211], v[136:139], v[64:67]\n"
    "v_exp_f32 v217, v217\n"
    "v_mfma_f32_16x16x32_bf16 v[68:71], v[208:211], v[140:143], v[68:71]\n"
    "s_nop 0\n"
    "ds_read_b64_tr_b16 v[208:209], v252 offset:49664\n"
    "ds_read_b64_tr_b16 v[210:211], v252 offset:53760\n"
    "v_mfma_f32_16x16x32_bf16 v[76:79], v[212:215], v[140:143], v[76:79]\n"
    "s_nop 0\n"
    "ds_read_b64_tr_b16 v[212:213], v253 offset:49664\n"
    "ds_read_b64_tr_b16 v[214:215], v253 offset:53760\n"
    "v_mfma_f32_16x16x32_bf16 v[220:223], v[176:179], a[32:35], v[164:167]\n"
    "v_exp_f32 v218, v218\n"
    "v_mfma_f32_16x16x32_bf16 v[220:223], v[180:183], a[36:39], v[220:223]\n"
    "s_add_u32 m0, s47, 22144\n"
    "v_exp_f32 v219, v219\n"
    "v_mfma_f32_16x16x32_bf16 v[220:223], v[184:187], a[40:43], v[220:223]\n"
    "buffer_load_dwordx4 v254, s[36:39], s44 offen offset:384 nt lds\n"
    "v_mfma_f32_16x16x32_bf16 v[220:223], v[188:191], a[44:47], v[220:223]\n"
    "v_add_f32 v224, v224, v216\n"
    "v_add_f32 v224, v224, v217\n"
    "v_mfma_f32_16x16x32_bf16 v[220:223], v[192:195], a[48:51], v[220:223]\n"
    "v_cvt_pk_bf16_f32 v146, v216, v217\n"
    "v_add_f32 v224, v224, v218\n"
    "v_mfma_f32_16x16x32_bf16 v[220:223], v[196:199], a[52:55], v[220:223]\n"
    "v_add_f32 v224, v224, v219\n"
    "v_cvt_pk_bf16_f32 v147, v218, v219\n"
    "v_mfma_f32_16x16x32_bf16 v[220:223], v[200:203], a[56:59], v[220:223]\n"
    "v_mfma_f32_16x16x32_bf16 v[220:223], v[204:207], a[60:63], v[220:223]\n"
    "s_waitcnt lgkmcnt(0)\n"
    "v_mfma_f32_16x16x32_bf16 a[216:219], v[212:215], v[128:131], a[216:219]\n"
    "v_mfma_f32_16x16x32_bf16 a[208:211], v[208:211], v[128:131], a[208:211]\n"
    "s_add_u32 m0, s48, 32768\n"
    "v_mfma_f32_16x16x32_bf16 a[212:215], v[208:211], v[132:135], a[212:215]\n"
    "buffer_load_dwordx4 v255, s[40:43], s45 offen offset:0 nt lds\n"
    "v_mfma_f32_16x16x32_bf16 a[220:223], v[212:215], v[132:135], a[220:223]\n"
    "s_nop 1\n"
    "v_max3_f32 v229, v229, v220, v221\n"
    "v_max3_f32 v229, v229, v222, v223\n"
    "v_mfma_f32_16x16x32_bf16 v[88:91], v[212:215], v[136:139], v[88:91]\n"
    "v_exp_f32 v220, v220\n"
    "v_mfma_f32_16x16x32_bf16 v[80:83], v[208:211], v[136:139], v[80:83]\n"
    "v_exp_f32 v221, v221\n"
    "v_mfma_f32_16x16x32_bf16 v[84:87], v[208:211], v[140:143], v[84:87]\n"
    "s_nop 0\n"
    "ds_read_b64_tr_b16 v[208:209], v252 offset:50176\n"
    "ds_read_b64_tr_b16 v[210:211], v252 offset:54272\n"
    "v_mfma_f32_16x16x32_bf16 v[92:95], v[212:215], v[140:143], v[92:95]\n"
    "s_nop 0\n"
    "ds_read_b64_tr_b16 v[212:213], v253 offset:50176\n"
    "ds_read_b64_tr_b16 v[214:215], v253 offset:54272\n"
    "v_mfma_f32_16x16x32_bf16 v[216:219], v[176:179], a[64:67], v[168:171]\n"
    "v_exp_f32 v222, v222\n"
    "v_mfma_f32_16x16x32_bf16 v[216:219], v[180:183], a[68:71], v[216:219]\n"
    "s_add_u32 m0, s48, 33664\n"
    "v_exp_f32 v223, v223\n"
    "v_mfma_f32_16x16x32_bf16 v[216:219], v[184:187], a[72:75], v[216:219]\n"
    "buffer_load_dwordx4 v255, s[40:43], s45 offen offset:128 nt lds\n"
    "v_mfma_f32_16x16x32_bf16 v[216:219], v[188:191], a[76:79], v[216:219]\n"
    "v_add_f32 v225, v225, v220\n"
    "v_add_f32 v225, v225, v221\n"
    "v_mfma_f32_16x16x32_bf16 v[216:219], v[192:195], a[80:83], v[216:219]\n"
    "v_cvt_pk_bf16_f32 v150, v220, v221\n"
    "v_add_f32 v225, v225, v222\n"
    "v_mfma_f32_16x16x32_bf16 v[216:219], v[196:199], a[84:87], v[216:219]\n"
    "s_add_u32 m0, s48, 34816\n"
    "v_add_f32 v225, v225, v223\n"
    "v_cvt_pk_bf16_f32 v151, v222, v223\n"
    "v_mfma_f32_16x16x32_bf16 v[216:219], v[200:203], a[88:91], v[216:219]\n"
    "buffer_load_dwordx4 v255, s[40:43], s46 offen offset:0 nt lds\n"
    "v_mfma_f32_16x16x32_bf16 v[216:219], v[204:207], a[92:95], v[216:219]\n"
    "s_waitcnt lgkmcnt(0)\n"
    "v_mfma_f32_16x16x32_bf16 a[232:235], v[212:215], v[128:131], a[232:235]\n"
    "s_sub_u32 s49, s49, 1\n"
    "v_mfma_f32_16x16x32_bf16 a[224:227], v[208:211], v[128:131], a[224:227]\n"
    "s_add_u32 m0, s48, 35712\n"
    "v_mfma_f32_16x16x32_bf16 a[228:231], v[208:211], v[132:135], a[228:231]\n"
    "buffer_load_dwordx4 v255, s[40:43], s46 offen offset:128 nt lds\n"
    "v_mfma_f32_16x16x32_bf16 a[236:239], v[212:215], v[132:135], a[236:239]\n"
    "s_nop 1\n"
    "v_max3_f32 v230, v230, v216, v217\n"
    "v_max3_f32 v230, v230, v218, v219\n"
    "v_mfma_f32_16x16x32_bf16 v[104:107], v[212:215], v[136:139], v[104:107]\n"
    "v_exp_f32 v216, v216\n"
    "v_mfma_f32_16x16x32_bf16 v[96:99], v[208:211], v[136:139], v[96:99]\n"
    "v_exp_f32 v217, v217\n"
    "v_mfma_f32_16x16x32_bf16 v[100:103], v[208:211], v[140:143], v[100:103]\n"
    "s_nop 0\n"
    "ds_read_b64_tr_b16 v[208:209], v252 offset:50688\n"
    "ds_read_b64_tr_b16 v[210:211], v252 offset:54784\n"
    "v_mfma_f32_16x16x32_bf16 v[108:111], v[212:215], v[140:143], v[108:111]\n"
    "s_nop 0\n"
    "ds_read_b64_tr_b16 v[212:213], v253 offset:50688\n"
    "ds_read_b64_tr_b16 v[214:215], v253 offset:54784\n"
    "v_mfma_f32_16x16x32_bf16 v[220:223], v[176:179], a[96:99], v[172:175]\n"
    "v_exp_f32 v218, v218\n"
    "v_mfma_f32_16x16x32_bf16 v[220:223], v[180:183], a[100:103], v[220:223]\n"
    "v_exp_f32 v219, v219\n"
    "v_mfma_f32_16x16x32_bf16 v[220:223], v[184:187], a[104:107], v[220:223]\n"
    "s_add_u32 s44, s44, 8192\n"
    "ds_read_b128 v[176:179], v251 offset:49152\n"
    "v_add_f32 v226, v226, v216\n"
    "v_mfma_f32_16x16x32_bf16 v[220:223], v[188:191], a[108:111], v[220:223]\n"
    "s_add_u32 s45, s45, 256\n"
    "ds_read_b128 v[180:183], v251 offset:49664\n"
    "v_add_f32 v226, v226, v217\n"
    "v_mfma_f32_16x16x32_bf16 v[220:223], v[192:195], a[112:115], v[220:223]\n"
    "s_add_u32 s46, s46, 256\n"
    "ds_read_b128 v[184:187], v251 offset:51200\n"
    "v_cvt_pk_bf16_f32 v154, v216, v217\n"
    "v_mfma_f32_16x16x32_bf16 v[220:223], v[196:199], a[116:119], v[220:223]\n"
    "ds_read_b128 v[188:191], v251 offset:51712\n"
    "v_add_f32 v226, v226, v218\n"
    "v_mfma_f32_16x16x32_bf16 v[220:223], v[200:203], a[120:123], v[220:223]\n"
    "s_cmp_lg_u32 s49, 0\n"
    "ds_read_b128 v[192:195], v251 offset:53248\n"
    "v_add_f32 v226, v226, v219\n"
    "v_mfma_f32_16x16x32_bf16 v[220:223], v[204:207], a[124:127], v[220:223]\n"
    "s_waitcnt lgkmcnt(5)\n"
    "ds_read_b128 v[196:199], v251 offset:53760\n"
    "v_cvt_pk_bf16_f32 v155, v218, v219\n"
    "v_mfma_f32_16x16x32_bf16 a[248:251], v[212:215], v[128:131], a[248:251]\n"
    "ds_read_b128 v[200:203], v251 offset:55296\n"
    "v_mfma_f32_16x16x32_bf16 a[240:243], v[208:211], v[128:131], a[240:243]\n"
    "ds_read_b128 v[204:207], v251 offset:55808\n"
    "v_mfma_f32_16x16x32_bf16 a[244:247], v[208:211], v[132:135], a[244:247]\n"
    "s_waitcnt vmcnt(13) lgkmcnt(8)\n"
    "s_barrier\n"
    "v_mfma_f32_16x16x32_bf16 a[252:255], v[212:215], v[132:135], a[252:255]\n"
    "s_nop 1\n"
    "v_max3_f32 v231, v231, v220, v221\n"
    "v_max3_f32 v231, v231, v222, v223\n"
    "v_mfma_f32_16x16x32_bf16 v[120:123], v[212:215], v[136:139], v[120:123]\n"
    "v_exp_f32 v220, v220\n"
    "v_mfma_f32_16x16x32_bf16 v[112:115], v[208:211], v[136:139], v[112:115]\n"
    "v_exp_f32 v221, v221\n"
    "v_mfma_f32_16x16x32_bf16 v[116:119], v[208:211], v[140:143], v[116:119]\n"
    "s_nop 0\n"
    "ds_read_b64_tr_b16 v[208:209], v252 offset:0\n"
    "ds_read_b64_tr_b16 v[210:211], v252 offset:4096\n"
    "v_mfma_f32_16x16x32_bf16 v[124:127], v[212:215], v[140:143], v[124:127]\n"
    "s_waitcnt lgkmcnt(6)\n"
    "ds_read_b64_tr_b16 v[212:213], v253 offset:0\n"
    "ds_read_b64_tr_b16 v[214:215], v253 offset:4096\n"
    "s_cbranch_scc1 L_meven_%=\n"
    "s_mov_b32 s91, 2\n"
    "v_mov_b32 v248, s64\n"
    "v_mov_b32 v249, s65\n"
    "s_nop 7\n"
    "s_branch L_again_%=\n"
    "L_end_%=:\n"
    :: "s"(__builtin_amdgcn_kernarg_segment_ptr()), "s"(b), "s"(w)
    : "memory", "scc", "vcc", "v0","v1","v2","v3","v4","v5","v6","v7","v8","v9","v10","v11","v12","v13","v14","v15","v16","v17","v18","v19","v20","v21","v22","v23","v24","v25","v26","v27","v28","v29","v30","v31","v32","v33","v34","v35","v36","v37","v38","v39","v40","v41","v42","v43","v44","v45","v46","v47","v48","v49","v50","v51","v52","v53","v54","v55","v56","v57","v58","v59","v60","v61","v62","v63","v64","v65","v66","v67","v68","v69","v70","v71","v72","v73","v74","v75","v76","v77","v78","v79","v80","v81","v82","v83","v84","v85","v86","v87","v88","v89","v90","v91","v92","v93","v94","v95","v96","v97","v98","v99","v100","v101","v102","v103","v104","v105","v106","v107","v108","v109","v110","v111","v112","v113","v114","v115","v116","v117","v118","v119","v120","v121","v122","v123","v124","v125","v126","v127","v128","v129","v130","v131","v132","v133","v134","v135","v136","v137","v138","v139","v140","v141","v142","v143","v144","v145","v146","v147","v148","v149","v150","v151","v152","v153","v154","v155","v156","v157","v158","v159","v160","v161","v162","v163","v164","v165","v166","v167","v168","v169","v170","v171","v172","v173","v174","v175","v176","v177","v178","v179","v180","v181","v182","v183","v184","v185","v186","v187","v188","v189","v190","v191","v192","v193","v194","v195","v196","v197","v198","v199","v200","v201","v202","v203","v204","v205","v206","v207","v208","v209","v210","v211","v212","v213","v214","v215","v216","v217","v218","v219","v220","v221","v222","v223","v224","v225","v226","v227","v228","v229","v230","v231","v232","v233","v234","v235","v236","v237","v238","v239","v240","v241","v242","v243","v244","v245","v246","v247","v248","v249","v250","v251","v252","v253","v254","v255","a0","a1","a2","a3","a4","a5","a6","a7","a8","a9","a10","a11","a12","a13","a14","a15","a16","a17","a18","a19","a20","a21","a22","a23","a24","a25","a26","a27","a28","a29","a30","a31","a32","a33","a34","a35","a36","a37","a38","a39","a40","a41","a42","a43","a44","a45","a46","a47","a48","a49","a50","a51","a52","a53","a54","a55","a56","a57","a58","a59","a60","a61","a62","a63","a64","a65","a66","a67","a68","a69","a70","a71","a72","a73","a74","a75","a76","a77","a78","a79","a80","a81","a82","a83","a84","a85","a86","a87","a88","a89","a90","a91","a92","a93","a94","a95","a96","a97","a98","a99","a100","a101","a102","a103","a104","a105","a106","a107","a108","a109","a110","a111","a112","a113","a114","a115","a116","a117","a118","a119","a120","a121","a122","a123","a124","a125","a126","a127","a128","a129","a130","a131","a132","a133","a134","a135","a136","a137","a138","a139","a140","a141","a142","a143","a144","a145","a146","a147","a148","a149","a150","a151","a152","a153","a154","a155","a156","a157","a158","a159","a160","a161","a162","a163","a164","a165","a166","a167","a168","a169","a170","a171","a172","a173","a174","a175","a176","a177","a178","a179","a180","a181","a182","a183","a184","a185","a186","a187","a188","a189","a190","a191","a192","a193","a194","a195","a196","a197","a198","a199","a200","a201","a202","a203","a204","a205","a206","a207","a208","a209","a210","a211","a212","a213","a214","a215","a216","a217","a218","a219","a220","a221","a222","a223","a224","a225","a226","a227","a228","a229","a230","a231","a232","a233","a234","a235","a236","a237","a238","a239","a240","a241","a242","a243","a244","a245","a246","a247","a248","a249","a250","a251","a252","a253","a254","a255","s4","s5","s6","s7","s8","s9","s10","s11","s12","s13","s14","s15","s16","s17","s18","s19","s20","s21","s22","s23","s24","s25","s26","s27","s28","s29","s30","s31","s36","s37","s38","s39","s40","s41","s42","s43","s44","s45","s46","s47","s48","s49","s50","s51","s52","s53","s54","s55","s56","s57","s58","s59","s60","s61","s62","s63","s64","s65","s66","s67","s68","s69","s70","s71","s72","s73","s74","s75","s76","s77","s78","s79","s80","s81","s82","s83","s84","s85","s86","s87","s88","s89","s90","s91","s92","s93","s94","s95","s96","s97","s98","s99");
}

extern "C" void kernel_launch(void* const* d_in, const int* in_sizes, int n_in, void* d_out, int out_size, void* d_ws, size_t ws_size, hipStream_t stream) {
  char* ws = (char*)d_ws + (ws_size - 54525952);
  (void)hipFuncSetAttribute((const void*)fused_block, hipFuncAttributeMaxDynamicSharedMemorySize, 163840);
  fused_block<<<256, 256, 163840, stream>>>(d_in[0], d_in[1], d_in[2], d_in[3], d_in[4], d_in[6], d_in[7], d_in[8], d_in[9], d_in[10], ws, d_out);
}
```
